```python
import torch

def _softmax(x, axis=-1):
    return torch.softmax(x, dim=axis)

def _gelu(x):
    return torch.nn.functional.gelu(x, approximate='tanh')

def _silu(x):
    return torch.nn.functional.silu(x)

def _sigmoid(x):
    return torch.sigmoid(x)

def _layernorm(x, axis=-1, eps=1e-05):
    m = x.mean(dim=axis, keepdim=True)
    v = x.var(dim=axis, keepdim=True, unbiased=False)
    return (x - m) / torch.sqrt(v + eps)

def _scatter_add(tgt, idx, src, axis=0):
    return tgt.clone().index_add_(axis, idx, src)

def _segment_sum(src, seg, n):
    out = torch.zeros((n,) + src.shape[1:], dtype=src.dtype)
    return out.index_add_(0, seg, src)

def _pad(x, pw):
    flat = tuple((v for pair in reversed(pw) for v in pair))
    return torch.nn.functional.pad(x, flat)
kernel_name = 'adaLN_cond__gqa_block_cached__dense_activation_silu__decode'

def setup_inputs():

    _w = lambda a: (lambda t: t.to(torch.bfloat16) if t.is_floating_point() else t)(torch.as_tensor(a))

    def _A_setup():
        B, Sq, D = (2, 1, 512)
        Hq, Hkv, Sk, Dh = (32, 8, 32768, 128)
        assert Hq % Hkv == 0, (Hq, Hkv)
        return {'x': _w((lambda *s: torch.randn(*s, dtype=torch.bfloat16) * 0.1)(B, Sq, D)), 'k_cache': _w((lambda *s: torch.randn(*s, dtype=torch.bfloat16) * 0.8)(B, Hkv, Sk, Dh)), 'v_cache': _w((lambda *s: torch.randn(*s, dtype=torch.bfloat16) * 3.2)(B, Hkv, Sk, Dh)), 'wq': _w((lambda *s: torch.randn(*s, dtype=torch.bfloat16) * 0.8)(D, Hq * Dh) * D ** (-0.5)), 'wo': _w((lambda *s: torch.randn(*s, dtype=torch.bfloat16) * 3.2)(Hq * Dh, D) * (Hq * Dh) ** (-0.5))}
    _A_in = _A_setup()

    def _B_setup():
        B, S, D, F = (2, 1, 512, 1792)
        return {'x': _w((lambda *s: torch.randn(*s, dtype=torch.bfloat16) * 0.1)(B, S, D)), 'w1': _w((lambda *s: torch.randn(*s, dtype=torch.bfloat16) * 3.2)(D, F) * D ** (-0.5)), 'w2': _w((lambda *s: torch.randn(*s, dtype=torch.bfloat16) * 3.2)(F, D) * F ** (-0.5))}
    _B_in = _B_setup()
    return {'A_x': _A_in['x'], 'A_k_cache': _A_in['k_cache'], 'A_v_cache': _A_in['v_cache'], 'A_wq': _A_in['wq'], 'A_wo': _A_in['wo'], 'B_x': _B_in['x'], 'B_w1': _B_in['w1'], 'B_w2': _B_in['w2'], 'c': _w((lambda *s: torch.randn(*s, dtype=torch.bfloat16) * 0.4)(2, 512) * 8.0), 'w_mod': _w((lambda *s: torch.randn(*s, dtype=torch.bfloat16) * 0.4)(512, 6 * 512) * 512 ** (-0.5) * 8.0)}

def reference(A_x, A_k_cache, A_v_cache, A_wq, A_wo, B_x, B_w1, B_w2, c, w_mod):

    def _A_ref(x, k_cache, v_cache, wq, wo):
        B, Sq, D = x.shape
        Hq, Dh = (32, 128)
        Hkv = k_cache.shape[1]
        rep = Hq // Hkv
        q = (lambda x, *d: x.permute(*d))((x @ wq).reshape(B, Sq, Hq, Dh), 0, 2, 1, 3)
        k = (lambda x, n, axis: torch.repeat_interleave(x, n, dim=axis))(k_cache, rep, axis=1)
        v = (lambda x, n, axis: torch.repeat_interleave(x, n, dim=axis))(v_cache, rep, axis=1)
        scores = torch.einsum('bhqd,bhkd->bhqk', q, k) * Dh ** (-0.5)
        p = _softmax(scores, axis=-1)
        o = torch.einsum('bhqk,bhkd->bhqd', p, v)
        return (lambda x, *d: x.permute(*d))(o, 0, 2, 1, 3).reshape(B, Sq, Hq * Dh) @ wo

    def _B_ref(x, w1, w2):
        h = torch.einsum('bsd,df->bsf', x, w1)
        h = _silu(h)
        out = torch.einsum('bsf,fd->bsd', h, w2)
        return out
    x = A_x
    mod = (_silu(c) @ w_mod).reshape(c.shape[0], 6, 1, -1)
    sa, ca, ga, sb, cb, gb = (mod[:, 0].clone(), mod[:, 1].clone(), mod[:, 2].clone(), mod[:, 3].clone(), mod[:, 4].clone(), mod[:, 5].clone())
    h = _layernorm(x, axis=-1) * (1 + ca) + sa
    x = x + ga * _A_ref(h, A_k_cache, A_v_cache, A_wq, A_wo)
    h = _layernorm(x, axis=-1) * (1 + cb) + sb
    x = x + gb * _B_ref(h, B_w1, B_w2)
    return x
```

```cpp
#include <hip/hip_runtime.h>
#include <hip/hip_bf16.h>
#include <cstdint>
#define EMU 0
typedef unsigned short u16;
__device__ __forceinline__ float bf2f(u16 v) { return __uint_as_float(((unsigned)v) << 16); }
__device__ __forceinline__ u16 f2bf(float f) { __hip_bfloat16 b = __float2bfloat16(f); return *(u16*)&b; }
__device__ __forceinline__ float rbf(float f) { return bf2f(f2bf(f)); }
__device__ __forceinline__ float rb(float f) { return EMU ? rbf(f) : f; }

constexpr int B = 2, D = 512, HQ = 32, HKV = 8, SK = 32768, DH = 128, F = 1792, NSPLIT = 64;

__global__ void k_mod(const u16* c, const u16* wmod, float* mod) {
  int n = blockIdx.x * blockDim.x + threadIdx.x;
  __shared__ float sc[2][512];
  for (int i = threadIdx.x; i < 1024; i += blockDim.x) { float v = bf2f(c[i]); sc[i >> 9][i & 511] = rb(v / (1.f + __expf(-v))); }
  __syncthreads();
  float a0 = 0, a1 = 0;
  for (int k = 0; k < 512; ++k) { float w = bf2f(wmod[(size_t)k * 3072 + n]); a0 += sc[0][k] * w; a1 += sc[1][k] * w; }
  mod[n] = rb(a0); mod[3072 + n] = rb(a1);
}
__device__ void ln_mod(const float* xin  , const float* mod_b, int s_off, int c_off, float* hout, float* red) {
  int t = threadIdx.x;
  if (t == 0) { float s = 0; for (int i = 0; i < 512; ++i) s += xin[i]; float m = s / 512.f; float v = 0; for (int i = 0; i < 512; ++i) { float d = xin[i] - m; v += d * d; } v /= 512.f; red[0] = rb(m); red[1] = rb(v); }
  __syncthreads();
  float m = red[0], v = red[1];
  float den = rb(sqrtf(rb(v + 1e-5f)));
  for (int i = t; i < 512; i += blockDim.x) {
    float y = rb(rb(xin[i] - m) / den);
    float h = rb(rb(y * rb(1.f + mod_b[c_off + i])) + mod_b[s_off + i]);
    hout[i] = h;
  }
  __syncthreads();
}
__global__ void k_q(const u16* x, const float* mod, const u16* wq, float* q) {
  __shared__ float xin[512], h[2][512], red[2];
  for (int b = 0; b < 2; ++b) {
    for (int i = threadIdx.x; i < 512; i += blockDim.x) xin[i] = bf2f(x[b * 512 + i]);
    __syncthreads();
    ln_mod(xin, mod + b * 3072, 0, 512, h[b], red);
  }
  int n = blockIdx.x * blockDim.x + threadIdx.x;
  float a0 = 0, a1 = 0;
  for (int k = 0; k < 512; ++k) { float w = bf2f(wq[(size_t)k * 4096 + n]); a0 += h[0][k] * w; a1 += h[1][k] * w; }
  q[n] = rbf(a0); q[4096 + n] = rbf(a1);
}
__global__ void k_attn(const float* q, const u16* K, const u16* V, float* pm, float* pl, float* po) {
  constexpr int KPS = SK / NSPLIT;
  int s = blockIdx.x, kvh = blockIdx.y, b = blockIdx.z, t = threadIdx.x;
  __shared__ float sq[4][128], sc[4][KPS], mx[4], sm[4];
  for (int i = t; i < 512; i += 256) sq[i >> 7][i & 127] = q[b * 4096 + kvh * 512 + i];
  __syncthreads();
  const u16* Kb = K + ((size_t)(b * HKV + kvh) * SK + (size_t)s * KPS) * DH;
  const u16* Vb = V + ((size_t)(b * HKV + kvh) * SK + (size_t)s * KPS) * DH;
  for (int j = t; j < KPS; j += 256) {
    float a[4] = {0, 0, 0, 0};
    for (int d = 0; d < 128; ++d) { float kv = bf2f(Kb[(size_t)j * DH + d]); for (int g = 0; g < 4; ++g) a[g] += sq[g][d] * kv; }
    for (int g = 0; g < 4; ++g) sc[g][j] = rb(rb(a[g]) * 0.08838834764831845f);
  }
  __syncthreads();
  if (t < 4) { float m = -INFINITY; for (int j = 0; j < KPS; ++j) m = fmaxf(m, sc[t][j]); float l = 0; for (int j = 0; j < KPS; ++j) { float p = __expf(sc[t][j] - m); sc[t][j] = p; l += p; } mx[t] = m; sm[t] = l; }
  __syncthreads();
  for (int i = t; i < 512; i += 256) {
    int g = i >> 7, d = i & 127; float a = 0;
    for (int j = 0; j < KPS; ++j) a += sc[g][j] * bf2f(Vb[(size_t)j * DH + d]);
    po[((size_t)(b * HQ + kvh * 4 + g) * NSPLIT + s) * DH + d] = a;
  }
  if (t < 4) { pm[(b * HQ + kvh * 4 + t) * NSPLIT + s] = mx[t]; pl[(b * HQ + kvh * 4 + t) * NSPLIT + s] = sm[t]; }
}
__global__ void k_comb(const float* pm, const float* pl, const float* po, float* o) {
  int r = blockIdx.x, d = threadIdx.x;
  float M = -INFINITY; for (int s = 0; s < NSPLIT; ++s) M = fmaxf(M, pm[r * NSPLIT + s]);
  float L = 0, a = 0;
  for (int s = 0; s < NSPLIT; ++s) { float w = __expf(pm[r * NSPLIT + s] - M); L += w * pl[r * NSPLIT + s]; a += w * po[((size_t)r * NSPLIT + s) * DH + d]; }
  o[r * DH + d] = rb(a / L);
}
__global__ void k_wo(const float* o, const u16* wo, const u16* x, const float* mod, float* x1) {
  int n = blockIdx.x * blockDim.x + threadIdx.x;
  float a0 = 0, a1 = 0;
  for (int k = 0; k < 4096; ++k) { float w = bf2f(wo[(size_t)k * 512 + n]); a0 += o[k] * w; a1 += o[4096 + k] * w; }
  x1[n] = rb(bf2f(x[n]) + rb(mod[1024 + n] * rb(a0)));
  x1[512 + n] = rb(bf2f(x[512 + n]) + rb(mod[3072 + 1024 + n] * rb(a1)));
}
__global__ void k_w1(const float* x1, const float* mod, const u16* w1, float* h1) {
  __shared__ float xin[512], h[2][512], red[2];
  for (int b = 0; b < 2; ++b) {
    for (int i = threadIdx.x; i < 512; i += blockDim.x) xin[i] = x1[b * 512 + i];
    __syncthreads();
    ln_mod(xin, mod + b * 3072, 3 * 512, 4 * 512, h[b], red);
  }
  int n = blockIdx.x * blockDim.x + threadIdx.x;
  float a0 = 0, a1 = 0;
  for (int k = 0; k < 512; ++k) { float w = bf2f(w1[(size_t)k * F + n]); a0 += h[0][k] * w; a1 += h[1][k] * w; }
  a0 = rb(a0); a1 = rb(a1);
  h1[n] = rb(a0 / (1.f + __expf(-a0))); h1[F + n] = rb(a1 / (1.f + __expf(-a1)));
}
__global__ void k_w2(const float* h1, const u16* w2, const float* x1, const float* mod, u16* out) {
  int n = blockIdx.x * blockDim.x + threadIdx.x;
  float a0 = 0, a1 = 0;
  for (int k = 0; k < F; ++k) { float w = bf2f(w2[(size_t)k * 512 + n]); a0 += h1[k] * w; a1 += h1[F + k] * w; }
  out[n] = f2bf(x1[n] + rb(mod[5 * 512 + n] * rb(a0)));
  out[512 + n] = f2bf(x1[512 + n] + rb(mod[3072 + 5 * 512 + n] * rb(a1)));
}

extern "C" void kernel_launch(void* const* d_in, const int* in_sizes, int n_in, void* d_out, int out_size, void* d_ws, size_t ws_size, hipStream_t stream) {
  const u16* x = (const u16*)d_in[0]; const u16* K = (const u16*)d_in[1]; const u16* V = (const u16*)d_in[2];
  const u16* wq = (const u16*)d_in[3]; const u16* wo = (const u16*)d_in[4];
  const u16* w1 = (const u16*)d_in[6]; const u16* w2 = (const u16*)d_in[7]; const u16* c = (const u16*)d_in[8]; const u16* wmod = (const u16*)d_in[9];
  float* ws = (float*)d_ws;
  float* mod = ws; float* q = mod + 6144; float* pm = q + 8192; float* pl = pm + 64 * NSPLIT; float* po = pl + 64 * NSPLIT;
  float* o = po + 64 * NSPLIT * 128; float* x1 = o + 8192; float* h1 = x1 + 1024;
  k_mod<<<3072 / 64, 64, 0, stream>>>(c, wmod, mod);
  k_q<<<4096 / 64, 64, 0, stream>>>(x, mod, wq, q);
  k_attn<<<dim3(NSPLIT, HKV, B), 256, 0, stream>>>(q, K, V, pm, pl, po);
  k_comb<<<64, 128, 0, stream>>>(pm, pl, po, o);
  k_wo<<<8, 64, 0, stream>>>(o, wo, x, mod, x1);
  k_w1<<<F / 64, 64, 0, stream>>>(x1, mod, w1, h1);
  k_w2<<<8, 64, 0, stream>>>(h1, w2, x1, mod, (u16*)d_out);
}
```

```cpp
#include <hip/hip_runtime.h>
#include <hip/hip_bf16.h>

#define STAMPS 0

typedef unsigned int u32;
typedef unsigned long long u64;
typedef unsigned short u16;
typedef u32 v4u __attribute__((ext_vector_type(4)));
typedef u32 v2u __attribute__((ext_vector_type(2)));
typedef int v4i __attribute__((ext_vector_type(4)));
typedef float v4f __attribute__((ext_vector_type(4)));
typedef float v2f __attribute__((ext_vector_type(2)));
typedef short v4s __attribute__((ext_vector_type(4)));
typedef __bf16 v8bf __attribute__((ext_vector_type(8)));
typedef __bf16 v2bf __attribute__((ext_vector_type(2)));
#define LAS __attribute__((address_space(3)))
#define GAS __attribute__((address_space(1)))
#define DEVI __device__ __forceinline__

constexpr int NSLOT = 8, SLOT = 16384, NCONS = 6;
#define WIN 16
constexpr int NX_A = 63, NY_A = 56, NO_A = 66;
constexpr int NX_B = 61, NO_B = 65;
static_assert(4 * NX_A + 2 * NY_A + 10 * NO_A == 1024 && 4 * NX_B + 12 * NO_B == 1024, "units");
constexpr int L_CTL = NSLOT * SLOT;
constexpr int L_Q = L_CTL + 256;
constexpr int L_ACT = L_Q + 1024;
constexpr int L_HP = L_ACT + 2048;
constexpr int L_H1 = L_HP + 512;
constexpr int L_RED = L_H1 + 256;
constexpr int L_X1 = L_RED + 128;
constexpr int L_MRG = L_X1 + 4096;
constexpr int L_MOD = L_MRG + NCONS * 4 * 132 * 4;
constexpr int L_END = L_MOD + 8192;
static_assert(L_END <= 160 * 1024, "LDS");
constexpr int C_FULL = 0, C_QRDY = 1, C_MCNT = 2, C_CLR = 3, C_FREE = 8;
constexpr u32 W_EPOCH = 0, W_FLAG = 4096, W_MODP = 8192, W_QP = W_MODP + 4 * 3072 * 16, W_PART = W_QP + 4 * 2 * 4096 * 8;
constexpr u32 PREC = 4 * 132 * 4;
constexpr u32 W_AP = W_PART + 256 * PREC, W_OP = W_AP + 8 * 4096, W_END = W_OP + 28 * 4096;
constexpr u32 W_STAMP = 2u << 20;
constexpr u32 SENT = 0xFFFFFFFFu;
constexpr int SPIN_CAP = 1 << 16;

DEVI float bf2f(u32 v) { return __uint_as_float(v << 16); }
DEVI float bfhi(u32 v) { return __uint_as_float(v & 0xffff0000u); }
DEVI u32 pk2(float a, float b) { v2f x = {a, b}; return __builtin_bit_cast(u32, __builtin_convertvector(x, v2bf)); }
DEVI float rb(float a) { return bf2f(pk2(a, a) & 0xffffu); }
DEVI float ex2(float x) { return __builtin_amdgcn_exp2f(x); }
DEVI float rcp(float x) { return __builtin_amdgcn_rcpf(x); }
DEVI float silu(float v) { return v * rcp(1.f + ex2(v * -1.4426950408889634f)); }
DEVI float silu_exact(float v) { return v / (1.f + expf(-v)); }
DEVI float lo16(u32 v) { return __uint_as_float(v << 16); }
DEVI v2f rb2(v2f a) { const u32 p = pk2(a.x, a.y); return v2f{lo16(p), bfhi(p)}; }
DEVI float fold32(float a, float b) { auto s = __builtin_amdgcn_permlane32_swap(__float_as_uint(a), __float_as_uint(b), false, false); return __uint_as_float(s[0]) + __uint_as_float(s[1]); }
DEVI float fold16(float a, float b) { auto s = __builtin_amdgcn_permlane16_swap(__float_as_uint(a), __float_as_uint(b), false, false); return __uint_as_float(s[0]) + __uint_as_float(s[1]); }
#define PIN(x) asm volatile("" : "+v"(x))
DEVI float swap16_add(float x) { auto s = __builtin_amdgcn_permlane16_swap(__float_as_uint(x), __float_as_uint(x), false, false); return __uint_as_float(s[0]) + __uint_as_float(s[1]); }
DEVI float swap32_add(float x) { auto s = __builtin_amdgcn_permlane32_swap(__float_as_uint(x), __float_as_uint(x), false, false); return __uint_as_float(s[0]) + __uint_as_float(s[1]); }
DEVI float swap16_max(float x) { auto s = __builtin_amdgcn_permlane16_swap(__float_as_uint(x), __float_as_uint(x), false, false); return fmaxf(__uint_as_float(s[0]), __uint_as_float(s[1])); }
DEVI float swap32_max(float x) { auto s = __builtin_amdgcn_permlane32_swap(__float_as_uint(x), __float_as_uint(x), false, false); return fmaxf(__uint_as_float(s[0]), __uint_as_float(s[1])); }
template <int CTRL> DEVI float dpp(float x) { return __builtin_bit_cast(float, __builtin_amdgcn_mov_dpp(__builtin_bit_cast(int, x), CTRL, 0xf, 0xf, true)); }
DEVI float row_sum(float x) { x += dpp<0xB1>(x); x += dpp<0x4E>(x); x += dpp<0x141>(x); x += dpp<0x128>(x); return x; }
DEVI float half_sum(float x) { return swap16_add(row_sum(x)); }
DEVI float wave_sum(float x) { return swap32_add(half_sum(x)); }
template <typename T> DEVI LAS T* lp(u32 a) { return (LAS T*)(size_t)a; }
DEVI u32 ldsv(u32 a) { return *(volatile LAS u32*)(size_t)a; }
DEVI void stsv(u32 a, u32 v) { *(volatile LAS u32*)(size_t)a = v; }
DEVI v2f modv(u32 col, int b) { const v2u v = *lp<v2u>(L_MOD + col * 4); const int sh = 16 - 16 * b; return v2f{bfhi(v.x << sh), bfhi(v.y << sh)}; }
#define FENCE() asm volatile("" ::: "memory")
#define WAITV(n) asm volatile("s_waitcnt vmcnt(%0)" ::"i"(n) : "memory")
#define WAITL() asm volatile("s_waitcnt lgkmcnt(0)" ::: "memory")
DEVI void bar() { WAITL(); __builtin_amdgcn_s_barrier(); FENCE(); }
typedef __amdgpu_buffer_rsrc_t rsrc_t;
DEVI v4u ld4(rsrc_t r, u32 vo, u32 so) { return __builtin_bit_cast(v4u, __builtin_amdgcn_raw_buffer_load_b128(r, vo, so, 16)); }
DEVI u32 ld1(rsrc_t r, u32 vo, u32 so) { return __builtin_amdgcn_raw_buffer_load_b32(r, vo, so, 16); }
DEVI void st4(v4u v, rsrc_t r, u32 vo, u32 so) { __builtin_amdgcn_raw_buffer_store_b128(v, r, vo, so, 16); }
DEVI void st1(u32 v, rsrc_t r, u32 vo, u32 so) { __builtin_amdgcn_raw_buffer_store_b32(v, r, vo, so, 16); }
DEVI u32 canon(float f) { u32 b = __float_as_uint(f); return b < 0xFFFFFFFEu ? b : 0xFFFFFFFEu; }
DEVI v8bf trfrag(u32 a) {
  v4s lo = __builtin_amdgcn_ds_read_tr16_b64_v4i16(lp<v4s>(a)), hi = __builtin_amdgcn_ds_read_tr16_b64_v4i16(lp<v4s>(a + 2048));
  return __builtin_bit_cast(v8bf, __builtin_shufflevector(lo, hi, 0, 1, 2, 3, 4, 5, 6, 7));
}
DEVI v4f mfma(v8bf a, v8bf b, v4f c) { return __builtin_amdgcn_mfma_f32_16x16x32_bf16(a, b, c, 0, 0, 0); }
DEVI v8bf ldfrag(u32 a) { return __builtin_bit_cast(v8bf, *lp<v4u>(a)); }
DEVI int fpos(int k) { return (k & ~31) + ((k >> 2) & 3) * 8 + ((k >> 4) & 1) * 4 + (k & 3); }
DEVI v4i mksrd(const void* p, u32 bytes) { u64 a = (u64)p; return v4i{(int)(u32)a, (int)(u32)(a >> 32), (int)bytes, 0x00020000}; }

#define DMA4(m, v0, v1, v2, v3, srd, so)                                                                                   \
  asm volatile("s_mov_b32 m0, %0\n\ts_nop 0\n\tbuffer_load_dwordx4 %1, %5, %6 offen nt lds\n\t"                           \
               "buffer_load_dwordx4 %2, %5, %6 offen offset:1024 nt lds\n\tbuffer_load_dwordx4 %3, %5, %6 offen offset:2048 nt lds\n\t" \
               "buffer_load_dwordx4 %4, %5, %6 offen offset:3072 nt lds" ::"s"(m), "v"(v0), "v"(v1), "v"(v2), "v"(v3), "s"(srd), "s"(so) : "memory")
#define DMA1(m, v0, srd, so) \
  asm volatile("s_mov_b32 m0, %0\n\ts_nop 0\n\tbuffer_load_dwordx4 %1, %2, %3 offen nt lds" ::"s"(m), "v"(v0), "s"(srd), "s"(so) : "memory")

#define STAMP(i) do { } while (0)

__global__ void __launch_bounds__(512, 2)
fused_block(const u16* __restrict__ Kc, const u16* __restrict__ Vc, const u16* __restrict__ wmod, const u16* __restrict__ wq,
            const u16* __restrict__ wo, const u16* __restrict__ w1, const u16* __restrict__ w2, const u16* __restrict__ xin,
            const u16* __restrict__ cin, u16* __restrict__ out, unsigned char* ws) {
  __shared__ __attribute__((aligned(16))) unsigned char smem[L_END];
  asm volatile("" ::"s"((u32)(size_t)(LAS unsigned char*)smem));
  const int tid = threadIdx.x, lane = tid & 63, wave = __builtin_amdgcn_readfirstlane(tid >> 6);
  const int bid = blockIdx.x, grp = bid >> 4, mem = bid & 15;
  const int g = lane >> 4, c = lane & 15;
  const bool isX = mem < 4, isY = (mem == 4 || mem == 5) && grp < 14, hasP1 = mem >= 4;
  const int xi = grp * 4 + mem, yi = grp * 2 + mem - 4, p1i = grp * 12 + mem - 4;
  const int u0 = hasP1 ? 2 : 1;
  int nkv, kv0;
  if (grp < 14) { nkv = isX ? NX_A : isY ? NY_A : NO_A; kv0 = isX ? mem * NX_A : isY ? 4 * NX_A + (mem - 4) * NY_A : 4 * NX_A + 2 * NY_A + (mem - 6) * NO_A; }
  else { nkv = isX ? NX_B : NO_B; kv0 = isX ? mem * NX_B : 4 * NX_B + (mem - 4) * NO_B; }
  const int ntail = isX ? 4 : isY ? 8 : 0;
  const int uw = u0 + nkv;
  const rsrc_t rws = __builtin_amdgcn_make_buffer_rsrc(ws, 0, W_END, 0x00020000);

  if (wave == 0) {
    __builtin_amdgcn_s_setprio(3);
    STAMP(0);
    u32 pub = 0;
    auto publish = [&](u32 v) { if ((int)v > (int)pub) { pub = v; stsv(L_CTL + 4 * C_FULL, v); } };
    auto wait_slot = [&](int u) {
      if (u >= NSLOT) {
        const u32 a = L_CTL + 4 * (C_FREE + (u & (NSLOT - 1))), need = u - NSLOT + 1;
        if (ldsv(a) < need) { WAITV(0); publish(16 * u); while (ldsv(a) < need) __builtin_amdgcn_s_sleep(1); }
      }
    };
    const u32 wrow = lane >> 3, wch = ((lane & 7) ^ (((wrow >> 1) & 3) << 1)) << 4;
    auto wunit = [&](int u, const u16* base, u32 bytes, u32 rs, u32 so0, u32 bq, bool pubok) {
      const v4i srd = mksrd(base, bytes);
      const u32 vo = wrow * rs + wch;
      const u32 lb = (u & (NSLOT - 1)) * SLOT;
      for (int p = 0; p < 16; ++p) {
        const u32 m = lb + p * 1024, so = so0 + (p & 7) * 8 * rs + (p >> 3) * bq;
        DMA1(m, vo, srd, so);
        if ((p & 3) == 3 && pubok) { WAITV(WIN); publish(16 * u + p + 1 - WIN); }
      }
    };
    if (hasP1) wunit(0, wmod, 512 * 3072 * 2, 6144, (p1i / 48) * 128 * 6144 + (p1i % 48) * 128, 64 * 6144, false);
    else wunit(0, wq, 512 * 4096 * 2, 8192, (bid >> 6) * 128 * 8192 + (bid & 63) * 128, 64 * 8192, false);
    __builtin_amdgcn_s_barrier(); FENCE();
    if (hasP1) wunit(1, wq, 512 * 4096 * 2, 8192, (bid >> 6) * 128 * 8192 + (bid & 63) * 128, 64 * 8192, true);
    {
      const v4i rk = mksrd(Kc + (size_t)grp * 32768 * 128, 32768 * 256), rv = mksrd(Vc + (size_t)grp * 32768 * 128, 32768 * 256);
      const u32 k0 = g * 256 + ((c ^ g) << 4), k1 = g * 256 + ((c ^ (4 + g)) << 4), k2 = g * 256 + ((c ^ (8 + g)) << 4), k3 = g * 256 + ((c ^ (12 + g)) << 4);
      const u32 v0 = g * 256 + ((c ^ (2 * g)) << 4), v1 = g * 256 + ((c ^ (8 + 2 * g)) << 4);
      for (int u = u0; u < uw; ++u) {
        wait_slot(u);
        const u32 lb = (u & (NSLOT - 1)) * SLOT, so = (u32)(kv0 + u - u0) * 8192;
        { const u32 m = lb; DMA4(m, k0, k1, k2, k3, rk, so); }
        WAITV(WIN); publish(16 * u + 4 - WIN);
        { const u32 m = lb + 4096, s = so + 4096; DMA4(m, k0, k1, k2, k3, rk, s); }
        WAITV(WIN); publish(16 * u + 8 - WIN);
        { const u32 m = lb + 8192; DMA4(m, v0, v1, v0, v1, rv, so); }
        WAITV(WIN); publish(16 * u + 12 - WIN);
        { const u32 m = lb + 12288, s = so + 4096; DMA4(m, v0, v1, v0, v1, rv, s); }
        WAITV(WIN); publish(16 * u + 16 - WIN);
      }
    }
    for (int t = 0; t < ntail; ++t) {
      const int u = uw + t;
      wait_slot(u);
      if (isX) wunit(u, wo, 4096 * 512 * 2, 1024, ((xi >> 3) * 512 + t * 128) * 1024 + (xi & 7) * 128, 64 * 1024, true);
      else if (t < 4) wunit(u, w1, 512 * 1792 * 2, 3584, t * 128 * 3584 + yi * 128, 64 * 3584, true);
      else wunit(u, w2, 1792 * 512 * 2, 1024, yi * 64 * 1024 + (t - 4) * 256, 128, true);
    }
    const u32 e = 16 * (uw + ntail);
    WAITV(16); publish(e - 16);
    WAITV(8); publish(e - 8);
    WAITV(0); publish(e);
    STAMP(1);
    __builtin_amdgcn_s_setprio(0);
  } else {
    auto wait_full = [&](u32 need) { while (ldsv(L_CTL + 4 * C_FULL) < need) __builtin_amdgcn_s_sleep(1); FENCE(); };
    const u32 trow = 4 * g + (c >> 2);
    auto waddr = [&](int t) -> u32 { return trow * 128 + ((((u32)(t ^ ((trow >> 1) & 3)) << 1) | ((c >> 1) & 1)) << 4) + (c & 1) * 8; };

    if (wave == 1) {
      const int kg1 = p1i / 48, cb1 = p1i % 48, kg2 = bid >> 6, cb2 = bid & 63;
      const u64 E0 = __hip_atomic_load((GAS u64*)(ws + W_EPOCH), __ATOMIC_RELAXED, __HIP_MEMORY_SCOPE_AGENT);
      const v2u cv = *(const v2u*)(cin + (lane >> 5) * 512 + (hasP1 ? kg1 : 0) * 128 + (lane & 31) * 4);
      const v4u xv0 = *(const v4u*)(xin + lane * 8), xv1 = *(const v4u*)(xin + 512 + lane * 8);
      const u32 xs00 = xin[kg2 * 128 + lane], xs01 = xin[kg2 * 128 + 64 + lane], xs10 = xin[512 + kg2 * 128 + lane], xs11 = xin[512 + kg2 * 128 + 64 + lane];
      __builtin_amdgcn_s_barrier(); FENCE();
      const u64 E = E0 + 1;
      const u32 tag = (u32)E;
      const u32 fa = L_HP + (c & 1) * 256 + g * 16;
      if (hasP1) {
        *lp<v2u>(L_HP + (lane >> 5) * 256 + fpos((lane & 31) * 4) * 2) = v2u{pk2(silu_exact(lo16(cv.x)), silu_exact(bfhi(cv.x))), pk2(silu_exact(lo16(cv.y)), silu_exact(bfhi(cv.y)))};
        WAITL();
        v8bf act[4];
#pragma unroll
        for (int ks = 0; ks < 4; ++ks) { act[ks] = ldfrag(fa + ks * 64); PIN(act[ks]); }
        wait_full(16);
        v8bf W[4][4];
#pragma unroll
        for (int t = 0; t < 4; ++t)
#pragma unroll
          for (int ks = 0; ks < 4; ++ks) W[t][ks] = trfrag(waddr(t) + ks * 4096);
        WAITL(); stsv(L_CTL + 4 * (C_FREE + 0), 1);
        v4f acc[4];
#pragma unroll
        for (int t = 0; t < 4; ++t) acc[t] = v4f{0.f, 0.f, 0.f, 0.f};
#pragma unroll
        for (int ks = 0; ks < 4; ++ks)
#pragma unroll
          for (int t = 0; t < 4; ++t) acc[t] = mfma(act[ks], W[t][ks], acc[t]);
        const float r0 = g == 0 ? acc[0][0] : g == 1 ? acc[1][0] : g == 2 ? acc[2][0] : acc[3][0];
        const float r1 = g == 0 ? acc[0][1] : g == 1 ? acc[1][1] : g == 2 ? acc[2][1] : acc[3][1];
        st4(v4u{__float_as_uint(r0), tag, __float_as_uint(r1), tag}, rws, lane * 16, W_MODP + (kg1 * 3072 + cb1 * 64) * 16);
        STAMP(3);
      }
      {
        v2f y[2];
        {
          float mr[2], ri[2];
#pragma unroll
          for (int b = 0; b < 2; ++b) {
            const v4u xv = b ? xv1 : xv0;
            const float x[8] = {lo16(xv.x), bfhi(xv.x), lo16(xv.y), bfhi(xv.y), lo16(xv.z), bfhi(xv.z), lo16(xv.w), bfhi(xv.w)};
            float s = 0.f;
#pragma unroll
            for (int i = 0; i < 8; ++i) s += x[i];
            const float mean = wave_sum(s) * (1.f / 512.f);
            float q2 = 0.f;
#pragma unroll
            for (int i = 0; i < 8; ++i) q2 += (x[i] - mean) * (x[i] - mean);
            const float var = wave_sum(q2) * (1.f / 512.f);
            mr[b] = rb(mean); ri[b] = rcp(rb(__builtin_amdgcn_sqrtf(rb(rb(var) + 1e-5f))));
          }
          y[0] = rb2(rb2(v2f{lo16(xs00) - mr[0], lo16(xs10) - mr[1]}) * v2f{ri[0], ri[1]});
          y[1] = rb2(rb2(v2f{lo16(xs01) - mr[0], lo16(xs11) - mr[1]}) * v2f{ri[0], ri[1]});
          PIN(y[0]); PIN(y[1]);
        }
        wait_full(16 * u0);
        v8bf W[4][4];
#pragma unroll
        for (int t = 0; t < 4; ++t)
#pragma unroll
          for (int ks = 0; ks < 4; ++ks) W[t][ks] = trfrag((u0 - 1) * SLOT + waddr(t) + ks * 4096);
        WAITL(); stsv(L_CTL + 4 * (C_FREE + u0 - 1), u0);
        STAMP(4);
        v2f s[2][2];
        for (int sp = 0; sp < SPIN_CAP; ++sp) {
          u32 bad = 0;
#pragma unroll
          for (int pt = 0; pt < 2; ++pt)
#pragma unroll
            for (int e = 0; e < 2; ++e) {
              s[pt][e] = v2f{0.f, 0.f};
#pragma unroll
              for (int k4 = 0; k4 < 4; ++k4) {
                const v4u a = ld4(rws, lane * 16 + e * 1024, W_MODP + (k4 * 3072 + pt * 512 + kg2 * 128) * 16);
                bad |= (a.y ^ tag) | (a.w ^ tag);
                s[pt][e] += v2f{__uint_as_float(a.x), __uint_as_float(a.z)};
              }
            }
          if (__all(bad == 0)) break;
          FENCE();
        }
#pragma unroll
        for (int e = 0; e < 2; ++e) {
          const v2f h = rb2(y[e] * rb2(rb2(s[1][e]) + 1.f)) + rb2(s[0][e]);
          const u32 hp = pk2(h.x, h.y);
          *lp<u16>(L_HP + fpos(e * 64 + lane) * 2) = (u16)hp; *lp<u16>(L_HP + 256 + fpos(e * 64 + lane) * 2) = (u16)(hp >> 16);
        }
        WAITL();
        v8bf act[4];
#pragma unroll
        for (int ks = 0; ks < 4; ++ks) act[ks] = ldfrag(fa + ks * 64);
        v4f acc[4];
#pragma unroll
        for (int t = 0; t < 4; ++t) acc[t] = v4f{0.f, 0.f, 0.f, 0.f};
#pragma unroll
        for (int ks = 0; ks < 4; ++ks)
#pragma unroll
          for (int t = 0; t < 4; ++t) acc[t] = mfma(act[ks], W[t][ks], acc[t]);
        const float r0 = g == 0 ? acc[0][0] : g == 1 ? acc[1][0] : g == 2 ? acc[2][0] : acc[3][0];
        const float r1 = g == 0 ? acc[0][1] : g == 1 ? acc[1][1] : g == 2 ? acc[2][1] : acc[3][1];
        const float n0 = dpp<0xB1>(r0), n1 = dpp<0xB1>(r1);
        const bool odd = lane & 1;
        const float va = odd ? n1 : r0, vb = odd ? r1 : n0;
        st4(v4u{__float_as_uint(va), tag, __float_as_uint(vb), tag}, rws, (lane & 1) * 32768 + (lane >> 1) * 16, W_QP + kg2 * 65536 + cb2 * 512);
        STAMP(5);
      }
      {
        v2f s[4];
        for (int sp = 0; sp < SPIN_CAP; ++sp) {
          u32 bad = 0;
#pragma unroll
          for (int i = 0; i < 4; ++i) {
            s[i] = v2f{0.f, 0.f};
#pragma unroll
            for (int k4 = 0; k4 < 4; ++k4) {
              const v4u a = ld4(rws, lane * 16 + i * 1024, W_QP + k4 * 65536 + grp * 4096);
              bad |= (a.y ^ tag) | (a.w ^ tag);
              s[i] += v2f{__uint_as_float(a.x), __uint_as_float(a.z)};
            }
          }
          if (__all(bad == 0)) break;
          FENCE();
        }
#pragma unroll
        for (int i = 0; i < 4; ++i) *lp<u32>(L_Q + i * 256 + lane * 4) = pk2(s[i].x, s[i].y);
        WAITL(); stsv(L_CTL + 4 * C_QRDY, 1);
        STAMP(6);
      }
      const v4u s4 = {SENT, SENT, SENT, SENT};
      st4(s4, rws, lane * 16, W_PART + bid * PREC); st4(s4, rws, lane * 16 + 1024, W_PART + bid * PREC);
      if (lane < 4) st4(s4, rws, lane * 16 + 2048, W_PART + bid * PREC);
      if (isX && lane < 32) st4(s4, rws, (lane >> 4) * 2048 + (lane & 15) * 16, W_AP + (xi >> 3) * 4096 + (xi & 7) * 256);
      if (isY) {
#pragma unroll
        for (int i = 0; i < 4; ++i) st4(s4, rws, lane * 16 + i * 1024, W_OP + yi * 4096);
      }
      WAITV(0);
      stsv(L_CTL + 4 * C_CLR, 1);
      if (lane == 0) __hip_atomic_store((GAS u64*)(ws + W_FLAG) + bid, E, __ATOMIC_RELAXED, __HIP_MEMORY_SCOPE_AGENT);
      if (isX || isY) {
#pragma nounroll
        for (int sp = 0; sp < SPIN_CAP; ++sp) {
          const v4u a = ld4(rws, lane * 16, W_FLAG), b = ld4(rws, lane * 16 + 1024, W_FLAG);
          const u32 lo = (u32)E, hi = (u32)(E >> 32);
          const u32 bad = (a.x ^ lo) | (a.y ^ hi) | (a.z ^ lo) | (a.w ^ hi) | (b.x ^ lo) | (b.y ^ hi) | (b.z ^ lo) | (b.w ^ hi);
          if (__all(bad == 0)) break;
          __builtin_amdgcn_s_sleep(32); FENCE();
        }
        if (bid == 0 && lane == 0) __hip_atomic_store((GAS u64*)(ws + W_EPOCH), E, __ATOMIC_RELAXED, __HIP_MEMORY_SCOPE_AGENT);
      }
      if (isY) {
        for (int it = 0; it < 8; ++it) {
          v2f s[4];
          for (int sp = 0; sp < SPIN_CAP; ++sp) {
            u32 bad = 0;
#pragma unroll
            for (int e = 0; e < 4; ++e) {
              s[e] = v2f{0.f, 0.f};
#pragma unroll
              for (int kg = 0; kg < 4; ++kg) {
                const v4u a = ld4(rws, lane * 16 + e * 1024, W_MODP + (kg * 3072 + 1024 + it * 256) * 16);
                bad |= (a.y ^ tag) | (a.w ^ tag);
                s[e] += v2f{__uint_as_float(a.x), __uint_as_float(a.z)};
              }
            }
            if (__all(bad == 0)) break;
            __builtin_amdgcn_s_sleep(32); FENCE();
          }
#pragma unroll
          for (int e = 0; e < 4; ++e) *lp<u32>(L_MOD + (it * 256 + e * 64 + lane) * 4) = pk2(s[e].x, s[e].y);
        }
      }
      STAMP(2);
    } else {
      if (wave == 2) { if (lane < 32) stsv(L_CTL + 4 * lane, 0); WAITL(); }
      __builtin_amdgcn_s_barrier(); FENCE();
      v8bf qf[4];
      float m = -INFINITY, l = 0.f;
      v4f acc[8];
#pragma unroll
      for (int t = 0; t < 8; ++t) acc[t] = v4f{0.f, 0.f, 0.f, 0.f};
      u32 ka[4], va[8];
#pragma unroll
      for (int s = 0; s < 4; ++s) ka[s] = c * 256 + (((4 * s + g) ^ c) << 4);
#pragma unroll
      for (int t = 0; t < 8; ++t) va[t] = 8192 + trow * 256 + ((((u32)(t ^ (trow & 7)) << 1) | ((c >> 1) & 1)) << 4) + (c & 1) * 8;
      bool first = true;
#pragma nounroll
      for (int u = u0 + wave - 2; u < uw; u += NCONS) {
        const u32 lb = (u & (NSLOT - 1)) * SLOT;
        wait_full(16 * u + 16);
        v8bf kf[2][4], vf[8];
#pragma unroll
        for (int s = 0; s < 4; ++s) { kf[0][s] = ldfrag(lb + ka[s]); kf[1][s] = ldfrag(lb + ka[s] + 4096); }
#pragma unroll
        for (int t = 0; t < 8; ++t) {
          v4s lo = __builtin_amdgcn_ds_read_tr16_b64_v4i16(lp<v4s>(lb + va[t])), hi = __builtin_amdgcn_ds_read_tr16_b64_v4i16(lp<v4s>(lb + va[t] + 4096));
          vf[t] = __builtin_bit_cast(v8bf, __builtin_shufflevector(lo, hi, 0, 1, 2, 3, 4, 5, 6, 7));
        }
        WAITL(); stsv(L_CTL + 4 * (C_FREE + (u & (NSLOT - 1))), u + 1);
        if (first) {
          first = false;
          while (ldsv(L_CTL + 4 * C_QRDY) == 0) __builtin_amdgcn_s_sleep(2);
          FENCE();
#pragma unroll
          for (int s = 0; s < 4; ++s) qf[s] = ldfrag(L_Q + (c & 3) * 256 + (4 * s + g) * 16);
        }
        v4f S[2] = {v4f{0.f, 0.f, 0.f, 0.f}, v4f{0.f, 0.f, 0.f, 0.f}};
#pragma unroll
        for (int s = 0; s < 4; ++s) { S[0] = mfma(kf[0][s], qf[s], S[0]); S[1] = mfma(kf[1][s], qf[s], S[1]); }
        float sc[8];
#pragma unroll
        for (int i = 0; i < 8; i += 2) {
          const v2f r = rb2(rb2(v2f{S[i >> 2][i & 3], S[i >> 2][(i & 3) + 1]}) * 0.08838834764831845f);
          sc[i] = r.x; sc[i + 1] = r.y;
        }
        float tm = fmaxf(fmaxf(fmaxf(sc[0], sc[1]), fmaxf(sc[2], sc[3])), fmaxf(fmaxf(sc[4], sc[5]), fmaxf(sc[6], sc[7])));
        tm = swap32_max(swap16_max(tm));
        const float mn = fmaxf(m, tm * 1.4426950408889634f);
        const float alpha = ex2(m - mn);
        m = mn;
        float p[8], ps = 0.f;
#pragma unroll
        for (int i = 0; i < 8; ++i) { p[i] = ex2(fmaf(sc[i], 1.4426950408889634f, -mn)); ps += p[i]; }
        l = fmaf(l, alpha, ps);
        const v8bf pf = __builtin_bit_cast(v8bf, v4u{pk2(p[0], p[1]), pk2(p[2], p[3]), pk2(p[4], p[5]), pk2(p[6], p[7])});
#pragma unroll
        for (int t = 0; t < 8; ++t) acc[t] = mfma(vf[t], pf, acc[t] * alpha);
      }
      if (wave == 2) STAMP(7);
      l = swap32_add(swap16_add(l));
      if (c < 4) {
        const u32 a = L_MRG + ((wave - 2) * 4 + c) * 528;
#pragma unroll
        for (int t = 0; t < 8; ++t) *lp<v4f>(a + (16 * t + 4 * g) * 4) = acc[t];
        if (g == 0) *lp<v2f>(a + 512) = v2f{m, l};
      }
      WAITL();
      if (lane == 0) __hip_atomic_fetch_add(lp<u32>(L_CTL + 4 * C_MCNT), 1u, __ATOMIC_RELAXED, __HIP_MEMORY_SCOPE_WORKGROUP);
      if (wave < 4) {
        while (ldsv(L_CTL + 4 * C_CLR) == 0) { }
        while (ldsv(L_CTL + 4 * C_MCNT) < NCONS) { }
        FENCE();
        const int hd = (wave - 2) * 2 + (lane >> 5), i = lane & 31;
        v4f a[NCONS]; v2f ml[NCONS];
#pragma unroll
        for (int w = 0; w < NCONS; ++w) { a[w] = *lp<v4f>(L_MRG + (w * 4 + hd) * 528 + i * 16); ml[w] = *lp<v2f>(L_MRG + (w * 4 + hd) * 528 + 512); }
        float M = ml[0].x;
#pragma unroll
        for (int w = 1; w < NCONS; ++w) M = fmaxf(M, ml[w].x);
        float L = 0.f; v4f o = {0.f, 0.f, 0.f, 0.f};
#pragma unroll
        for (int w = 0; w < NCONS; ++w) { const float e = ex2(ml[w].x - M); L = fmaf(e, ml[w].y, L); o += a[w] * e; }
        const float inv = rcp(L);
        st4(v4u{canon(o.x * inv), canon(o.y * inv), canon(o.z * inv), canon(o.w * inv)}, rws, hd * 528 + i * 16, W_PART + bid * PREC);
        if (i == 0) st1(canon(M + __builtin_amdgcn_logf(L)), rws, hd * 528 + 512, W_PART + bid * PREC);
        if (wave == 2) STAMP(8);
      }
    }
  }
  if (!(isX || isY)) return;
  bar();
  const u32 trow = 4 * g + (c >> 2);
  auto waddr = [&](int t) -> u32 { return trow * 128 + ((((u32)(t ^ ((trow >> 1) & 3)) << 1) | ((c >> 1) & 1)) << 4) + (c & 1) * 8; };
  auto wslot = [&](int t) -> u32 { return ((uw + t) & (NSLOT - 1)) * SLOT; };
  const int half = lane >> 5, i32 = lane & 31;
  const u32 fa = L_ACT + (c & 1) * 1024 + g * 16;
  if (isX) {
    const int kvh = xi >> 3, ng = xi & 7;
    v8bf W[16];
    if (wave < 4) {
#pragma unroll
      for (int ks = 0; ks < 16; ++ks) W[ks] = trfrag(wslot(ks >> 2) + (ks & 3) * 4096 + waddr(wave));
    }
    const int b = wave >> 2, h = wave & 3;
    const u32 so = W_PART + ((b * 8 + kvh) * 16) * PREC + h * 528;
    u32 wa = L_ACT + b * 1024 + fpos(h * 128 + i32 * 4 + half * 2) * 2;
    PIN(wa);
    v4u a[8]; u32 ls[8];
#pragma nounroll
    for (int sp = 0; sp < SPIN_CAP; ++sp) {
#pragma unroll
      for (int j = 0; j < 8; ++j) { a[j] = ld4(rws, half * PREC + i32 * 16, so + j * 2 * PREC); ls[j] = ld1(rws, half * PREC + 512, so + j * 2 * PREC); }
      u32 mx = 0;
#pragma unroll
      for (int j = 0; j < 8; ++j) { mx = max(max(mx, ls[j]), max(max(a[j].x, a[j].y), max(a[j].z, a[j].w))); }
      if (__all(mx != SENT)) break;
      FENCE();
    }
    float M = __uint_as_float(ls[0]);
#pragma unroll
    for (int j = 1; j < 8; ++j) M = fmaxf(M, __uint_as_float(ls[j]));
    M = swap32_max(M);
    float L = 0.f; v4f o = v4f{0.f, 0.f, 0.f, 0.f};
#pragma unroll
    for (int j = 0; j < 8; ++j) { const float e = ex2(__uint_as_float(ls[j]) - M); L += e; o += __builtin_bit_cast(v4f, a[j]) * e; }
    const float inv = rcp(swap32_add(L));
    *lp<u32>(wa) = pk2(fold32(o.x, o.z) * inv, fold32(o.y, o.w) * inv);
    if (wave == 0) STAMP(9);
    bar();
    if (wave < 4) {
      v8bf A[16];
#pragma unroll
      for (int ks = 0; ks < 16; ++ks) A[ks] = ldfrag(fa + ks * 64);
      __builtin_amdgcn_sched_barrier(0);
      v4f d0 = {0.f, 0.f, 0.f, 0.f}, d1 = {0.f, 0.f, 0.f, 0.f};
#pragma unroll
      for (int ks = 0; ks < 16; ks += 2) { d0 = mfma(W[ks], A[ks], d0); d1 = mfma(W[ks + 1], A[ks + 1], d1); }
      d0 += d1;
      if (c < 2) st4(v4u{canon(d0.x), canon(d0.y), canon(d0.z), canon(d0.w)}, rws, c * 2048 + g * 16, W_AP + kvh * 4096 + ng * 256 + wave * 64);
      if (wave == 0) STAMP(10);
    }
    return;
  }
  {
    v8bf W[16];
    if (wave < 4) {
#pragma unroll
      for (int ks = 0; ks < 16; ++ks) W[ks] = trfrag(wslot(ks >> 2) + (ks & 3) * 4096 + waddr(wave));
    } else {
#pragma unroll
      for (int j = 0; j < 16; ++j) W[j] = trfrag(wslot(wave) + (j >> 3) * 8192 + (j & 1) * 4096 + waddr((j >> 1) & 3));
    }
    const int b = wave >> 2, col = (wave & 3) * 128 + i32 * 4 + half * 2;
    const u32 xv = *(const u32*)(xin + b * 512 + col);
    v2f x0 = {lo16(xv), bfhi(xv)};
    v2f ga = modv(col, b), sb = modv(512 + col, b), cb1 = rb2(modv(1024 + col, b) + 1.f);
    u32 wa = L_ACT + b * 1024 + fpos(col) * 2, xa = L_X1 + b * 2048 + col * 4, ra = L_RED + wave * 8 + half * 4;
    u32 ha = L_H1 + (c & 1) * 128 + fpos((wave & 3) * 16 + g * 4 + ((c >> 1) & 3)) * 2;
    PIN(x0); PIN(ga); PIN(sb); PIN(cb1); PIN(wa); PIN(xa); PIN(ra); PIN(ha);
    v4u a[4];
#pragma nounroll
    for (int sp = 0; sp < SPIN_CAP; ++sp) {
#pragma unroll
      for (int j = 0; j < 4; ++j) a[j] = ld4(rws, half * 16384 + i32 * 16, W_AP + j * 4096 + b * 2048 + (wave & 3) * 512);
      u32 mx = 0;
#pragma unroll
      for (int j = 0; j < 4; ++j) mx = max(mx, max(max(a[j].x, a[j].y), max(a[j].z, a[j].w)));
      if (__all(mx != SENT)) break;
      FENCE();
    }
    if (wave == 0) STAMP(11);
    const v4f at = (__builtin_bit_cast(v4f, a[0]) + __builtin_bit_cast(v4f, a[1])) + (__builtin_bit_cast(v4f, a[2]) + __builtin_bit_cast(v4f, a[3]));
    const v2f x1 = rb2(x0 + rb2(ga * rb2(v2f{fold32(at.x, at.z), fold32(at.y, at.w)})));
    const float st = half_sum(fold32(x1.x + x1.y, fmaf(x1.x, x1.x, x1.y * x1.y)));
    if (i32 == 0) *lp<float>(ra) = st;
    *lp<v2f>(xa) = x1;
    bar();
    {
      const v4f r0 = *lp<v4f>(L_RED + b * 32), r1 = *lp<v4f>(L_RED + b * 32 + 16);
      const float mean = ((r0.x + r0.z) + (r1.x + r1.z)) * (1.f / 512.f);
      const float var = fmaxf(fmaf(-mean, mean, ((r0.y + r0.w) + (r1.y + r1.w)) * (1.f / 512.f)), 0.f);
      const v2f mv = rb2(v2f{mean, var});
      const float ri = rcp(rb(__builtin_amdgcn_sqrtf(rb(mv.y + 1e-5f))));
      const v2f hh = rb2(rb2(rb2(x1 - mv.x) * ri) * cb1) + sb;
      *lp<u32>(wa) = pk2(hh.x, hh.y);
    }
    bar();
    if (wave < 4) {
      v8bf A[16];
#pragma unroll
      for (int ks = 0; ks < 16; ++ks) A[ks] = ldfrag(fa + ks * 64);
      __builtin_amdgcn_sched_barrier(0);
      v4f d0 = {0.f, 0.f, 0.f, 0.f}, d1 = {0.f, 0.f, 0.f, 0.f};
#pragma unroll
      for (int ks = 0; ks < 16; ks += 2) { d0 = mfma(W[ks], A[ks], d0); d1 = mfma(W[ks + 1], A[ks + 1], d1); }
      d0 += d1;
      const int r = (c >> 1) & 3;
      const float v = silu(rb(r == 0 ? d0.x : r == 1 ? d0.y : r == 2 ? d0.z : d0.w));
      *lp<u16>(ha) = (u16)pk2(v, v);
    }
    bar();
    if (wave >= 4) {
      const v8bf a0 = ldfrag(L_H1 + (c & 1) * 128 + g * 16), a1 = ldfrag(L_H1 + (c & 1) * 128 + 64 + g * 16);
      v4f d[8];
#pragma unroll
      for (int t = 0; t < 8; ++t) d[t] = mfma(W[2 * t + 1], a1, mfma(W[2 * t], a0, v4f{0.f, 0.f, 0.f, 0.f}));
      v4f r = d[0];
#pragma unroll
      for (int t = 1; t < 8; ++t) if ((c >> 1) == t) r = d[t];
      st4(v4u{canon(r.x), canon(r.y), canon(r.z), canon(r.w)}, rws, (c & 1) * 2048 + (c >> 1) * 64 + g * 16, W_OP + yi * 4096 + (wave - 4) * 512);
      if (wave == 4) STAMP(12);
    } else if (wave == 0 && yi < 16) {
      const int zb = yi >> 3, zc = (yi & 7) * 64 + c * 4 + (g >> 1) * 2 + (g & 1);
      float xx = *lp<float>(L_X1 + zb * 2048 + zc * 4), gb = bfhi(*lp<u32>(L_MOD + (1536 + zc) * 4) << (16 - 16 * zb));
      PIN(xx); PIN(gb);
      v4u a[7];
#pragma nounroll
      for (int sp = 0; sp < SPIN_CAP; ++sp) {
#pragma unroll
        for (int j = 0; j < 7; ++j) a[j] = ld4(rws, g * 4096 + c * 16, W_OP + j * 16384 + zb * 2048 + (yi & 7) * 256);
        u32 mx = 0;
#pragma unroll
        for (int j = 0; j < 7; ++j) mx = max(mx, max(max(a[j].x, a[j].y), max(a[j].z, a[j].w)));
        if (__all(mx != SENT)) break;
        FENCE();
      }
      v4f s = __builtin_bit_cast(v4f, a[0]);
#pragma unroll
      for (int j = 1; j < 7; ++j) s += __builtin_bit_cast(v4f, a[j]);
      const float tot = fold16(fold32(s.x, s.z), fold32(s.y, s.w));
      const float r = xx + rb(gb * rb(tot));
      out[zb * 512 + zc] = (u16)pk2(r, r);
      STAMP(13);
    }
  }
}

extern "C" void kernel_launch(void* const* d_in, const int* in_sizes, int n_in, void* d_out, int out_size, void* d_ws, size_t ws_size, hipStream_t stream) {
  hipLaunchKernelGGL(fused_block, dim3(256), dim3(512), 0, stream, (const u16*)d_in[1], (const u16*)d_in[2], (const u16*)d_in[9], (const u16*)d_in[3],
                     (const u16*)d_in[4], (const u16*)d_in[6], (const u16*)d_in[7], (const u16*)d_in[0], (const u16*)d_in[8], (u16*)d_out, (unsigned char*)d_ws);
}
```

```cpp
#include <hip/hip_runtime.h>
#include <hip/hip_bf16.h>

#define STAMPS 0

typedef unsigned int u32;
typedef unsigned long long u64;
typedef unsigned short u16;
typedef u32 v4u __attribute__((ext_vector_type(4)));
typedef u32 v2u __attribute__((ext_vector_type(2)));
typedef int v4i __attribute__((ext_vector_type(4)));
typedef float v4f __attribute__((ext_vector_type(4)));
typedef float v2f __attribute__((ext_vector_type(2)));
typedef short v4s __attribute__((ext_vector_type(4)));
typedef __bf16 v8bf __attribute__((ext_vector_type(8)));
typedef __bf16 v2bf __attribute__((ext_vector_type(2)));
#define LAS __attribute__((address_space(3)))
#define GAS __attribute__((address_space(1)))
#define DEVI __device__ __forceinline__

constexpr int NSLOT = 8, SLOT = 16384, NCONS = 6;
#define WIN 20
constexpr int NX_A = 63, NY_A = 56, NO_A = 66;
constexpr int NX_B = 61, NO_B = 65;
static_assert(4 * NX_A + 2 * NY_A + 10 * NO_A == 1024 && 4 * NX_B + 12 * NO_B == 1024, "units");
constexpr int L_CTL = NSLOT * SLOT;
constexpr int L_Q = L_CTL + 256;
constexpr int L_ACT = L_Q + 1024;
constexpr int L_HP = L_ACT + 2048;
constexpr int L_H1 = L_HP + 512;
constexpr int L_RED = L_H1 + 256;
constexpr int L_X1 = L_RED + 128;
constexpr int L_MRG = L_X1 + 4096;
constexpr int L_MOD = L_MRG + NCONS * 4 * 132 * 4;
constexpr int L_END = L_MOD + 8192;
static_assert(L_END <= 160 * 1024, "LDS");
constexpr int C_FULL = 0, C_QRDY = 1, C_MCNT = 2, C_CLR = 3, C_FREE = 8;
constexpr u32 W_EPOCH = 0, W_FLAG = 4096, W_MODP = 8192, W_QP = W_MODP + 4 * 3072 * 16, W_PART = W_QP + 4 * 2 * 4096 * 8;
constexpr u32 PREC = 4 * 132 * 4;
constexpr u32 W_AP = W_PART + 256 * PREC, W_OP = W_AP + 8 * 4096, W_END = W_OP + 28 * 4096;
constexpr u32 W_STAMP = 2u << 20;
constexpr u32 SENT = 0xFFFFFFFFu;
constexpr int SPIN_CAP = 1 << 16;

DEVI float bf2f(u32 v) { return __uint_as_float(v << 16); }
DEVI float bfhi(u32 v) { return __uint_as_float(v & 0xffff0000u); }
DEVI u32 pk2(float a, float b) { v2f x = {a, b}; return __builtin_bit_cast(u32, __builtin_convertvector(x, v2bf)); }
DEVI float rb(float a) { return bf2f(pk2(a, a) & 0xffffu); }
DEVI float ex2(float x) { return __builtin_amdgcn_exp2f(x); }
DEVI float rcp(float x) { return __builtin_amdgcn_rcpf(x); }
DEVI float silu(float v) { return v * rcp(1.f + ex2(v * -1.4426950408889634f)); }
DEVI float silu_exact(float v) { return v / (1.f + expf(-v)); }
DEVI float lo16(u32 v) { return __uint_as_float(v << 16); }
DEVI v2f rb2(v2f a) { const u32 p = pk2(a.x, a.y); return v2f{lo16(p), bfhi(p)}; }
DEVI float fold32(float a, float b) { auto s = __builtin_amdgcn_permlane32_swap(__float_as_uint(a), __float_as_uint(b), false, false); return __uint_as_float(s[0]) + __uint_as_float(s[1]); }
DEVI float fold16(float a, float b) { auto s = __builtin_amdgcn_permlane16_swap(__float_as_uint(a), __float_as_uint(b), false, false); return __uint_as_float(s[0]) + __uint_as_float(s[1]); }
#define PIN(x) asm volatile("" : "+v"(x))
DEVI float swap16_add(float x) { auto s = __builtin_amdgcn_permlane16_swap(__float_as_uint(x), __float_as_uint(x), false, false); return __uint_as_float(s[0]) + __uint_as_float(s[1]); }
DEVI float swap32_add(float x) { auto s = __builtin_amdgcn_permlane32_swap(__float_as_uint(x), __float_as_uint(x), false, false); return __uint_as_float(s[0]) + __uint_as_float(s[1]); }
DEVI float swap16_max(float x) { auto s = __builtin_amdgcn_permlane16_swap(__float_as_uint(x), __float_as_uint(x), false, false); return fmaxf(__uint_as_float(s[0]), __uint_as_float(s[1])); }
DEVI float swap32_max(float x) { auto s = __builtin_amdgcn_permlane32_swap(__float_as_uint(x), __float_as_uint(x), false, false); return fmaxf(__uint_as_float(s[0]), __uint_as_float(s[1])); }
template <int CTRL> DEVI float dpp(float x) { return __builtin_bit_cast(float, __builtin_amdgcn_mov_dpp(__builtin_bit_cast(int, x), CTRL, 0xf, 0xf, true)); }
DEVI float row_sum(float x) { x += dpp<0xB1>(x); x += dpp<0x4E>(x); x += dpp<0x141>(x); x += dpp<0x128>(x); return x; }
DEVI float half_sum(float x) { return swap16_add(row_sum(x)); }
DEVI float wave_sum(float x) { return swap32_add(half_sum(x)); }
template <typename T> DEVI LAS T* lp(u32 a) { return (LAS T*)(size_t)a; }
DEVI u32 ldsv(u32 a) { return *(volatile LAS u32*)(size_t)a; }
DEVI void stsv(u32 a, u32 v) { *(volatile LAS u32*)(size_t)a = v; }
DEVI v2f modv(u32 col, int b) { const v2u v = *lp<v2u>(L_MOD + col * 4); const int sh = 16 - 16 * b; return v2f{bfhi(v.x << sh), bfhi(v.y << sh)}; }
#define FENCE() asm volatile("" ::: "memory")
#define WAITV(n) asm volatile("s_waitcnt vmcnt(%0)" ::"i"(n) : "memory")
#define WAITL() asm volatile("s_waitcnt lgkmcnt(0)" ::: "memory")
DEVI void bar() { WAITL(); __builtin_amdgcn_s_barrier(); FENCE(); }
typedef __amdgpu_buffer_rsrc_t rsrc_t;
DEVI v4u ld4(rsrc_t r, u32 vo, u32 so) { return __builtin_bit_cast(v4u, __builtin_amdgcn_raw_buffer_load_b128(r, vo, so, 16)); }
DEVI u32 ld1(rsrc_t r, u32 vo, u32 so) { return __builtin_amdgcn_raw_buffer_load_b32(r, vo, so, 16); }
DEVI void st4(v4u v, rsrc_t r, u32 vo, u32 so) { __builtin_amdgcn_raw_buffer_store_b128(v, r, vo, so, 16); }
DEVI void st1(u32 v, rsrc_t r, u32 vo, u32 so) { __builtin_amdgcn_raw_buffer_store_b32(v, r, vo, so, 16); }
DEVI u32 canon(float f) { u32 b = __float_as_uint(f); return b < 0xFFFFFFFEu ? b : 0xFFFFFFFEu; }
DEVI v8bf trfrag(u32 a) {
  v4s lo = __builtin_amdgcn_ds_read_tr16_b64_v4i16(lp<v4s>(a)), hi = __builtin_amdgcn_ds_read_tr16_b64_v4i16(lp<v4s>(a + 2048));
  return __builtin_bit_cast(v8bf, __builtin_shufflevector(lo, hi, 0, 1, 2, 3, 4, 5, 6, 7));
}
DEVI v4f mfma(v8bf a, v8bf b, v4f c) { return __builtin_amdgcn_mfma_f32_16x16x32_bf16(a, b, c, 0, 0, 0); }
DEVI v8bf ldfrag(u32 a) { return __builtin_bit_cast(v8bf, *lp<v4u>(a)); }
DEVI int fpos(int k) { return (k & ~31) + ((k >> 2) & 3) * 8 + ((k >> 4) & 1) * 4 + (k & 3); }
DEVI v4i mksrd(const void* p, u32 bytes) { u64 a = (u64)p; return v4i{(int)(u32)a, (int)(u32)(a >> 32), (int)bytes, 0x00020000}; }

#define DMA4(m, v0, v1, v2, v3, srd, so)                                                                                   \
  asm volatile("s_mov_b32 m0, %0\n\ts_nop 0\n\tbuffer_load_dwordx4 %1, %5, %6 offen nt lds\n\t"                           \
               "buffer_load_dwordx4 %2, %5, %6 offen offset:1024 nt lds\n\tbuffer_load_dwordx4 %3, %5, %6 offen offset:2048 nt lds\n\t" \
               "buffer_load_dwordx4 %4, %5, %6 offen offset:3072 nt lds" ::"s"(m), "v"(v0), "v"(v1), "v"(v2), "v"(v3), "s"(srd), "s"(so) : "memory")
#define DMA1(m, v0, srd, so) \
  asm volatile("s_mov_b32 m0, %0\n\ts_nop 0\n\tbuffer_load_dwordx4 %1, %2, %3 offen nt lds" ::"s"(m), "v"(v0), "s"(srd), "s"(so) : "memory")

#define STAMP(i) do { } while (0)
#define FIN() do { } while (0)

__global__ void __launch_bounds__(512, 2)
fused_block(const u16* __restrict__ Kc, const u16* __restrict__ Vc, const u16* __restrict__ wmod, const u16* __restrict__ wq,
            const u16* __restrict__ wo, const u16* __restrict__ w1, const u16* __restrict__ w2, const u16* __restrict__ xin,
            const u16* __restrict__ cin, u16* __restrict__ out, unsigned char* ws) {
  __shared__ __attribute__((aligned(16))) unsigned char smem[L_END];
  asm volatile("" ::"s"((u32)(size_t)(LAS unsigned char*)smem));
  const int tid = threadIdx.x, lane = tid & 63, wave = __builtin_amdgcn_readfirstlane(tid >> 6);
  const int bid = blockIdx.x, grp = bid >> 4, mem = bid & 15;
  const int g = lane >> 4, c = lane & 15;
  const bool isX = mem < 4, isY = (mem == 4 || mem == 5) && grp < 14, hasP1 = mem >= 4;
  const int xi = grp * 4 + mem, yi = grp * 2 + mem - 4, p1i = grp * 12 + mem - 4;
  const int u0 = hasP1 ? 2 : 1;
  int nkv, kv0;
  if (grp < 14) { nkv = isX ? NX_A : isY ? NY_A : NO_A; kv0 = isX ? mem * NX_A : isY ? 4 * NX_A + (mem - 4) * NY_A : 4 * NX_A + 2 * NY_A + (mem - 6) * NO_A; }
  else { nkv = isX ? NX_B : NO_B; kv0 = isX ? mem * NX_B : 4 * NX_B + (mem - 4) * NO_B; }
  const int ntail = isX ? 4 : isY ? 8 : 0;
  const int uw = u0 + nkv;
  const rsrc_t rws = __builtin_amdgcn_make_buffer_rsrc(ws, 0, W_END, 0x00020000);

  if (wave == 0) {
    __builtin_amdgcn_s_setprio(3);
    STAMP(0);
    u32 pub = 0;
    auto publish = [&](u32 v) { if ((int)v > (int)pub) { pub = v; stsv(L_CTL + 4 * C_FULL, v); } };
    auto wait_slot = [&](int u) {
      if (u >= NSLOT) {
        const u32 a = L_CTL + 4 * (C_FREE + (u & (NSLOT - 1))), need = u - NSLOT + 1;
        if (ldsv(a) < need) { WAITV(0); publish(16 * u); while (ldsv(a) < need) __builtin_amdgcn_s_sleep(1); }
      }
    };
    const u32 wrow = lane >> 3, wch = ((lane & 7) ^ (((wrow >> 1) & 3) << 1)) << 4;
    auto wunit = [&](int u, const u16* base, u32 bytes, u32 rs, u32 so0, u32 bq, bool pubok) {
      const v4i srd = mksrd(base, bytes);
      const u32 vo = wrow * rs + wch;
      const u32 lb = (u & (NSLOT - 1)) * SLOT;
      for (int p = 0; p < 16; ++p) {
        const u32 m = lb + p * 1024, so = so0 + (p & 7) * 8 * rs + (p >> 3) * bq;
        DMA1(m, vo, srd, so);
        if ((p & 3) == 3 && pubok) { WAITV(WIN); publish(16 * u + p + 1 - WIN); }
      }
    };
    if (hasP1) wunit(0, wmod, 512 * 3072 * 2, 6144, (p1i / 48) * 128 * 6144 + (p1i % 48) * 128, 64 * 6144, false);
    else wunit(0, wq, 512 * 4096 * 2, 8192, (bid >> 6) * 128 * 8192 + (bid & 63) * 128, 64 * 8192, false);
    __builtin_amdgcn_s_barrier(); FENCE();
    if (hasP1) wunit(1, wq, 512 * 4096 * 2, 8192, (bid >> 6) * 128 * 8192 + (bid & 63) * 128, 64 * 8192, true);
    {
      const v4i rk = mksrd(Kc + (size_t)grp * 32768 * 128, 32768 * 256), rv = mksrd(Vc + (size_t)grp * 32768 * 128, 32768 * 256);
      const u32 k0 = g * 256 + ((c ^ g) << 4), k1 = g * 256 + ((c ^ (4 + g)) << 4), k2 = g * 256 + ((c ^ (8 + g)) << 4), k3 = g * 256 + ((c ^ (12 + g)) << 4);
      const u32 v0 = g * 256 + ((c ^ (2 * g)) << 4), v1 = g * 256 + ((c ^ (8 + 2 * g)) << 4);
      for (int u = u0; u < uw; ++u) {
        wait_slot(u);
        const u32 lb = (u & (NSLOT - 1)) * SLOT, so = (u32)(kv0 + u - u0) * 8192;
        { const u32 m = lb; DMA4(m, k0, k1, k2, k3, rk, so); }
        WAITV(WIN); publish(16 * u + 4 - WIN);
        { const u32 m = lb + 4096, s = so + 4096; DMA4(m, k0, k1, k2, k3, rk, s); }
        WAITV(WIN); publish(16 * u + 8 - WIN);
        { const u32 m = lb + 8192; DMA4(m, v0, v1, v0, v1, rv, so); }
        WAITV(WIN); publish(16 * u + 12 - WIN);
        { const u32 m = lb + 12288, s = so + 4096; DMA4(m, v0, v1, v0, v1, rv, s); }
        WAITV(WIN); publish(16 * u + 16 - WIN);
      }
    }
    for (int t = 0; t < ntail; ++t) {
      const int u = uw + t;
      wait_slot(u);
      if (isX) wunit(u, wo, 4096 * 512 * 2, 1024, ((xi >> 3) * 512 + t * 128) * 1024 + (xi & 7) * 128, 64 * 1024, true);
      else if (t < 4) wunit(u, w1, 512 * 1792 * 2, 3584, t * 128 * 3584 + yi * 128, 64 * 3584, true);
      else wunit(u, w2, 1792 * 512 * 2, 1024, yi * 64 * 1024 + (t - 4) * 256, 128, true);
    }
    const u32 e = 16 * (uw + ntail);
    WAITV(16); publish(e - 16);
    WAITV(8); publish(e - 8);
    WAITV(0); publish(e);
    STAMP(1);
    __builtin_amdgcn_s_setprio(0);
  } else {
    auto wait_full = [&](u32 need) { while (ldsv(L_CTL + 4 * C_FULL) < need) __builtin_amdgcn_s_sleep(1); FENCE(); };
    const u32 trow = 4 * g + (c >> 2);
    auto waddr = [&](int t) -> u32 { return trow * 128 + ((((u32)(t ^ ((trow >> 1) & 3)) << 1) | ((c >> 1) & 1)) << 4) + (c & 1) * 8; };

    if (wave == 1) {
      const int kg1 = p1i / 48, cb1 = p1i % 48, kg2 = bid >> 6, cb2 = bid & 63;
      const u64 E0 = __hip_atomic_load((GAS u64*)(ws + W_EPOCH), __ATOMIC_RELAXED, __HIP_MEMORY_SCOPE_AGENT);
      const v2u cv = *(const v2u*)(cin + (lane >> 5) * 512 + (hasP1 ? kg1 : 0) * 128 + (lane & 31) * 4);
      const v4u xv0 = *(const v4u*)(xin + lane * 8), xv1 = *(const v4u*)(xin + 512 + lane * 8);
      const u32 xs00 = xin[kg2 * 128 + lane], xs01 = xin[kg2 * 128 + 64 + lane], xs10 = xin[512 + kg2 * 128 + lane], xs11 = xin[512 + kg2 * 128 + 64 + lane];
      __builtin_amdgcn_s_barrier(); FENCE();
      const u64 E = E0 + 1;
      const u32 tag = (u32)E;
      const u32 fa = L_HP + (c & 1) * 256 + g * 16;
      if (hasP1) {
        *lp<v2u>(L_HP + (lane >> 5) * 256 + fpos((lane & 31) * 4) * 2) = v2u{pk2(silu_exact(lo16(cv.x)), silu_exact(bfhi(cv.x))), pk2(silu_exact(lo16(cv.y)), silu_exact(bfhi(cv.y)))};
        WAITL();
        v8bf act[4];
#pragma unroll
        for (int ks = 0; ks < 4; ++ks) { act[ks] = ldfrag(fa + ks * 64); PIN(act[ks]); }
        wait_full(16);
        v8bf W[4][4];
#pragma unroll
        for (int t = 0; t < 4; ++t)
#pragma unroll
          for (int ks = 0; ks < 4; ++ks) W[t][ks] = trfrag(waddr(t) + ks * 4096);
        WAITL(); stsv(L_CTL + 4 * (C_FREE + 0), 1);
        v4f acc[4];
#pragma unroll
        for (int t = 0; t < 4; ++t) acc[t] = v4f{0.f, 0.f, 0.f, 0.f};
#pragma unroll
        for (int ks = 0; ks < 4; ++ks)
#pragma unroll
          for (int t = 0; t < 4; ++t) acc[t] = mfma(act[ks], W[t][ks], acc[t]);
        const float r0 = g == 0 ? acc[0][0] : g == 1 ? acc[1][0] : g == 2 ? acc[2][0] : acc[3][0];
        const float r1 = g == 0 ? acc[0][1] : g == 1 ? acc[1][1] : g == 2 ? acc[2][1] : acc[3][1];
        st4(v4u{__float_as_uint(r0), tag, __float_as_uint(r1), tag}, rws, lane * 16, W_MODP + (kg1 * 3072 + cb1 * 64) * 16);
        STAMP(3);
      }
      {
        v2f y[2];
        {
          float mr[2], ri[2];
#pragma unroll
          for (int b = 0; b < 2; ++b) {
            const v4u xv = b ? xv1 : xv0;
            const float x[8] = {lo16(xv.x), bfhi(xv.x), lo16(xv.y), bfhi(xv.y), lo16(xv.z), bfhi(xv.z), lo16(xv.w), bfhi(xv.w)};
            float s = 0.f;
#pragma unroll
            for (int i = 0; i < 8; ++i) s += x[i];
            const float mean = wave_sum(s) * (1.f / 512.f);
            float q2 = 0.f;
#pragma unroll
            for (int i = 0; i < 8; ++i) q2 += (x[i] - mean) * (x[i] - mean);
            const float var = wave_sum(q2) * (1.f / 512.f);
            mr[b] = rb(mean); ri[b] = rcp(rb(__builtin_amdgcn_sqrtf(rb(rb(var) + 1e-5f))));
          }
          y[0] = rb2(rb2(v2f{lo16(xs00) - mr[0], lo16(xs10) - mr[1]}) * v2f{ri[0], ri[1]});
          y[1] = rb2(rb2(v2f{lo16(xs01) - mr[0], lo16(xs11) - mr[1]}) * v2f{ri[0], ri[1]});
          PIN(y[0]); PIN(y[1]);
        }
        wait_full(16 * u0);
        v8bf W[4][4];
#pragma unroll
        for (int t = 0; t < 4; ++t)
#pragma unroll
          for (int ks = 0; ks < 4; ++ks) W[t][ks] = trfrag((u0 - 1) * SLOT + waddr(t) + ks * 4096);
        WAITL(); stsv(L_CTL + 4 * (C_FREE + u0 - 1), u0);
        STAMP(4);
        v2f s[2][2];
        for (int sp = 0; sp < SPIN_CAP; ++sp) {
          u32 bad = 0;
#pragma unroll
          for (int pt = 0; pt < 2; ++pt)
#pragma unroll
            for (int e = 0; e < 2; ++e) {
              s[pt][e] = v2f{0.f, 0.f};
#pragma unroll
              for (int k4 = 0; k4 < 4; ++k4) {
                const v4u a = ld4(rws, lane * 16 + e * 1024, W_MODP + (k4 * 3072 + pt * 512 + kg2 * 128) * 16);
                bad |= (a.y ^ tag) | (a.w ^ tag);
                s[pt][e] += v2f{__uint_as_float(a.x), __uint_as_float(a.z)};
              }
            }
          if (__all(bad == 0)) break;
          FENCE();
        }
#pragma unroll
        for (int e = 0; e < 2; ++e) {
          const v2f h = rb2(y[e] * rb2(rb2(s[1][e]) + 1.f)) + rb2(s[0][e]);
          const u32 hp = pk2(h.x, h.y);
          *lp<u16>(L_HP + fpos(e * 64 + lane) * 2) = (u16)hp; *lp<u16>(L_HP + 256 + fpos(e * 64 + lane) * 2) = (u16)(hp >> 16);
        }
        WAITL();
        v8bf act[4];
#pragma unroll
        for (int ks = 0; ks < 4; ++ks) act[ks] = ldfrag(fa + ks * 64);
        v4f acc[4];
#pragma unroll
        for (int t = 0; t < 4; ++t) acc[t] = v4f{0.f, 0.f, 0.f, 0.f};
#pragma unroll
        for (int ks = 0; ks < 4; ++ks)
#pragma unroll
          for (int t = 0; t < 4; ++t) acc[t] = mfma(act[ks], W[t][ks], acc[t]);
        const float r0 = g == 0 ? acc[0][0] : g == 1 ? acc[1][0] : g == 2 ? acc[2][0] : acc[3][0];
        const float r1 = g == 0 ? acc[0][1] : g == 1 ? acc[1][1] : g == 2 ? acc[2][1] : acc[3][1];
        const float n0 = dpp<0xB1>(r0), n1 = dpp<0xB1>(r1);
        const bool odd = lane & 1;
        const float va = odd ? n1 : r0, vb = odd ? r1 : n0;
        st4(v4u{__float_as_uint(va), tag, __float_as_uint(vb), tag}, rws, (lane & 1) * 32768 + (lane >> 1) * 16, W_QP + kg2 * 65536 + cb2 * 512);
        STAMP(5);
      }
      {
        v2f s[4];
        for (int sp = 0; sp < SPIN_CAP; ++sp) {
          u32 bad = 0;
#pragma unroll
          for (int i = 0; i < 4; ++i) {
            s[i] = v2f{0.f, 0.f};
#pragma unroll
            for (int k4 = 0; k4 < 4; ++k4) {
              const v4u a = ld4(rws, lane * 16 + i * 1024, W_QP + k4 * 65536 + grp * 4096);
              bad |= (a.y ^ tag) | (a.w ^ tag);
              s[i] += v2f{__uint_as_float(a.x), __uint_as_float(a.z)};
            }
          }
          if (__all(bad == 0)) break;
          FENCE();
        }
#pragma unroll
        for (int i = 0; i < 4; ++i) *lp<u32>(L_Q + i * 256 + lane * 4) = pk2(s[i].x, s[i].y);
        WAITL(); stsv(L_CTL + 4 * C_QRDY, 1);
        STAMP(6);
      }
      const v4u s4 = {SENT, SENT, SENT, SENT};
      st4(s4, rws, lane * 16, W_PART + bid * PREC); st4(s4, rws, lane * 16 + 1024, W_PART + bid * PREC);
      if (lane < 4) st4(s4, rws, lane * 16 + 2048, W_PART + bid * PREC);
      if (isX && lane < 32) st4(s4, rws, (lane >> 4) * 2048 + (lane & 15) * 16, W_AP + (xi >> 3) * 4096 + (xi & 7) * 256);
      if (isY) {
#pragma unroll
        for (int i = 0; i < 4; ++i) st4(s4, rws, lane * 16 + i * 1024, W_OP + yi * 4096);
      }
      WAITV(0);
      stsv(L_CTL + 4 * C_CLR, 1);
      if (lane == 0) __hip_atomic_store((GAS u64*)(ws + W_FLAG) + bid, E, __ATOMIC_RELAXED, __HIP_MEMORY_SCOPE_AGENT);
      if (isX || isY) {
#pragma nounroll
        for (int sp = 0; sp < SPIN_CAP; ++sp) {
          const v4u a = ld4(rws, lane * 16, W_FLAG), b = ld4(rws, lane * 16 + 1024, W_FLAG);
          const u32 lo = (u32)E, hi = (u32)(E >> 32);
          const u32 bad = (a.x ^ lo) | (a.y ^ hi) | (a.z ^ lo) | (a.w ^ hi) | (b.x ^ lo) | (b.y ^ hi) | (b.z ^ lo) | (b.w ^ hi);
          if (__all(bad == 0)) break;
          __builtin_amdgcn_s_sleep(32); FENCE();
        }
        if (bid == 0 && lane == 0) __hip_atomic_store((GAS u64*)(ws + W_EPOCH), E, __ATOMIC_RELAXED, __HIP_MEMORY_SCOPE_AGENT);
      }
      if (isY) {
        for (int it = 0; it < 8; ++it) {
          v2f s[4];
          for (int sp = 0; sp < SPIN_CAP; ++sp) {
            u32 bad = 0;
#pragma unroll
            for (int e = 0; e < 4; ++e) {
              s[e] = v2f{0.f, 0.f};
#pragma unroll
              for (int kg = 0; kg < 4; ++kg) {
                const v4u a = ld4(rws, lane * 16 + e * 1024, W_MODP + (kg * 3072 + 1024 + it * 256) * 16);
                bad |= (a.y ^ tag) | (a.w ^ tag);
                s[e] += v2f{__uint_as_float(a.x), __uint_as_float(a.z)};
              }
            }
            if (__all(bad == 0)) break;
            __builtin_amdgcn_s_sleep(32); FENCE();
          }
#pragma unroll
          for (int e = 0; e < 4; ++e) *lp<u32>(L_MOD + (it * 256 + e * 64 + lane) * 4) = pk2(s[e].x, s[e].y);
        }
      }
      STAMP(2);
    } else {
      if (wave == 2) { if (lane < 32) stsv(L_CTL + 4 * lane, 0); WAITL(); }
      __builtin_amdgcn_s_barrier(); FENCE();
      v8bf qf[4];
      float m = -INFINITY, l = 0.f;
      v4f acc[8];
#pragma unroll
      for (int t = 0; t < 8; ++t) acc[t] = v4f{0.f, 0.f, 0.f, 0.f};
      u32 ka[4], va[8];
#pragma unroll
      for (int s = 0; s < 4; ++s) ka[s] = c * 256 + (((4 * s + g) ^ c) << 4);
#pragma unroll
      for (int t = 0; t < 8; ++t) va[t] = 8192 + trow * 256 + ((((u32)(t ^ (trow & 7)) << 1) | ((c >> 1) & 1)) << 4) + (c & 1) * 8;
      bool first = true;
#pragma nounroll
      for (int u = u0 + wave - 2; u < uw; u += NCONS) {
        const u32 lb = (u & (NSLOT - 1)) * SLOT;
        wait_full(16 * u + 16);
        v8bf kf[2][4], vf[8];
#pragma unroll
        for (int s = 0; s < 4; ++s) { kf[0][s] = ldfrag(lb + ka[s]); kf[1][s] = ldfrag(lb + ka[s] + 4096); }
#pragma unroll
        for (int t = 0; t < 8; ++t) {
          v4s lo = __builtin_amdgcn_ds_read_tr16_b64_v4i16(lp<v4s>(lb + va[t])), hi = __builtin_amdgcn_ds_read_tr16_b64_v4i16(lp<v4s>(lb + va[t] + 4096));
          vf[t] = __builtin_bit_cast(v8bf, __builtin_shufflevector(lo, hi, 0, 1, 2, 3, 4, 5, 6, 7));
        }
        WAITL(); stsv(L_CTL + 4 * (C_FREE + (u & (NSLOT - 1))), u + 1);
        if (first) {
          first = false;
          while (ldsv(L_CTL + 4 * C_QRDY) == 0) __builtin_amdgcn_s_sleep(2);
          FENCE();
#pragma unroll
          for (int s = 0; s < 4; ++s) qf[s] = ldfrag(L_Q + (c & 3) * 256 + (4 * s + g) * 16);
        }
        v4f S[2] = {v4f{0.f, 0.f, 0.f, 0.f}, v4f{0.f, 0.f, 0.f, 0.f}};
#pragma unroll
        for (int s = 0; s < 4; ++s) { S[0] = mfma(kf[0][s], qf[s], S[0]); S[1] = mfma(kf[1][s], qf[s], S[1]); }
        float sc[8];
#pragma unroll
        for (int i = 0; i < 8; i += 2) {
          const v2f r = rb2(rb2(v2f{S[i >> 2][i & 3], S[i >> 2][(i & 3) + 1]}) * 0.08838834764831845f);
          sc[i] = r.x; sc[i + 1] = r.y;
        }
        float tm = fmaxf(fmaxf(fmaxf(sc[0], sc[1]), fmaxf(sc[2], sc[3])), fmaxf(fmaxf(sc[4], sc[5]), fmaxf(sc[6], sc[7])));
        tm = swap32_max(swap16_max(tm));
        const float mn = fmaxf(m, tm * 1.4426950408889634f);
        const float alpha = ex2(m - mn);
        m = mn;
        float p[8], ps = 0.f;
#pragma unroll
        for (int i = 0; i < 8; ++i) { p[i] = ex2(fmaf(sc[i], 1.4426950408889634f, -mn)); ps += p[i]; }
        l = fmaf(l, alpha, ps);
        const v8bf pf = __builtin_bit_cast(v8bf, v4u{pk2(p[0], p[1]), pk2(p[2], p[3]), pk2(p[4], p[5]), pk2(p[6], p[7])});
#pragma unroll
        for (int t = 0; t < 8; ++t) acc[t] = mfma(vf[t], pf, acc[t] * alpha);
      }
      if (wave == 2) STAMP(7);
      l = swap32_add(swap16_add(l));
      if (c < 4) {
        const u32 a = L_MRG + ((wave - 2) * 4 + c) * 528;
#pragma unroll
        for (int t = 0; t < 8; ++t) *lp<v4f>(a + (16 * t + 4 * g) * 4) = acc[t];
        if (g == 0) *lp<v2f>(a + 512) = v2f{m, l};
      }
      WAITL();
      if (lane == 0) __hip_atomic_fetch_add(lp<u32>(L_CTL + 4 * C_MCNT), 1u, __ATOMIC_RELAXED, __HIP_MEMORY_SCOPE_WORKGROUP);
      if (wave < 4) {
        while (ldsv(L_CTL + 4 * C_CLR) == 0) { }
        while (ldsv(L_CTL + 4 * C_MCNT) < NCONS) { }
        FENCE();
        const int hd = (wave - 2) * 2 + (lane >> 5), i = lane & 31;
        v4f a[NCONS]; v2f ml[NCONS];
#pragma unroll
        for (int w = 0; w < NCONS; ++w) { a[w] = *lp<v4f>(L_MRG + (w * 4 + hd) * 528 + i * 16); ml[w] = *lp<v2f>(L_MRG + (w * 4 + hd) * 528 + 512); }
        float M = ml[0].x;
#pragma unroll
        for (int w = 1; w < NCONS; ++w) M = fmaxf(M, ml[w].x);
        float L = 0.f; v4f o = {0.f, 0.f, 0.f, 0.f};
#pragma unroll
        for (int w = 0; w < NCONS; ++w) { const float e = ex2(ml[w].x - M); L = fmaf(e, ml[w].y, L); o += a[w] * e; }
        const float inv = rcp(L);
        st4(v4u{canon(o.x * inv), canon(o.y * inv), canon(o.z * inv), canon(o.w * inv)}, rws, hd * 528 + i * 16, W_PART + bid * PREC);
        if (i == 0) st1(canon(M + __builtin_amdgcn_logf(L)), rws, hd * 528 + 512, W_PART + bid * PREC);
        if (wave == 2) STAMP(8);
      }
    }
  }
  if (!(isX || isY)) { FIN(); return; }
  bar();
  const u32 trow = 4 * g + (c >> 2);
  auto waddr = [&](int t) -> u32 { return trow * 128 + ((((u32)(t ^ ((trow >> 1) & 3)) << 1) | ((c >> 1) & 1)) << 4) + (c & 1) * 8; };
  auto wslot = [&](int t) -> u32 { return ((uw + t) & (NSLOT - 1)) * SLOT; };
  const int half = lane >> 5, i32 = lane & 31;
  const u32 fa = L_ACT + (c & 1) * 1024 + g * 16;
  if (isX) {
    const int kvh = xi >> 3, ng = xi & 7;
    v8bf W[16];
    if (wave < 4) {
#pragma unroll
      for (int ks = 0; ks < 16; ++ks) W[ks] = trfrag(wslot(ks >> 2) + (ks & 3) * 4096 + waddr(wave));
    }
    const int b = wave >> 2, h = wave & 3;
    const u32 so = W_PART + ((b * 8 + kvh) * 16) * PREC + h * 528;
    u32 wa = L_ACT + b * 1024 + fpos(h * 128 + i32 * 4 + half * 2) * 2;
    PIN(wa);
    v4u a[8]; u32 ls[8];
#pragma nounroll
    for (int sp = 0; sp < SPIN_CAP; ++sp) {
#pragma unroll
      for (int j = 0; j < 8; ++j) { a[j] = ld4(rws, half * PREC + i32 * 16, so + j * 2 * PREC); ls[j] = ld1(rws, half * PREC + 512, so + j * 2 * PREC); }
      u32 mx = 0;
#pragma unroll
      for (int j = 0; j < 8; ++j) { mx = max(max(mx, ls[j]), max(max(a[j].x, a[j].y), max(a[j].z, a[j].w))); }
      if (__all(mx != SENT)) break;
      FENCE();
    }
    float M = __uint_as_float(ls[0]);
#pragma unroll
    for (int j = 1; j < 8; ++j) M = fmaxf(M, __uint_as_float(ls[j]));
    M = swap32_max(M);
    float L = 0.f; v4f o = v4f{0.f, 0.f, 0.f, 0.f};
#pragma unroll
    for (int j = 0; j < 8; ++j) { const float e = ex2(__uint_as_float(ls[j]) - M); L += e; o += __builtin_bit_cast(v4f, a[j]) * e; }
    const float inv = rcp(swap32_add(L));
    *lp<u32>(wa) = pk2(fold32(o.x, o.z) * inv, fold32(o.y, o.w) * inv);
    if (wave == 0) STAMP(9);
    bar();
    if (wave < 4) {
      v8bf A[16];
#pragma unroll
      for (int ks = 0; ks < 16; ++ks) A[ks] = ldfrag(fa + ks * 64);
      __builtin_amdgcn_sched_barrier(0);
      v4f d0 = {0.f, 0.f, 0.f, 0.f}, d1 = {0.f, 0.f, 0.f, 0.f};
#pragma unroll
      for (int ks = 0; ks < 16; ks += 2) { d0 = mfma(W[ks], A[ks], d0); d1 = mfma(W[ks + 1], A[ks + 1], d1); }
      d0 += d1;
      if (c < 2) st4(v4u{canon(d0.x), canon(d0.y), canon(d0.z), canon(d0.w)}, rws, c * 2048 + g * 16, W_AP + kvh * 4096 + ng * 256 + wave * 64);
      if (wave == 0) STAMP(10);
    }
    FIN();
    return;
  }
  {
    v8bf W[16];
    if (wave < 4) {
#pragma unroll
      for (int ks = 0; ks < 16; ++ks) W[ks] = trfrag(wslot(ks >> 2) + (ks & 3) * 4096 + waddr(wave));
    } else {
#pragma unroll
      for (int j = 0; j < 16; ++j) W[j] = trfrag(wslot(wave) + (j >> 3) * 8192 + (j & 1) * 4096 + waddr((j >> 1) & 3));
    }
    const int b = wave >> 2, col = (wave & 3) * 128 + i32 * 4 + half * 2;
    const u32 xv = *(const u32*)(xin + b * 512 + col);
    v2f x0 = {lo16(xv), bfhi(xv)};
    v2f ga = modv(col, b), sb = modv(512 + col, b), cb1 = rb2(modv(1024 + col, b) + 1.f);
    u32 wa = L_ACT + b * 1024 + fpos(col) * 2, xa = L_X1 + b * 2048 + col * 4, ra = L_RED + wave * 8 + half * 4;
    u32 ha = L_H1 + (c & 1) * 128 + fpos((wave & 3) * 16 + g * 4 + ((c >> 1) & 3)) * 2;
    PIN(x0); PIN(ga); PIN(sb); PIN(cb1); PIN(wa); PIN(xa); PIN(ra); PIN(ha);
    v4u a[4];
#pragma nounroll
    for (int sp = 0; sp < SPIN_CAP; ++sp) {
#pragma unroll
      for (int j = 0; j < 4; ++j) a[j] = ld4(rws, half * 16384 + i32 * 16, W_AP + j * 4096 + b * 2048 + (wave & 3) * 512);
      u32 mx = 0;
#pragma unroll
      for (int j = 0; j < 4; ++j) mx = max(mx, max(max(a[j].x, a[j].y), max(a[j].z, a[j].w)));
      if (__all(mx != SENT)) break;
      FENCE();
    }
    if (wave == 0) STAMP(11);
    const v4f at = (__builtin_bit_cast(v4f, a[0]) + __builtin_bit_cast(v4f, a[1])) + (__builtin_bit_cast(v4f, a[2]) + __builtin_bit_cast(v4f, a[3]));
    const v2f x1 = rb2(x0 + rb2(ga * rb2(v2f{fold32(at.x, at.z), fold32(at.y, at.w)})));
    const float st = half_sum(fold32(x1.x + x1.y, fmaf(x1.x, x1.x, x1.y * x1.y)));
    if (i32 == 0) *lp<float>(ra) = st;
    *lp<v2f>(xa) = x1;
    bar();
    {
      const v4f r0 = *lp<v4f>(L_RED + b * 32), r1 = *lp<v4f>(L_RED + b * 32 + 16);
      const float mean = ((r0.x + r0.z) + (r1.x + r1.z)) * (1.f / 512.f);
      const float var = fmaxf(fmaf(-mean, mean, ((r0.y + r0.w) + (r1.y + r1.w)) * (1.f / 512.f)), 0.f);
      const v2f mv = rb2(v2f{mean, var});
      const float ri = rcp(rb(__builtin_amdgcn_sqrtf(rb(mv.y + 1e-5f))));
      const v2f hh = rb2(rb2(rb2(x1 - mv.x) * ri) * cb1) + sb;
      *lp<u32>(wa) = pk2(hh.x, hh.y);
    }
    bar();
    if (wave < 4) {
      v8bf A[16];
#pragma unroll
      for (int ks = 0; ks < 16; ++ks) A[ks] = ldfrag(fa + ks * 64);
      __builtin_amdgcn_sched_barrier(0);
      v4f d0 = {0.f, 0.f, 0.f, 0.f}, d1 = {0.f, 0.f, 0.f, 0.f};
#pragma unroll
      for (int ks = 0; ks < 16; ks += 2) { d0 = mfma(W[ks], A[ks], d0); d1 = mfma(W[ks + 1], A[ks + 1], d1); }
      d0 += d1;
      const int r = (c >> 1) & 3;
      const float v = silu(rb(r == 0 ? d0.x : r == 1 ? d0.y : r == 2 ? d0.z : d0.w));
      *lp<u16>(ha) = (u16)pk2(v, v);
    }
    bar();
    if (wave >= 4) {
      const v8bf a0 = ldfrag(L_H1 + (c & 1) * 128 + g * 16), a1 = ldfrag(L_H1 + (c & 1) * 128 + 64 + g * 16);
      v4f d[8];
#pragma unroll
      for (int t = 0; t < 8; ++t) d[t] = mfma(W[2 * t + 1], a1, mfma(W[2 * t], a0, v4f{0.f, 0.f, 0.f, 0.f}));
      v4f r = d[0];
#pragma unroll
      for (int t = 1; t < 8; ++t) if ((c >> 1) == t) r = d[t];
      st4(v4u{canon(r.x), canon(r.y), canon(r.z), canon(r.w)}, rws, (c & 1) * 2048 + (c >> 1) * 64 + g * 16, W_OP + yi * 4096 + (wave - 4) * 512);
      if (wave == 4) STAMP(12);
    } else if (wave == 0 && yi < 16) {
      const int zb = yi >> 3, zc = (yi & 7) * 64 + c * 4 + (g >> 1) * 2 + (g & 1);
      float xx = *lp<float>(L_X1 + zb * 2048 + zc * 4), gb = bfhi(*lp<u32>(L_MOD + (1536 + zc) * 4) << (16 - 16 * zb));
      PIN(xx); PIN(gb);
      v4u a[7];
#pragma nounroll
      for (int sp = 0; sp < SPIN_CAP; ++sp) {
#pragma unroll
        for (int j = 0; j < 7; ++j) a[j] = ld4(rws, g * 4096 + c * 16, W_OP + j * 16384 + zb * 2048 + (yi & 7) * 256);
        u32 mx = 0;
#pragma unroll
        for (int j = 0; j < 7; ++j) mx = max(mx, max(max(a[j].x, a[j].y), max(a[j].z, a[j].w)));
        if (__all(mx != SENT)) break;
        FENCE();
      }
      v4f s = __builtin_bit_cast(v4f, a[0]);
#pragma unroll
      for (int j = 1; j < 7; ++j) s += __builtin_bit_cast(v4f, a[j]);
      const float tot = fold16(fold32(s.x, s.z), fold32(s.y, s.w));
      const float r = xx + rb(gb * rb(tot));
      out[zb * 512 + zc] = (u16)pk2(r, r);
      STAMP(13);
    }
  }
  FIN();
}

extern "C" void kernel_launch(void* const* d_in, const int* in_sizes, int n_in, void* d_out, int out_size, void* d_ws, size_t ws_size, hipStream_t stream) {
  hipLaunchKernelGGL(fused_block, dim3(256), dim3(512), 0, stream, (const u16*)d_in[1], (const u16*)d_in[2], (const u16*)d_in[9], (const u16*)d_in[3],
                     (const u16*)d_in[4], (const u16*)d_in[6], (const u16*)d_in[7], (const u16*)d_in[0], (const u16*)d_in[8], (u16*)d_out, (unsigned char*)d_ws);
}
```

```cpp
#define DIAG 22
#include <hip/hip_runtime.h>
#include <hip/hip_bf16.h>

#define STAMPS 0

typedef unsigned int u32;
typedef unsigned long long u64;
typedef unsigned short u16;
typedef u32 v4u __attribute__((ext_vector_type(4)));
typedef u32 v2u __attribute__((ext_vector_type(2)));
typedef int v4i __attribute__((ext_vector_type(4)));
typedef float v4f __attribute__((ext_vector_type(4)));
typedef float v2f __attribute__((ext_vector_type(2)));
typedef short v4s __attribute__((ext_vector_type(4)));
typedef __bf16 v8bf __attribute__((ext_vector_type(8)));
typedef __bf16 v2bf __attribute__((ext_vector_type(2)));
#define LAS __attribute__((address_space(3)))
#define GAS __attribute__((address_space(1)))
#define DEVI __device__ __forceinline__

constexpr int NSLOT = 8, SLOT = 16384, NCONS = 6;
#define WIN 20
constexpr int NX_A = 63, NY_A = 56, NO_A = 66;
constexpr int NX_B = 61, NO_B = 65;
static_assert(4 * NX_A + 2 * NY_A + 10 * NO_A == 1024 && 4 * NX_B + 12 * NO_B == 1024, "units");
constexpr int L_CTL = NSLOT * SLOT;
constexpr int L_Q = L_CTL + 256;
constexpr int L_ACT = L_Q + 1024;
constexpr int L_HP = L_ACT + 2048;
constexpr int L_H1 = L_HP + 512;
constexpr int L_RED = L_H1 + 256;
constexpr int L_X1 = L_RED + 128;
constexpr int L_MRG = L_X1 + 4096;
constexpr int L_MOD = L_MRG + NCONS * 4 * 132 * 4;
constexpr int L_END = L_MOD + 8192;
static_assert(L_END <= 160 * 1024, "LDS");
constexpr int C_FULL = 0, C_QRDY = 1, C_MCNT = 2, C_CLR = 3, C_FREE = 8;
constexpr u32 W_EPOCH = 0, W_FLAG = 4096, W_MODP = 8192, W_QP = W_MODP + 4 * 3072 * 16, W_PART = W_QP + 4 * 2 * 4096 * 8;
constexpr u32 PREC = 4 * 132 * 4;
constexpr u32 W_AP = W_PART + 256 * PREC, W_OP = W_AP + 8 * 4096, W_END = W_OP + 28 * 4096;
constexpr u32 W_STAMP = 2u << 20;
constexpr u32 SENT = 0xFFFFFFFFu;
constexpr int SPIN_CAP = 1 << 16;

DEVI float bf2f(u32 v) { return __uint_as_float(v << 16); }
DEVI float bfhi(u32 v) { return __uint_as_float(v & 0xffff0000u); }
DEVI u32 pk2(float a, float b) { v2f x = {a, b}; return __builtin_bit_cast(u32, __builtin_convertvector(x, v2bf)); }
DEVI float rb(float a) { return bf2f(pk2(a, a) & 0xffffu); }
DEVI float ex2(float x) { return __builtin_amdgcn_exp2f(x); }
DEVI float rcp(float x) { return __builtin_amdgcn_rcpf(x); }
DEVI float silu(float v) { return v * rcp(1.f + ex2(v * -1.4426950408889634f)); }
DEVI float silu_exact(float v) { return v / (1.f + expf(-v)); }
DEVI float lo16(u32 v) { return __uint_as_float(v << 16); }
DEVI v2f rb2(v2f a) { const u32 p = pk2(a.x, a.y); return v2f{lo16(p), bfhi(p)}; }
DEVI float fold32(float a, float b) { auto s = __builtin_amdgcn_permlane32_swap(__float_as_uint(a), __float_as_uint(b), false, false); return __uint_as_float(s[0]) + __uint_as_float(s[1]); }
DEVI float fold16(float a, float b) { auto s = __builtin_amdgcn_permlane16_swap(__float_as_uint(a), __float_as_uint(b), false, false); return __uint_as_float(s[0]) + __uint_as_float(s[1]); }
#define PIN(x) asm volatile("" : "+v"(x))
DEVI float swap16_add(float x) { auto s = __builtin_amdgcn_permlane16_swap(__float_as_uint(x), __float_as_uint(x), false, false); return __uint_as_float(s[0]) + __uint_as_float(s[1]); }
DEVI float swap32_add(float x) { auto s = __builtin_amdgcn_permlane32_swap(__float_as_uint(x), __float_as_uint(x), false, false); return __uint_as_float(s[0]) + __uint_as_float(s[1]); }
DEVI float swap16_max(float x) { auto s = __builtin_amdgcn_permlane16_swap(__float_as_uint(x), __float_as_uint(x), false, false); return fmaxf(__uint_as_float(s[0]), __uint_as_float(s[1])); }
DEVI float swap32_max(float x) { auto s = __builtin_amdgcn_permlane32_swap(__float_as_uint(x), __float_as_uint(x), false, false); return fmaxf(__uint_as_float(s[0]), __uint_as_float(s[1])); }
template <int CTRL> DEVI float dpp(float x) { return __builtin_bit_cast(float, __builtin_amdgcn_mov_dpp(__builtin_bit_cast(int, x), CTRL, 0xf, 0xf, true)); }
DEVI float row_sum(float x) { x += dpp<0xB1>(x); x += dpp<0x4E>(x); x += dpp<0x141>(x); x += dpp<0x128>(x); return x; }
DEVI float half_sum(float x) { return swap16_add(row_sum(x)); }
DEVI float wave_sum(float x) { return swap32_add(half_sum(x)); }
template <typename T> DEVI LAS T* lp(u32 a) { return (LAS T*)(size_t)a; }
DEVI u32 ldsv(u32 a) { return *(volatile LAS u32*)(size_t)a; }
DEVI void stsv(u32 a, u32 v) { *(volatile LAS u32*)(size_t)a = v; }
DEVI v2f modv(u32 col, int b) { const v2u v = *lp<v2u>(L_MOD + col * 4); const int sh = 16 - 16 * b; return v2f{bfhi(v.x << sh), bfhi(v.y << sh)}; }
#define FENCE() asm volatile("" ::: "memory")
#define WAITV(n) asm volatile("s_waitcnt vmcnt(%0)" ::"i"(n) : "memory")
#define WAITL() asm volatile("s_waitcnt lgkmcnt(0)" ::: "memory")
DEVI void bar() { WAITL(); __builtin_amdgcn_s_barrier(); FENCE(); }
typedef __amdgpu_buffer_rsrc_t rsrc_t;
DEVI v4u ld4(rsrc_t r, u32 vo, u32 so) { return __builtin_bit_cast(v4u, __builtin_amdgcn_raw_buffer_load_b128(r, vo, so, 16)); }
DEVI u32 ld1(rsrc_t r, u32 vo, u32 so) { return __builtin_amdgcn_raw_buffer_load_b32(r, vo, so, 16); }
DEVI void st4(v4u v, rsrc_t r, u32 vo, u32 so) { __builtin_amdgcn_raw_buffer_store_b128(v, r, vo, so, 16); }
DEVI void st1(u32 v, rsrc_t r, u32 vo, u32 so) { __builtin_amdgcn_raw_buffer_store_b32(v, r, vo, so, 16); }
DEVI u32 canon(float f) { u32 b = __float_as_uint(f); return b < 0xFFFFFFFEu ? b : 0xFFFFFFFEu; }
DEVI v8bf trfrag(u32 a) {
  v4s lo = __builtin_amdgcn_ds_read_tr16_b64_v4i16(lp<v4s>(a)), hi = __builtin_amdgcn_ds_read_tr16_b64_v4i16(lp<v4s>(a + 2048));
  return __builtin_bit_cast(v8bf, __builtin_shufflevector(lo, hi, 0, 1, 2, 3, 4, 5, 6, 7));
}
DEVI v4f mfma(v8bf a, v8bf b, v4f c) { return __builtin_amdgcn_mfma_f32_16x16x32_bf16(a, b, c, 0, 0, 0); }
DEVI v8bf ldfrag(u32 a) { return __builtin_bit_cast(v8bf, *lp<v4u>(a)); }
DEVI int fpos(int k) { return (k & ~31) + ((k >> 2) & 3) * 8 + ((k >> 4) & 1) * 4 + (k & 3); }
DEVI v4i mksrd(const void* p, u32 bytes) { u64 a = (u64)p; return v4i{(int)(u32)a, (int)(u32)(a >> 32), (int)bytes, 0x00020000}; }

#define DMA4(m, v0, v1, v2, v3, srd, so)                                                                                   \
  asm volatile("s_mov_b32 m0, %0\n\ts_nop 0\n\tbuffer_load_dwordx4 %1, %5, %6 offen nt lds\n\t"                           \
               "buffer_load_dwordx4 %2, %5, %6 offen offset:1024 nt lds\n\tbuffer_load_dwordx4 %3, %5, %6 offen offset:2048 nt lds\n\t" \
               "buffer_load_dwordx4 %4, %5, %6 offen offset:3072 nt lds" ::"s"(m), "v"(v0), "v"(v1), "v"(v2), "v"(v3), "s"(srd), "s"(so) : "memory")
#define DMA1(m, v0, srd, so) \
  asm volatile("s_mov_b32 m0, %0\n\ts_nop 0\n\tbuffer_load_dwordx4 %1, %2, %3 offen nt lds" ::"s"(m), "v"(v0), "s"(srd), "s"(so) : "memory")

#define STAMP(i) do { if ((i) == DIAG) tdiag = __builtin_amdgcn_s_memrealtime(); } while (0)
#define FIN() do { if (tdiag) while (__builtin_amdgcn_s_memrealtime() < tdiag + 10000) __builtin_amdgcn_s_sleep(8); } while (0)

__global__ void __launch_bounds__(512, 2)
fused_block(const u16* __restrict__ Kc, const u16* __restrict__ Vc, const u16* __restrict__ wmod, const u16* __restrict__ wq,
            const u16* __restrict__ wo, const u16* __restrict__ w1, const u16* __restrict__ w2, const u16* __restrict__ xin,
            const u16* __restrict__ cin, u16* __restrict__ out, unsigned char* ws) {
  __shared__ __attribute__((aligned(16))) unsigned char smem[L_END];
  asm volatile("" ::"s"((u32)(size_t)(LAS unsigned char*)smem));
  u64 tdiag = 0;
  if (DIAG >= 20) { asm volatile("s_memrealtime %0\n\ts_waitcnt lgkmcnt(0)" : "=s"(tdiag)); }
  if (DIAG == 21 && blockIdx.x != 0) tdiag = 0;
  if (DIAG == 22 && blockIdx.x != 255) tdiag = 0;
  const int tid = threadIdx.x, lane = tid & 63, wave = __builtin_amdgcn_readfirstlane(tid >> 6);
  const int bid = blockIdx.x, grp = bid >> 4, mem = bid & 15;
  const int g = lane >> 4, c = lane & 15;
  const bool isX = mem < 4, isY = (mem == 4 || mem == 5) && grp < 14, hasP1 = mem >= 4;
  const int xi = grp * 4 + mem, yi = grp * 2 + mem - 4, p1i = grp * 12 + mem - 4;
  const int u0 = hasP1 ? 2 : 1;
  int nkv, kv0;
  if (grp < 14) { nkv = isX ? NX_A : isY ? NY_A : NO_A; kv0 = isX ? mem * NX_A : isY ? 4 * NX_A + (mem - 4) * NY_A : 4 * NX_A + 2 * NY_A + (mem - 6) * NO_A; }
  else { nkv = isX ? NX_B : NO_B; kv0 = isX ? mem * NX_B : 4 * NX_B + (mem - 4) * NO_B; }
  const int ntail = isX ? 4 : isY ? 8 : 0;
  const int uw = u0 + nkv;
  const rsrc_t rws = __builtin_amdgcn_make_buffer_rsrc(ws, 0, W_END, 0x00020000);

  if (wave == 0) {
    __builtin_amdgcn_s_setprio(3);
    STAMP(0);
    u32 pub = 0;
    auto publish = [&](u32 v) { if ((int)v > (int)pub) { pub = v; stsv(L_CTL + 4 * C_FULL, v); } };
    auto wait_slot = [&](int u) {
      if (u >= NSLOT) {
        const u32 a = L_CTL + 4 * (C_FREE + (u & (NSLOT - 1))), need = u - NSLOT + 1;
        if (ldsv(a) < need) { WAITV(0); publish(16 * u); while (ldsv(a) < need) __builtin_amdgcn_s_sleep(1); }
      }
    };
    const u32 wrow = lane >> 3, wch = ((lane & 7) ^ (((wrow >> 1) & 3) << 1)) << 4;
    auto wunit = [&](int u, const u16* base, u32 bytes, u32 rs, u32 so0, u32 bq, bool pubok) {
      const v4i srd = mksrd(base, bytes);
      const u32 vo = wrow * rs + wch;
      const u32 lb = (u & (NSLOT - 1)) * SLOT;
      for (int p = 0; p < 16; ++p) {
        const u32 m = lb + p * 1024, so = so0 + (p & 7) * 8 * rs + (p >> 3) * bq;
        DMA1(m, vo, srd, so);
        if ((p & 3) == 3 && pubok) { WAITV(WIN); publish(16 * u + p + 1 - WIN); }
      }
    };
    if (hasP1) wunit(0, wmod, 512 * 3072 * 2, 6144, (p1i / 48) * 128 * 6144 + (p1i % 48) * 128, 64 * 6144, false);
    else wunit(0, wq, 512 * 4096 * 2, 8192, (bid >> 6) * 128 * 8192 + (bid & 63) * 128, 64 * 8192, false);
    __builtin_amdgcn_s_barrier(); FENCE();
    if (hasP1) wunit(1, wq, 512 * 4096 * 2, 8192, (bid >> 6) * 128 * 8192 + (bid & 63) * 128, 64 * 8192, true);
    {
      const v4i rk = mksrd(Kc + (size_t)grp * 32768 * 128, 32768 * 256), rv = mksrd(Vc + (size_t)grp * 32768 * 128, 32768 * 256);
      const u32 k0 = g * 256 + ((c ^ g) << 4), k1 = g * 256 + ((c ^ (4 + g)) << 4), k2 = g * 256 + ((c ^ (8 + g)) << 4), k3 = g * 256 + ((c ^ (12 + g)) << 4);
      const u32 v0 = g * 256 + ((c ^ (2 * g)) << 4), v1 = g * 256 + ((c ^ (8 + 2 * g)) << 4);
      for (int u = u0; u < uw; ++u) {
        wait_slot(u);
        const u32 lb = (u & (NSLOT - 1)) * SLOT, so = (u32)(kv0 + u - u0) * 8192;
        { const u32 m = lb; DMA4(m, k0, k1, k2, k3, rk, so); }
        WAITV(WIN); publish(16 * u + 4 - WIN);
        { const u32 m = lb + 4096, s = so + 4096; DMA4(m, k0, k1, k2, k3, rk, s); }
        WAITV(WIN); publish(16 * u + 8 - WIN);
        { const u32 m = lb + 8192; DMA4(m, v0, v1, v0, v1, rv, so); }
        WAITV(WIN); publish(16 * u + 12 - WIN);
        { const u32 m = lb + 12288, s = so + 4096; DMA4(m, v0, v1, v0, v1, rv, s); }
        WAITV(WIN); publish(16 * u + 16 - WIN);
      }
    }
    for (int t = 0; t < ntail; ++t) {
      const int u = uw + t;
      wait_slot(u);
      if (isX) wunit(u, wo, 4096 * 512 * 2, 1024, ((xi >> 3) * 512 + t * 128) * 1024 + (xi & 7) * 128, 64 * 1024, true);
      else if (t < 4) wunit(u, w1, 512 * 1792 * 2, 3584, t * 128 * 3584 + yi * 128, 64 * 3584, true);
      else wunit(u, w2, 1792 * 512 * 2, 1024, yi * 64 * 1024 + (t - 4) * 256, 128, true);
    }
    const u32 e = 16 * (uw + ntail);
    WAITV(16); publish(e - 16);
    WAITV(8); publish(e - 8);
    WAITV(0); publish(e);
    STAMP(1);
    __builtin_amdgcn_s_setprio(0);
  } else {
    auto wait_full = [&](u32 need) { while (ldsv(L_CTL + 4 * C_FULL) < need) __builtin_amdgcn_s_sleep(1); FENCE(); };
    const u32 trow = 4 * g + (c >> 2);
    auto waddr = [&](int t) -> u32 { return trow * 128 + ((((u32)(t ^ ((trow >> 1) & 3)) << 1) | ((c >> 1) & 1)) << 4) + (c & 1) * 8; };

    if (wave == 1) {
      const int kg1 = p1i / 48, cb1 = p1i % 48, kg2 = bid >> 6, cb2 = bid & 63;
      const u64 E0 = __hip_atomic_load((GAS u64*)(ws + W_EPOCH), __ATOMIC_RELAXED, __HIP_MEMORY_SCOPE_AGENT);
      const v2u cv = *(const v2u*)(cin + (lane >> 5) * 512 + (hasP1 ? kg1 : 0) * 128 + (lane & 31) * 4);
      const v4u xv0 = *(const v4u*)(xin + lane * 8), xv1 = *(const v4u*)(xin + 512 + lane * 8);
      const u32 xs00 = xin[kg2 * 128 + lane], xs01 = xin[kg2 * 128 + 64 + lane], xs10 = xin[512 + kg2 * 128 + lane], xs11 = xin[512 + kg2 * 128 + 64 + lane];
      __builtin_amdgcn_s_barrier(); FENCE();
      const u64 E = E0 + 1;
      const u32 tag = (u32)E;
      const u32 fa = L_HP + (c & 1) * 256 + g * 16;
      if (hasP1) {
        *lp<v2u>(L_HP + (lane >> 5) * 256 + fpos((lane & 31) * 4) * 2) = v2u{pk2(silu_exact(lo16(cv.x)), silu_exact(bfhi(cv.x))), pk2(silu_exact(lo16(cv.y)), silu_exact(bfhi(cv.y)))};
        WAITL();
        v8bf act[4];
#pragma unroll
        for (int ks = 0; ks < 4; ++ks) { act[ks] = ldfrag(fa + ks * 64); PIN(act[ks]); }
        wait_full(16);
        v8bf W[4][4];
#pragma unroll
        for (int t = 0; t < 4; ++t)
#pragma unroll
          for (int ks = 0; ks < 4; ++ks) W[t][ks] = trfrag(waddr(t) + ks * 4096);
        WAITL(); stsv(L_CTL + 4 * (C_FREE + 0), 1);
        v4f acc[4];
#pragma unroll
        for (int t = 0; t < 4; ++t) acc[t] = v4f{0.f, 0.f, 0.f, 0.f};
#pragma unroll
        for (int ks = 0; ks < 4; ++ks)
#pragma unroll
          for (int t = 0; t < 4; ++t) acc[t] = mfma(act[ks], W[t][ks], acc[t]);
        const float r0 = g == 0 ? acc[0][0] : g == 1 ? acc[1][0] : g == 2 ? acc[2][0] : acc[3][0];
        const float r1 = g == 0 ? acc[0][1] : g == 1 ? acc[1][1] : g == 2 ? acc[2][1] : acc[3][1];
        st4(v4u{__float_as_uint(r0), tag, __float_as_uint(r1), tag}, rws, lane * 16, W_MODP + (kg1 * 3072 + cb1 * 64) * 16);
        STAMP(3);
      }
      {
        v2f y[2];
        {
          float mr[2], ri[2];
#pragma unroll
          for (int b = 0; b < 2; ++b) {
            const v4u xv = b ? xv1 : xv0;
            const float x[8] = {lo16(xv.x), bfhi(xv.x), lo16(xv.y), bfhi(xv.y), lo16(xv.z), bfhi(xv.z), lo16(xv.w), bfhi(xv.w)};
            float s = 0.f;
#pragma unroll
            for (int i = 0; i < 8; ++i) s += x[i];
            const float mean = wave_sum(s) * (1.f / 512.f);
            float q2 = 0.f;
#pragma unroll
            for (int i = 0; i < 8; ++i) q2 += (x[i] - mean) * (x[i] - mean);
            const float var = wave_sum(q2) * (1.f / 512.f);
            mr[b] = rb(mean); ri[b] = rcp(rb(__builtin_amdgcn_sqrtf(rb(rb(var) + 1e-5f))));
          }
          y[0] = rb2(rb2(v2f{lo16(xs00) - mr[0], lo16(xs10) - mr[1]}) * v2f{ri[0], ri[1]});
          y[1] = rb2(rb2(v2f{lo16(xs01) - mr[0], lo16(xs11) - mr[1]}) * v2f{ri[0], ri[1]});
          PIN(y[0]); PIN(y[1]);
        }
        wait_full(16 * u0);
        v8bf W[4][4];
#pragma unroll
        for (int t = 0; t < 4; ++t)
#pragma unroll
          for (int ks = 0; ks < 4; ++ks) W[t][ks] = trfrag((u0 - 1) * SLOT + waddr(t) + ks * 4096);
        WAITL(); stsv(L_CTL + 4 * (C_FREE + u0 - 1), u0);
        STAMP(4);
        v2f s[2][2];
        for (int sp = 0; sp < SPIN_CAP; ++sp) {
          u32 bad = 0;
#pragma unroll
          for (int pt = 0; pt < 2; ++pt)
#pragma unroll
            for (int e = 0; e < 2; ++e) {
              s[pt][e] = v2f{0.f, 0.f};
#pragma unroll
              for (int k4 = 0; k4 < 4; ++k4) {
                const v4u a = ld4(rws, lane * 16 + e * 1024, W_MODP + (k4 * 3072 + pt * 512 + kg2 * 128) * 16);
                bad |= (a.y ^ tag) | (a.w ^ tag);
                s[pt][e] += v2f{__uint_as_float(a.x), __uint_as_float(a.z)};
              }
            }
          if (__all(bad == 0)) break;
          FENCE();
        }
#pragma unroll
        for (int e = 0; e < 2; ++e) {
          const v2f h = rb2(y[e] * rb2(rb2(s[1][e]) + 1.f)) + rb2(s[0][e]);
          const u32 hp = pk2(h.x, h.y);
          *lp<u16>(L_HP + fpos(e * 64 + lane) * 2) = (u16)hp; *lp<u16>(L_HP + 256 + fpos(e * 64 + lane) * 2) = (u16)(hp >> 16);
        }
        WAITL();
        v8bf act[4];
#pragma unroll
        for (int ks = 0; ks < 4; ++ks) act[ks] = ldfrag(fa + ks * 64);
        v4f acc[4];
#pragma unroll
        for (int t = 0; t < 4; ++t) acc[t] = v4f{0.f, 0.f, 0.f, 0.f};
#pragma unroll
        for (int ks = 0; ks < 4; ++ks)
#pragma unroll
          for (int t = 0; t < 4; ++t) acc[t] = mfma(act[ks], W[t][ks], acc[t]);
        const float r0 = g == 0 ? acc[0][0] : g == 1 ? acc[1][0] : g == 2 ? acc[2][0] : acc[3][0];
        const float r1 = g == 0 ? acc[0][1] : g == 1 ? acc[1][1] : g == 2 ? acc[2][1] : acc[3][1];
        const float n0 = dpp<0xB1>(r0), n1 = dpp<0xB1>(r1);
        const bool odd = lane & 1;
        const float va = odd ? n1 : r0, vb = odd ? r1 : n0;
        st4(v4u{__float_as_uint(va), tag, __float_as_uint(vb), tag}, rws, (lane & 1) * 32768 + (lane >> 1) * 16, W_QP + kg2 * 65536 + cb2 * 512);
        STAMP(5);
      }
      {
        v2f s[4];
        for (int sp = 0; sp < SPIN_CAP; ++sp) {
          u32 bad = 0;
#pragma unroll
          for (int i = 0; i < 4; ++i) {
            s[i] = v2f{0.f, 0.f};
#pragma unroll
            for (int k4 = 0; k4 < 4; ++k4) {
              const v4u a = ld4(rws, lane * 16 + i * 1024, W_QP + k4 * 65536 + grp * 4096);
              bad |= (a.y ^ tag) | (a.w ^ tag);
              s[i] += v2f{__uint_as_float(a.x), __uint_as_float(a.z)};
            }
          }
          if (__all(bad == 0)) break;
          FENCE();
        }
#pragma unroll
        for (int i = 0; i < 4; ++i) *lp<u32>(L_Q + i * 256 + lane * 4) = pk2(s[i].x, s[i].y);
        WAITL(); stsv(L_CTL + 4 * C_QRDY, 1);
        STAMP(6);
      }
      const v4u s4 = {SENT, SENT, SENT, SENT};
      st4(s4, rws, lane * 16, W_PART + bid * PREC); st4(s4, rws, lane * 16 + 1024, W_PART + bid * PREC);
      if (lane < 4) st4(s4, rws, lane * 16 + 2048, W_PART + bid * PREC);
      if (isX && lane < 32) st4(s4, rws, (lane >> 4) * 2048 + (lane & 15) * 16, W_AP + (xi >> 3) * 4096 + (xi & 7) * 256);
      if (isY) {
#pragma unroll
        for (int i = 0; i < 4; ++i) st4(s4, rws, lane * 16 + i * 1024, W_OP + yi * 4096);
      }
      WAITV(0);
      stsv(L_CTL + 4 * C_CLR, 1);
      if (lane == 0) __hip_atomic_store((GAS u64*)(ws + W_FLAG) + bid, E, __ATOMIC_RELAXED, __HIP_MEMORY_SCOPE_AGENT);
      if (isX || isY) {
#pragma nounroll
        for (int sp = 0; sp < SPIN_CAP; ++sp) {
          const v4u a = ld4(rws, lane * 16, W_FLAG), b = ld4(rws, lane * 16 + 1024, W_FLAG);
          const u32 lo = (u32)E, hi = (u32)(E >> 32);
          const u32 bad = (a.x ^ lo) | (a.y ^ hi) | (a.z ^ lo) | (a.w ^ hi) | (b.x ^ lo) | (b.y ^ hi) | (b.z ^ lo) | (b.w ^ hi);
          if (__all(bad == 0)) break;
          __builtin_amdgcn_s_sleep(32); FENCE();
        }
        if (bid == 0 && lane == 0) __hip_atomic_store((GAS u64*)(ws + W_EPOCH), E, __ATOMIC_RELAXED, __HIP_MEMORY_SCOPE_AGENT);
      }
      if (isY) {
        for (int it = 0; it < 8; ++it) {
          v2f s[4];
          for (int sp = 0; sp < SPIN_CAP; ++sp) {
            u32 bad = 0;
#pragma unroll
            for (int e = 0; e < 4; ++e) {
              s[e] = v2f{0.f, 0.f};
#pragma unroll
              for (int kg = 0; kg < 4; ++kg) {
                const v4u a = ld4(rws, lane * 16 + e * 1024, W_MODP + (kg * 3072 + 1024 + it * 256) * 16);
                bad |= (a.y ^ tag) | (a.w ^ tag);
                s[e] += v2f{__uint_as_float(a.x), __uint_as_float(a.z)};
              }
            }
            if (__all(bad == 0)) break;
            __builtin_amdgcn_s_sleep(32); FENCE();
          }
#pragma unroll
          for (int e = 0; e < 4; ++e) *lp<u32>(L_MOD + (it * 256 + e * 64 + lane) * 4) = pk2(s[e].x, s[e].y);
        }
      }
      STAMP(2);
    } else {
      if (wave == 2) { if (lane < 32) stsv(L_CTL + 4 * lane, 0); WAITL(); }
      __builtin_amdgcn_s_barrier(); FENCE();
      v8bf qf[4];
      float m = -INFINITY, l = 0.f;
      v4f acc[8];
#pragma unroll
      for (int t = 0; t < 8; ++t) acc[t] = v4f{0.f, 0.f, 0.f, 0.f};
      u32 ka[4], va[8];
#pragma unroll
      for (int s = 0; s < 4; ++s) ka[s] = c * 256 + (((4 * s + g) ^ c) << 4);
#pragma unroll
      for (int t = 0; t < 8; ++t) va[t] = 8192 + trow * 256 + ((((u32)(t ^ (trow & 7)) << 1) | ((c >> 1) & 1)) << 4) + (c & 1) * 8;
      bool first = true;
#pragma nounroll
      for (int u = u0 + wave - 2; u < uw; u += NCONS) {
        const u32 lb = (u & (NSLOT - 1)) * SLOT;
        wait_full(16 * u + 16);
        v8bf kf[2][4], vf[8];
#pragma unroll
        for (int s = 0; s < 4; ++s) { kf[0][s] = ldfrag(lb + ka[s]); kf[1][s] = ldfrag(lb + ka[s] + 4096); }
#pragma unroll
        for (int t = 0; t < 8; ++t) {
          v4s lo = __builtin_amdgcn_ds_read_tr16_b64_v4i16(lp<v4s>(lb + va[t])), hi = __builtin_amdgcn_ds_read_tr16_b64_v4i16(lp<v4s>(lb + va[t] + 4096));
          vf[t] = __builtin_bit_cast(v8bf, __builtin_shufflevector(lo, hi, 0, 1, 2, 3, 4, 5, 6, 7));
        }
        WAITL(); stsv(L_CTL + 4 * (C_FREE + (u & (NSLOT - 1))), u + 1);
        if (first) {
          first = false;
          while (ldsv(L_CTL + 4 * C_QRDY) == 0) __builtin_amdgcn_s_sleep(2);
          FENCE();
#pragma unroll
          for (int s = 0; s < 4; ++s) qf[s] = ldfrag(L_Q + (c & 3) * 256 + (4 * s + g) * 16);
        }
        v4f S[2] = {v4f{0.f, 0.f, 0.f, 0.f}, v4f{0.f, 0.f, 0.f, 0.f}};
#pragma unroll
        for (int s = 0; s < 4; ++s) { S[0] = mfma(kf[0][s], qf[s], S[0]); S[1] = mfma(kf[1][s], qf[s], S[1]); }
        float sc[8];
#pragma unroll
        for (int i = 0; i < 8; i += 2) {
          const v2f r = rb2(rb2(v2f{S[i >> 2][i & 3], S[i >> 2][(i & 3) + 1]}) * 0.08838834764831845f);
          sc[i] = r.x; sc[i + 1] = r.y;
        }
        float tm = fmaxf(fmaxf(fmaxf(sc[0], sc[1]), fmaxf(sc[2], sc[3])), fmaxf(fmaxf(sc[4], sc[5]), fmaxf(sc[6], sc[7])));
        tm = swap32_max(swap16_max(tm));
        const float mn = fmaxf(m, tm * 1.4426950408889634f);
        const float alpha = ex2(m - mn);
        m = mn;
        float p[8], ps = 0.f;
#pragma unroll
        for (int i = 0; i < 8; ++i) { p[i] = ex2(fmaf(sc[i], 1.4426950408889634f, -mn)); ps += p[i]; }
        l = fmaf(l, alpha, ps);
        const v8bf pf = __builtin_bit_cast(v8bf, v4u{pk2(p[0], p[1]), pk2(p[2], p[3]), pk2(p[4], p[5]), pk2(p[6], p[7])});
#pragma unroll
        for (int t = 0; t < 8; ++t) acc[t] = mfma(vf[t], pf, acc[t] * alpha);
      }
      if (wave == 2) STAMP(7);
      l = swap32_add(swap16_add(l));
      if (c < 4) {
        const u32 a = L_MRG + ((wave - 2) * 4 + c) * 528;
#pragma unroll
        for (int t = 0; t < 8; ++t) *lp<v4f>(a + (16 * t + 4 * g) * 4) = acc[t];
        if (g == 0) *lp<v2f>(a + 512) = v2f{m, l};
      }
      WAITL();
      if (lane == 0) __hip_atomic_fetch_add(lp<u32>(L_CTL + 4 * C_MCNT), 1u, __ATOMIC_RELAXED, __HIP_MEMORY_SCOPE_WORKGROUP);
      if (wave < 4) {
        while (ldsv(L_CTL + 4 * C_CLR) == 0) { }
        while (ldsv(L_CTL + 4 * C_MCNT) < NCONS) { }
        FENCE();
        const int hd = (wave - 2) * 2 + (lane >> 5), i = lane & 31;
        v4f a[NCONS]; v2f ml[NCONS];
#pragma unroll
        for (int w = 0; w < NCONS; ++w) { a[w] = *lp<v4f>(L_MRG + (w * 4 + hd) * 528 + i * 16); ml[w] = *lp<v2f>(L_MRG + (w * 4 + hd) * 528 + 512); }
        float M = ml[0].x;
#pragma unroll
        for (int w = 1; w < NCONS; ++w) M = fmaxf(M, ml[w].x);
        float L = 0.f; v4f o = {0.f, 0.f, 0.f, 0.f};
#pragma unroll
        for (int w = 0; w < NCONS; ++w) { const float e = ex2(ml[w].x - M); L = fmaf(e, ml[w].y, L); o += a[w] * e; }
        const float inv = rcp(L);
        st4(v4u{canon(o.x * inv), canon(o.y * inv), canon(o.z * inv), canon(o.w * inv)}, rws, hd * 528 + i * 16, W_PART + bid * PREC);
        if (i == 0) st1(canon(M + __builtin_amdgcn_logf(L)), rws, hd * 528 + 512, W_PART + bid * PREC);
        if (wave == 2) STAMP(8);
      }
    }
  }
  if (!(isX || isY)) { FIN(); return; }
  bar();
  const u32 trow = 4 * g + (c >> 2);
  auto waddr = [&](int t) -> u32 { return trow * 128 + ((((u32)(t ^ ((trow >> 1) & 3)) << 1) | ((c >> 1) & 1)) << 4) + (c & 1) * 8; };
  auto wslot = [&](int t) -> u32 { return ((uw + t) & (NSLOT - 1)) * SLOT; };
  const int half = lane >> 5, i32 = lane & 31;
  const u32 fa = L_ACT + (c & 1) * 1024 + g * 16;
  if (isX) {
    const int kvh = xi >> 3, ng = xi & 7;
    v8bf W[16];
    if (wave < 4) {
#pragma unroll
      for (int ks = 0; ks < 16; ++ks) W[ks] = trfrag(wslot(ks >> 2) + (ks & 3) * 4096 + waddr(wave));
    }
    const int b = wave >> 2, h = wave & 3;
    const u32 so = W_PART + ((b * 8 + kvh) * 16) * PREC + h * 528;
    u32 wa = L_ACT + b * 1024 + fpos(h * 128 + i32 * 4 + half * 2) * 2;
    PIN(wa);
    v4u a[8]; u32 ls[8];
#pragma nounroll
    for (int sp = 0; sp < SPIN_CAP; ++sp) {
#pragma unroll
      for (int j = 0; j < 8; ++j) { a[j] = ld4(rws, half * PREC + i32 * 16, so + j * 2 * PREC); ls[j] = ld1(rws, half * PREC + 512, so + j * 2 * PREC); }
      u32 mx = 0;
#pragma unroll
      for (int j = 0; j < 8; ++j) { mx = max(max(mx, ls[j]), max(max(a[j].x, a[j].y), max(a[j].z, a[j].w))); }
      if (__all(mx != SENT)) break;
      FENCE();
    }
    float M = __uint_as_float(ls[0]);
#pragma unroll
    for (int j = 1; j < 8; ++j) M = fmaxf(M, __uint_as_float(ls[j]));
    M = swap32_max(M);
    float L = 0.f; v4f o = v4f{0.f, 0.f, 0.f, 0.f};
#pragma unroll
    for (int j = 0; j < 8; ++j) { const float e = ex2(__uint_as_float(ls[j]) - M); L += e; o += __builtin_bit_cast(v4f, a[j]) * e; }
    const float inv = rcp(swap32_add(L));
    *lp<u32>(wa) = pk2(fold32(o.x, o.z) * inv, fold32(o.y, o.w) * inv);
    if (wave == 0) STAMP(9);
    bar();
    if (wave < 4) {
      v8bf A[16];
#pragma unroll
      for (int ks = 0; ks < 16; ++ks) A[ks] = ldfrag(fa + ks * 64);
      __builtin_amdgcn_sched_barrier(0);
      v4f d0 = {0.f, 0.f, 0.f, 0.f}, d1 = {0.f, 0.f, 0.f, 0.f};
#pragma unroll
      for (int ks = 0; ks < 16; ks += 2) { d0 = mfma(W[ks], A[ks], d0); d1 = mfma(W[ks + 1], A[ks + 1], d1); }
      d0 += d1;
      if (c < 2) st4(v4u{canon(d0.x), canon(d0.y), canon(d0.z), canon(d0.w)}, rws, c * 2048 + g * 16, W_AP + kvh * 4096 + ng * 256 + wave * 64);
      if (wave == 0) STAMP(10);
    }
    FIN();
    return;
  }
  {
    v8bf W[16];
    if (wave < 4) {
#pragma unroll
      for (int ks = 0; ks < 16; ++ks) W[ks] = trfrag(wslot(ks >> 2) + (ks & 3) * 4096 + waddr(wave));
    } else {
#pragma unroll
      for (int j = 0; j < 16; ++j) W[j] = trfrag(wslot(wave) + (j >> 3) * 8192 + (j & 1) * 4096 + waddr((j >> 1) & 3));
    }
    const int b = wave >> 2, col = (wave & 3) * 128 + i32 * 4 + half * 2;
    const u32 xv = *(const u32*)(xin + b * 512 + col);
    v2f x0 = {lo16(xv), bfhi(xv)};
    v2f ga = modv(col, b), sb = modv(512 + col, b), cb1 = rb2(modv(1024 + col, b) + 1.f);
    u32 wa = L_ACT + b * 1024 + fpos(col) * 2, xa = L_X1 + b * 2048 + col * 4, ra = L_RED + wave * 8 + half * 4;
    u32 ha = L_H1 + (c & 1) * 128 + fpos((wave & 3) * 16 + g * 4 + ((c >> 1) & 3)) * 2;
    PIN(x0); PIN(ga); PIN(sb); PIN(cb1); PIN(wa); PIN(xa); PIN(ra); PIN(ha);
    v4u a[4];
#pragma nounroll
    for (int sp = 0; sp < SPIN_CAP; ++sp) {
#pragma unroll
      for (int j = 0; j < 4; ++j) a[j] = ld4(rws, half * 16384 + i32 * 16, W_AP + j * 4096 + b * 2048 + (wave & 3) * 512);
      u32 mx = 0;
#pragma unroll
      for (int j = 0; j < 4; ++j) mx = max(mx, max(max(a[j].x, a[j].y), max(a[j].z, a[j].w)));
      if (__all(mx != SENT)) break;
      FENCE();
    }
    if (wave == 0) STAMP(11);
    const v4f at = (__builtin_bit_cast(v4f, a[0]) + __builtin_bit_cast(v4f, a[1])) + (__builtin_bit_cast(v4f, a[2]) + __builtin_bit_cast(v4f, a[3]));
    const v2f x1 = rb2(x0 + rb2(ga * rb2(v2f{fold32(at.x, at.z), fold32(at.y, at.w)})));
    const float st = half_sum(fold32(x1.x + x1.y, fmaf(x1.x, x1.x, x1.y * x1.y)));
    if (i32 == 0) *lp<float>(ra) = st;
    *lp<v2f>(xa) = x1;
    bar();
    {
      const v4f r0 = *lp<v4f>(L_RED + b * 32), r1 = *lp<v4f>(L_RED + b * 32 + 16);
      const float mean = ((r0.x + r0.z) + (r1.x + r1.z)) * (1.f / 512.f);
      const float var = fmaxf(fmaf(-mean, mean, ((r0.y + r0.w) + (r1.y + r1.w)) * (1.f / 512.f)), 0.f);
      const v2f mv = rb2(v2f{mean, var});
      const float ri = rcp(rb(__builtin_amdgcn_sqrtf(rb(mv.y + 1e-5f))));
      const v2f hh = rb2(rb2(rb2(x1 - mv.x) * ri) * cb1) + sb;
      *lp<u32>(wa) = pk2(hh.x, hh.y);
    }
    bar();
    if (wave < 4) {
      v8bf A[16];
#pragma unroll
      for (int ks = 0; ks < 16; ++ks) A[ks] = ldfrag(fa + ks * 64);
      __builtin_amdgcn_sched_barrier(0);
      v4f d0 = {0.f, 0.f, 0.f, 0.f}, d1 = {0.f, 0.f, 0.f, 0.f};
#pragma unroll
      for (int ks = 0; ks < 16; ks += 2) { d0 = mfma(W[ks], A[ks], d0); d1 = mfma(W[ks + 1], A[ks + 1], d1); }
      d0 += d1;
      const int r = (c >> 1) & 3;
      const float v = silu(rb(r == 0 ? d0.x : r == 1 ? d0.y : r == 2 ? d0.z : d0.w));
      *lp<u16>(ha) = (u16)pk2(v, v);
    }
    bar();
    if (wave >= 4) {
      const v8bf a0 = ldfrag(L_H1 + (c & 1) * 128 + g * 16), a1 = ldfrag(L_H1 + (c & 1) * 128 + 64 + g * 16);
      v4f d[8];
#pragma unroll
      for (int t = 0; t < 8; ++t) d[t] = mfma(W[2 * t + 1], a1, mfma(W[2 * t], a0, v4f{0.f, 0.f, 0.f, 0.f}));
      v4f r = d[0];
#pragma unroll
      for (int t = 1; t < 8; ++t) if ((c >> 1) == t) r = d[t];
      st4(v4u{canon(r.x), canon(r.y), canon(r.z), canon(r.w)}, rws, (c & 1) * 2048 + (c >> 1) * 64 + g * 16, W_OP + yi * 4096 + (wave - 4) * 512);
      if (wave == 4) STAMP(12);
    } else if (wave == 0 && yi < 16) {
      const int zb = yi >> 3, zc = (yi & 7) * 64 + c * 4 + (g >> 1) * 2 + (g & 1);
      float xx = *lp<float>(L_X1 + zb * 2048 + zc * 4), gb = bfhi(*lp<u32>(L_MOD + (1536 + zc) * 4) << (16 - 16 * zb));
      PIN(xx); PIN(gb);
      v4u a[7];
#pragma nounroll
      for (int sp = 0; sp < SPIN_CAP; ++sp) {
#pragma unroll
        for (int j = 0; j < 7; ++j) a[j] = ld4(rws, g * 4096 + c * 16, W_OP + j * 16384 + zb * 2048 + (yi & 7) * 256);
        u32 mx = 0;
#pragma unroll
        for (int j = 0; j < 7; ++j) mx = max(mx, max(max(a[j].x, a[j].y), max(a[j].z, a[j].w)));
        if (__all(mx != SENT)) break;
        FENCE();
      }
      v4f s = __builtin_bit_cast(v4f, a[0]);
#pragma unroll
      for (int j = 1; j < 7; ++j) s += __builtin_bit_cast(v4f, a[j]);
      const float tot = fold16(fold32(s.x, s.z), fold32(s.y, s.w));
      const float r = xx + rb(gb * rb(tot));
      out[zb * 512 + zc] = (u16)pk2(r, r);
      STAMP(13);
    }
  }
  FIN();
}

extern "C" void kernel_launch(void* const* d_in, const int* in_sizes, int n_in, void* d_out, int out_size, void* d_ws, size_t ws_size, hipStream_t stream) {
  hipLaunchKernelGGL(fused_block, dim3(256), dim3(512), 0, stream, (const u16*)d_in[1], (const u16*)d_in[2], (const u16*)d_in[9], (const u16*)d_in[3],
                     (const u16*)d_in[4], (const u16*)d_in[6], (const u16*)d_in[7], (const u16*)d_in[0], (const u16*)d_in[8], (u16*)d_out, (unsigned char*)d_ws);
}
```

```cpp
#define DIAG 20
#include <hip/hip_runtime.h>
#include <hip/hip_bf16.h>

#define STAMPS 0

typedef unsigned int u32;
typedef unsigned long long u64;
typedef unsigned short u16;
typedef u32 v4u __attribute__((ext_vector_type(4)));
typedef u32 v2u __attribute__((ext_vector_type(2)));
typedef int v4i __attribute__((ext_vector_type(4)));
typedef float v4f __attribute__((ext_vector_type(4)));
typedef float v2f __attribute__((ext_vector_type(2)));
typedef short v4s __attribute__((ext_vector_type(4)));
typedef __bf16 v8bf __attribute__((ext_vector_type(8)));
typedef __bf16 v2bf __attribute__((ext_vector_type(2)));
#define LAS __attribute__((address_space(3)))
#define GAS __attribute__((address_space(1)))
#define DEVI __device__ __forceinline__

constexpr int NSLOT = 8, SLOT = 16384, NCONS = 6;
#define WIN 20
constexpr int NX_A = 63, NY_A = 56, NO_A = 66;
constexpr int NX_B = 61, NO_B = 65;
static_assert(4 * NX_A + 2 * NY_A + 10 * NO_A == 1024 && 4 * NX_B + 12 * NO_B == 1024, "units");
constexpr int L_CTL = NSLOT * SLOT;
constexpr int L_Q = L_CTL + 256;
constexpr int L_ACT = L_Q + 1024;
constexpr int L_HP = L_ACT + 2048;
constexpr int L_H1 = L_HP + 512;
constexpr int L_RED = L_H1 + 256;
constexpr int L_X1 = L_RED + 128;
constexpr int L_MRG = L_X1 + 4096;
constexpr int L_MOD = L_MRG + NCONS * 4 * 132 * 4;
constexpr int L_END = L_MOD + 8192;
static_assert(L_END <= 160 * 1024, "LDS");
constexpr int C_FULL = 0, C_QRDY = 1, C_MCNT = 2, C_CLR = 3, C_FREE = 8;
constexpr u32 W_EPOCH = 0, W_FLAG = 4096, W_MODP = 8192, W_QP = W_MODP + 4 * 3072 * 16, W_PART = W_QP + 4 * 2 * 4096 * 8;
constexpr u32 PREC = 4 * 132 * 4;
constexpr u32 W_AP = W_PART + 256 * PREC, W_OP = W_AP + 8 * 4096, W_END = W_OP + 28 * 4096;
constexpr u32 W_STAMP = 2u << 20;
constexpr u32 SENT = 0xFFFFFFFFu;
constexpr int SPIN_CAP = 1 << 16;

DEVI float bf2f(u32 v) { return __uint_as_float(v << 16); }
DEVI float bfhi(u32 v) { return __uint_as_float(v & 0xffff0000u); }
DEVI u32 pk2(float a, float b) { v2f x = {a, b}; return __builtin_bit_cast(u32, __builtin_convertvector(x, v2bf)); }
DEVI float rb(float a) { return bf2f(pk2(a, a) & 0xffffu); }
DEVI float ex2(float x) { return __builtin_amdgcn_exp2f(x); }
DEVI float rcp(float x) { return __builtin_amdgcn_rcpf(x); }
DEVI float silu(float v) { return v * rcp(1.f + ex2(v * -1.4426950408889634f)); }
DEVI float silu_exact(float v) { return v / (1.f + expf(-v)); }
DEVI float lo16(u32 v) { return __uint_as_float(v << 16); }
DEVI v2f rb2(v2f a) { const u32 p = pk2(a.x, a.y); return v2f{lo16(p), bfhi(p)}; }
DEVI float fold32(float a, float b) { auto s = __builtin_amdgcn_permlane32_swap(__float_as_uint(a), __float_as_uint(b), false, false); return __uint_as_float(s[0]) + __uint_as_float(s[1]); }
DEVI float fold16(float a, float b) { auto s = __builtin_amdgcn_permlane16_swap(__float_as_uint(a), __float_as_uint(b), false, false); return __uint_as_float(s[0]) + __uint_as_float(s[1]); }
#define PIN(x) asm volatile("" : "+v"(x))
DEVI float swap16_add(float x) { auto s = __builtin_amdgcn_permlane16_swap(__float_as_uint(x), __float_as_uint(x), false, false); return __uint_as_float(s[0]) + __uint_as_float(s[1]); }
DEVI float swap32_add(float x) { auto s = __builtin_amdgcn_permlane32_swap(__float_as_uint(x), __float_as_uint(x), false, false); return __uint_as_float(s[0]) + __uint_as_float(s[1]); }
DEVI float swap16_max(float x) { auto s = __builtin_amdgcn_permlane16_swap(__float_as_uint(x), __float_as_uint(x), false, false); return fmaxf(__uint_as_float(s[0]), __uint_as_float(s[1])); }
DEVI float swap32_max(float x) { auto s = __builtin_amdgcn_permlane32_swap(__float_as_uint(x), __float_as_uint(x), false, false); return fmaxf(__uint_as_float(s[0]), __uint_as_float(s[1])); }
template <int CTRL> DEVI float dpp(float x) { return __builtin_bit_cast(float, __builtin_amdgcn_mov_dpp(__builtin_bit_cast(int, x), CTRL, 0xf, 0xf, true)); }
DEVI float row_sum(float x) { x += dpp<0xB1>(x); x += dpp<0x4E>(x); x += dpp<0x141>(x); x += dpp<0x128>(x); return x; }
DEVI float half_sum(float x) { return swap16_add(row_sum(x)); }
DEVI float wave_sum(float x) { return swap32_add(half_sum(x)); }
template <typename T> DEVI LAS T* lp(u32 a) { return (LAS T*)(size_t)a; }
DEVI u32 ldsv(u32 a) { return *(volatile LAS u32*)(size_t)a; }
DEVI void stsv(u32 a, u32 v) { *(volatile LAS u32*)(size_t)a = v; }
DEVI v2f modv(u32 col, int b) { const v2u v = *lp<v2u>(L_MOD + col * 4); const int sh = 16 - 16 * b; return v2f{bfhi(v.x << sh), bfhi(v.y << sh)}; }
#define FENCE() asm volatile("" ::: "memory")
#define WAITV(n) asm volatile("s_waitcnt vmcnt(%0)" ::"i"(n) : "memory")
#define WAITL() asm volatile("s_waitcnt lgkmcnt(0)" ::: "memory")
DEVI void bar() { WAITL(); __builtin_amdgcn_s_barrier(); FENCE(); }
typedef __amdgpu_buffer_rsrc_t rsrc_t;
DEVI v4u ld4(rsrc_t r, u32 vo, u32 so) { return __builtin_bit_cast(v4u, __builtin_amdgcn_raw_buffer_load_b128(r, vo, so, 16)); }
DEVI u32 ld1(rsrc_t r, u32 vo, u32 so) { return __builtin_amdgcn_raw_buffer_load_b32(r, vo, so, 16); }
DEVI void st4(v4u v, rsrc_t r, u32 vo, u32 so) { __builtin_amdgcn_raw_buffer_store_b128(v, r, vo, so, 16); }
DEVI void st1(u32 v, rsrc_t r, u32 vo, u32 so) { __builtin_amdgcn_raw_buffer_store_b32(v, r, vo, so, 16); }
DEVI u32 canon(float f) { u32 b = __float_as_uint(f); return b < 0xFFFFFFFEu ? b : 0xFFFFFFFEu; }
DEVI v8bf trfrag(u32 a) {
  v4s lo = __builtin_amdgcn_ds_read_tr16_b64_v4i16(lp<v4s>(a)), hi = __builtin_amdgcn_ds_read_tr16_b64_v4i16(lp<v4s>(a + 2048));
  return __builtin_bit_cast(v8bf, __builtin_shufflevector(lo, hi, 0, 1, 2, 3, 4, 5, 6, 7));
}
DEVI v4f mfma(v8bf a, v8bf b, v4f c) { return __builtin_amdgcn_mfma_f32_16x16x32_bf16(a, b, c, 0, 0, 0); }
DEVI v8bf ldfrag(u32 a) { return __builtin_bit_cast(v8bf, *lp<v4u>(a)); }
DEVI int fpos(int k) { return (k & ~31) + ((k >> 2) & 3) * 8 + ((k >> 4) & 1) * 4 + (k & 3); }
DEVI v4i mksrd(const void* p, u32 bytes) { u64 a = (u64)p; return v4i{(int)(u32)a, (int)(u32)(a >> 32), (int)bytes, 0x00020000}; }

#define DMA4(m, v0, v1, v2, v3, srd, so)                                                                                   \
  asm volatile("s_mov_b32 m0, %0\n\ts_nop 0\n\tbuffer_load_dwordx4 %1, %5, %6 offen nt lds\n\t"                           \
               "buffer_load_dwordx4 %2, %5, %6 offen offset:1024 nt lds\n\tbuffer_load_dwordx4 %3, %5, %6 offen offset:2048 nt lds\n\t" \
               "buffer_load_dwordx4 %4, %5, %6 offen offset:3072 nt lds" ::"s"(m), "v"(v0), "v"(v1), "v"(v2), "v"(v3), "s"(srd), "s"(so) : "memory")
#define DMA1(m, v0, srd, so) \
  asm volatile("s_mov_b32 m0, %0\n\ts_nop 0\n\tbuffer_load_dwordx4 %1, %2, %3 offen nt lds" ::"s"(m), "v"(v0), "s"(srd), "s"(so) : "memory")

#define STAMP(i) do { if ((i) == DIAG) tdiag = __builtin_amdgcn_s_memrealtime(); } while (0)
#define FIN() do { if (tdiag) while (__builtin_amdgcn_s_memrealtime() < tdiag + 10000) __builtin_amdgcn_s_sleep(8); } while (0)

__global__ void __launch_bounds__(512, 2)
fused_block(const u16* __restrict__ Kc, const u16* __restrict__ Vc, const u16* __restrict__ wmod, const u16* __restrict__ wq,
            const u16* __restrict__ wo, const u16* __restrict__ w1, const u16* __restrict__ w2, const u16* __restrict__ xin,
            const u16* __restrict__ cin, u16* __restrict__ out, unsigned char* ws) {
  __shared__ __attribute__((aligned(16))) unsigned char smem[L_END];
  asm volatile("" ::"s"((u32)(size_t)(LAS unsigned char*)smem));
  u64 tdiag = 0;
  if (DIAG >= 20) { asm volatile("s_memrealtime %0\n\ts_waitcnt lgkmcnt(0)" : "=s"(tdiag)); }
  if (DIAG == 21 && blockIdx.x != 0) tdiag = 0;
  if (DIAG == 22 && blockIdx.x != 255) tdiag = 0;
  const int tid = threadIdx.x, lane = tid & 63, wave = __builtin_amdgcn_readfirstlane(tid >> 6);
  const int bid = blockIdx.x, grp = bid >> 4, mem = bid & 15;
  const int g = lane >> 4, c = lane & 15;
  const bool isX = mem < 4, isY = (mem == 4 || mem == 5) && grp < 14, hasP1 = mem >= 4;
  const int xi = grp * 4 + mem, yi = grp * 2 + mem - 4, p1i = grp * 12 + mem - 4;
  const int u0 = hasP1 ? 2 : 1;
  int nkv, kv0;
  if (grp < 14) { nkv = isX ? NX_A : isY ? NY_A : NO_A; kv0 = isX ? mem * NX_A : isY ? 4 * NX_A + (mem - 4) * NY_A : 4 * NX_A + 2 * NY_A + (mem - 6) * NO_A; }
  else { nkv = isX ? NX_B : NO_B; kv0 = isX ? mem * NX_B : 4 * NX_B + (mem - 4) * NO_B; }
  const int ntail = isX ? 4 : isY ? 8 : 0;
  const int uw = u0 + nkv;
  const rsrc_t rws = __builtin_amdgcn_make_buffer_rsrc(ws, 0, W_END, 0x00020000);

  if (wave == 0) {
    __builtin_amdgcn_s_setprio(3);
    STAMP(0);
    u32 pub = 0;
    auto publish = [&](u32 v) { if ((int)v > (int)pub) { pub = v; stsv(L_CTL + 4 * C_FULL, v); } };
    auto wait_slot = [&](int u) {
      if (u >= NSLOT) {
        const u32 a = L_CTL + 4 * (C_FREE + (u & (NSLOT - 1))), need = u - NSLOT + 1;
        if (ldsv(a) < need) { WAITV(0); publish(16 * u); while (ldsv(a) < need) __builtin_amdgcn_s_sleep(1); }
      }
    };
    const u32 wrow = lane >> 3, wch = ((lane & 7) ^ (((wrow >> 1) & 3) << 1)) << 4;
    auto wunit = [&](int u, const u16* base, u32 bytes, u32 rs, u32 so0, u32 bq, bool pubok) {
      const v4i srd = mksrd(base, bytes);
      const u32 vo = wrow * rs + wch;
      const u32 lb = (u & (NSLOT - 1)) * SLOT;
      for (int p = 0; p < 16; ++p) {
        const u32 m = lb + p * 1024, so = so0 + (p & 7) * 8 * rs + (p >> 3) * bq;
        DMA1(m, vo, srd, so);
        if ((p & 3) == 3 && pubok) { WAITV(WIN); publish(16 * u + p + 1 - WIN); }
      }
    };
    if (hasP1) wunit(0, wmod, 512 * 3072 * 2, 6144, (p1i / 48) * 128 * 6144 + (p1i % 48) * 128, 64 * 6144, false);
    else wunit(0, wq, 512 * 4096 * 2, 8192, (bid >> 6) * 128 * 8192 + (bid & 63) * 128, 64 * 8192, false);
    __builtin_amdgcn_s_barrier(); FENCE();
    if (hasP1) wunit(1, wq, 512 * 4096 * 2, 8192, (bid >> 6) * 128 * 8192 + (bid & 63) * 128, 64 * 8192, true);
    {
      const v4i rk = mksrd(Kc + (size_t)grp * 32768 * 128, 32768 * 256), rv = mksrd(Vc + (size_t)grp * 32768 * 128, 32768 * 256);
      const u32 k0 = g * 256 + ((c ^ g) << 4), k1 = g * 256 + ((c ^ (4 + g)) << 4), k2 = g * 256 + ((c ^ (8 + g)) << 4), k3 = g * 256 + ((c ^ (12 + g)) << 4);
      const u32 v0 = g * 256 + ((c ^ (2 * g)) << 4), v1 = g * 256 + ((c ^ (8 + 2 * g)) << 4);
      for (int u = u0; u < uw; ++u) {
        wait_slot(u);
        const u32 lb = (u & (NSLOT - 1)) * SLOT, so = (u32)(kv0 + u - u0) * 8192;
        { const u32 m = lb; DMA4(m, k0, k1, k2, k3, rk, so); }
        WAITV(WIN); publish(16 * u + 4 - WIN);
        { const u32 m = lb + 4096, s = so + 4096; DMA4(m, k0, k1, k2, k3, rk, s); }
        WAITV(WIN); publish(16 * u + 8 - WIN);
        { const u32 m = lb + 8192; DMA4(m, v0, v1, v0, v1, rv, so); }
        WAITV(WIN); publish(16 * u + 12 - WIN);
        { const u32 m = lb + 12288, s = so + 4096; DMA4(m, v0, v1, v0, v1, rv, s); }
        WAITV(WIN); publish(16 * u + 16 - WIN);
      }
    }
    for (int t = 0; t < ntail; ++t) {
      const int u = uw + t;
      wait_slot(u);
      if (isX) wunit(u, wo, 4096 * 512 * 2, 1024, ((xi >> 3) * 512 + t * 128) * 1024 + (xi & 7) * 128, 64 * 1024, true);
      else if (t < 4) wunit(u, w1, 512 * 1792 * 2, 3584, t * 128 * 3584 + yi * 128, 64 * 3584, true);
      else wunit(u, w2, 1792 * 512 * 2, 1024, yi * 64 * 1024 + (t - 4) * 256, 128, true);
    }
    const u32 e = 16 * (uw + ntail);
    WAITV(16); publish(e - 16);
    WAITV(8); publish(e - 8);
    WAITV(0); publish(e);
    STAMP(1);
    __builtin_amdgcn_s_setprio(0);
  } else {
    auto wait_full = [&](u32 need) { while (ldsv(L_CTL + 4 * C_FULL) < need) __builtin_amdgcn_s_sleep(1); FENCE(); };
    const u32 trow = 4 * g + (c >> 2);
    auto waddr = [&](int t) -> u32 { return trow * 128 + ((((u32)(t ^ ((trow >> 1) & 3)) << 1) | ((c >> 1) & 1)) << 4) + (c & 1) * 8; };

    if (wave == 1) {
      const int kg1 = p1i / 48, cb1 = p1i % 48, kg2 = bid >> 6, cb2 = bid & 63;
      const u64 E0 = __hip_atomic_load((GAS u64*)(ws + W_EPOCH), __ATOMIC_RELAXED, __HIP_MEMORY_SCOPE_AGENT);
      const v2u cv = *(const v2u*)(cin + (lane >> 5) * 512 + (hasP1 ? kg1 : 0) * 128 + (lane & 31) * 4);
      const v4u xv0 = *(const v4u*)(xin + lane * 8), xv1 = *(const v4u*)(xin + 512 + lane * 8);
      const u32 xs00 = xin[kg2 * 128 + lane], xs01 = xin[kg2 * 128 + 64 + lane], xs10 = xin[512 + kg2 * 128 + lane], xs11 = xin[512 + kg2 * 128 + 64 + lane];
      __builtin_amdgcn_s_barrier(); FENCE();
      const u64 E = E0 + 1;
      const u32 tag = (u32)E;
      const u32 fa = L_HP + (c & 1) * 256 + g * 16;
      if (hasP1) {
        *lp<v2u>(L_HP + (lane >> 5) * 256 + fpos((lane & 31) * 4) * 2) = v2u{pk2(silu_exact(lo16(cv.x)), silu_exact(bfhi(cv.x))), pk2(silu_exact(lo16(cv.y)), silu_exact(bfhi(cv.y)))};
        WAITL();
        v8bf act[4];
#pragma unroll
        for (int ks = 0; ks < 4; ++ks) { act[ks] = ldfrag(fa + ks * 64); PIN(act[ks]); }
        wait_full(16);
        v8bf W[4][4];
#pragma unroll
        for (int t = 0; t < 4; ++t)
#pragma unroll
          for (int ks = 0; ks < 4; ++ks) W[t][ks] = trfrag(waddr(t) + ks * 4096);
        WAITL(); stsv(L_CTL + 4 * (C_FREE + 0), 1);
        v4f acc[4];
#pragma unroll
        for (int t = 0; t < 4; ++t) acc[t] = v4f{0.f, 0.f, 0.f, 0.f};
#pragma unroll
        for (int ks = 0; ks < 4; ++ks)
#pragma unroll
          for (int t = 0; t < 4; ++t) acc[t] = mfma(act[ks], W[t][ks], acc[t]);
        const float r0 = g == 0 ? acc[0][0] : g == 1 ? acc[1][0] : g == 2 ? acc[2][0] : acc[3][0];
        const float r1 = g == 0 ? acc[0][1] : g == 1 ? acc[1][1] : g == 2 ? acc[2][1] : acc[3][1];
        st4(v4u{__float_as_uint(r0), tag, __float_as_uint(r1), tag}, rws, lane * 16, W_MODP + (kg1 * 3072 + cb1 * 64) * 16);
        STAMP(3);
      }
      {
        v2f y[2];
        {
          float mr[2], ri[2];
#pragma unroll
          for (int b = 0; b < 2; ++b) {
            const v4u xv = b ? xv1 : xv0;
            const float x[8] = {lo16(xv.x), bfhi(xv.x), lo16(xv.y), bfhi(xv.y), lo16(xv.z), bfhi(xv.z), lo16(xv.w), bfhi(xv.w)};
            float s = 0.f;
#pragma unroll
            for (int i = 0; i < 8; ++i) s += x[i];
            const float mean = wave_sum(s) * (1.f / 512.f);
            float q2 = 0.f;
#pragma unroll
            for (int i = 0; i < 8; ++i) q2 += (x[i] - mean) * (x[i] - mean);
            const float var = wave_sum(q2) * (1.f / 512.f);
            mr[b] = rb(mean); ri[b] = rcp(rb(__builtin_amdgcn_sqrtf(rb(rb(var) + 1e-5f))));
          }
          y[0] = rb2(rb2(v2f{lo16(xs00) - mr[0], lo16(xs10) - mr[1]}) * v2f{ri[0], ri[1]});
          y[1] = rb2(rb2(v2f{lo16(xs01) - mr[0], lo16(xs11) - mr[1]}) * v2f{ri[0], ri[1]});
          PIN(y[0]); PIN(y[1]);
        }
        wait_full(16 * u0);
        v8bf W[4][4];
#pragma unroll
        for (int t = 0; t < 4; ++t)
#pragma unroll
          for (int ks = 0; ks < 4; ++ks) W[t][ks] = trfrag((u0 - 1) * SLOT + waddr(t) + ks * 4096);
        WAITL(); stsv(L_CTL + 4 * (C_FREE + u0 - 1), u0);
        STAMP(4);
        v2f s[2][2];
        for (int sp = 0; sp < SPIN_CAP; ++sp) {
          u32 bad = 0;
#pragma unroll
          for (int pt = 0; pt < 2; ++pt)
#pragma unroll
            for (int e = 0; e < 2; ++e) {
              s[pt][e] = v2f{0.f, 0.f};
#pragma unroll
              for (int k4 = 0; k4 < 4; ++k4) {
                const v4u a = ld4(rws, lane * 16 + e * 1024, W_MODP + (k4 * 3072 + pt * 512 + kg2 * 128) * 16);
                bad |= (a.y ^ tag) | (a.w ^ tag);
                s[pt][e] += v2f{__uint_as_float(a.x), __uint_as_float(a.z)};
              }
            }
          if (__all(bad == 0)) break;
          FENCE();
        }
#pragma unroll
        for (int e = 0; e < 2; ++e) {
          const v2f h = rb2(y[e] * rb2(rb2(s[1][e]) + 1.f)) + rb2(s[0][e]);
          const u32 hp = pk2(h.x, h.y);
          *lp<u16>(L_HP + fpos(e * 64 + lane) * 2) = (u16)hp; *lp<u16>(L_HP + 256 + fpos(e * 64 + lane) * 2) = (u16)(hp >> 16);
        }
        WAITL();
        v8bf act[4];
#pragma unroll
        for (int ks = 0; ks < 4; ++ks) act[ks] = ldfrag(fa + ks * 64);
        v4f acc[4];
#pragma unroll
        for (int t = 0; t < 4; ++t) acc[t] = v4f{0.f, 0.f, 0.f, 0.f};
#pragma unroll
        for (int ks = 0; ks < 4; ++ks)
#pragma unroll
          for (int t = 0; t < 4; ++t) acc[t] = mfma(act[ks], W[t][ks], acc[t]);
        const float r0 = g == 0 ? acc[0][0] : g == 1 ? acc[1][0] : g == 2 ? acc[2][0] : acc[3][0];
        const float r1 = g == 0 ? acc[0][1] : g == 1 ? acc[1][1] : g == 2 ? acc[2][1] : acc[3][1];
        const float n0 = dpp<0xB1>(r0), n1 = dpp<0xB1>(r1);
        const bool odd = lane & 1;
        const float va = odd ? n1 : r0, vb = odd ? r1 : n0;
        st4(v4u{__float_as_uint(va), tag, __float_as_uint(vb), tag}, rws, (lane & 1) * 32768 + (lane >> 1) * 16, W_QP + kg2 * 65536 + cb2 * 512);
        STAMP(5);
      }
      {
        v2f s[4];
        for (int sp = 0; sp < SPIN_CAP; ++sp) {
          u32 bad = 0;
#pragma unroll
          for (int i = 0; i < 4; ++i) {
            s[i] = v2f{0.f, 0.f};
#pragma unroll
            for (int k4 = 0; k4 < 4; ++k4) {
              const v4u a = ld4(rws, lane * 16 + i * 1024, W_QP + k4 * 65536 + grp * 4096);
              bad |= (a.y ^ tag) | (a.w ^ tag);
              s[i] += v2f{__uint_as_float(a.x), __uint_as_float(a.z)};
            }
          }
          if (__all(bad == 0)) break;
          FENCE();
        }
#pragma unroll
        for (int i = 0; i < 4; ++i) *lp<u32>(L_Q + i * 256 + lane * 4) = pk2(s[i].x, s[i].y);
        WAITL(); stsv(L_CTL + 4 * C_QRDY, 1);
        STAMP(6);
      }
      const v4u s4 = {SENT, SENT, SENT, SENT};
      st4(s4, rws, lane * 16, W_PART + bid * PREC); st4(s4, rws, lane * 16 + 1024, W_PART + bid * PREC);
      if (lane < 4) st4(s4, rws, lane * 16 + 2048, W_PART + bid * PREC);
      if (isX && lane < 32) st4(s4, rws, (lane >> 4) * 2048 + (lane & 15) * 16, W_AP + (xi >> 3) * 4096 + (xi & 7) * 256);
      if (isY) {
#pragma unroll
        for (int i = 0; i < 4; ++i) st4(s4, rws, lane * 16 + i * 1024, W_OP + yi * 4096);
      }
      WAITV(0);
      stsv(L_CTL + 4 * C_CLR, 1);
      if (lane == 0) __hip_atomic_store((GAS u64*)(ws + W_FLAG) + bid, E, __ATOMIC_RELAXED, __HIP_MEMORY_SCOPE_AGENT);
      if (isX || isY) {
#pragma nounroll
        for (int sp = 0; sp < SPIN_CAP; ++sp) {
          const v4u a = ld4(rws, lane * 16, W_FLAG), b = ld4(rws, lane * 16 + 1024, W_FLAG);
          const u32 lo = (u32)E, hi = (u32)(E >> 32);
          const u32 bad = (a.x ^ lo) | (a.y ^ hi) | (a.z ^ lo) | (a.w ^ hi) | (b.x ^ lo) | (b.y ^ hi) | (b.z ^ lo) | (b.w ^ hi);
          if (__all(bad == 0)) break;
          __builtin_amdgcn_s_sleep(32); FENCE();
        }
        if (bid == 0 && lane == 0) __hip_atomic_store((GAS u64*)(ws + W_EPOCH), E, __ATOMIC_RELAXED, __HIP_MEMORY_SCOPE_AGENT);
      }
      if (isY) {
        for (int it = 0; it < 8; ++it) {
          v2f s[4];
          for (int sp = 0; sp < SPIN_CAP; ++sp) {
            u32 bad = 0;
#pragma unroll
            for (int e = 0; e < 4; ++e) {
              s[e] = v2f{0.f, 0.f};
#pragma unroll
              for (int kg = 0; kg < 4; ++kg) {
                const v4u a = ld4(rws, lane * 16 + e * 1024, W_MODP + (kg * 3072 + 1024 + it * 256) * 16);
                bad |= (a.y ^ tag) | (a.w ^ tag);
                s[e] += v2f{__uint_as_float(a.x), __uint_as_float(a.z)};
              }
            }
            if (__all(bad == 0)) break;
            __builtin_amdgcn_s_sleep(32); FENCE();
          }
#pragma unroll
          for (int e = 0; e < 4; ++e) *lp<u32>(L_MOD + (it * 256 + e * 64 + lane) * 4) = pk2(s[e].x, s[e].y);
        }
      }
      STAMP(2);
    } else {
      if (wave == 2) { if (lane < 32) stsv(L_CTL + 4 * lane, 0); WAITL(); }
      __builtin_amdgcn_s_barrier(); FENCE();
      v8bf qf[4];
      float m = -INFINITY, l = 0.f;
      v4f acc[8];
#pragma unroll
      for (int t = 0; t < 8; ++t) acc[t] = v4f{0.f, 0.f, 0.f, 0.f};
      u32 ka[4], va[8];
#pragma unroll
      for (int s = 0; s < 4; ++s) ka[s] = c * 256 + (((4 * s + g) ^ c) << 4);
#pragma unroll
      for (int t = 0; t < 8; ++t) va[t] = 8192 + trow * 256 + ((((u32)(t ^ (trow & 7)) << 1) | ((c >> 1) & 1)) << 4) + (c & 1) * 8;
      bool first = true;
#pragma nounroll
      for (int u = u0 + wave - 2; u < uw; u += NCONS) {
        const u32 lb = (u & (NSLOT - 1)) * SLOT;
        wait_full(16 * u + 16);
        v8bf kf[2][4], vf[8];
#pragma unroll
        for (int s = 0; s < 4; ++s) { kf[0][s] = ldfrag(lb + ka[s]); kf[1][s] = ldfrag(lb + ka[s] + 4096); }
#pragma unroll
        for (int t = 0; t < 8; ++t) {
          v4s lo = __builtin_amdgcn_ds_read_tr16_b64_v4i16(lp<v4s>(lb + va[t])), hi = __builtin_amdgcn_ds_read_tr16_b64_v4i16(lp<v4s>(lb + va[t] + 4096));
          vf[t] = __builtin_bit_cast(v8bf, __builtin_shufflevector(lo, hi, 0, 1, 2, 3, 4, 5, 6, 7));
        }
        WAITL(); stsv(L_CTL + 4 * (C_FREE + (u & (NSLOT - 1))), u + 1);
        if (first) {
          first = false;
          while (ldsv(L_CTL + 4 * C_QRDY) == 0) __builtin_amdgcn_s_sleep(2);
          FENCE();
#pragma unroll
          for (int s = 0; s < 4; ++s) qf[s] = ldfrag(L_Q + (c & 3) * 256 + (4 * s + g) * 16);
        }
        v4f S[2] = {v4f{0.f, 0.f, 0.f, 0.f}, v4f{0.f, 0.f, 0.f, 0.f}};
#pragma unroll
        for (int s = 0; s < 4; ++s) { S[0] = mfma(kf[0][s], qf[s], S[0]); S[1] = mfma(kf[1][s], qf[s], S[1]); }
        float sc[8];
#pragma unroll
        for (int i = 0; i < 8; i += 2) {
          const v2f r = rb2(rb2(v2f{S[i >> 2][i & 3], S[i >> 2][(i & 3) + 1]}) * 0.08838834764831845f);
          sc[i] = r.x; sc[i + 1] = r.y;
        }
        float tm = fmaxf(fmaxf(fmaxf(sc[0], sc[1]), fmaxf(sc[2], sc[3])), fmaxf(fmaxf(sc[4], sc[5]), fmaxf(sc[6], sc[7])));
        tm = swap32_max(swap16_max(tm));
        const float mn = fmaxf(m, tm * 1.4426950408889634f);
        const float alpha = ex2(m - mn);
        m = mn;
        float p[8], ps = 0.f;
#pragma unroll
        for (int i = 0; i < 8; ++i) { p[i] = ex2(fmaf(sc[i], 1.4426950408889634f, -mn)); ps += p[i]; }
        l = fmaf(l, alpha, ps);
        const v8bf pf = __builtin_bit_cast(v8bf, v4u{pk2(p[0], p[1]), pk2(p[2], p[3]), pk2(p[4], p[5]), pk2(p[6], p[7])});
#pragma unroll
        for (int t = 0; t < 8; ++t) acc[t] = mfma(vf[t], pf, acc[t] * alpha);
      }
      if (wave == 2) STAMP(7);
      l = swap32_add(swap16_add(l));
      if (c < 4) {
        const u32 a = L_MRG + ((wave - 2) * 4 + c) * 528;
#pragma unroll
        for (int t = 0; t < 8; ++t) *lp<v4f>(a + (16 * t + 4 * g) * 4) = acc[t];
        if (g == 0) *lp<v2f>(a + 512) = v2f{m, l};
      }
      WAITL();
      if (lane == 0) __hip_atomic_fetch_add(lp<u32>(L_CTL + 4 * C_MCNT), 1u, __ATOMIC_RELAXED, __HIP_MEMORY_SCOPE_WORKGROUP);
      if (wave < 4) {
        while (ldsv(L_CTL + 4 * C_CLR) == 0) { }
        while (ldsv(L_CTL + 4 * C_MCNT) < NCONS) { }
        FENCE();
        const int hd = (wave - 2) * 2 + (lane >> 5), i = lane & 31;
        v4f a[NCONS]; v2f ml[NCONS];
#pragma unroll
        for (int w = 0; w < NCONS; ++w) { a[w] = *lp<v4f>(L_MRG + (w * 4 + hd) * 528 + i * 16); ml[w] = *lp<v2f>(L_MRG + (w * 4 + hd) * 528 + 512); }
        float M = ml[0].x;
#pragma unroll
        for (int w = 1; w < NCONS; ++w) M = fmaxf(M, ml[w].x);
        float L = 0.f; v4f o = {0.f, 0.f, 0.f, 0.f};
#pragma unroll
        for (int w = 0; w < NCONS; ++w) { const float e = ex2(ml[w].x - M); L = fmaf(e, ml[w].y, L); o += a[w] * e; }
        const float inv = rcp(L);
        st4(v4u{canon(o.x * inv), canon(o.y * inv), canon(o.z * inv), canon(o.w * inv)}, rws, hd * 528 + i * 16, W_PART + bid * PREC);
        if (i == 0) st1(canon(M + __builtin_amdgcn_logf(L)), rws, hd * 528 + 512, W_PART + bid * PREC);
        if (wave == 2) STAMP(8);
      }
    }
  }
  if (!(isX || isY)) { FIN(); return; }
  bar();
  const u32 trow = 4 * g + (c >> 2);
  auto waddr = [&](int t) -> u32 { return trow * 128 + ((((u32)(t ^ ((trow >> 1) & 3)) << 1) | ((c >> 1) & 1)) << 4) + (c & 1) * 8; };
  auto wslot = [&](int t) -> u32 { return ((uw + t) & (NSLOT - 1)) * SLOT; };
  const int half = lane >> 5, i32 = lane & 31;
  const u32 fa = L_ACT + (c & 1) * 1024 + g * 16;
  if (isX) {
    const int kvh = xi >> 3, ng = xi & 7;
    v8bf W[16];
    if (wave < 4) {
#pragma unroll
      for (int ks = 0; ks < 16; ++ks) W[ks] = trfrag(wslot(ks >> 2) + (ks & 3) * 4096 + waddr(wave));
    }
    const int b = wave >> 2, h = wave & 3;
    const u32 so = W_PART + ((b * 8 + kvh) * 16) * PREC + h * 528;
    u32 wa = L_ACT + b * 1024 + fpos(h * 128 + i32 * 4 + half * 2) * 2;
    PIN(wa);
    v4u a[8]; u32 ls[8];
#pragma nounroll
    for (int sp = 0; sp < SPIN_CAP; ++sp) {
#pragma unroll
      for (int j = 0; j < 8; ++j) { a[j] = ld4(rws, half * PREC + i32 * 16, so + j * 2 * PREC); ls[j] = ld1(rws, half * PREC + 512, so + j * 2 * PREC); }
      u32 mx = 0;
#pragma unroll
      for (int j = 0; j < 8; ++j) { mx = max(max(mx, ls[j]), max(max(a[j].x, a[j].y), max(a[j].z, a[j].w))); }
      if (__all(mx != SENT)) break;
      FENCE();
    }
    float M = __uint_as_float(ls[0]);
#pragma unroll
    for (int j = 1; j < 8; ++j) M = fmaxf(M, __uint_as_float(ls[j]));
    M = swap32_max(M);
    float L = 0.f; v4f o = v4f{0.f, 0.f, 0.f, 0.f};
#pragma unroll
    for (int j = 0; j < 8; ++j) { const float e = ex2(__uint_as_float(ls[j]) - M); L += e; o += __builtin_bit_cast(v4f, a[j]) * e; }
    const float inv = rcp(swap32_add(L));
    *lp<u32>(wa) = pk2(fold32(o.x, o.z) * inv, fold32(o.y, o.w) * inv);
    if (wave == 0) STAMP(9);
    bar();
    if (wave < 4) {
      v8bf A[16];
#pragma unroll
      for (int ks = 0; ks < 16; ++ks) A[ks] = ldfrag(fa + ks * 64);
      __builtin_amdgcn_sched_barrier(0);
      v4f d0 = {0.f, 0.f, 0.f, 0.f}, d1 = {0.f, 0.f, 0.f, 0.f};
#pragma unroll
      for (int ks = 0; ks < 16; ks += 2) { d0 = mfma(W[ks], A[ks], d0); d1 = mfma(W[ks + 1], A[ks + 1], d1); }
      d0 += d1;
      if (c < 2) st4(v4u{canon(d0.x), canon(d0.y), canon(d0.z), canon(d0.w)}, rws, c * 2048 + g * 16, W_AP + kvh * 4096 + ng * 256 + wave * 64);
      if (wave == 0) STAMP(10);
    }
    FIN();
    return;
  }
  {
    v8bf W[16];
    if (wave < 4) {
#pragma unroll
      for (int ks = 0; ks < 16; ++ks) W[ks] = trfrag(wslot(ks >> 2) + (ks & 3) * 4096 + waddr(wave));
    } else {
#pragma unroll
      for (int j = 0; j < 16; ++j) W[j] = trfrag(wslot(wave) + (j >> 3) * 8192 + (j & 1) * 4096 + waddr((j >> 1) & 3));
    }
    const int b = wave >> 2, col = (wave & 3) * 128 + i32 * 4 + half * 2;
    const u32 xv = *(const u32*)(xin + b * 512 + col);
    v2f x0 = {lo16(xv), bfhi(xv)};
    v2f ga = modv(col, b), sb = modv(512 + col, b), cb1 = rb2(modv(1024 + col, b) + 1.f);
    u32 wa = L_ACT + b * 1024 + fpos(col) * 2, xa = L_X1 + b * 2048 + col * 4, ra = L_RED + wave * 8 + half * 4;
    u32 ha = L_H1 + (c & 1) * 128 + fpos((wave & 3) * 16 + g * 4 + ((c >> 1) & 3)) * 2;
    PIN(x0); PIN(ga); PIN(sb); PIN(cb1); PIN(wa); PIN(xa); PIN(ra); PIN(ha);
    v4u a[4];
#pragma nounroll
    for (int sp = 0; sp < SPIN_CAP; ++sp) {
#pragma unroll
      for (int j = 0; j < 4; ++j) a[j] = ld4(rws, half * 16384 + i32 * 16, W_AP + j * 4096 + b * 2048 + (wave & 3) * 512);
      u32 mx = 0;
#pragma unroll
      for (int j = 0; j < 4; ++j) mx = max(mx, max(max(a[j].x, a[j].y), max(a[j].z, a[j].w)));
      if (__all(mx != SENT)) break;
      FENCE();
    }
    if (wave == 0) STAMP(11);
    const v4f at = (__builtin_bit_cast(v4f, a[0]) + __builtin_bit_cast(v4f, a[1])) + (__builtin_bit_cast(v4f, a[2]) + __builtin_bit_cast(v4f, a[3]));
    const v2f x1 = rb2(x0 + rb2(ga * rb2(v2f{fold32(at.x, at.z), fold32(at.y, at.w)})));
    const float st = half_sum(fold32(x1.x + x1.y, fmaf(x1.x, x1.x, x1.y * x1.y)));
    if (i32 == 0) *lp<float>(ra) = st;
    *lp<v2f>(xa) = x1;
    bar();
    {
      const v4f r0 = *lp<v4f>(L_RED + b * 32), r1 = *lp<v4f>(L_RED + b * 32 + 16);
      const float mean = ((r0.x + r0.z) + (r1.x + r1.z)) * (1.f / 512.f);
      const float var = fmaxf(fmaf(-mean, mean, ((r0.y + r0.w) + (r1.y + r1.w)) * (1.f / 512.f)), 0.f);
      const v2f mv = rb2(v2f{mean, var});
      const float ri = rcp(rb(__builtin_amdgcn_sqrtf(rb(mv.y + 1e-5f))));
      const v2f hh = rb2(rb2(rb2(x1 - mv.x) * ri) * cb1) + sb;
      *lp<u32>(wa) = pk2(hh.x, hh.y);
    }
    bar();
    if (wave < 4) {
      v8bf A[16];
#pragma unroll
      for (int ks = 0; ks < 16; ++ks) A[ks] = ldfrag(fa + ks * 64);
      __builtin_amdgcn_sched_barrier(0);
      v4f d0 = {0.f, 0.f, 0.f, 0.f}, d1 = {0.f, 0.f, 0.f, 0.f};
#pragma unroll
      for (int ks = 0; ks < 16; ks += 2) { d0 = mfma(W[ks], A[ks], d0); d1 = mfma(W[ks + 1], A[ks + 1], d1); }
      d0 += d1;
      const int r = (c >> 1) & 3;
      const float v = silu(rb(r == 0 ? d0.x : r == 1 ? d0.y : r == 2 ? d0.z : d0.w));
      *lp<u16>(ha) = (u16)pk2(v, v);
    }
    bar();
    if (wave >= 4) {
      const v8bf a0 = ldfrag(L_H1 + (c & 1) * 128 + g * 16), a1 = ldfrag(L_H1 + (c & 1) * 128 + 64 + g * 16);
      v4f d[8];
#pragma unroll
      for (int t = 0; t < 8; ++t) d[t] = mfma(W[2 * t + 1], a1, mfma(W[2 * t], a0, v4f{0.f, 0.f, 0.f, 0.f}));
      v4f r = d[0];
#pragma unroll
      for (int t = 1; t < 8; ++t) if ((c >> 1) == t) r = d[t];
      st4(v4u{canon(r.x), canon(r.y), canon(r.z), canon(r.w)}, rws, (c & 1) * 2048 + (c >> 1) * 64 + g * 16, W_OP + yi * 4096 + (wave - 4) * 512);
      if (wave == 4) STAMP(12);
    } else if (wave == 0 && yi < 16) {
      const int zb = yi >> 3, zc = (yi & 7) * 64 + c * 4 + (g >> 1) * 2 + (g & 1);
      float xx = *lp<float>(L_X1 + zb * 2048 + zc * 4), gb = bfhi(*lp<u32>(L_MOD + (1536 + zc) * 4) << (16 - 16 * zb));
      PIN(xx); PIN(gb);
      v4u a[7];
#pragma nounroll
      for (int sp = 0; sp < SPIN_CAP; ++sp) {
#pragma unroll
        for (int j = 0; j < 7; ++j) a[j] = ld4(rws, g * 4096 + c * 16, W_OP + j * 16384 + zb * 2048 + (yi & 7) * 256);
        u32 mx = 0;
#pragma unroll
        for (int j = 0; j < 7; ++j) mx = max(mx, max(max(a[j].x, a[j].y), max(a[j].z, a[j].w)));
        if (__all(mx != SENT)) break;
        FENCE();
      }
      v4f s = __builtin_bit_cast(v4f, a[0]);
#pragma unroll
      for (int j = 1; j < 7; ++j) s += __builtin_bit_cast(v4f, a[j]);
      const float tot = fold16(fold32(s.x, s.z), fold32(s.y, s.w));
      const float r = xx + rb(gb * rb(tot));
      out[zb * 512 + zc] = (u16)pk2(r, r);
      STAMP(13);
    }
  }
  FIN();
}

template <int LDSB> __global__ void __launch_bounds__(512) probe_k(int ticks) {
  __shared__ unsigned char sm[LDSB];
  asm volatile("" ::"s"((u32)(size_t)(LAS unsigned char*)sm));
  u64 t; asm volatile("s_memrealtime %0\n\ts_waitcnt lgkmcnt(0)" : "=s"(t));
  while (__builtin_amdgcn_s_memrealtime() < t + ticks) __builtin_amdgcn_s_sleep(8);
}
extern "C" void kernel_launch(void* const* d_in, const int* in_sizes, int n_in, void* d_out, int out_size, void* d_ws, size_t ws_size, hipStream_t stream) {
  probe_k<16><<<256, 64, 0, stream>>>(0);
  hipLaunchKernelGGL(fused_block, dim3(256), dim3(512), 0, stream, (const u16*)d_in[1], (const u16*)d_in[2], (const u16*)d_in[9], (const u16*)d_in[3],
                     (const u16*)d_in[4], (const u16*)d_in[6], (const u16*)d_in[7], (const u16*)d_in[0], (const u16*)d_in[8], (u16*)d_out, (unsigned char*)d_ws);

}
```

```cpp
#include <hip/hip_runtime.h>
#include <hip/hip_bf16.h>

#define STAMPS 0

typedef unsigned int u32;
typedef unsigned long long u64;
typedef unsigned short u16;
typedef u32 v4u __attribute__((ext_vector_type(4)));
typedef u32 v2u __attribute__((ext_vector_type(2)));
typedef int v4i __attribute__((ext_vector_type(4)));
typedef float v4f __attribute__((ext_vector_type(4)));
typedef float v2f __attribute__((ext_vector_type(2)));
typedef short v4s __attribute__((ext_vector_type(4)));
typedef __bf16 v8bf __attribute__((ext_vector_type(8)));
typedef __bf16 v2bf __attribute__((ext_vector_type(2)));
#define LAS __attribute__((address_space(3)))
#define GAS __attribute__((address_space(1)))
#define DEVI __device__ __forceinline__

constexpr int NSLOT = 8, SLOT = 16384, NCONS = 6;
#define WIN 20
#define QAT 12
#define HDELAY 400
#define POLLS 16
constexpr int NA_A = 64, NY_A = 58, NS_A = 67, NA_B = 63, NY_B = 65, NS_B = 66;
static_assert(10 * NA_A + 2 * NY_A + 4 * NS_A == 1024 && 10 * NA_B + 2 * NY_B + 4 * NS_B == 1024, "units");
constexpr int L_CTL = NSLOT * SLOT;
constexpr int L_Q = L_CTL + 256;
constexpr int L_ACT = L_Q + 1024;
constexpr int L_H1 = L_ACT + 2048;
constexpr int L_RED = L_H1 + 256;
constexpr int L_X1 = L_RED + 128;
constexpr int L_MRG = L_X1 + 4096;
constexpr int L_MOD = L_MRG + 2560;
constexpr int L_END = L_MOD + 8192;
static_assert(L_END <= 160 * 1024, "LDS");
constexpr int C_FULL = 0, C_QRDY = 1, C_MCNT = 2, C_CLR = 3, C_ARDY = 4, C_TAG = 5, C_WRD = 6, C_FREE = 8;
constexpr u32 W_EPOCH = 0, W_SP = 8192, W_HG = W_SP + 4 * 1024 * 16, W_QG = W_HG + 512 * 8, W_MG = W_QG + 4096 * 8, W_PART = W_MG + 2048 * 8;
constexpr u32 PREC = 4 * 132 * 4;
constexpr u32 W_AP = W_PART + 256 * PREC, W_OP = W_AP + 8 * 4096, W_END = W_OP + 28 * 4096;
constexpr u32 CLRB = (W_END - W_PART) / 64;
static_assert(CLRB * 64 == W_END - W_PART && CLRB % 1024 == 512, "clear split");
constexpr u32 W_STAMP = 2u << 20;
constexpr u32 SENT = 0xFFFFFFFFu;
constexpr int SPIN_CAP = 1 << 16;

DEVI float bf2f(u32 v) { return __uint_as_float(v << 16); }
DEVI float bfhi(u32 v) { return __uint_as_float(v & 0xffff0000u); }
DEVI u32 pk2(float a, float b) { v2f x = {a, b}; return __builtin_bit_cast(u32, __builtin_convertvector(x, v2bf)); }
DEVI float rb(float a) { return bf2f(pk2(a, a) & 0xffffu); }
DEVI float ex2(float x) { return __builtin_amdgcn_exp2f(x); }
DEVI float rcp(float x) { return __builtin_amdgcn_rcpf(x); }
DEVI float silu(float v) { return v * rcp(1.f + ex2(v * -1.4426950408889634f)); }
DEVI float silu_exact(float v) {
  const float t = fminf(v * -1.4426950216293335f, 126.f);
  const float r = fmaf(v, -1.92596299112662e-08f, fmaf(v, -1.4426950216293335f, -t));
  float e = ex2(t);
  e = fmaf(e, fminf(r, 1.f) * 0.6931471805599453f, e);
  const float d = 1.f + e, rc = rcp(d), q = v * rc;
  return fmaf(fmaf(-q, d, v), rc, q);
}
DEVI float lo16(u32 v) { return __uint_as_float(v << 16); }
DEVI v2f rb2(v2f a) { const u32 p = pk2(a.x, a.y); return v2f{lo16(p), bfhi(p)}; }
DEVI float fold32(float a, float b) { auto s = __builtin_amdgcn_permlane32_swap(__float_as_uint(a), __float_as_uint(b), false, false); return __uint_as_float(s[0]) + __uint_as_float(s[1]); }
DEVI float fold16(float a, float b) { auto s = __builtin_amdgcn_permlane16_swap(__float_as_uint(a), __float_as_uint(b), false, false); return __uint_as_float(s[0]) + __uint_as_float(s[1]); }
#define PIN(x) asm volatile("" : "+v"(x))
DEVI float swap16_add(float x) { auto s = __builtin_amdgcn_permlane16_swap(__float_as_uint(x), __float_as_uint(x), false, false); return __uint_as_float(s[0]) + __uint_as_float(s[1]); }
DEVI float swap32_add(float x) { auto s = __builtin_amdgcn_permlane32_swap(__float_as_uint(x), __float_as_uint(x), false, false); return __uint_as_float(s[0]) + __uint_as_float(s[1]); }
DEVI float swap16_max(float x) { auto s = __builtin_amdgcn_permlane16_swap(__float_as_uint(x), __float_as_uint(x), false, false); return fmaxf(__uint_as_float(s[0]), __uint_as_float(s[1])); }
DEVI float swap32_max(float x) { auto s = __builtin_amdgcn_permlane32_swap(__float_as_uint(x), __float_as_uint(x), false, false); return fmaxf(__uint_as_float(s[0]), __uint_as_float(s[1])); }
template <int CTRL> DEVI float dpp(float x) { return __builtin_bit_cast(float, __builtin_amdgcn_mov_dpp(__builtin_bit_cast(int, x), CTRL, 0xf, 0xf, true)); }
DEVI float row_sum(float x) { x += dpp<0xB1>(x); x += dpp<0x4E>(x); x += dpp<0x141>(x); x += dpp<0x128>(x); return x; }
DEVI float half_sum(float x) { return swap16_add(row_sum(x)); }
DEVI float wave_sum(float x) { return swap32_add(half_sum(x)); }
template <typename T> DEVI LAS T* lp(u32 a) { return (LAS T*)(size_t)a; }
DEVI u32 ldsv(u32 a) { return *(volatile LAS u32*)(size_t)a; }
DEVI void stsv(u32 a, u32 v) { *(volatile LAS u32*)(size_t)a = v; }
DEVI v2f modv(u32 col, int b) { const v2u v = *lp<v2u>(L_MOD + col * 4); const int sh = 16 - 16 * b; return v2f{bfhi(v.x << sh), bfhi(v.y << sh)}; }
#define FENCE() asm volatile("" ::: "memory")
#define WAITV(n) asm volatile("s_waitcnt vmcnt(%0)" ::"i"(n) : "memory")
#define WAITL() asm volatile("s_waitcnt lgkmcnt(0)" ::: "memory")
DEVI void bar() { WAITL(); __builtin_amdgcn_s_barrier(); FENCE(); }
typedef __amdgpu_buffer_rsrc_t rsrc_t;
DEVI v4u ld4(rsrc_t r, u32 vo, u32 so) { return __builtin_bit_cast(v4u, __builtin_amdgcn_raw_buffer_load_b128(r, vo, so, 16)); }
DEVI u32 ld1(rsrc_t r, u32 vo, u32 so) { return __builtin_amdgcn_raw_buffer_load_b32(r, vo, so, 16); }
DEVI void st4(v4u v, rsrc_t r, u32 vo, u32 so) { __builtin_amdgcn_raw_buffer_store_b128(v, r, vo, so, 16); }
DEVI void st2(v2u v, rsrc_t r, u32 vo, u32 so) { __builtin_amdgcn_raw_buffer_store_b64(v, r, vo, so, 16); }
DEVI void st1(u32 v, rsrc_t r, u32 vo, u32 so) { __builtin_amdgcn_raw_buffer_store_b32(v, r, vo, so, 16); }
DEVI u32 canon(float f) { u32 b = __float_as_uint(f); return b < 0xFFFFFFFEu ? b : 0xFFFFFFFEu; }
DEVI v8bf trfrag(u32 a) {
  v4s lo = __builtin_amdgcn_ds_read_tr16_b64_v4i16(lp<v4s>(a)), hi = __builtin_amdgcn_ds_read_tr16_b64_v4i16(lp<v4s>(a + 2048));
  return __builtin_bit_cast(v8bf, __builtin_shufflevector(lo, hi, 0, 1, 2, 3, 4, 5, 6, 7));
}
DEVI v4f mfma(v8bf a, v8bf b, v4f c) { return __builtin_amdgcn_mfma_f32_16x16x32_bf16(a, b, c, 0, 0, 0); }
DEVI v8bf ldfrag(u32 a) { return __builtin_bit_cast(v8bf, *lp<v4u>(a)); }
DEVI int fpos(int k) { return (k & ~31) + ((k >> 2) & 3) * 8 + ((k >> 4) & 1) * 4 + (k & 3); }
DEVI v4i mksrd(const void* p, u32 bytes) { u64 a = (u64)p; return v4i{(int)(u32)a, (int)(u32)(a >> 32), (int)bytes, 0x00020000}; }

#define DMA4(m, v0, v1, v2, v3, srd, so)                                                                                   \
  asm volatile("s_mov_b32 m0, %0\n\ts_nop 0\n\tbuffer_load_dwordx4 %1, %5, %6 offen nt lds\n\t"                           \
               "buffer_load_dwordx4 %2, %5, %6 offen offset:1024 nt lds\n\tbuffer_load_dwordx4 %3, %5, %6 offen offset:2048 nt lds\n\t" \
               "buffer_load_dwordx4 %4, %5, %6 offen offset:3072 nt lds" ::"s"(m), "v"(v0), "v"(v1), "v"(v2), "v"(v3), "s"(srd), "s"(so) : "memory")
#define DMA1(m, v0, srd, so) \
  asm volatile("s_mov_b32 m0, %0\n\ts_nop 0\n\tbuffer_load_dwordx4 %1, %2, %3 offen nt lds" ::"s"(m), "v"(v0), "s"(srd), "s"(so) : "memory")

#define STAMP(i) do { } while (0)
#define FIN() do { } while (0)

__global__ void __launch_bounds__(512, 2)
fused_block(const u16* __restrict__ Kc, const u16* __restrict__ Vc, const u16* __restrict__ wmod, const u16* __restrict__ wq,
            const u16* __restrict__ wo, const u16* __restrict__ w1, const u16* __restrict__ w2, const u16* __restrict__ xin,
            const u16* __restrict__ cin, u16* __restrict__ out, unsigned char* ws) {
  __shared__ __attribute__((aligned(16))) unsigned char smem[L_END];
  asm volatile("" ::"s"((u32)(size_t)(LAS unsigned char*)smem));
  const int tid = threadIdx.x, lane = tid & 63, wave = __builtin_amdgcn_readfirstlane(tid >> 6);
  const int bid = blockIdx.x, grp = bid >> 4, mem = bid & 15;
  const int g = lane >> 4, c = lane & 15;
  if (tid < 64) stsv(L_CTL + 4 * tid, 0);
  WAITL(); __builtin_amdgcn_s_barrier(); FENCE();
  const int r = (mem + (grp >> 1)) & 15;
  const bool isX = r < 4, isQ = r >= 4 && r < 8, isM = r == 8 || r == 9, isY = (r == 10 || r == 11) && grp < 14, isS = r >= 12, isR = r == 0 && (grp & 1);
  const int xi = grp * 4 + r, qi = grp * 4 + r - 4, mi = grp * 2 + r - 8, yi = grp * 2 + r - 10;
  const int u0 = (isQ || isM) ? 4 : isS ? 1 : 0;
  asm volatile("" ::"s"(Kc), "s"(Vc), "s"(wmod), "s"(wq), "s"(wo), "s"(w1), "s"(w2), "s"(xin), "s"(cin), "s"(out), "s"(ws));
  const int na = grp < 14 ? NA_A : NA_B, ny = grp < 14 ? NY_A : NY_B, ns = grp < 14 ? NS_A : NS_B;
  const int nkv = r < 10 ? na : r < 12 ? ny : ns;
  const int kv0 = min(r, 10) * na + min(max(r - 10, 0), 2) * ny + max(r - 12, 0) * ns;
  const int ntail = isX ? 4 : isY ? 8 : 0;
  const int uw = u0 + nkv;
  const rsrc_t rws = __builtin_amdgcn_make_buffer_rsrc(ws, 0, W_END, 0x00020000);

  u64 E = 0;
  if (wave == 0) {
    __builtin_amdgcn_s_setprio(3);
    STAMP(0);
    int pub = 0, iss = 0;
    auto publish = [&](int v) { if (v > pub) { pub = v; stsv(L_CTL + 4 * C_FULL, v); } };
#define PACE(W_) do { WAITV(W_); publish(iss - (W_)); } while (0)
    auto wait_slot = [&](int u) {
      if (u >= NSLOT) {
        const u32 a = L_CTL + 4 * (C_FREE + (u & (NSLOT - 1))), need = u - NSLOT + 1;
        if (ldsv(a) < need) {
          PACE(0); while (ldsv(a) < need) __builtin_amdgcn_s_sleep(1);
        }
      }
    };
    const u32 wrow = lane >> 3, wlc = (lane & 7) ^ (((wrow >> 1) & 3) << 1);
#define WUNIT(u, srd, rs, so0, bq, W_)                                                               \
  {                                                                                                        \
    const u32 st_ = 8 * (rs) - 1024;                                                                       \
    const u32 a0 = wrow * (rs) + wlc * 16, a1 = a0 + st_, a2 = a1 + st_, a3 = a2 + st_; \
    const u32 lb = ((u) & (NSLOT - 1)) * SLOT;                                                             \
    { const u32 m = lb, so = (so0); DMA4(m, a0, a1, a2, a3, srd, so); }                                    \
    iss += 4; W_;                                                                                          \
    { const u32 m = lb + 4096, so = (so0) + 32 * (rs); DMA4(m, a0, a1, a2, a3, srd, so); }                 \
    iss += 4; W_;                                                                                          \
    { const u32 m = lb + 8192, so = (so0) + (bq); DMA4(m, a0, a1, a2, a3, srd, so); }                      \
    iss += 4; W_;                                                                                          \
    { const u32 m = lb + 12288, so = (so0) + (bq) + 32 * (rs); DMA4(m, a0, a1, a2, a3, srd, so); }         \
    iss += 4; W_;                                                                                          \
  }
    const v4i rk = mksrd(Kc + (size_t)grp * 32768 * 128, 32768 * 256), rv = mksrd(Vc + (size_t)grp * 32768 * 128, 32768 * 256);
    const u32 k0 = g * 256 + ((c ^ g) << 4), k1 = g * 256 + ((c ^ (4 + g)) << 4), k2 = g * 256 + ((c ^ (8 + g)) << 4), k3 = g * 256 + ((c ^ (12 + g)) << 4);
    const u32 v0 = g * 256 + ((c ^ (2 * g)) << 4), v1 = g * 256 + ((c ^ (8 + 2 * g)) << 4);
    if (u0) {
      const v4i srd = isQ ? mksrd(wq, 512 * 4096 * 2) : mksrd(wmod, 512 * 3072 * 2);
      const u32 rs = isQ ? 8192 : 6144, col = isQ ? qi * 128 : isM ? 2048 + mi * 128 : (r - 12) * 128 * 6144 + grp * 128;
#pragma nounroll
      for (int u = 0; u < u0; ++u) {
        WUNIT(u, srd, rs, u * 128 * rs + col, 64 * rs, WAITV(28))
      }
    }
    {
      const u32 so = (u32)kv0 * 8192, lb = u0 * SLOT;
      { const u32 m = lb; DMA4(m, k0, k1, k2, k3, rk, so); }
      { const u32 m = lb + 4096, s = so + 4096; DMA4(m, k0, k1, k2, k3, rk, s); }
      iss += 8;
    }
#define TPS()
#define AFTER() do { publish(iss - WIN); iss += 4; } while (0)
    u32 fr = 0;
    WAITV(WIN);
#pragma nounroll
    for (int u = u0; u < uw; ++u) {
      const u32 so = (u32)(kv0 + u - u0) * 8192;
      TPS();
      if (u + 1 < uw) {
        if ((int)fr < u + 2 - NSLOT) wait_slot(u + 1);
        const u32 lb = ((u + 1) & (NSLOT - 1)) * SLOT;
        { const u32 m = lb, s = so + 8192; DMA4(m, k0, k1, k2, k3, rk, s); }
        AFTER(); WAITV(WIN);
        { const u32 m = lb + 4096, s = so + 12288; DMA4(m, k0, k1, k2, k3, rk, s); }
        AFTER(); WAITV(WIN);
      }
      const u32 lb = (u & (NSLOT - 1)) * SLOT;
      { const u32 m = lb + 8192; DMA4(m, v0, v1, v0, v1, rv, so); }
      AFTER(); WAITV(WIN);
      { const u32 m = lb + 12288, s = so + 4096; DMA4(m, v0, v1, v0, v1, rv, s); }
      AFTER();
      fr = ldsv(L_CTL + 4 * (C_FREE + ((u + 2) & (NSLOT - 1))));
      WAITV(WIN);
    }
    publish(iss - WIN);
#pragma nounroll
    for (int t = 0; t < ntail; ++t) {
      const int u = uw + t;
      wait_slot(u);
      const bool y1 = !isX && t < 4;
      const v4i srd = mksrd(isX ? wo : y1 ? w1 : w2, isX ? 4096 * 512 * 2 : 512 * 1792 * 2);
      const u32 rs = y1 ? 3584 : 1024;
      const u32 so0 = isX ? ((xi >> 3) * 512 + t * 128) * 1024 + (xi & 7) * 128 : y1 ? t * 128 * 3584 + yi * 128 : yi * 64 * 1024 + (t - 4) * 256;
      WUNIT(u, srd, rs, so0, (isX || y1) ? 64 * rs : 128, PACE(WIN))
    }
    PACE(16); PACE(8); PACE(0);
    STAMP(1);
    __builtin_amdgcn_s_setprio(0);
  } else {
    auto wait_full = [&](u32 need) { while (ldsv(L_CTL + 4 * C_FULL) < need) __builtin_amdgcn_s_sleep(1); FENCE(); };
    const u32 trow = 4 * g + (c >> 2);
    auto waddr = [&](int t) -> u32 { return trow * 128 + ((((u32)(t ^ ((trow >> 1) & 3)) << 1) | ((c >> 1) & 1)) << 4) + (c & 1) * 8; };

    if (wave == 1) {
      const u64 tstart = __builtin_amdgcn_s_memrealtime();
      u64 E0 = __hip_atomic_load((GAS u64*)(ws + W_EPOCH), __ATOMIC_RELAXED, __HIP_MEMORY_SCOPE_AGENT);
      u32 tag;
#define CHAIN_START() do { E = E0 + 1; tag = (u32)E; } while (0)
      auto silu4 = [&](u32 x, u32 y) { return v2u{pk2(silu_exact(lo16(x)), silu_exact(bfhi(x))), pk2(silu_exact(lo16(y)), silu_exact(bfhi(y)))}; };
      if (isS) {
        const v2u cv = *(const v2u*)(cin + (lane >> 5) * 512 + (r - 12) * 128 + (lane & 31) * 4);
        CHAIN_START();
        *lp<v2u>(L_ACT + (lane >> 5) * 1024 + fpos((lane & 31) * 4) * 2) = silu4(cv.x, cv.y);
        FENCE();
        v8bf act[4];
#pragma unroll
        for (int ks = 0; ks < 4; ++ks) { act[ks] = ldfrag(L_ACT + (c & 1) * 1024 + g * 16 + ks * 64); PIN(act[ks]); }
        wait_full(16);
        v8bf W[16];
#pragma unroll
        for (int j = 0; j < 16; ++j) W[j] = trfrag(waddr(j & 3) + (j >> 2) * 4096);
        FENCE(); stsv(L_CTL + 4 * C_FREE, 1);
        v4f acc[4];
#pragma unroll
        for (int t = 0; t < 4; ++t) acc[t] = v4f{0.f, 0.f, 0.f, 0.f};
#pragma unroll
        for (int j = 0; j < 16; ++j) acc[j & 3] = mfma(act[j >> 2], W[j], acc[j & 3]);
        const float r0 = g == 0 ? acc[0][0] : g == 1 ? acc[1][0] : g == 2 ? acc[2][0] : acc[3][0];
        const float r1 = g == 0 ? acc[0][1] : g == 1 ? acc[1][1] : g == 2 ? acc[2][1] : acc[3][1];
        while (ldsv(L_CTL + 4 * C_CLR) == 0) { }
        st4(v4u{__float_as_uint(r0), tag, __float_as_uint(r1), tag}, rws, lane * 16, W_SP + ((r - 12) * 1024 + grp * 64) * 16);
        STAMP(3);
      } else if (isR) {
        const v4u in0 = *(const v4u*)(xin + lane * 8), in1 = *(const v4u*)(xin + 512 + lane * 8);
        u32 xs0 = *(const u32*)(xin + (grp >> 1) * 64 + (lane & ~1)), xs1 = *(const u32*)(xin + 512 + (grp >> 1) * 64 + (lane & ~1));
        CHAIN_START();
        PIN(xs0); PIN(xs1);
        xs0 >>= 16 * (lane & 1); xs1 >>= 16 * (lane & 1);
        float mr[2], ri[2];
#pragma unroll
        for (int b = 0; b < 2; ++b) {
          const v4u xv = b ? in1 : in0;
          const float x[8] = {lo16(xv.x), bfhi(xv.x), lo16(xv.y), bfhi(xv.y), lo16(xv.z), bfhi(xv.z), lo16(xv.w), bfhi(xv.w)};
          float s = 0.f;
#pragma unroll
          for (int i = 0; i < 8; ++i) s += x[i];
          const float mean = wave_sum(s) * (1.f / 512.f);
          float q2 = 0.f;
#pragma unroll
          for (int i = 0; i < 8; ++i) q2 += (x[i] - mean) * (x[i] - mean);
          const float var = wave_sum(q2) * (1.f / 512.f);
          mr[b] = rb(mean); ri[b] = rcp(rb(__builtin_amdgcn_sqrtf(rb(rb(var) + 1e-5f))));
        }
        v2f y = rb2(rb2(v2f{lo16(xs0) - mr[0], lo16(xs1) - mr[1]}) * v2f{ri[0], ri[1]});
        PIN(y);
        v4u a[8];
#pragma nounroll
        for (int sp = 0; sp < SPIN_CAP; ++sp) {
          u32 bad = 0;
#pragma unroll
          for (int i = 0; i < 8; ++i) { a[i] = ld4(rws, lane * 16, W_SP + ((i & 3) * 1024 + (i >> 2) * 512 + (grp >> 1) * 64) * 16); bad |= (a[i].y ^ tag) | (a[i].w ^ tag); }
          if (__all(bad == 0)) break;
          FENCE();
        }
        v2f sc[2];
#pragma unroll
        for (int pt = 0; pt < 2; ++pt) {
          sc[pt] = v2f{0.f, 0.f};
#pragma unroll
          for (int k4 = 0; k4 < 4; ++k4) sc[pt] += v2f{__uint_as_float(a[pt * 4 + k4].x), __uint_as_float(a[pt * 4 + k4].z)};
        }
        const v2f h = rb2(y * rb2(rb2(sc[1]) + 1.f)) + rb2(sc[0]);
        st2(v2u{pk2(h.x, h.y), tag}, rws, lane * 8, W_HG + (grp >> 1) * 512);
        STAMP(23);
      } else if (isM || isQ) {
        if (isM) {
          const v4u in0 = *(const v4u*)(cin + lane * 8), in1 = *(const v4u*)(cin + 512 + lane * 8);
          CHAIN_START();
          *lp<v2u>(L_ACT + fpos(lane * 8) * 2) = silu4(in0.x, in0.y); *lp<v2u>(L_ACT + fpos(lane * 8 + 4) * 2) = silu4(in0.z, in0.w);
          *lp<v2u>(L_ACT + 1024 + fpos(lane * 8) * 2) = silu4(in1.x, in1.y); *lp<v2u>(L_ACT + 1024 + fpos(lane * 8 + 4) * 2) = silu4(in1.z, in1.w);
        } else {
          CHAIN_START();
          while (__builtin_amdgcn_s_memrealtime() < tstart + HDELAY) __builtin_amdgcn_s_sleep(4);
          v4u a[4];
#pragma nounroll
          for (int sp = 0; sp < SPIN_CAP; ++sp) {
            u32 bad = 0;
#pragma unroll
            for (int i = 0; i < 4; ++i) { a[i] = ld4(rws, lane * 16 + i * 1024, W_HG); bad |= (a[i].y ^ tag) | (a[i].w ^ tag); }
            if (__all(bad == 0)) break;
            FENCE();
          }
#pragma unroll
          for (int i = 0; i < 4; ++i) {
            const u32 wa = L_ACT + fpos(i * 128 + lane * 2) * 2;
            *lp<u32>(wa) = (a[i].x & 0xffffu) | (a[i].z << 16); *lp<u32>(wa + 1024) = (a[i].x >> 16) | (a[i].z & 0xffff0000u);
          }
          STAMP(4);
        }
        FENCE(); stsv(L_CTL + 4 * C_TAG, tag); stsv(L_CTL + 4 * C_ARDY, 1);
      } else CHAIN_START();
      {
        wait_full(16 * (u0 + QAT));
        v4u a[2];
#pragma nounroll
        for (int sp = 0; sp < SPIN_CAP; ++sp) {
          a[0] = ld4(rws, lane * 16, W_QG + grp * 2048); a[1] = ld4(rws, lane * 16 + 1024, W_QG + grp * 2048);
          if (__all(((a[0].y ^ tag) | (a[0].w ^ tag) | (a[1].y ^ tag) | (a[1].w ^ tag)) == 0)) break;
          __builtin_amdgcn_s_sleep(POLLS); FENCE();
        }
        *lp<v2u>(L_Q + lane * 8) = v2u{a[0].x, a[0].z}; *lp<v2u>(L_Q + 512 + lane * 8) = v2u{a[1].x, a[1].z};
        FENCE(); stsv(L_CTL + 4 * C_QRDY, 1);
        STAMP(6);
      }
      if (isY) {
        wait_full(16 * 24);
        v4u a[16];
#pragma nounroll
        for (int sp = 0; sp < SPIN_CAP; ++sp) {
          u32 bad = 0;
#pragma unroll
          for (int i = 0; i < 16; ++i) { a[i] = ld4(rws, lane * 16 + (i & 3) * 1024, W_MG + (i >> 2) * 4096); bad |= (a[i].y ^ tag) | (a[i].w ^ tag); }
          if (__all(bad == 0)) break;
          __builtin_amdgcn_s_sleep(32); FENCE();
        }
#pragma unroll
        for (int i = 0; i < 16; ++i) *lp<v2u>(L_MOD + i * 512 + lane * 8) = v2u{a[i].x, a[i].z};
      }
      STAMP(2);
    } else {
      if (isS && wave == 7) {
        const v4u s4 = {SENT, SENT, SENT, SENT};
        const u32 so = W_PART + (grp * 4 + r - 12) * CLRB;
#pragma unroll
        for (int i = 0; i < CLRB / 1024; ++i) st4(s4, rws, lane * 16 + (i & 3) * 1024, so + (i >> 2) * 4096);
        if (lane < 32) st4(s4, rws, lane * 16, so + CLRB - 512);
        WAITV(0); stsv(L_CTL + 4 * C_CLR, 1);
      }

      if (u0 == 4 && wave < 6) {
        wait_full(64);
        v8bf W[16];
#pragma unroll
        for (int ks = 0; ks < 16; ++ks) W[ks] = trfrag((ks >> 2) * SLOT + (ks & 3) * 4096 + waddr(wave - 2));
        WAITL();
        u32 old = 0;
        if (lane == 0) old = __hip_atomic_fetch_add(lp<u32>(L_CTL + 4 * C_WRD), 1u, __ATOMIC_RELAXED, __HIP_MEMORY_SCOPE_WORKGROUP);
        if (__builtin_amdgcn_readfirstlane(old) == 3 && lane < 4) stsv(L_CTL + 4 * (C_FREE + lane), lane + 1);
        while (ldsv(L_CTL + 4 * C_ARDY) == 0) __builtin_amdgcn_s_sleep(1);
        FENCE();
        const u32 tag = ldsv(L_CTL + 4 * C_TAG);
        v8bf A[16];
#pragma unroll
        for (int ks = 0; ks < 16; ++ks) A[ks] = ldfrag(L_ACT + (c & 1) * 1024 + g * 16 + ks * 64);
        __builtin_amdgcn_sched_barrier(0);
        v4f d0 = {0.f, 0.f, 0.f, 0.f}, d1 = {0.f, 0.f, 0.f, 0.f};
#pragma unroll
        for (int ks = 0; ks < 16; ks += 2) { d0 = mfma(A[ks], W[ks], d0); d1 = mfma(A[ks + 1], W[ks + 1], d1); }
        const float r0 = d0[0] + d1[0], r1 = d0[1] + d1[1];
        const float n0 = dpp<0xB1>(r0), n1 = dpp<0xB1>(r1);
        const bool odd = lane & 1;
        if (lane < 16) {
          if (isM) st2(v2u{pk2(r0, r1), tag}, rws, lane * 8, W_MG + mi * 512 + (wave - 2) * 128);
          else st2(v2u{pk2(odd ? n1 : r0, odd ? r1 : n0), tag}, rws, (lane & 1) * 16384 + (lane >> 1) * 8, W_QG + qi * 256 + (wave - 2) * 64);
        }
        if (isQ && wave == 2) STAMP(5);
      }
      if (wave == 6) { *lp<v4f>(L_MRG + lane * 16) = v4f{0.f, 0.f, 0.f, 0.f}; *lp<v4f>(L_MRG + 1024 + lane * 16) = v4f{0.f, 0.f, 0.f, 0.f}; *lp<v2f>(L_MRG + 2048 + lane * 8) = v2f{-INFINITY, 0.f}; }
      v8bf qf[4];
      float m = -INFINITY, l = 0.f;
      v4f acc[8];
#pragma unroll
      for (int t = 0; t < 8; ++t) acc[t] = v4f{0.f, 0.f, 0.f, 0.f};
      u32 ka[4], va[8];
#pragma unroll
      for (int s = 0; s < 4; ++s) ka[s] = c * 256 + (((4 * s + g) ^ c) << 4);
#pragma unroll
      for (int t = 0; t < 8; ++t) va[t] = 8192 + trow * 256 + ((((u32)(t ^ (trow & 7)) << 1) | ((c >> 1) & 1)) << 4) + (c & 1) * 8;
      int u = u0 + wave - 2;
      auto rdunit = [&](v8bf (&kf)[2][4], v8bf (&vf)[8]) {
        const u32 lb = (u & (NSLOT - 1)) * SLOT;
        wait_full(16 * u + 24);
#pragma unroll
        for (int s = 0; s < 4; ++s) { kf[0][s] = ldfrag(lb + ka[s]); kf[1][s] = ldfrag(lb + ka[s] + 4096); }
#pragma unroll
        for (int t = 0; t < 8; ++t) {
          v4s lo = __builtin_amdgcn_ds_read_tr16_b64_v4i16(lp<v4s>(lb + va[t])), hi = __builtin_amdgcn_ds_read_tr16_b64_v4i16(lp<v4s>(lb + va[t] + 4096));
          vf[t] = __builtin_bit_cast(v8bf, __builtin_shufflevector(lo, hi, 0, 1, 2, 3, 4, 5, 6, 7));
        }
        FENCE(); stsv(L_CTL + 4 * (C_FREE + (u & (NSLOT - 1))), u + 1);
        u += NCONS;
      };
      v8bf sk[2][4], sv[8];
      rdunit(sk, sv);
#pragma nounroll
      for (int it = 0; it < 2 || u + NCONS < uw; ++it) {
        v8bf kf[2][4], vf[8];
        if (it == 1) {
#pragma unroll
          for (int s = 0; s < 4; ++s) { kf[0][s] = sk[0][s]; kf[1][s] = sk[1][s]; }
#pragma unroll
          for (int t = 0; t < 8; ++t) vf[t] = sv[t];
        } else rdunit(kf, vf);
        if (it == 0) {
          while (ldsv(L_CTL + 4 * C_QRDY) == 0) __builtin_amdgcn_s_sleep(2);
          FENCE();
#pragma unroll
          for (int s = 0; s < 4; ++s) qf[s] = ldfrag(L_Q + (c & 3) * 256 + (4 * s + g) * 16);
        }
        v4f S[2] = {v4f{0.f, 0.f, 0.f, 0.f}, v4f{0.f, 0.f, 0.f, 0.f}};
#pragma unroll
        for (int s = 0; s < 4; ++s) { S[0] = mfma(kf[0][s], qf[s], S[0]); S[1] = mfma(kf[1][s], qf[s], S[1]); }
        float sc[8];
#pragma unroll
        for (int i = 0; i < 8; i += 2) {
          const v2f r = rb2(rb2(v2f{S[i >> 2][i & 3], S[i >> 2][(i & 3) + 1]}) * 0.08838834764831845f);
          sc[i] = r.x; sc[i + 1] = r.y;
        }
        float tm = fmaxf(fmaxf(fmaxf(sc[0], sc[1]), fmaxf(sc[2], sc[3])), fmaxf(fmaxf(sc[4], sc[5]), fmaxf(sc[6], sc[7])));
        tm = swap32_max(swap16_max(tm));
        const float mn = fmaxf(m, tm * 1.4426950408889634f);
        const float alpha = ex2(m - mn);
        m = mn;
        float p[8], ps = 0.f;
#pragma unroll
        for (int i = 0; i < 8; ++i) { p[i] = ex2(fmaf(sc[i], 1.4426950408889634f, -mn)); ps += p[i]; }
        l = fmaf(l, alpha, ps);
        const v8bf pf = __builtin_bit_cast(v8bf, v4u{pk2(p[0], p[1]), pk2(p[2], p[3]), pk2(p[4], p[5]), pk2(p[6], p[7])});
#pragma unroll
        for (int t = 0; t < 8; ++t) acc[t] = mfma(vf[t], pf, acc[t] * alpha);
      }
      {
        const int link = u - (uw - NCONS), d = c >> 2;
        const u32 lb = (u & (NSLOT - 1)) * SLOT;
        v4f e0 = d == 0 ? acc[0] : d == 1 ? acc[2] : d == 2 ? acc[4] : acc[6];
        v4f e1 = d == 0 ? acc[1] : d == 1 ? acc[3] : d == 2 ? acc[5] : acc[7];
        PIN(e0); PIN(e1);
        wait_full(16 * u);
        v8bf kf[2][4];
#pragma unroll
        for (int s = 0; s < 4; ++s) { kf[0][s] = ldfrag(lb + ka[s]); kf[1][s] = ldfrag(lb + ka[s] + 4096); }
        v4f S[2] = {v4f{0.f, 0.f, 0.f, 0.f}, v4f{0.f, 0.f, 0.f, 0.f}};
#pragma unroll
        for (int s = 0; s < 4; ++s) { S[0] = mfma(kf[0][s], qf[s], S[0]); S[1] = mfma(kf[1][s], qf[s], S[1]); }
        float sc[8];
#pragma unroll
        for (int i = 0; i < 8; i += 2) {
          const v2f r = rb2(rb2(v2f{S[i >> 2][i & 3], S[i >> 2][(i & 3) + 1]}) * 0.08838834764831845f);
          sc[i] = r.x; sc[i + 1] = r.y;
        }
        float tm = fmaxf(fmaxf(fmaxf(sc[0], sc[1]), fmaxf(sc[2], sc[3])), fmaxf(fmaxf(sc[4], sc[5]), fmaxf(sc[6], sc[7])));
        const float mn = fmaxf(m, swap32_max(swap16_max(tm)) * 1.4426950408889634f);
        const float al = ex2(m - mn);
        float p[8], ps = 0.f;
#pragma unroll
        for (int i = 0; i < 8; ++i) { p[i] = ex2(fmaf(sc[i], 1.4426950408889634f, -mn)); ps += p[i]; }
        float Lo = swap32_add(swap16_add(fmaf(l, al, ps)));
        e0 *= al; e1 *= al;
        const v4u pf = {pk2(p[0], p[1]), pk2(p[2], p[3]), pk2(p[4], p[5]), pk2(p[6], p[7])};
        v4u pm[4];
#pragma unroll
        for (int j = 0; j < 4; ++j) { pm[j] = d == j ? pf : v4u{0u, 0u, 0u, 0u}; PIN(pm[j]); }
        u32 oa = (c & 3) * 528 + d * 128 + g * 16;
        u32 vb[8];
#pragma unroll
        for (int t = 0; t < 8; ++t) { vb[t] = lb + va[t]; PIN(vb[t]); }
        PIN(e0); PIN(e1); PIN(Lo); PIN(oa);
        while (ldsv(L_CTL + 4 * C_FULL) < (u32)min(16 * u + 24, 16 * uw)) { }
        FENCE();
        v8bf vf[8];
#pragma unroll
        for (int t = 0; t < 8; ++t) {
          v4s lo = __builtin_amdgcn_ds_read_tr16_b64_v4i16(lp<v4s>(vb[t])), hi = __builtin_amdgcn_ds_read_tr16_b64_v4i16(lp<v4s>(vb[t] + 4096));
          vf[t] = __builtin_bit_cast(v8bf, __builtin_shufflevector(lo, hi, 0, 1, 2, 3, 4, 5, 6, 7));
        }
        v4f r0, r1; v2f ml;
        for (;;) {
          const u32 cnt = ldsv(L_CTL + 4 * C_MCNT);
          FENCE();
          r0 = *lp<v4f>(L_MRG + lane * 16); r1 = *lp<v4f>(L_MRG + 1024 + lane * 16); ml = *lp<v2f>(L_MRG + 2048 + lane * 8);
          FENCE();
          if (cnt >= (u32)link) break;
        }
#pragma unroll
        for (int j = 0; j < 4; ++j) { e0 = mfma(vf[2 * j], __builtin_bit_cast(v8bf, pm[j]), e0); e1 = mfma(vf[2 * j + 1], __builtin_bit_cast(v8bf, pm[j]), e1); }
        const float M = fmaxf(mn, ml.x);
        float alpha = ex2(mn - M), beta = ex2(ml.x - M);
        const float L = fmaf(ml.y, beta, Lo * alpha);
        const float inv = link == NCONS - 1 ? rcp(L) : 1.f;
        alpha *= inv; beta *= inv;
        r0 *= beta; r1 *= beta;
        e0 = e0 * alpha + r0; e1 = e1 * alpha + r1;
        if (link == NCONS - 1) {
          st4(v4u{canon(e0.x), canon(e0.y), canon(e0.z), canon(e0.w)}, rws, oa, W_PART + bid * PREC);
          st4(v4u{canon(e1.x), canon(e1.y), canon(e1.z), canon(e1.w)}, rws, oa + 64, W_PART + bid * PREC);
          if (lane < 4) st1(canon(M + __builtin_amdgcn_logf(L)), rws, lane * 528 + 512, W_PART + bid * PREC);
          STAMP(8);
        } else {
          *lp<v4f>(L_MRG + lane * 16) = e0; *lp<v4f>(L_MRG + 1024 + lane * 16) = e1; *lp<v2f>(L_MRG + 2048 + lane * 8) = v2f{M, L};
          FENCE(); stsv(L_CTL + 4 * C_MCNT, link + 1);
        }
        stsv(L_CTL + 4 * (C_FREE + (u & (NSLOT - 1))), u + 1);
      }
    }
  }
  if (!(isX || isY)) { FIN(); return; }
  bar();
  const u32 trow = 4 * g + (c >> 2);
  auto waddr = [&](int t) -> u32 { return trow * 128 + ((((u32)(t ^ ((trow >> 1) & 3)) << 1) | ((c >> 1) & 1)) << 4) + (c & 1) * 8; };
  auto wslot = [&](int t) -> u32 { return ((uw + t) & (NSLOT - 1)) * SLOT; };
  const int half = lane >> 5, i32 = lane & 31;
  const u32 fa = L_ACT + (c & 1) * 1024 + g * 16;
  if (isX) {
    const int kvh = xi >> 3, ng = xi & 7;
    v8bf W[16];
    if (wave < 4) {
#pragma unroll
      for (int ks = 0; ks < 16; ++ks) W[ks] = trfrag(wslot(ks >> 2) + (ks & 3) * 4096 + waddr(wave));
    }
    const int b = wave >> 2, h = wave & 3;
    const u32 so = W_PART + ((b * 8 + kvh) * 16) * PREC + h * 528;
    u32 wa = L_ACT + b * 1024 + fpos(h * 128 + i32 * 4 + half * 2) * 2;
    PIN(wa);
    v4u a[8]; u32 ls[8];
#pragma nounroll
    for (int sp = 0; sp < SPIN_CAP; ++sp) {
#pragma unroll
      for (int j = 0; j < 8; ++j) { a[j] = ld4(rws, half * PREC + i32 * 16, so + j * 2 * PREC); ls[j] = ld1(rws, half * PREC + 512, so + j * 2 * PREC); }
      u32 mx = 0;
#pragma unroll
      for (int j = 0; j < 8; ++j) { mx = max(max(mx, ls[j]), max(max(a[j].x, a[j].y), max(a[j].z, a[j].w))); }
      if (__all(mx != SENT)) break;
      FENCE();
    }
    float M = __uint_as_float(ls[0]);
#pragma unroll
    for (int j = 1; j < 8; ++j) M = fmaxf(M, __uint_as_float(ls[j]));
    M = swap32_max(M);
    float L = 0.f; v4f o = v4f{0.f, 0.f, 0.f, 0.f};
#pragma unroll
    for (int j = 0; j < 8; ++j) { const float e = ex2(__uint_as_float(ls[j]) - M); L += e; o += __builtin_bit_cast(v4f, a[j]) * e; }
    const float inv = rcp(swap32_add(L));
    *lp<u32>(wa) = pk2(fold32(o.x, o.z) * inv, fold32(o.y, o.w) * inv);
    if (wave == 0) STAMP(9);
    bar();
    if (wave < 4) {
      v8bf A[16];
#pragma unroll
      for (int ks = 0; ks < 16; ++ks) A[ks] = ldfrag(fa + ks * 64);
      __builtin_amdgcn_sched_barrier(0);
      v4f d0 = {0.f, 0.f, 0.f, 0.f}, d1 = {0.f, 0.f, 0.f, 0.f};
#pragma unroll
      for (int ks = 0; ks < 16; ks += 2) { d0 = mfma(W[ks], A[ks], d0); d1 = mfma(W[ks + 1], A[ks + 1], d1); }
      d0 += d1;
      if (c < 2) st4(v4u{canon(d0.x), canon(d0.y), canon(d0.z), canon(d0.w)}, rws, c * 2048 + g * 16, W_AP + kvh * 4096 + ng * 256 + wave * 64);
      if (wave == 0) STAMP(10);
    }
    FIN();
    return;
  }
  {
    v8bf W[16];
    if (wave < 4) {
#pragma unroll
      for (int ks = 0; ks < 16; ++ks) W[ks] = trfrag(wslot(ks >> 2) + (ks & 3) * 4096 + waddr(wave));
    } else {
#pragma unroll
      for (int j = 0; j < 16; ++j) W[j] = trfrag(wslot(wave) + (j >> 3) * 8192 + (j & 1) * 4096 + waddr((j >> 1) & 3));
    }
    const int b = wave >> 2, col = (wave & 3) * 128 + i32 * 4 + half * 2;
    const u32 xv = *(const u32*)(xin + b * 512 + col);
    v2f x0 = {lo16(xv), bfhi(xv)};
    v2f ga = modv(col, b), sb = modv(512 + col, b), cb1 = rb2(modv(1024 + col, b) + 1.f);
    u32 wa = L_ACT + b * 1024 + fpos(col) * 2, xa = L_X1 + b * 2048 + col * 4, ra = L_RED + wave * 8 + half * 4;
    u32 ha = L_H1 + (c & 1) * 128 + fpos((wave & 3) * 16 + g * 4 + ((c >> 1) & 3)) * 2;
    PIN(x0); PIN(ga); PIN(sb); PIN(cb1); PIN(wa); PIN(xa); PIN(ra); PIN(ha);
    v4u a[4];
#pragma nounroll
    for (int sp = 0; sp < SPIN_CAP; ++sp) {
#pragma unroll
      for (int j = 0; j < 4; ++j) a[j] = ld4(rws, half * 16384 + i32 * 16, W_AP + j * 4096 + b * 2048 + (wave & 3) * 512);
      u32 mx = 0;
#pragma unroll
      for (int j = 0; j < 4; ++j) mx = max(mx, max(max(a[j].x, a[j].y), max(a[j].z, a[j].w)));
      if (__all(mx != SENT)) break;
      FENCE();
    }
    if (wave == 0) STAMP(11);
    const v4f at = (__builtin_bit_cast(v4f, a[0]) + __builtin_bit_cast(v4f, a[1])) + (__builtin_bit_cast(v4f, a[2]) + __builtin_bit_cast(v4f, a[3]));
    const v2f x1 = rb2(x0 + rb2(ga * rb2(v2f{fold32(at.x, at.z), fold32(at.y, at.w)})));
    const float st = half_sum(fold32(x1.x + x1.y, fmaf(x1.x, x1.x, x1.y * x1.y)));
    if (i32 == 0) *lp<float>(ra) = st;
    *lp<v2f>(xa) = x1;
    bar();
    if (wave == 1 && yi == 0 && lane == 0) __hip_atomic_store((GAS u64*)(ws + W_EPOCH), E, __ATOMIC_RELAXED, __HIP_MEMORY_SCOPE_AGENT);
    {
      const v4f r0 = *lp<v4f>(L_RED + b * 32), r1 = *lp<v4f>(L_RED + b * 32 + 16);
      const float mean = ((r0.x + r0.z) + (r1.x + r1.z)) * (1.f / 512.f);
      const float var = fmaxf(fmaf(-mean, mean, ((r0.y + r0.w) + (r1.y + r1.w)) * (1.f / 512.f)), 0.f);
      const v2f mv = rb2(v2f{mean, var});
      const float ri = rcp(rb(__builtin_amdgcn_sqrtf(rb(mv.y + 1e-5f))));
      const v2f hh = rb2(rb2(rb2(x1 - mv.x) * ri) * cb1) + sb;
      *lp<u32>(wa) = pk2(hh.x, hh.y);
    }
    bar();
    if (wave < 4) {
      v8bf A[16];
#pragma unroll
      for (int ks = 0; ks < 16; ++ks) A[ks] = ldfrag(fa + ks * 64);
      __builtin_amdgcn_sched_barrier(0);
      v4f d0 = {0.f, 0.f, 0.f, 0.f}, d1 = {0.f, 0.f, 0.f, 0.f};
#pragma unroll
      for (int ks = 0; ks < 16; ks += 2) { d0 = mfma(W[ks], A[ks], d0); d1 = mfma(W[ks + 1], A[ks + 1], d1); }
      d0 += d1;
      const int r = (c >> 1) & 3;
      const float v = silu(rb(r == 0 ? d0.x : r == 1 ? d0.y : r == 2 ? d0.z : d0.w));
      *lp<u16>(ha) = (u16)pk2(v, v);
    }
    bar();
    if (wave >= 4) {
      const v8bf a0 = ldfrag(L_H1 + (c & 1) * 128 + g * 16), a1 = ldfrag(L_H1 + (c & 1) * 128 + 64 + g * 16);
      v4f d[8];
#pragma unroll
      for (int t = 0; t < 8; ++t) d[t] = mfma(W[2 * t + 1], a1, mfma(W[2 * t], a0, v4f{0.f, 0.f, 0.f, 0.f}));
      v4f r = d[0];
#pragma unroll
      for (int t = 1; t < 8; ++t) if ((c >> 1) == t) r = d[t];
      st4(v4u{canon(r.x), canon(r.y), canon(r.z), canon(r.w)}, rws, (c & 1) * 2048 + (c >> 1) * 64 + g * 16, W_OP + yi * 4096 + (wave - 4) * 512);
      if (wave == 4) STAMP(12);
    } else if (wave == 0 && yi < 16) {
      const int zb = yi >> 3, zc = (yi & 7) * 64 + c * 4 + (g >> 1) * 2 + (g & 1);
      float xx = *lp<float>(L_X1 + zb * 2048 + zc * 4), gb = bfhi(*lp<u32>(L_MOD + (1536 + zc) * 4) << (16 - 16 * zb));
      PIN(xx); PIN(gb);
      v4u a[7];
#pragma nounroll
      for (int sp = 0; sp < SPIN_CAP; ++sp) {
#pragma unroll
        for (int j = 0; j < 7; ++j) a[j] = ld4(rws, g * 4096 + c * 16, W_OP + j * 16384 + zb * 2048 + (yi & 7) * 256);
        u32 mx = 0;
#pragma unroll
        for (int j = 0; j < 7; ++j) mx = max(mx, max(max(a[j].x, a[j].y), max(a[j].z, a[j].w)));
        if (__all(mx != SENT)) break;
        FENCE();
      }
      v4f s = __builtin_bit_cast(v4f, a[0]);
#pragma unroll
      for (int j = 1; j < 7; ++j) s += __builtin_bit_cast(v4f, a[j]);
      const float tot = fold16(fold32(s.x, s.z), fold32(s.y, s.w));
      const float r = xx + rb(gb * rb(tot));
      out[zb * 512 + zc] = (u16)pk2(r, r);
      STAMP(13);
    }
  }
  FIN();
}

extern "C" void kernel_launch(void* const* d_in, const int* in_sizes, int n_in, void* d_out, int out_size, void* d_ws, size_t ws_size, hipStream_t stream) {
  hipLaunchKernelGGL(fused_block, dim3(256), dim3(512), 0, stream, (const u16*)d_in[1], (const u16*)d_in[2], (const u16*)d_in[9], (const u16*)d_in[3],
                     (const u16*)d_in[4], (const u16*)d_in[6], (const u16*)d_in[7], (const u16*)d_in[0], (const u16*)d_in[8], (u16*)d_out, (unsigned char*)d_ws);
}
```

```cpp
#include <hip/hip_runtime.h>
#include <hip/hip_bf16.h>

#define STAMPS 0

typedef unsigned int u32;
typedef unsigned long long u64;
typedef unsigned short u16;
typedef u32 v4u __attribute__((ext_vector_type(4)));
typedef u32 v2u __attribute__((ext_vector_type(2)));
typedef int v4i __attribute__((ext_vector_type(4)));
typedef float v4f __attribute__((ext_vector_type(4)));
typedef float v2f __attribute__((ext_vector_type(2)));
typedef short v4s __attribute__((ext_vector_type(4)));
typedef __bf16 v8bf __attribute__((ext_vector_type(8)));
typedef __bf16 v2bf __attribute__((ext_vector_type(2)));
#define LAS __attribute__((address_space(3)))
#define GAS __attribute__((address_space(1)))
#define DEVI __device__ __forceinline__

constexpr int NSLOT = 16, SLOT = 8192, NCONS = 6, NSTASH = 4;
#define WIN 28
#define QAT 12
#define HDELAY 400
#define MAP 2
#define TOUCH 1
#define DRY 1
#define TAU16 443
#define TOFF 25
#define NF 2
#define CODEPF 0
#define CODE_BYTES 22912
#define PSTART 6
#define POLLS 16
#define NX_A 65
#define NQ_A 63
#define NY_A 61
#define NS_A 66
constexpr int NX_B = 64, NQ_B = 63, NY_B = 65, NS_B = 65, NMAX = 72;
static_assert(4 * NX_A + 6 * NQ_A + 2 * NY_A + 4 * NS_A == 1024 && 4 * NX_B + 6 * NQ_B + 2 * NY_B + 4 * NS_B == 1024, "blocks");
#define BIAS 8
constexpr u64 start_table(int nx, int nq, int ny, int ns) {
  u64 t = 0; int s = 0;
  for (int r = 0; r < 16; ++r) { const int dl = s - 64 * r + BIAS; if (dl < 0 || dl > 15) return ~0ull; t |= (u64)dl << (4 * r); s += r < 4 ? nx : r < 10 ? nq : r < 12 ? ny : ns; }
  return t;
}
constexpr u64 ST_A = start_table(NX_A, NQ_A, NY_A, NS_A), ST_B = start_table(NX_B, NQ_B, NY_B, NS_B);
static_assert(ST_A != ~0ull && ST_B != ~0ull, "start table");
constexpr int L_CTL = NSLOT * SLOT;
constexpr int L_Q = L_CTL + 256;
constexpr int L_ACT = L_Q + 1024;
constexpr int L_MRG = L_ACT + 2048;
constexpr int L_ML = L_MRG + 2048;
constexpr int L_SC = L_ML + 256;
constexpr int L_JUNK = L_SC + NMAX * 256;
constexpr int L_END = L_JUNK + 256;
constexpr int L_H1 = L_SC;
constexpr int L_RED = L_H1 + 256;
constexpr int L_X1 = L_RED + 128;
static_assert(L_END <= 160 * 1024 && L_X1 + 4096 <= L_END, "LDS");
constexpr int C_FULL = 0, C_QRDY = 1, C_MCNT = 2, C_CLR = 3, C_ARDY = 4, C_TAG = 5, C_WRD = 6, C_MX = 7, C_SM = 8, C_FREE = 16;
constexpr u32 W_EPOCH = 0, W_SP = 8192, W_HG = W_SP + 4 * 1024 * 16, W_QG = W_HG + 512 * 8, W_MG = W_QG + 4096 * 8, W_PART = W_MG + 2048 * 8;
constexpr u32 W_LSE = W_PART + 64 * 8192;
constexpr u32 W_AP = W_LSE + 64 * 256, W_OP = W_AP + 8 * 4096, W_END = W_OP + 28 * 4096;
constexpr u32 CLRB = (W_END - W_PART) / 64;
static_assert(CLRB * 64 == W_END - W_PART && CLRB % 1024 == 512, "clear split");
constexpr u32 W_STAMP = 2u << 20;
constexpr u32 SENT = 0xFFFFFFFFu;
constexpr int SPIN_CAP = 1 << 16;

DEVI float bf2f(u32 v) { return __uint_as_float(v << 16); }
DEVI float bfhi(u32 v) { return __uint_as_float(v & 0xffff0000u); }
DEVI u32 pk2(float a, float b) { v2f x = {a, b}; return __builtin_bit_cast(u32, __builtin_convertvector(x, v2bf)); }
DEVI float rb(float a) { return bf2f(pk2(a, a) & 0xffffu); }
DEVI float ex2(float x) { return __builtin_amdgcn_exp2f(x); }
DEVI float rcp(float x) { return __builtin_amdgcn_rcpf(x); }
DEVI float silu(float v) { return v * rcp(1.f + ex2(v * -1.4426950408889634f)); }
DEVI float silu_exact(float v) {
  const float t = fminf(v * -1.4426950216293335f, 126.f);
  const float r = fmaf(v, -1.92596299112662e-08f, fmaf(v, -1.4426950216293335f, -t));
  float e = ex2(t);
  e = fmaf(e, fminf(r, 1.f) * 0.6931471805599453f, e);
  const float d = 1.f + e, rc = rcp(d), q = v * rc;
  return fmaf(fmaf(-q, d, v), rc, q);
}
DEVI float lo16(u32 v) { return __uint_as_float(v << 16); }
DEVI v2f rb2(v2f a) { const u32 p = pk2(a.x, a.y); return v2f{lo16(p), bfhi(p)}; }
DEVI float fold32(float a, float b) { auto s = __builtin_amdgcn_permlane32_swap(__float_as_uint(a), __float_as_uint(b), false, false); return __uint_as_float(s[0]) + __uint_as_float(s[1]); }
DEVI float fold16(float a, float b) { auto s = __builtin_amdgcn_permlane16_swap(__float_as_uint(a), __float_as_uint(b), false, false); return __uint_as_float(s[0]) + __uint_as_float(s[1]); }
#define PIN(x) asm volatile("" : "+v"(x))
DEVI float swap16_add(float x) { auto s = __builtin_amdgcn_permlane16_swap(__float_as_uint(x), __float_as_uint(x), false, false); return __uint_as_float(s[0]) + __uint_as_float(s[1]); }
DEVI float swap32_add(float x) { auto s = __builtin_amdgcn_permlane32_swap(__float_as_uint(x), __float_as_uint(x), false, false); return __uint_as_float(s[0]) + __uint_as_float(s[1]); }
DEVI float swap16_max(float x) { auto s = __builtin_amdgcn_permlane16_swap(__float_as_uint(x), __float_as_uint(x), false, false); return fmaxf(__uint_as_float(s[0]), __uint_as_float(s[1])); }
DEVI float swap32_max(float x) { auto s = __builtin_amdgcn_permlane32_swap(__float_as_uint(x), __float_as_uint(x), false, false); return fmaxf(__uint_as_float(s[0]), __uint_as_float(s[1])); }
template <int CTRL> DEVI float dpp(float x) { return __builtin_bit_cast(float, __builtin_amdgcn_mov_dpp(__builtin_bit_cast(int, x), CTRL, 0xf, 0xf, true)); }
DEVI float row_sum(float x) { x += dpp<0xB1>(x); x += dpp<0x4E>(x); x += dpp<0x141>(x); x += dpp<0x128>(x); return x; }
DEVI float half_sum(float x) { return swap16_add(row_sum(x)); }
DEVI float wave_sum(float x) { return swap32_add(half_sum(x)); }
template <typename T> DEVI LAS T* lp(u32 a) { return (LAS T*)(size_t)a; }
DEVI u32 ldsv(u32 a) { return *(volatile LAS u32*)(size_t)a; }
DEVI void stsv(u32 a, u32 v) { *(volatile LAS u32*)(size_t)a = v; }
#define FENCE() asm volatile("" ::: "memory")
#define WAITV(n) asm volatile("s_waitcnt vmcnt(%0)" ::"i"(n) : "memory")
#define WAITL() asm volatile("s_waitcnt lgkmcnt(0)" ::: "memory")
DEVI void bar() { WAITL(); __builtin_amdgcn_s_barrier(); FENCE(); }
typedef __amdgpu_buffer_rsrc_t rsrc_t;
DEVI v4u ld4(rsrc_t r, u32 vo, u32 so) { return __builtin_bit_cast(v4u, __builtin_amdgcn_raw_buffer_load_b128(r, vo, so, 16)); }
DEVI u32 ld1(rsrc_t r, u32 vo, u32 so) { return __builtin_amdgcn_raw_buffer_load_b32(r, vo, so, 16); }
DEVI void st4(v4u v, rsrc_t r, u32 vo, u32 so) { __builtin_amdgcn_raw_buffer_store_b128(v, r, vo, so, 16); }
DEVI void st2(v2u v, rsrc_t r, u32 vo, u32 so) { __builtin_amdgcn_raw_buffer_store_b64(v, r, vo, so, 16); }
DEVI void st1(u32 v, rsrc_t r, u32 vo, u32 so) { __builtin_amdgcn_raw_buffer_store_b32(v, r, vo, so, 16); }
DEVI u32 canon(float f) { u32 b = __float_as_uint(f); return b < 0xFFFFFFFEu ? b : 0xFFFFFFFEu; }
DEVI v8bf trfrag(u32 a) {
  v4s lo = __builtin_amdgcn_ds_read_tr16_b64_v4i16(lp<v4s>(a)), hi = __builtin_amdgcn_ds_read_tr16_b64_v4i16(lp<v4s>(a + 2048));
  return __builtin_bit_cast(v8bf, __builtin_shufflevector(lo, hi, 0, 1, 2, 3, 4, 5, 6, 7));
}
DEVI v4f mfma(v8bf a, v8bf b, v4f c) { return __builtin_amdgcn_mfma_f32_16x16x32_bf16(a, b, c, 0, 0, 0); }
DEVI v8bf ldfrag(u32 a) { return __builtin_bit_cast(v8bf, *lp<v4u>(a)); }
DEVI int fpos(int k) { return (k & ~31) + ((k >> 2) & 3) * 8 + ((k >> 4) & 1) * 4 + (k & 3); }
DEVI v4i mksrd(const void* p, u32 bytes) { u64 a = (u64)p; return v4i{(int)(u32)a, (int)(u32)(a >> 32), (int)bytes, 0x00020000}; }

#define DMA4(m, v0, v1, v2, v3, srd, so)                                                                                   \
  asm volatile("s_mov_b32 m0, %0\n\ts_nop 0\n\tbuffer_load_dwordx4 %1, %5, %6 offen nt lds\n\t"                           \
               "buffer_load_dwordx4 %2, %5, %6 offen offset:1024 nt lds\n\tbuffer_load_dwordx4 %3, %5, %6 offen offset:2048 nt lds\n\t" \
               "buffer_load_dwordx4 %4, %5, %6 offen offset:3072 nt lds" ::"s"(m), "v"(v0), "v"(v1), "v"(v2), "v"(v3), "s"(srd), "s"(so) : "memory")
#define DMA1(m, v0, srd, so) \
  asm volatile("s_mov_b32 m0, %0\n\ts_nop 0\n\tbuffer_load_dwordx4 %1, %2, %3 offen nt lds" ::"s"(m), "v"(v0), "s"(srd), "s"(so) : "memory")

#define STAMP(i) do { } while (0)
#define FIN() do { } while (0)

__global__ void __launch_bounds__(512, 2)
fused_block(const u16* __restrict__ Kc, const u16* __restrict__ Vc, const u16* __restrict__ wmod, const u16* __restrict__ wq,
            const u16* __restrict__ wo, const u16* __restrict__ w1, const u16* __restrict__ w2, const u16* __restrict__ xin,
            const u16* __restrict__ cin, u16* __restrict__ out, unsigned char* ws) {
  __shared__ __attribute__((aligned(16))) unsigned char smem[L_END + 256 * STAMPS];
  asm volatile("" ::"s"((u32)(size_t)(LAS unsigned char*)smem));
  const int tid = threadIdx.x, lane = tid & 63, wave = __builtin_amdgcn_readfirstlane(tid >> 6);
  const int bid = (blockIdx.x & ~31) | ((blockIdx.x & 7) << 2) | ((blockIdx.x >> 3) & 3);
  const int grp = bid >> 4, mem = bid & 15;
  const int g = lane >> 4, c = lane & 15;
  const int r = MAP ? mem : (mem + (grp >> 1)) & 15;
  const u64 stt = (grp < 14 ? ST_A : ST_B) >> (4 * r);
  const int kv0 = 64 * r + ((int)stt & 15) - BIAS;
  const bool isS = r >= 12;
  if (wave == 0) {
    const v4i srd = isS ? mksrd(wmod, 512 * 6144) : mksrd(Kc, 1u << 27);
    const u32 k0 = g * 256 + ((c ^ g) << 4), w0 = (lane >> 3) * 6144 + (((lane & 7) ^ ((lane >> 3) & 6)) << 4);
    const u32 a0 = isS ? w0 : k0, a1 = isS ? w0 + 48128 : k0 ^ 64, a2 = isS ? w0 + 2 * 48128 : k0 ^ 128, a3 = isS ? w0 + 3 * 48128 : k0 ^ 192;
    const u32 so = isS ? (r - 12) * 128 * 6144 + grp * 128 : (u32)(grp * 1024 + kv0) << 13, hs = isS ? 32 * 6144 : 4096;
#pragma unroll
    for (int i = 0; i < 4; ++i) { const u32 m = i * 4096, s1 = so + i * hs; DMA4(m, a0, a1, a2, a3, srd, s1); }
  }
  const bool isX = r < 4, isQ = r >= 4 && r < 8, isM = r == 8 || r == 9, isY = (r == 10 || r == 11) && grp < 14, isR = r == 0 && (grp & 1);
  const int xi = grp * 4 + r, qi = grp * 4 + r - 4, mi = grp * 2 + r - 8, yi = grp * 2 + r - 10;
  const int u0 = (int)(0x2222008888880000ull >> (4 * r)) & 15;
  const int nb = 64 + (r == 15 ? BIAS : (int)(stt >> 4) & 15) - ((int)stt & 15);
  const int nf = isS ? 0 : NF;
  const int ntail = isX ? 8 : isY ? 16 : 0;
  const int uv = u0 + nb, uw = uv + nb;
  u64 E = 0;
  if (__builtin_expect(wave == 0, 1)) {
    __builtin_amdgcn_s_setprio(3);
    STAMP(0);
    int pub = 0, iss = 8 * NF;
#define TPS()
    auto publish = [&](int v) { if (v > pub) { pub = v; stsv(L_CTL + 4 * C_FULL, v); } };
#define PACE(W_) do { WAITV(W_); publish(iss - (W_)); } while (0)
    u32 fr = 0, now = (u32)__builtin_amdgcn_s_memrealtime(), due = (now + TOFF) * 16 + NF * TAU16;
    __builtin_amdgcn_s_barrier(); FENCE();
    const u32 wrow = lane >> 3, wlc = ((lane & 7) ^ (((wrow >> 1) & 3) << 1)) << 4;
    int u = NF;
#pragma nounroll
    for (int seg = u0 == 8 ? 0 : 1; seg < 3 + (ntail >> 3); ++seg) {
      const bool kv = seg == 1 || seg == 2;
      const u16* base = seg == 0 ? (isQ ? wq : wmod) : seg == 1 ? Kc : seg == 2 ? Vc : seg == 4 ? w2 : isX ? wo : w1;
      const u32 rs = seg == 0 ? (isQ ? 8192 : 6144) : (seg == 3 && !isX) ? 3584 : 1024;
      const v4i srd = mksrd(base, kv ? 1u << 27 : seg == 4 ? 1792 * 1024 : (seg == 3 && isX) ? 4096 * 1024 : 512 * rs);
      u32 so = kv ? (u32)(grp * 1024 + kv0 + (seg == 1 ? nf : 0)) << 13
                  : seg == 0 ? (isQ ? qi : 16 + mi) * 128
                             : seg == 4 ? yi << 16 : isX ? ((xi >> 3) << 19) + (xi & 7) * 128 : yi * 128;
      const u32 hstep = kv ? 4096 : 32 * rs, ustep = seg == 4 ? 128 : 2 * hstep;
      const u32 sw = g << (seg - 1), sx = seg == 1 ? 4 : 8;
      const u32 w0 = wrow * rs + wlc, wd = 8 * rs - 1024;
      const u32 a0 = kv ? g * 256 + ((c ^ sw) << 4) : w0, a1 = kv ? g * 256 + ((c ^ sw ^ sx) << 4) : w0 + wd;
      const u32 a2 = kv ? g * 256 + ((c ^ sw ^ ((2 * sx) & 15)) << 4) : w0 + 2 * wd, a3 = kv ? g * 256 + ((c ^ sw ^ ((3 * sx) & 15)) << 4) : w0 + 3 * wd;
      const int ue = seg == 0 ? NF + u0 : seg == 1 ? uv : seg == 2 ? uw : u + 8;
#pragma nounroll
      for (; u < ue; ++u, so += ustep) {
        TPS();
        if (u >= NSLOT && (int)fr < u + 1 - NSLOT) {
          const u32 a = L_CTL + 4 * (C_FREE + (u & (NSLOT - 1)));
          if ((int)ldsv(a) < u + 1 - NSLOT) {
            PACE(0); while ((int)ldsv(a) < u + 1 - NSLOT) __builtin_amdgcn_s_sleep(1);
          }
        }
        if (u >= PSTART && seg < 3) while ((int)(now * 16 - due) < 0) now = (u32)__builtin_amdgcn_s_memrealtime();
        const u32 lb = (u & (NSLOT - 1)) * SLOT;
        DMA4(lb, a0, a1, a2, a3, srd, so);
        publish(iss - WIN); iss += 4; WAITV(WIN);
        { const u32 m = lb + 4096, s1 = so + hstep; DMA4(m, a0, a1, a2, a3, srd, s1); }
        publish(iss - WIN); iss += 4;
        now = (u32)__builtin_amdgcn_s_memrealtime();
        due += TAU16;
        fr = ldsv(L_CTL + 4 * (C_FREE + ((u + 1) & (NSLOT - 1))));
        WAITV(WIN);
        if (u == 3 && isS) PACE(16);
      }
    }
    PACE(24); PACE(16); PACE(8); PACE(0);
    STAMP(1);
    __builtin_amdgcn_s_setprio(0);
  } else {
    if (wave == 2) { stsv(L_CTL + 4 * lane, 0); WAITL(); }
    __builtin_amdgcn_s_barrier(); FENCE();
    const rsrc_t rws = __builtin_amdgcn_make_buffer_rsrc(ws, 0, W_END, 0x00020000);
    auto wait_full = [&](u32 need) { while (ldsv(L_CTL + 4 * C_FULL) < need) __builtin_amdgcn_s_sleep(1); FENCE(); };
    const u32 trow = 4 * g + (c >> 2);
    auto waddr = [&](int t) -> u32 { return trow * 128 + ((((u32)(t ^ ((trow >> 1) & 3)) << 1) | ((c >> 1) & 1)) << 4) + (c & 1) * 8; };

    if (wave == 1) {
      const u64 tstart = __builtin_amdgcn_s_memrealtime();
      u64 E0 = __hip_atomic_load((GAS u64*)(ws + W_EPOCH), __ATOMIC_RELAXED, __HIP_MEMORY_SCOPE_AGENT);
      u32 tag;
#define CHAIN_START() do { E = E0 + 1; tag = (u32)E; } while (0)
      auto silu4 = [&](u32 x, u32 y) { return v2u{pk2(silu_exact(lo16(x)), silu_exact(bfhi(x))), pk2(silu_exact(lo16(y)), silu_exact(bfhi(y)))}; };
      if (isS) {
        const v2u cv = *(const v2u*)(cin + (lane >> 5) * 512 + (r - 12) * 128 + (lane & 31) * 4);
        CHAIN_START();
        *lp<v2u>(L_ACT + (lane >> 5) * 1024 + fpos((lane & 31) * 4) * 2) = silu4(cv.x, cv.y);
        FENCE();
        v8bf act[4];
#pragma unroll
        for (int ks = 0; ks < 4; ++ks) { act[ks] = ldfrag(L_ACT + (c & 1) * 1024 + g * 16 + ks * 64); PIN(act[ks]); }
        wait_full(16);
        v8bf W[16];
#pragma unroll
        for (int j = 0; j < 16; ++j) W[j] = trfrag(waddr(j & 3) + (j >> 2) * 4096);
        FENCE(); stsv(L_CTL + 4 * C_FREE, 1); stsv(L_CTL + 4 * (C_FREE + 1), 2);
        v4f acc[4];
#pragma unroll
        for (int t = 0; t < 4; ++t) acc[t] = v4f{0.f, 0.f, 0.f, 0.f};
#pragma unroll
        for (int j = 0; j < 16; ++j) acc[j & 3] = mfma(act[j >> 2], W[j], acc[j & 3]);
        const float r0 = g == 0 ? acc[0][0] : g == 1 ? acc[1][0] : g == 2 ? acc[2][0] : acc[3][0];
        const float r1 = g == 0 ? acc[0][1] : g == 1 ? acc[1][1] : g == 2 ? acc[2][1] : acc[3][1];
        while (ldsv(L_CTL + 4 * C_CLR) == 0) { }
        st4(v4u{__float_as_uint(r0), tag, __float_as_uint(r1), tag}, rws, lane * 16, W_SP + ((r - 12) * 1024 + grp * 64) * 16);
        STAMP(3);
      } else if (isR) {
        const v4u in0 = *(const v4u*)(xin + lane * 8), in1 = *(const v4u*)(xin + 512 + lane * 8);
        u32 xs0 = *(const u32*)(xin + (grp >> 1) * 64 + (lane & ~1)), xs1 = *(const u32*)(xin + 512 + (grp >> 1) * 64 + (lane & ~1));
        CHAIN_START();
        PIN(xs0); PIN(xs1);
        xs0 >>= 16 * (lane & 1); xs1 >>= 16 * (lane & 1);
        float mr[2], ri[2];
#pragma unroll
        for (int b = 0; b < 2; ++b) {
          const v4u xv = b ? in1 : in0;
          const float x[8] = {lo16(xv.x), bfhi(xv.x), lo16(xv.y), bfhi(xv.y), lo16(xv.z), bfhi(xv.z), lo16(xv.w), bfhi(xv.w)};
          float s = 0.f;
#pragma unroll
          for (int i = 0; i < 8; ++i) s += x[i];
          const float mean = wave_sum(s) * (1.f / 512.f);
          float q2 = 0.f;
#pragma unroll
          for (int i = 0; i < 8; ++i) q2 += (x[i] - mean) * (x[i] - mean);
          const float var = wave_sum(q2) * (1.f / 512.f);
          mr[b] = rb(mean); ri[b] = rcp(rb(__builtin_amdgcn_sqrtf(rb(rb(var) + 1e-5f))));
        }
        v2f y = rb2(rb2(v2f{lo16(xs0) - mr[0], lo16(xs1) - mr[1]}) * v2f{ri[0], ri[1]});
        PIN(y);
        v4u a[8];
#pragma nounroll
        for (int sp = 0; sp < SPIN_CAP; ++sp) {
          u32 bad = 0;
#pragma unroll
          for (int i = 0; i < 8; ++i) { a[i] = ld4(rws, lane * 16, W_SP + ((i & 3) * 1024 + (i >> 2) * 512 + (grp >> 1) * 64) * 16); bad |= (a[i].y ^ tag) | (a[i].w ^ tag); }
          if (__all(bad == 0)) break;
          FENCE();
        }
        v2f sc[2];
#pragma unroll
        for (int pt = 0; pt < 2; ++pt) {
          sc[pt] = v2f{0.f, 0.f};
#pragma unroll
          for (int k4 = 0; k4 < 4; ++k4) sc[pt] += v2f{__uint_as_float(a[pt * 4 + k4].x), __uint_as_float(a[pt * 4 + k4].z)};
        }
        const v2f h = rb2(y * rb2(rb2(sc[1]) + 1.f)) + rb2(sc[0]);
        st2(v2u{pk2(h.x, h.y), tag}, rws, lane * 8, W_HG + (grp >> 1) * 512);
        STAMP(23);
      } else if (isM || isQ) {
        if (isM) {
          const v4u in0 = *(const v4u*)(cin + lane * 8), in1 = *(const v4u*)(cin + 512 + lane * 8);
          CHAIN_START();
          *lp<v2u>(L_ACT + fpos(lane * 8) * 2) = silu4(in0.x, in0.y); *lp<v2u>(L_ACT + fpos(lane * 8 + 4) * 2) = silu4(in0.z, in0.w);
          *lp<v2u>(L_ACT + 1024 + fpos(lane * 8) * 2) = silu4(in1.x, in1.y); *lp<v2u>(L_ACT + 1024 + fpos(lane * 8 + 4) * 2) = silu4(in1.z, in1.w);
        } else {
          CHAIN_START();
          while (__builtin_amdgcn_s_memrealtime() < tstart + HDELAY) __builtin_amdgcn_s_sleep(4);
          v4u a[4];
#pragma nounroll
          for (int sp = 0; sp < SPIN_CAP; ++sp) {
            u32 bad = 0;
#pragma unroll
            for (int i = 0; i < 4; ++i) { a[i] = ld4(rws, lane * 16 + i * 1024, W_HG); bad |= (a[i].y ^ tag) | (a[i].w ^ tag); }
            if (__all(bad == 0)) break;
            FENCE();
          }
#pragma unroll
          for (int i = 0; i < 4; ++i) {
            const u32 wa = L_ACT + fpos(i * 128 + lane * 2) * 2;
            *lp<u32>(wa) = (a[i].x & 0xffffu) | (a[i].z << 16); *lp<u32>(wa + 1024) = (a[i].x >> 16) | (a[i].z & 0xffff0000u);
          }
          STAMP(4);
        }
        FENCE(); stsv(L_CTL + 4 * C_TAG, tag); stsv(L_CTL + 4 * C_ARDY, 1);
      } else CHAIN_START();
      {
        wait_full(8 * u0 + 16 * QAT);
        v4u a[2];
#pragma nounroll
        for (int sp = 0; sp < SPIN_CAP; ++sp) {
          a[0] = ld4(rws, lane * 16, W_QG + grp * 2048); a[1] = ld4(rws, lane * 16 + 1024, W_QG + grp * 2048);
          if (__all(((a[0].y ^ tag) | (a[0].w ^ tag) | (a[1].y ^ tag) | (a[1].w ^ tag)) == 0)) break;
          __builtin_amdgcn_s_sleep(POLLS); FENCE();
        }
        *lp<v2u>(L_Q + lane * 8) = v2u{a[0].x, a[0].z}; *lp<v2u>(L_Q + 512 + lane * 8) = v2u{a[1].x, a[1].z};
        FENCE(); stsv(L_CTL + 4 * C_TAG, tag); stsv(L_CTL + 4 * C_QRDY, 1);
        STAMP(6);
      }
      {
        const u16* p = Vc + ((size_t)grp * 32768 + (size_t)(kv0 + (lane & 1) * (nb - 1)) * 32) * 128;
        if (lane >= 2) p = isX ? wo + (xi >> 3) * 512 * 512 : lane == 2 ? out : lane == 3 ? xin : lane == 4 ? w2 + max(yi, 0) % 28 * 64 * 512 : w1 + (lane & 7) * 64 * 1792;
        if (lane < 2 || (isX && lane == 2) || (isY && lane < 16)) { u32 t = __builtin_nontemporal_load((const u32*)p); PIN(t); }
      }
      STAMP(2);
    } else {
      if (isS && wave == 7) {
        const v4u s4 = {SENT, SENT, SENT, SENT};
        const u32 so = W_PART + (grp * 4 + r - 12) * CLRB;
#pragma unroll
        for (int i = 0; i < CLRB / 1024; ++i) st4(s4, rws, lane * 16 + (i & 3) * 1024, so + (i >> 2) * 4096);
        if (lane < 32) st4(s4, rws, lane * 16, so + CLRB - 512);
        WAITV(0); stsv(L_CTL + 4 * C_CLR, 1);
      }

      if (wave == 6) { *lp<v4f>(L_MRG + lane * 16) = v4f{0.f, 0.f, 0.f, 0.f}; *lp<v4f>(L_MRG + 1024 + lane * 16) = v4f{0.f, 0.f, 0.f, 0.f}; }
      const int d = c >> 2, cw = wave - 2;
      const u32 sca = L_SC + (g * 4 + (c & 3)) * 16;
      {
        u32 ka[4];
#pragma unroll
        for (int s = 0; s < 4; ++s) ka[s] = c * 256 + (((4 * s + g) ^ c) << 4);
        v8bf qf[4];
        float mx = -INFINITY;
        auto rdk = [&](v8bf (&kf)[8], int j) {
          const int u = j < nf ? j : u0 + j;
          const u32 lb = (u & (NSLOT - 1)) * SLOT;
          wait_full(8 * u + 8);
#pragma unroll
          for (int s = 0; s < 4; ++s) { kf[s] = ldfrag(lb + ka[s]); kf[4 + s] = ldfrag(lb + ka[s] + 4096); }
          FENCE(); stsv(L_CTL + 4 * (C_FREE + (u & (NSLOT - 1))), u + 1);
        };
        auto qk = [&](v8bf (&kf)[8], int j) {
          v4f S0 = {0.f, 0.f, 0.f, 0.f}, S1 = {0.f, 0.f, 0.f, 0.f};
#pragma unroll
          for (int s = 0; s < 4; ++s) { S0 = mfma(kf[s], qf[s], S0); S1 = mfma(kf[4 + s], qf[s], S1); }
          const v2f a = rb2(v2f{S0.x, S0.y}) * 0.08838834764831845f, b = rb2(v2f{S0.z, S0.w}) * 0.08838834764831845f;
          const v2f e = rb2(v2f{S1.x, S1.y}) * 0.08838834764831845f, f = rb2(v2f{S1.z, S1.w}) * 0.08838834764831845f;
          mx = fmaxf(fmaxf(fmaxf(mx, fmaxf(a.x, a.y)), fmaxf(b.x, b.y)), fmaxf(fmaxf(e.x, e.y), fmaxf(f.x, f.y)));
          if (c < 4) *lp<v4u>(sca + j * 256) = v4u{pk2(a.x, a.y), pk2(b.x, b.y), pk2(e.x, e.y), pk2(f.x, f.y)};
        };
        v8bf sk[NSTASH][8];
        rdk(sk[0], cw);
      if (u0 == 8 && wave < 6) {
        wait_full(8 * NF + 64);
        v8bf W[16];
#pragma unroll
        for (int ks = 0; ks < 16; ++ks) W[ks] = trfrag(NF * SLOT + ks * 4096 + waddr(wave - 2));
        WAITL();
        u32 old = 0;
        if (lane == 0) old = __hip_atomic_fetch_add(lp<u32>(L_CTL + 4 * C_WRD), 1u, __ATOMIC_RELAXED, __HIP_MEMORY_SCOPE_WORKGROUP);
        if (__builtin_amdgcn_readfirstlane(old) == 3 && lane < 8) stsv(L_CTL + 4 * (C_FREE + NF + lane), NF + lane + 1);
        while (ldsv(L_CTL + 4 * C_ARDY) == 0) __builtin_amdgcn_s_sleep(1);
        FENCE();
        const u32 tag = ldsv(L_CTL + 4 * C_TAG);
        v8bf A[16];
#pragma unroll
        for (int ks = 0; ks < 16; ++ks) A[ks] = ldfrag(L_ACT + (c & 1) * 1024 + g * 16 + ks * 64);
        __builtin_amdgcn_sched_barrier(0);
        v4f d0 = {0.f, 0.f, 0.f, 0.f}, d1 = {0.f, 0.f, 0.f, 0.f};
#pragma unroll
        for (int ks = 0; ks < 16; ks += 2) { d0 = mfma(A[ks], W[ks], d0); d1 = mfma(A[ks + 1], W[ks + 1], d1); }
        const float r0 = d0[0] + d1[0], r1 = d0[1] + d1[1];
        const float n0 = dpp<0xB1>(r0), n1 = dpp<0xB1>(r1);
        const bool odd = lane & 1;
        if (lane < 16) {
          if (isM) st2(v2u{pk2(r0, r1), tag}, rws, lane * 8, W_MG + mi * 512 + (wave - 2) * 128);
          else st2(v2u{pk2(odd ? n1 : r0, odd ? r1 : n0), tag}, rws, (lane & 1) * 16384 + (lane >> 1) * 8, W_QG + qi * 256 + (wave - 2) * 64);
        }
        if (isQ && wave == 2) STAMP(5);
      }
#pragma unroll
        for (int i = 1; i < NSTASH; ++i) rdk(sk[i], cw + i * NCONS);
        while (ldsv(L_CTL + 4 * C_QRDY) == 0) __builtin_amdgcn_s_sleep(2);
        FENCE();
#pragma unroll
        for (int s = 0; s < 4; ++s) qf[s] = ldfrag(L_Q + (c & 3) * 256 + (4 * s + g) * 16);
#pragma unroll
        for (int i = 0; i < NSTASH; ++i) qk(sk[i], cw + i * NCONS);
#pragma nounroll
        for (int j = cw + NSTASH * NCONS; j < nb; j += NCONS) { v8bf kf[8]; rdk(kf, j); qk(kf, j); }
        mx = swap32_max(swap16_max(rb(mx)));
        if (lane < 4) *lp<float>(L_ML + cw * 16 + lane * 4) = mx;
        FENCE();
        if (lane == 0) __hip_atomic_fetch_add(lp<u32>(L_CTL + 4 * C_MX), 1u, __ATOMIC_RELAXED, __HIP_MEMORY_SCOPE_WORKGROUP);
      }
      float M2;
      {
        while (ldsv(L_CTL + 4 * C_MX) < NCONS) __builtin_amdgcn_s_sleep(1);
        FENCE();
        float M = *lp<float>(L_ML + (c & 3) * 4);
#pragma unroll
        for (int w = 1; w < NCONS; ++w) M = fmaxf(M, *lp<float>(L_ML + w * 16 + (c & 3) * 4));
        M2 = M * 1.4426950408889634f;
        float l = 0.f;
#pragma nounroll
        for (int j = cw + d * NCONS; j < nb; j += 4 * NCONS) {
          const v4u s = *lp<v4u>(sca + j * 256);
          float p[8];
#pragma unroll
          for (int i = 0; i < 4; ++i) { p[2 * i] = ex2(fmaf(lo16(s[i]), 1.4426950408889634f, -M2)); p[2 * i + 1] = ex2(fmaf(bfhi(s[i]), 1.4426950408889634f, -M2)); }
          l += ((p[0] + p[1]) + (p[2] + p[3])) + ((p[4] + p[5]) + (p[6] + p[7]));
          *lp<v4u>(sca + j * 256) = v4u{pk2(p[0], p[1]), pk2(p[2], p[3]), pk2(p[4], p[5]), pk2(p[6], p[7])};
        }
        l += dpp<0x124>(l); l += dpp<0x128>(l);
        l = swap32_add(swap16_add(l));
        if (lane < 4) *lp<float>(L_ML + 128 + cw * 16 + lane * 4) = l;
        FENCE();
        if (lane == 0) __hip_atomic_fetch_add(lp<u32>(L_CTL + 4 * C_SM), 1u, __ATOMIC_RELAXED, __HIP_MEMORY_SCOPE_WORKGROUP);
      }
      {
        u32 va[8];
#pragma unroll
        for (int t = 0; t < 8; ++t) va[t] = trow * 256 + ((((u32)(t ^ (trow & 7)) << 1) | ((c >> 1) & 1)) << 4) + (c & 1) * 8;
        v4f acc[8];
#pragma unroll
        for (int t = 0; t < 8; ++t) acc[t] = v4f{0.f, 0.f, 0.f, 0.f};
        int j = cw;
#pragma nounroll
        for (; j + NCONS < nb; j += NCONS) {
          const int u = uv + j;
          const u32 lb = (u & (NSLOT - 1)) * SLOT;
          wait_full(8 * u + 8);
          const v8bf pf = ldfrag(sca + j * 256);
          v8bf vf[8];
#pragma unroll
          for (int t = 0; t < 8; ++t) {
            v4s lo = __builtin_amdgcn_ds_read_tr16_b64_v4i16(lp<v4s>(lb + va[t])), hi = __builtin_amdgcn_ds_read_tr16_b64_v4i16(lp<v4s>(lb + va[t] + 4096));
            vf[t] = __builtin_bit_cast(v8bf, __builtin_shufflevector(lo, hi, 0, 1, 2, 3, 4, 5, 6, 7));
          }
          FENCE(); stsv(L_CTL + 4 * (C_FREE + (u & (NSLOT - 1))), u + 1);
#pragma unroll
          for (int t = 0; t < 8; ++t) acc[t] = mfma(vf[t], pf, acc[t]);
        }
        const int link = j - (nb - NCONS), u = uv + j;
        const u32 lb = (u & (NSLOT - 1)) * SLOT;
        v4f e0 = d == 0 ? acc[0] : d == 1 ? acc[2] : d == 2 ? acc[4] : acc[6];
        v4f e1 = d == 0 ? acc[1] : d == 1 ? acc[3] : d == 2 ? acc[5] : acc[7];
        const v4u pf = *lp<v4u>(sca + j * 256);
        v4u pm[4];
#pragma unroll
        for (int i = 0; i < 4; ++i) { pm[i] = d == i ? pf : v4u{0u, 0u, 0u, 0u}; PIN(pm[i]); }
        float inv = 1.f;
        if (link == NCONS - 1) {
          while (ldsv(L_CTL + 4 * C_SM) < NCONS) { }
          FENCE();
          float L = *lp<float>(L_ML + 128 + (c & 3) * 4);
#pragma unroll
          for (int w = 1; w < NCONS; ++w) L += *lp<float>(L_ML + 128 + w * 16 + (c & 3) * 4);
          inv = rcp(L);
          M2 += __builtin_amdgcn_logf(L);
        }
        u32 lse = canon(M2);
        PIN(lse);
        u32 oa = (c & 3) * 8192 + d * 128 + g * 16, ra = L_MRG + lane * 16;
        u32 vb[8];
#pragma unroll
        for (int t = 0; t < 8; ++t) { vb[t] = lb + va[t]; PIN(vb[t]); }
        PIN(e0); PIN(e1); PIN(oa); PIN(ra); PIN(inv);
        while (ldsv(L_CTL + 4 * C_FULL) < (u32)(8 * u + 8)) { }
        FENCE();
        v8bf vf[8];
#pragma unroll
        for (int t = 0; t < 8; ++t) {
          v4s lo = __builtin_amdgcn_ds_read_tr16_b64_v4i16(lp<v4s>(vb[t])), hi = __builtin_amdgcn_ds_read_tr16_b64_v4i16(lp<v4s>(vb[t] + 4096));
          vf[t] = __builtin_bit_cast(v8bf, __builtin_shufflevector(lo, hi, 0, 1, 2, 3, 4, 5, 6, 7));
        }
        __builtin_amdgcn_sched_barrier(0);
#pragma unroll
        for (int i = 0; i < 4; ++i) { e0 = mfma(vf[2 * i], __builtin_bit_cast(v8bf, pm[i]), e0); e1 = mfma(vf[2 * i + 1], __builtin_bit_cast(v8bf, pm[i]), e1); }
        __builtin_amdgcn_sched_barrier(0);
        v4f r0, r1;
        for (;;) {
          const u32 cnt = ldsv(L_CTL + 4 * C_MCNT);
          FENCE();
          r0 = *lp<v4f>(ra); r1 = *lp<v4f>(ra + 1024);
          FENCE();
          if (cnt >= (u32)link) break;
        }
        e0 = (e0 + r0) * inv; e1 = (e1 + r1) * inv;
        if (link == NCONS - 1) {
          st4(v4u{canon(e0.x), canon(e0.y), canon(e0.z), canon(e0.w)}, rws, oa, W_PART + grp * 32768 + mem * 512);
          st4(v4u{canon(e1.x), canon(e1.y), canon(e1.z), canon(e1.w)}, rws, oa + 64, W_PART + grp * 32768 + mem * 512);
          if (lane < 4) st1(lse, rws, lane * 4, W_LSE + bid * 64);
          STAMP(8);
        } else {
          *lp<v4f>(ra) = e0; *lp<v4f>(ra + 1024) = e1;
          FENCE(); stsv(L_CTL + 4 * C_MCNT, link + 1);
        }
        stsv(L_CTL + 4 * (C_FREE + (u & (NSLOT - 1))), u + 1);
      }
    }
  }
  if (!(isX || isY)) { FIN(); return; }
  const u32 trow = 4 * g + (c >> 2);
  auto waddr = [&](int t) -> u32 { return trow * 128 + ((((u32)(t ^ ((trow >> 1) & 3)) << 1) | ((c >> 1) & 1)) << 4) + (c & 1) * 8; };
  auto wslot = [&](int t) -> u32 { return ((uw + t) & (NSLOT - 1)) * SLOT; };
  const int half = lane >> 5, i32 = lane & 31;
  const u32 fa = L_ACT + (c & 1) * 1024 + g * 16;
#pragma nounroll
  for (int pass = (DRY && wave == 1) ? 0 : 1; pass < 2; ++pass) {
    const bool dry = pass == 0;
    const int cap = dry ? 1 : SPIN_CAP;
    const rsrc_t rws = __builtin_amdgcn_make_buffer_rsrc(ws, 0, dry ? 0 : W_END, 0x00020000);
    const v4i sws = mksrd(ws, dry ? 0 : W_END);
    auto tbar = [&]() { WAITL(); if (!dry) __builtin_amdgcn_s_barrier(); FENCE(); };
    if (!dry) while (ldsv(L_CTL + 4 * C_FULL) < (u32)(8 * (uw + ntail))) __builtin_amdgcn_s_sleep(1);
    FENCE();
    if (isX) {
      const int kvh = xi >> 3, ng = xi & 7;
      const int b = wave >> 2, h = wave & 3;
      v8bf W[16];
      if (wave >= 4 || dry) {
#pragma unroll
        for (int ks = 0; ks < 16; ++ks) W[ks] = trfrag(wslot(ks >> 1) + (ks & 1) * 4096 + waddr(wave & 3));
      }
      u32 wa = L_ACT + b * 1024 + fpos(h * 128 + i32 * 4 + half * 2) * 2;
      PIN(wa);
      v4u a[8]; u32 ls = 0;
#pragma unroll
      for (int j = 0; j < 8; ++j) a[j] = v4u{0u, 0u, 0u, 0u};
      {
        const u32 vo = lane * 16, s0 = W_PART + (b * 8 + kvh) * 32768 + h * 8192, s1 = s0 + 4096;
        const u32 vl = ((c & 7) * 2 + ((c >> 3) ^ half)) * 64, s2 = W_LSE + (b * 8 + kvh) * 1024 + h * 4;
        u32 pend = 0xFF;
#pragma nounroll
        for (int sp = 0; sp < cap; ++sp) {
#define SEL(j, so, off) "s_bitcmp1_b32 %9, " #j "\n\ts_cbranch_scc0 .Lsk%=_" #j "\n\tbuffer_load_dwordx4 %" #j ", %10, %11, " so " offen offset:" #off " sc1\n.Lsk%=_" #j ":\n\t"
          asm volatile("buffer_load_dword %8, %14, %11, %15 offen sc1\n\t" SEL(0, "%12", 0) SEL(1, "%12", 1024) SEL(2, "%12", 2048) SEL(3, "%12", 3072)
                       SEL(4, "%13", 0) SEL(5, "%13", 1024) SEL(6, "%13", 2048) SEL(7, "%13", 3072) "s_waitcnt vmcnt(0)"
                       : "+v"(a[0]), "+v"(a[1]), "+v"(a[2]), "+v"(a[3]), "+v"(a[4]), "+v"(a[5]), "+v"(a[6]), "+v"(a[7]), "+v"(ls)
                       : "s"(pend), "v"(vo), "s"(sws), "s"(s0), "s"(s1), "v"(vl), "s"(s2) : "memory", "scc");
#pragma unroll
          for (int j = 0; j < 8; ++j)
            if (pend & (1u << j)) { if (__all(max(max(a[j].x, a[j].y), max(a[j].z, a[j].w)) != SENT)) pend &= ~(1u << j); }
          if (pend == 0 && __all(ls != SENT)) break;
        }
      }
      float M = __uint_as_float(ls);
      M = fmaxf(M, dpp<0xB1>(M)); M = fmaxf(M, dpp<0x4E>(M)); M = fmaxf(M, dpp<0x141>(M)); M = fmaxf(M, dpp<0x128>(M));
      const float cf = ex2(__uint_as_float(ls) - M);
      const float inv = rcp(row_sum(cf));
      const float cj[8] = {dpp<0x150>(cf), dpp<0x151>(cf), dpp<0x152>(cf), dpp<0x153>(cf), dpp<0x154>(cf), dpp<0x155>(cf), dpp<0x156>(cf), dpp<0x157>(cf)};
      v4f o = __builtin_bit_cast(v4f, a[0]) * cj[0];
#pragma unroll
      for (int j = 1; j < 8; ++j) o += __builtin_bit_cast(v4f, a[j]) * cj[j];
      *lp<u32>(wa) = pk2(fold32(o.x, o.z) * inv, fold32(o.y, o.w) * inv);
      if (wave == 4) STAMP(9);
      tbar();
      if (wave >= 4 || dry) {
        v8bf A[16];
#pragma unroll
        for (int ks = 0; ks < 16; ++ks) A[ks] = ldfrag(fa + ks * 64);
        __builtin_amdgcn_sched_barrier(0);
        v4f d0 = {0.f, 0.f, 0.f, 0.f}, d1 = {0.f, 0.f, 0.f, 0.f};
#pragma unroll
        for (int ks = 0; ks < 16; ks += 2) { d0 = mfma(W[ks], A[ks], d0); d1 = mfma(W[ks + 1], A[ks + 1], d1); }
        d0 += d1;
        v4u ov = {canon(d0.x), canon(d0.y), canon(d0.z), canon(d0.w)};
        if (c < 2) st4(ov, rws, c * 2048 + g * 16, W_AP + kvh * 4096 + ng * 256 + (wave & 3) * 64);
        if (wave == 4) STAMP(10);
      }
    } else {
      const int b = wave >> 2, col = (wave & 3) * 128 + i32 * 4 + half * 2;
      const u32 xv = __builtin_amdgcn_raw_buffer_load_b32(__builtin_amdgcn_make_buffer_rsrc((void*)xin, 0, dry ? 0 : 2048, 0x00020000), (b * 512 + col) * 2, 0, 0);
      v2f x0 = {lo16(xv), bfhi(xv)};
      const int zb = yi >> 3, zc = (yi & 7) * 64 + c * 4 + g;
      v2f ga, sb, cb1; float gb;
      {
        const u32 tag = ldsv(L_CTL + 4 * C_TAG);
        v4u mg[3]; v2u mz;
#pragma nounroll
        for (int sp = 0; sp < cap; ++sp) {
#pragma unroll
          for (int i = 0; i < 3; ++i) mg[i] = ld4(rws, col * 8, W_MG + i * 4096);
          mz = __builtin_bit_cast(v2u, __builtin_amdgcn_raw_buffer_load_b64(rws, zc * 8, W_MG + 3 * 4096, 16));
          if (__all(((mg[0].y ^ tag) | (mg[0].w ^ tag) | (mg[1].y ^ tag) | (mg[1].w ^ tag) | (mg[2].y ^ tag) | (mg[2].w ^ tag) | (mz.y ^ tag)) == 0)) break;
          FENCE();
        }
        const int sh = 16 - 16 * b;
        ga = v2f{bfhi(mg[0].x << sh), bfhi(mg[0].z << sh)}; sb = v2f{bfhi(mg[1].x << sh), bfhi(mg[1].z << sh)};
        cb1 = rb2(v2f{bfhi(mg[2].x << sh), bfhi(mg[2].z << sh)} + 1.f);
        gb = bfhi(mz.x << (16 - 16 * (zb & 1)));
      }
      v8bf W[16];
#pragma unroll
      for (int j = 0; j < 16; ++j)
        W[j] = trfrag((wave >= 4 ? wslot(j >> 1) + waddr(wave & 3) : wslot(8 + 2 * wave + (j >> 3)) + waddr((j >> 1) & 3)) + (j & 1) * 4096);
      u32 wa = L_ACT + b * 1024 + fpos(col) * 2, xa = L_X1 + b * 2048 + col * 4, ra = L_RED + wave * 8 + half * 4;
      u32 ha = L_H1 + (c & 1) * 128 + fpos((wave & 3) * 16 + g * 4 + ((c >> 1) & 3)) * 2;
      if (dry) xa = ra = ha = L_ACT;
      PIN(x0); PIN(ga); PIN(sb); PIN(cb1); PIN(gb); PIN(wa); PIN(xa); PIN(ra); PIN(ha);
      v4u a[4];
#pragma nounroll
      for (int sp = 0; sp < cap; ++sp) {
#pragma unroll
        for (int j = 0; j < 4; ++j) a[j] = ld4(rws, half * 16384 + i32 * 16, W_AP + j * 4096 + b * 2048 + (wave & 3) * 512);
        u32 mx = 0;
#pragma unroll
        for (int j = 0; j < 4; ++j) mx = max(mx, max(max(a[j].x, a[j].y), max(a[j].z, a[j].w)));
        if (__all(mx != SENT)) break;
        FENCE();
      }
      if (wave == 4) STAMP(11);
      const v4f at = (__builtin_bit_cast(v4f, a[0]) + __builtin_bit_cast(v4f, a[1])) + (__builtin_bit_cast(v4f, a[2]) + __builtin_bit_cast(v4f, a[3]));
      const v2f x1 = rb2(x0 + rb2(ga * rb2(v2f{fold32(at.x, at.z), fold32(at.y, at.w)})));
      const float st = half_sum(fold32(x1.x + x1.y, fmaf(x1.x, x1.x, x1.y * x1.y)));
      if (i32 == 0) *lp<float>(ra) = st;
      *lp<v2f>(xa) = x1;
      tbar();
      {
        const v4f r0 = *lp<v4f>(L_RED + b * 32), r1 = *lp<v4f>(L_RED + b * 32 + 16);
        const float mean = ((r0.x + r0.z) + (r1.x + r1.z)) * (1.f / 512.f);
        const float var = fmaxf(fmaf(-mean, mean, ((r0.y + r0.w) + (r1.y + r1.w)) * (1.f / 512.f)), 0.f);
        const v2f mv = rb2(v2f{mean, var});
        const float ri = rcp(rb(__builtin_amdgcn_sqrtf(rb(mv.y + 1e-5f))));
        const v2f hh = rb2(rb2(rb2(x1 - mv.x) * ri) * cb1) + sb;
        *lp<u32>(wa) = pk2(hh.x, hh.y);
      }
      tbar();
      if (wave >= 4 || dry) {
        v8bf A[16];
#pragma unroll
        for (int ks = 0; ks < 16; ++ks) A[ks] = ldfrag(fa + ks * 64);
        __builtin_amdgcn_sched_barrier(0);
        v4f d0 = {0.f, 0.f, 0.f, 0.f}, d1 = {0.f, 0.f, 0.f, 0.f};
#pragma unroll
        for (int ks = 0; ks < 16; ks += 2) { d0 = mfma(W[ks], A[ks], d0); d1 = mfma(W[ks + 1], A[ks + 1], d1); }
        d0 += d1;
        const int rr = (c >> 1) & 3;
        const float v = silu(rb(rr == 0 ? d0.x : rr == 1 ? d0.y : rr == 2 ? d0.z : d0.w));
        *lp<u16>(ha) = (u16)pk2(v, v);
      }
      tbar();
      if (wave < 4) {
        const v8bf a0 = ldfrag(L_H1 + (c & 1) * 128 + g * 16), a1 = ldfrag(L_H1 + (c & 1) * 128 + 64 + g * 16);
        v4f dd[8];
#pragma unroll
        for (int t = 0; t < 8; ++t) dd[t] = mfma(W[2 * t + 1], a1, mfma(W[2 * t], a0, v4f{0.f, 0.f, 0.f, 0.f}));
        v4f rv = dd[0];
#pragma unroll
        for (int t = 1; t < 8; ++t) if ((c >> 1) == t) rv = dd[t];
        v4u ov = {canon(rv.x), canon(rv.y), canon(rv.z), canon(rv.w)};
        st4(ov, rws, (c & 1) * 2048 + (c >> 1) * 64 + g * 16, W_OP + yi * 4096 + wave * 512);
        if (wave == 1 && yi == 27 && lane == 0) st2(v2u{(u32)E, (u32)(E >> 32)}, rws, 0, W_EPOCH);
        if (wave == 0) STAMP(12);
      }
      if ((wave == 4 || dry) && yi < 16) {
        float xx = *lp<float>(L_X1 + zb * 2048 + zc * 4);
        PIN(xx);
        v4u z[7];
#pragma nounroll
        for (int sp = 0; sp < cap; ++sp) {
#pragma unroll
          for (int j = 0; j < 7; ++j) z[j] = ld4(rws, g * 4096 + c * 16, W_OP + j * 16384 + zb * 2048 + (yi & 7) * 256);
          u32 mx = 0;
#pragma unroll
          for (int j = 0; j < 7; ++j) mx = max(mx, max(max(z[j].x, z[j].y), max(z[j].z, z[j].w)));
          if (__all(mx != SENT)) break;
          FENCE();
        }
        v4f s = __builtin_bit_cast(v4f, z[0]);
#pragma unroll
        for (int j = 1; j < 7; ++j) s += __builtin_bit_cast(v4f, z[j]);
        const float tot = fold16(fold32(s.x, s.z), fold32(s.y, s.w));
        const float rr = xx + rb(gb * rb(tot));
        u32 ob = pk2(rr, rr);
        PIN(ob);
        if (!dry) { out[zb * 512 + zc] = (u16)ob; STAMP(13); }
      }
    }
  }
  FIN();
}

extern "C" void kernel_launch(void* const* d_in, const int* in_sizes, int n_in, void* d_out, int out_size, void* d_ws, size_t ws_size, hipStream_t stream) {
  hipLaunchKernelGGL(fused_block, dim3(256), dim3(512), 0, stream, (const u16*)d_in[1], (const u16*)d_in[2], (const u16*)d_in[9], (const u16*)d_in[3],
                     (const u16*)d_in[4], (const u16*)d_in[6], (const u16*)d_in[7], (const u16*)d_in[0], (const u16*)d_in[8], (u16*)d_out, (unsigned char*)d_ws);
}
```

```cpp
#include <hip/hip_runtime.h>
#include <hip/hip_bf16.h>

#define STAMPS 0

typedef unsigned int u32;
typedef unsigned long long u64;
typedef unsigned short u16;
typedef u32 v4u __attribute__((ext_vector_type(4)));
typedef u32 v2u __attribute__((ext_vector_type(2)));
typedef int v4i __attribute__((ext_vector_type(4)));
typedef float v4f __attribute__((ext_vector_type(4)));
typedef float v2f __attribute__((ext_vector_type(2)));
typedef short v4s __attribute__((ext_vector_type(4)));
typedef __bf16 v8bf __attribute__((ext_vector_type(8)));
typedef __bf16 v2bf __attribute__((ext_vector_type(2)));
#define LAS __attribute__((address_space(3)))
#define GAS __attribute__((address_space(1)))
#define DEVI __device__ __forceinline__

constexpr int NSLOT = 16, SLOT = 8192, NCONS = 6, NSTASH = 4;
#define WIN 28
#define QAT 12
#define HDELAY 400
#define MAP 2
#define TOUCH 1
#define TAU16 443
#define TOFF 0
#define NF 2
#define CODEPF 1
#define CODE_BYTES 22784
#define PSTART 6
#define TMODE 0
#define PEND 0
#define ZR 8
#define WA 16
#define WB 20
#define LA 25
#define LB 50
#define COFF -45
#define COMMON 1
#define POLLS 16
#define NX_A 64
#define NQ_A 63
#define NY_A 63
#define NS_A 66
constexpr int NX_B = 64, NQ_B = 63, NY_B = 65, NS_B = 65, NMAX = 72;
static_assert(4 * NX_A + 6 * NQ_A + 2 * NY_A + 4 * NS_A == 1024 && 4 * NX_B + 6 * NQ_B + 2 * NY_B + 4 * NS_B == 1024, "blocks");
#define BIAS 8
constexpr u64 start_table(int nx, int nq, int ny, int ns) {
  u64 t = 0; int s = 0;
  for (int r = 0; r < 16; ++r) { const int dl = s - 64 * r + BIAS; if (dl < 0 || dl > 15) return ~0ull; t |= (u64)dl << (4 * r); s += r < 4 ? nx : r < 10 ? nq : r < 12 ? ny : ns; }
  return t;
}
constexpr u64 ST_A = start_table(NX_A, NQ_A, NY_A, NS_A), ST_B = start_table(NX_B, NQ_B, NY_B, NS_B);
static_assert(ST_A != ~0ull && ST_B != ~0ull, "start table");
constexpr int L_CTL = NSLOT * SLOT;
constexpr int L_Q = L_CTL + 256;
constexpr int L_ACT = L_Q + 1024;
constexpr int ASTR = 1024 + 64;
constexpr int L_MRG = L_ACT + 2 * ASTR;
constexpr int L_ML = L_MRG + 2048;
constexpr int L_SC = L_ML + 256;
constexpr int L_JUNK = L_SC + NMAX * 256;
constexpr int L_END = L_JUNK + 256;
constexpr int L_H1 = L_SC;
constexpr int L_RED = L_H1 + 256;
static_assert(L_END <= 160 * 1024, "LDS");
constexpr int C_FULL = 0, C_QRDY = 1, C_MCNT = 2, C_CLR = 3, C_ARDY = 4, C_TAG = 5, C_WRD = 6, C_MX = 7, C_SM = 8, C_T0 = 9, C_FREE = 16;
constexpr u32 W_EPOCH = 0, W_T0 = 4096, W_SP = 8192, W_HG = W_SP + 4 * 1024 * 16, W_QG = W_HG + 512 * 8, W_MG = W_QG + 4096 * 8, W_PART = W_MG + 2048 * 8;
constexpr u32 W_LSE = W_PART + 64 * 4096;
constexpr u32 W_AP = W_LSE + 256 * 128, W_OP = W_AP + 8 * 4096, W_END = W_OP + 28 * 4096;
constexpr u32 CLRB = (W_END - W_PART) / 64;
static_assert(CLRB * 64 == W_END - W_PART && CLRB % 16 == 0, "clear split");
constexpr u32 W_STAMP = 2u << 20;
constexpr u32 SENT = 0xFFFFFFFFu;
constexpr int SPIN_CAP = 1 << 16;

DEVI float bf2f(u32 v) { return __uint_as_float(v << 16); }
DEVI float bfhi(u32 v) { return __uint_as_float(v & 0xffff0000u); }
DEVI u32 pk2(float a, float b) { v2f x = {a, b}; return __builtin_bit_cast(u32, __builtin_convertvector(x, v2bf)); }
DEVI float rb(float a) { return bf2f(pk2(a, a) & 0xffffu); }
DEVI float ex2(float x) { return __builtin_amdgcn_exp2f(x); }
DEVI float rcp(float x) { return __builtin_amdgcn_rcpf(x); }
DEVI float silu(float v) { return v * rcp(1.f + ex2(v * -1.4426950408889634f)); }
DEVI float silu_exact(float v) {
  const float t = fminf(v * -1.4426950216293335f, 126.f);
  const float r = fmaf(v, -1.92596299112662e-08f, fmaf(v, -1.4426950216293335f, -t));
  float e = ex2(t);
  e = fmaf(e, fminf(r, 1.f) * 0.6931471805599453f, e);
  const float d = 1.f + e, rc = rcp(d), q = v * rc;
  return fmaf(fmaf(-q, d, v), rc, q);
}
DEVI float lo16(u32 v) { return __uint_as_float(v << 16); }
DEVI v2f rb2(v2f a) { const u32 p = pk2(a.x, a.y); return v2f{lo16(p), bfhi(p)}; }
DEVI float fold32(float a, float b) { auto s = __builtin_amdgcn_permlane32_swap(__float_as_uint(a), __float_as_uint(b), false, false); return __uint_as_float(s[0]) + __uint_as_float(s[1]); }
DEVI float fold16(float a, float b) { auto s = __builtin_amdgcn_permlane16_swap(__float_as_uint(a), __float_as_uint(b), false, false); return __uint_as_float(s[0]) + __uint_as_float(s[1]); }
#define PIN(x) asm volatile("" : "+v"(x))
DEVI float swap16_add(float x) { auto s = __builtin_amdgcn_permlane16_swap(__float_as_uint(x), __float_as_uint(x), false, false); return __uint_as_float(s[0]) + __uint_as_float(s[1]); }
DEVI float swap32_add(float x) { auto s = __builtin_amdgcn_permlane32_swap(__float_as_uint(x), __float_as_uint(x), false, false); return __uint_as_float(s[0]) + __uint_as_float(s[1]); }
DEVI float swap16_max(float x) { auto s = __builtin_amdgcn_permlane16_swap(__float_as_uint(x), __float_as_uint(x), false, false); return fmaxf(__uint_as_float(s[0]), __uint_as_float(s[1])); }
DEVI float swap32_max(float x) { auto s = __builtin_amdgcn_permlane32_swap(__float_as_uint(x), __float_as_uint(x), false, false); return fmaxf(__uint_as_float(s[0]), __uint_as_float(s[1])); }
template <int CTRL> DEVI float dpp(float x) { return __builtin_bit_cast(float, __builtin_amdgcn_mov_dpp(__builtin_bit_cast(int, x), CTRL, 0xf, 0xf, true)); }
DEVI float row_sum(float x) { x += dpp<0xB1>(x); x += dpp<0x4E>(x); x += dpp<0x141>(x); x += dpp<0x128>(x); return x; }
DEVI float half_sum(float x) { return swap16_add(row_sum(x)); }
DEVI float wave_sum(float x) { return swap32_add(half_sum(x)); }
template <typename T> DEVI LAS T* lp(u32 a) { return (LAS T*)(size_t)a; }
DEVI u32 ldsv(u32 a) { return *(volatile LAS u32*)(size_t)a; }
DEVI void stsv(u32 a, u32 v) { *(volatile LAS u32*)(size_t)a = v; }
#define FENCE() asm volatile("" ::: "memory")
#define WAITV(n) asm volatile("s_waitcnt vmcnt(%0)" ::"i"(n) : "memory")
#define WAITL() asm volatile("s_waitcnt lgkmcnt(0)" ::: "memory")
DEVI void bar() { WAITL(); __builtin_amdgcn_s_barrier(); FENCE(); }
typedef __amdgpu_buffer_rsrc_t rsrc_t;
DEVI v4u ld4(rsrc_t r, u32 vo, u32 so) { return __builtin_bit_cast(v4u, __builtin_amdgcn_raw_buffer_load_b128(r, vo, so, 16)); }
DEVI v2u ld2(rsrc_t r, u32 vo, u32 so) { return __builtin_bit_cast(v2u, __builtin_amdgcn_raw_buffer_load_b64(r, vo, so, 16)); }
DEVI u32 ld1(rsrc_t r, u32 vo, u32 so) { return __builtin_amdgcn_raw_buffer_load_b32(r, vo, so, 16); }
DEVI void st4(v4u v, rsrc_t r, u32 vo, u32 so) { __builtin_amdgcn_raw_buffer_store_b128(v, r, vo, so, 16); }
DEVI void st2(v2u v, rsrc_t r, u32 vo, u32 so) { __builtin_amdgcn_raw_buffer_store_b64(v, r, vo, so, 16); }
DEVI void st1(u32 v, rsrc_t r, u32 vo, u32 so) { __builtin_amdgcn_raw_buffer_store_b32(v, r, vo, so, 16); }
DEVI u32 canon(float f) { u32 b = __float_as_uint(f); return b < 0xFFFFFFFEu ? b : 0xFFFFFFFEu; }
DEVI v8bf trfrag(u32 a) {
  v4s lo = __builtin_amdgcn_ds_read_tr16_b64_v4i16(lp<v4s>(a)), hi = __builtin_amdgcn_ds_read_tr16_b64_v4i16(lp<v4s>(a + 2048));
  return __builtin_bit_cast(v8bf, __builtin_shufflevector(lo, hi, 0, 1, 2, 3, 4, 5, 6, 7));
}
DEVI v4f mfma(v8bf a, v8bf b, v4f c) { return __builtin_amdgcn_mfma_f32_16x16x32_bf16(a, b, c, 0, 0, 0); }
DEVI v8bf ldfrag(u32 a) { return __builtin_bit_cast(v8bf, *lp<v4u>(a)); }
DEVI int fpos(int k) { return (k & ~31) + ((k >> 2) & 3) * 8 + ((k >> 4) & 1) * 4 + (k & 3); }
DEVI v4i mksrd(const void* p, u32 bytes) { u64 a = (u64)p; return v4i{(int)(u32)a, (int)(u32)(a >> 32), (int)bytes, 0x00020000}; }

#define DMA4(m, v0, v1, v2, v3, srd, so)                                                                                   \
  asm volatile("s_mov_b32 m0, %0\n\ts_nop 0\n\tbuffer_load_dwordx4 %1, %5, %6 offen nt lds\n\t"                           \
               "buffer_load_dwordx4 %2, %5, %6 offen offset:1024 nt lds\n\tbuffer_load_dwordx4 %3, %5, %6 offen offset:2048 nt lds\n\t" \
               "buffer_load_dwordx4 %4, %5, %6 offen offset:3072 nt lds" ::"s"(m), "v"(v0), "v"(v1), "v"(v2), "v"(v3), "s"(srd), "s"(so) : "memory")
#define DMA1(m, v0, srd, so) \
  asm volatile("s_mov_b32 m0, %0\n\ts_nop 0\n\tbuffer_load_dwordx4 %1, %2, %3 offen nt lds" ::"s"(m), "v"(v0), "s"(srd), "s"(so) : "memory")

#define STAMP(i) do { } while (0)
#define FIN() do { } while (0)

__global__ void __launch_bounds__(512, 2)
fused_block(const u16* __restrict__ Kc, const u16* __restrict__ Vc, const u16* __restrict__ wmod, const u16* __restrict__ wq,
            const u16* __restrict__ wo, const u16* __restrict__ w1, const u16* __restrict__ w2, const u16* __restrict__ xin,
            const u16* __restrict__ cin, u16* __restrict__ out, unsigned char* ws) {
  __shared__ __attribute__((aligned(16))) unsigned char smem[L_END + 256 * STAMPS];
  asm volatile("" ::"s"((u32)(size_t)(LAS unsigned char*)smem));
  const int tid = threadIdx.x, lane = tid & 63, wave = __builtin_amdgcn_readfirstlane(tid >> 6);
  if (wave == 1 && blockIdx.x < 32) {
    u64 pc; asm volatile("s_getpc_b64 %0" : "=s"(pc));
    const v4i rc = mksrd((const void*)(pc & ~127ull), CODE_BYTES);
    const u32 vo = lane * 128, m = L_JUNK, s1 = 8192, s2 = 16384;
    asm volatile("s_mov_b32 m0, %0\n\ts_nop 0\n\tbuffer_load_dword %1, %2, 0 offen lds\n\tbuffer_load_dword %1, %2, %3 offen lds\n\tbuffer_load_dword %1, %2, %4 offen lds"
                 ::"s"(m), "v"(vo), "s"(rc), "s"(s1), "s"(s2) : "memory");
  }
  const int bid = (blockIdx.x & ~31) | ((blockIdx.x & 7) << 2) | ((blockIdx.x >> 3) & 3);
  const int grp = bid >> 4, mem = bid & 15;
  const int g = lane >> 4, c = lane & 15;
  const int r = MAP ? mem : (mem + (grp >> 1)) & 15;
  const u64 stt = (grp < 14 ? ST_A : ST_B) >> (4 * r);
  const int kv0 = 64 * r + ((int)stt & 15) - BIAS;
  const bool isS = r >= 12;
  if (wave == 0) {
    const v4i srd = isS ? mksrd(wmod, 512 * 6144) : mksrd(Kc, 1u << 27);
    const u32 k0 = g * 256 + ((c ^ g) << 4), w0 = (lane >> 3) * 6144 + (((lane & 7) ^ ((lane >> 3) & 6)) << 4);
    const u32 a0 = isS ? w0 : k0, a1 = isS ? w0 + 48128 : k0 ^ 64, a2 = isS ? w0 + 2 * 48128 : k0 ^ 128, a3 = isS ? w0 + 3 * 48128 : k0 ^ 192;
    const u32 so = isS ? (r - 12) * 128 * 6144 + grp * 128 : (u32)(grp * 1024 + kv0) << 13, hs = isS ? 32 * 6144 : 4096;
#pragma unroll
    for (int i = 0; i < 4; ++i) { const u32 m = i * 4096, s1 = so + i * hs; DMA4(m, a0, a1, a2, a3, srd, s1); }
  }
  const bool isX = r < 4, isQ = r >= 4 && r < 8, isM = r == 8 || r == 9, isY = (r == 10 || r == 11) && grp < 14, isR = r == 0 && (grp & 1);
  const int xi = grp * 4 + r, qi = grp * 4 + r - 4, mi = grp * 2 + r - 8, yi = grp * 2 + r - 10;
  const int u0 = (int)(0x2222008888880000ull >> (4 * r)) & 15;
  const int nb = 64 + (r == 15 ? BIAS : (int)(stt >> 4) & 15) - ((int)stt & 15);
  const int nf = isS ? 0 : NF;
  const int ntail = isX ? 8 : isY ? 16 : 0;
  const int uv = u0 + nb, uw = uv + nb;
  u64 E = 0;
  if (__builtin_expect(wave == 0, 1)) {
    __builtin_amdgcn_s_setprio(3);
    STAMP(0);
    int iss, pub = 0;
#define TPS()
    auto publish = [&](int v) { pub = max(pub, v); stsv(L_CTL + 4 * C_FULL, pub); };
#define PACE(W_) do { WAITV(W_); publish(iss - (W_)); } while (0)
    u32 due = ((u32)__builtin_amdgcn_s_memrealtime() + TOFF) * 16 + NF * TAU16;
    __builtin_amdgcn_s_barrier(); FENCE();
    const u32 wrow = lane >> 3, wlc = ((lane & 7) ^ (((wrow >> 1) & 3) << 1)) << 4;
    int u = NF;
#pragma nounroll
    for (int seg = u0 == 8 ? 0 : 1; seg < 3 + (ntail >> 3); ++seg) {
      const bool kv = seg == 1 || seg == 2;
      const u16* base = seg == 0 ? (isQ ? wq : wmod) : seg == 1 ? Kc : seg == 2 ? Vc : seg == 4 ? w2 : isX ? wo : w1;
      const u32 rs = seg == 0 ? (isQ ? 8192 : 6144) : (seg == 3 && !isX) ? 3584 : 1024;
      const v4i srd = mksrd(base, kv ? 1u << 27 : seg == 4 ? 1792 * 1024 : (seg == 3 && isX) ? 4096 * 1024 : 512 * rs);
      u32 so = kv ? (u32)(grp * 1024 + kv0 + (seg == 1 ? nf : 0)) << 13
                  : seg == 0 ? (isQ ? qi : 16 + mi) * 128
                             : seg == 4 ? yi << 16 : isX ? ((xi >> 3) << 19) + (xi & 7) * 128 : yi * 128;
      const u32 hstep = kv ? 4096 : 32 * rs, ustep = seg == 4 ? 128 : 2 * hstep;
      const u32 sw = g << (seg - 1), sx = seg == 1 ? 4 : 8;
      const u32 w0 = wrow * rs + wlc, wd = 8 * rs - 1024;
      const u32 a0 = kv ? g * 256 + ((c ^ sw) << 4) : w0, a1 = kv ? g * 256 + ((c ^ sw ^ sx) << 4) : w0 + wd;
      const u32 a2 = kv ? g * 256 + ((c ^ sw ^ ((2 * sx) & 15)) << 4) : w0 + 2 * wd, a3 = kv ? g * 256 + ((c ^ sw ^ ((3 * sx) & 15)) << 4) : w0 + 3 * wd;
      const int ue = seg == 0 ? NF + u0 : seg == 1 ? uv : seg == 2 ? uw : u + 8;
#pragma nounroll
      while (u < ue) {
        const bool paced = u >= 4 && (seg < 3 || u < PEND);
        const int stop = min(ue, u < 4 ? 4 : (COMMON && u < 48) ? 48 : (seg >= 3 && u < PEND) ? PEND : 1 << 20);
        u32 dd = paced ? due : due - (1u << 28);
        const int u_in = u;
        u32 t0, t1, lag; u32 vf, vt;
#define DMAS(so) "buffer_load_dwordx4 %[a0], %[srd], " so " offen nt lds\n\tbuffer_load_dwordx4 %[a1], %[srd], " so " offen offset:1024 nt lds\n\t" \
                 "buffer_load_dwordx4 %[a2], %[srd], " so " offen offset:2048 nt lds\n\tbuffer_load_dwordx4 %[a3], %[srd], " so " offen offset:3072 nt lds\n\t"
#define RDFREE "s_lshl_b32 %[t1], %[u], 2\n\ts_and_b32 %[t1], %[t1], 60\n\ts_or_b32 %[t1], %[t1], %[fb]\n\tv_mov_b32 %[vf], %[t1]\n\tds_read_b32 %[vf], %[vf]\n\t"
#define REST(W) "s_waitcnt vmcnt(" #W ")\n\ts_mov_b32 m0, %[t0]\n\ts_lshl_b32 %[t0], %[u], 3\n\t" DMAS("%[t1]")                                   \
                "s_sub_i32 %[t0], %[t0], " #W " - 4\n\ts_max_i32 %[pub], %[pub], %[t0]\n\tv_mov_b32 %[vt], %[pub]\n\tds_write_b32 %[va], %[vt]\n\t"                                      \
                "s_add_i32 %[u], %[u], 1\n\ts_add_u32 %[so], %[so], %[us]\n\ts_add_u32 %[dd], %[dd], %[tau]\n\t" RDFREE                             \
                "s_memrealtime s[98:99]\n\ts_cmp_lt_i32 %[u], %[stop]\n\ts_waitcnt vmcnt(" #W ")\n\ts_cbranch_scc1 1b\n\ts_branch 9f\n"
#define XREST(W) REST(W)
        asm volatile(RDFREE "s_memrealtime s[98:99]\n"
                     "1:\n\ts_sub_i32 %[t1], %[u], 15\n\ts_waitcnt lgkmcnt(0)\n\tv_cmp_gt_i32 vcc, %[t1], %[vf]\n\ts_lshl_b32 s98, s98, 4\n\t"
                     "s_cbranch_vccnz 8f\n"
                     "2:\n\ts_sub_i32 %[lag], s98, %[dd]\n\ts_cmp_lt_i32 %[lag], 0\n\ts_cbranch_scc1 7f\n\t"
                     "s_lshl_b32 %[t0], %[u], 13\n\ts_and_b32 %[t0], %[t0], 0x1e000\n\ts_mov_b32 m0, %[t0]\n\ts_add_u32 %[t1], %[so], %[hs]\n\t" DMAS("%[so]")
                     "s_or_b32 %[t0], %[t0], 0x1000\n\ts_cmp_lt_i32 %[lag], %[la]\n\ts_cbranch_scc1 4f\n\ts_cmp_lt_i32 %[lag], %[lb]\n\ts_cbranch_scc1 5f\n\t"
                     XREST(WIN) "4:\n\t" XREST(WA) "5:\n\t" XREST(WB)
                     "8:\n\t" RDFREE "s_sub_i32 %[t1], %[u], 15\n\ts_waitcnt lgkmcnt(0)\n\tv_cmp_gt_i32 vcc, %[t1], %[vf]\n\ts_nop 0\n\ts_cbranch_vccz 7f\n\t"
                     "s_waitcnt vmcnt(0)\n\ts_lshl_b32 %[pub], %[u], 3\n\tv_mov_b32 %[vt], %[pub]\n\tds_write_b32 %[va], %[vt]\n"
                     "6:\n\t" RDFREE "s_sub_i32 %[t1], %[u], 15\n\ts_waitcnt lgkmcnt(0)\n\tv_cmp_gt_i32 vcc, %[t1], %[vf]\n\ts_sleep 1\n\ts_cbranch_vccnz 6b\n"
                     "7:\n\ts_memrealtime s[98:99]\n\ts_waitcnt lgkmcnt(0)\n\ts_lshl_b32 s98, s98, 4\n\ts_branch 2b\n"
                     "9:\n\ts_waitcnt lgkmcnt(0)"
                     : [u] "+s"(u), [so] "+s"(so), [dd] "+s"(dd), [pub] "+s"(pub), [t0] "=&s"(t0), [t1] "=&s"(t1), [lag] "=&s"(lag), [vf] "=&v"(vf), [vt] "=&v"(vt)
                     : [a0] "v"(a0), [a1] "v"(a1), [a2] "v"(a2), [a3] "v"(a3), [srd] "s"(srd), [hs] "s"(hstep), [us] "s"(ustep), [stop] "s"(stop),
                       [va] "v"(L_CTL + 4 * C_FULL), [fb] "i"(L_CTL + 4 * C_FREE), [tau] "i"(TAU16), [la] "i"(LA * 16), [lb] "i"(LB * 16)
                     : "memory", "vcc", "scc", "s98", "s99");
        due += (u - u_in) * TAU16;
        if (u == 4 && isS) { WAITV(16); publish(16); }
        if (COMMON && u == 48) { const u32 t = __builtin_amdgcn_readfirstlane(ldsv(L_CTL + 4 * C_T0)); if (t) due = t * 16 + u * TAU16; }
      }
    }
    iss = 8 * u;
    PACE(24); PACE(16); PACE(8); PACE(0);
    STAMP(1);
    __builtin_amdgcn_s_setprio(0);
  } else {
    if (wave == 2) { stsv(L_CTL + 4 * lane, 0); WAITL(); }
    __builtin_amdgcn_s_barrier(); FENCE();
    const rsrc_t rws = __builtin_amdgcn_make_buffer_rsrc(ws, 0, W_END, 0x00020000);
    auto wait_full = [&](u32 need) { while (ldsv(L_CTL + 4 * C_FULL) < need) __builtin_amdgcn_s_sleep(1); FENCE(); };
    const u32 trow = 4 * g + (c >> 2);
    auto waddr = [&](int t) -> u32 { return trow * 128 + ((((u32)(t ^ ((trow >> 1) & 3)) << 1) | ((c >> 1) & 1)) << 4) + (c & 1) * 8; };

    if (wave == 1) {
      const u64 tstart = __builtin_amdgcn_s_memrealtime();
      u64 E0 = __hip_atomic_load((GAS u64*)(ws + W_EPOCH), __ATOMIC_RELAXED, __HIP_MEMORY_SCOPE_AGENT);
      u32 tag;
#define CHAIN_START() do { E = E0 + 1; tag = (u32)E; } while (0)
      auto silu4 = [&](u32 x, u32 y) { return v2u{pk2(silu_exact(lo16(x)), silu_exact(bfhi(x))), pk2(silu_exact(lo16(y)), silu_exact(bfhi(y)))}; };
      if (isS) {
        const v2u cv = *(const v2u*)(cin + (lane >> 5) * 512 + (r - 12) * 128 + (lane & 31) * 4);
        CHAIN_START();
        *lp<v2u>(L_ACT + (lane >> 5) * ASTR + fpos((lane & 31) * 4) * 2) = silu4(cv.x, cv.y);
        FENCE();
        v8bf act[4];
#pragma unroll
        for (int ks = 0; ks < 4; ++ks) { act[ks] = ldfrag(L_ACT + (c & 1) * ASTR + g * 16 + ks * 64); PIN(act[ks]); }
        wait_full(16);
        v8bf W[16];
#pragma unroll
        for (int j = 0; j < 16; ++j) W[j] = trfrag(waddr(j & 3) + (j >> 2) * 4096);
        FENCE(); stsv(L_CTL + 4 * C_FREE, 1); stsv(L_CTL + 4 * (C_FREE + 1), 2);
        v4f acc[4];
#pragma unroll
        for (int t = 0; t < 4; ++t) acc[t] = v4f{0.f, 0.f, 0.f, 0.f};
#pragma unroll
        for (int j = 0; j < 16; ++j) acc[j & 3] = mfma(act[j >> 2], W[j], acc[j & 3]);
        const float r0 = g == 0 ? acc[0][0] : g == 1 ? acc[1][0] : g == 2 ? acc[2][0] : acc[3][0];
        const float r1 = g == 0 ? acc[0][1] : g == 1 ? acc[1][1] : g == 2 ? acc[2][1] : acc[3][1];
        while (ldsv(L_CTL + 4 * C_CLR) == 0) { }
        st4(v4u{__float_as_uint(r0), tag, __float_as_uint(r1), tag}, rws, lane * 16, W_SP + ((r - 12) * 1024 + grp * 64) * 16);
        STAMP(3);
      } else if (isR) {
        const v4u in0 = *(const v4u*)(xin + lane * 8), in1 = *(const v4u*)(xin + 512 + lane * 8);
        u32 xs0 = *(const u32*)(xin + (grp >> 1) * 64 + (lane & ~1)), xs1 = *(const u32*)(xin + 512 + (grp >> 1) * 64 + (lane & ~1));
        CHAIN_START();
        PIN(xs0); PIN(xs1);
        xs0 >>= 16 * (lane & 1); xs1 >>= 16 * (lane & 1);
        float mr[2], ri[2];
#pragma unroll
        for (int b = 0; b < 2; ++b) {
          const v4u xv = b ? in1 : in0;
          const float x[8] = {lo16(xv.x), bfhi(xv.x), lo16(xv.y), bfhi(xv.y), lo16(xv.z), bfhi(xv.z), lo16(xv.w), bfhi(xv.w)};
          float s = 0.f;
#pragma unroll
          for (int i = 0; i < 8; ++i) s += x[i];
          const float mean = wave_sum(s) * (1.f / 512.f);
          float q2 = 0.f;
#pragma unroll
          for (int i = 0; i < 8; ++i) q2 += (x[i] - mean) * (x[i] - mean);
          const float var = wave_sum(q2) * (1.f / 512.f);
          mr[b] = rb(mean); ri[b] = rcp(rb(__builtin_amdgcn_sqrtf(rb(rb(var) + 1e-5f))));
        }
        v2f y = rb2(rb2(v2f{lo16(xs0) - mr[0], lo16(xs1) - mr[1]}) * v2f{ri[0], ri[1]});
        PIN(y);
        v4u a[8];
#pragma nounroll
        for (int sp = 0; sp < SPIN_CAP; ++sp) {
          u32 bad = 0;
#pragma unroll
          for (int i = 0; i < 8; ++i) { a[i] = ld4(rws, lane * 16, W_SP + ((i & 3) * 1024 + (i >> 2) * 512 + (grp >> 1) * 64) * 16); bad |= (a[i].y ^ tag) | (a[i].w ^ tag); }
          if (__all(bad == 0)) break;
          FENCE();
        }
        v2f sc[2];
#pragma unroll
        for (int pt = 0; pt < 2; ++pt) {
          sc[pt] = v2f{0.f, 0.f};
#pragma unroll
          for (int k4 = 0; k4 < 4; ++k4) sc[pt] += v2f{__uint_as_float(a[pt * 4 + k4].x), __uint_as_float(a[pt * 4 + k4].z)};
        }
        const v2f h = rb2(y * rb2(rb2(sc[1]) + 1.f)) + rb2(sc[0]);
        st2(v2u{pk2(h.x, h.y), tag}, rws, lane * 8, W_HG + (grp >> 1) * 512);
        STAMP(23);
      } else if (isM || isQ) {
        if (isM) {
          const v4u in0 = *(const v4u*)(cin + lane * 8), in1 = *(const v4u*)(cin + 512 + lane * 8);
          CHAIN_START();
          *lp<v2u>(L_ACT + fpos(lane * 8) * 2) = silu4(in0.x, in0.y); *lp<v2u>(L_ACT + fpos(lane * 8 + 4) * 2) = silu4(in0.z, in0.w);
          *lp<v2u>(L_ACT + ASTR + fpos(lane * 8) * 2) = silu4(in1.x, in1.y); *lp<v2u>(L_ACT + ASTR + fpos(lane * 8 + 4) * 2) = silu4(in1.z, in1.w);
        } else {
          CHAIN_START();
          while (__builtin_amdgcn_s_memrealtime() < tstart + HDELAY) __builtin_amdgcn_s_sleep(4);
          v4u a[4];
#pragma nounroll
          for (int sp = 0; sp < SPIN_CAP; ++sp) {
            u32 bad = 0;
#pragma unroll
            for (int i = 0; i < 4; ++i) { a[i] = ld4(rws, lane * 16 + i * 1024, W_HG); bad |= (a[i].y ^ tag) | (a[i].w ^ tag); }
            if (__all(bad == 0)) break;
            FENCE();
          }
#pragma unroll
          for (int i = 0; i < 4; ++i) {
            const u32 wa = L_ACT + fpos(i * 128 + lane * 2) * 2;
            *lp<u32>(wa) = (a[i].x & 0xffffu) | (a[i].z << 16); *lp<u32>(wa + ASTR) = (a[i].x >> 16) | (a[i].z & 0xffff0000u);
          }
          STAMP(4);
        }
        FENCE(); stsv(L_CTL + 4 * C_TAG, tag); stsv(L_CTL + 4 * C_ARDY, 1);
      } else CHAIN_START();
      if (COMMON && blockIdx.x < 8 && lane == 0) st2(v2u{(u32)tstart, tag}, rws, blockIdx.x * 8, W_T0);
      {
        wait_full(8 * u0 + 16 * QAT);
        v4u a[2];
#pragma nounroll
        for (int sp = 0; sp < SPIN_CAP; ++sp) {
          a[0] = ld4(rws, lane * 16, W_QG + grp * 2048); a[1] = ld4(rws, lane * 16 + 1024, W_QG + grp * 2048);
          if (__all(((a[0].y ^ tag) | (a[0].w ^ tag) | (a[1].y ^ tag) | (a[1].w ^ tag)) == 0)) break;
          __builtin_amdgcn_s_sleep(POLLS); FENCE();
        }
        *lp<v2u>(L_Q + lane * 8) = v2u{a[0].x, a[0].z}; *lp<v2u>(L_Q + 512 + lane * 8) = v2u{a[1].x, a[1].z};
        FENCE(); stsv(L_CTL + 4 * C_TAG, tag); stsv(L_CTL + 4 * C_QRDY, 1);
        STAMP(6);
        if (COMMON) {
          const v2u t = ld2(rws, (lane & 7) * 8, W_T0);
          if (__all(t.y == tag)) {
            int dt = (int)(t.x - (u32)tstart);
            dt += __builtin_amdgcn_mov_dpp(dt, 0xB1, 0xf, 0xf, true); dt += __builtin_amdgcn_mov_dpp(dt, 0x4E, 0xf, 0xf, true); dt += __builtin_amdgcn_mov_dpp(dt, 0x141, 0xf, 0xf, true);
            stsv(L_CTL + 4 * C_T0, ((u32)tstart + (dt >> 3) + COFF) | 1);
          }
        }
      }
      {
        const u16* p = Vc + ((size_t)grp * 32768 + (size_t)(kv0 + (lane & 1) * (nb - 1)) * 32) * 128;
        if (lane >= 2) p = isX ? wo + (xi >> 3) * 512 * 512 : lane == 2 ? out : lane == 3 ? xin : lane == 4 ? w2 + max(yi, 0) % 28 * 64 * 512 : w1 + (lane & 7) * 64 * 1792;
        if (lane < 2 || ((isX || r == ZR) && lane == 2) || (isY && lane < 16)) { u32 t = __builtin_nontemporal_load((const u32*)p); PIN(t); }
      }
      STAMP(2);
      if (r == ZR) {
        const int zb = grp >> 3, zc = (grp & 7) * 64 + c * 4 + g;
        wait_full(8 * uw);
        const u32 xv = xin[zb * 512 + zc];
        v2u m0, m1;
#pragma nounroll
        for (int sp = 0; sp < SPIN_CAP; ++sp) {
          m0 = ld2(rws, zc * 8, W_MG); m1 = ld2(rws, zc * 8, W_MG + 3 * 4096);
          if (__all(((m0.y ^ tag) | (m1.y ^ tag)) == 0)) break;
          FENCE();
        }
        v4u z[7];
#pragma nounroll
        for (int sp = 0; sp < SPIN_CAP; ++sp) {
          z[0] = ld4(rws, g * 4096 + c * 16, W_AP + grp * 256); z[1] = ld4(rws, g * 4096 + c * 16, W_AP + 16384 + grp * 256);
          if (__all(max(max(max(z[0].x, z[0].y), max(z[0].z, z[0].w)), max(max(z[1].x, z[1].y), max(z[1].z, z[1].w))) != SENT)) break;
          __builtin_amdgcn_s_sleep(2); FENCE();
        }
        v4f s0 = __builtin_bit_cast(v4f, z[0]), s1 = __builtin_bit_cast(v4f, z[1]);
        const float at = fold32(fold16(s0.x, s0.y), fold16(s0.z, s0.w)) + fold32(fold16(s1.x, s1.y), fold16(s1.z, s1.w));
        float x1 = rb(lo16(xv) + rb(bfhi(m0.x << (16 - 16 * zb)) * rb(at))), gb = bfhi(m1.x << (16 - 16 * zb));
        GAS u16* op = (GAS u16*)out + zb * 512 + zc;
        PIN(x1); PIN(gb); PIN(op);
#pragma nounroll
        for (int sp = 0; sp < SPIN_CAP; ++sp) {
#pragma unroll
          for (int j = 0; j < 7; ++j) z[j] = ld4(rws, g * 4096 + c * 16, W_OP + j * 16384 + grp * 256);
          u32 mx = 0;
#pragma unroll
          for (int j = 0; j < 7; ++j) mx = max(mx, max(max(z[j].x, z[j].y), max(z[j].z, z[j].w)));
          if (__all(mx != SENT)) break;
          FENCE();
        }
        const v4f s = ((__builtin_bit_cast(v4f, z[0]) + __builtin_bit_cast(v4f, z[1])) + (__builtin_bit_cast(v4f, z[2]) + __builtin_bit_cast(v4f, z[3]))) +
                      ((__builtin_bit_cast(v4f, z[4]) + __builtin_bit_cast(v4f, z[5])) + __builtin_bit_cast(v4f, z[6]));
        const float rr = x1 + rb(gb * rb(fold16(fold32(s.x, s.z), fold32(s.y, s.w))));
        *op = (u16)pk2(rr, rr);
        STAMP(13);
      }
    } else {
      if (isS && wave == 7) {
        const v4u s4 = {SENT, SENT, SENT, SENT};
        const u32 so = W_PART + (grp * 4 + r - 12) * CLRB;
#pragma unroll
        for (int i = 0; i < CLRB / 1024; ++i) st4(s4, rws, lane * 16 + (i & 3) * 1024, so + (i >> 2) * 4096);
        if (lane < (CLRB % 1024) / 16) st4(s4, rws, lane * 16, so + CLRB / 1024 * 1024);
        WAITV(0); stsv(L_CTL + 4 * C_CLR, 1);
      }

      if (wave == 6) { *lp<v4f>(L_MRG + lane * 16) = v4f{0.f, 0.f, 0.f, 0.f}; *lp<v4f>(L_MRG + 1024 + lane * 16) = v4f{0.f, 0.f, 0.f, 0.f}; }
      const int d = c >> 2, cw = wave - 2;
      const u32 sca = L_SC + (g * 4 + (c & 3)) * 16;
      {
        u32 ka[4];
#pragma unroll
        for (int s = 0; s < 4; ++s) ka[s] = c * 256 + (((4 * s + g) ^ c) << 4);
        v8bf qf[4];
        float mx = -INFINITY;
        auto rdk = [&](v8bf (&kf)[8], int j) {
          const int u = j < nf ? j : u0 + j;
          const u32 lb = (u & (NSLOT - 1)) * SLOT;
          wait_full(8 * u + 8);
#pragma unroll
          for (int s = 0; s < 4; ++s) { kf[s] = ldfrag(lb + ka[s]); kf[4 + s] = ldfrag(lb + ka[s] + 4096); }
          FENCE(); stsv(L_CTL + 4 * (C_FREE + (u & (NSLOT - 1))), u + 1);
        };
        auto qk = [&](v8bf (&kf)[8], int j) {
          v4f S0 = {0.f, 0.f, 0.f, 0.f}, S1 = {0.f, 0.f, 0.f, 0.f};
#pragma unroll
          for (int s = 0; s < 4; ++s) { S0 = mfma(kf[s], qf[s], S0); S1 = mfma(kf[4 + s], qf[s], S1); }
          const v2f a = rb2(v2f{S0.x, S0.y}) * 0.08838834764831845f, b = rb2(v2f{S0.z, S0.w}) * 0.08838834764831845f;
          const v2f e = rb2(v2f{S1.x, S1.y}) * 0.08838834764831845f, f = rb2(v2f{S1.z, S1.w}) * 0.08838834764831845f;
          mx = fmaxf(fmaxf(fmaxf(mx, fmaxf(a.x, a.y)), fmaxf(b.x, b.y)), fmaxf(fmaxf(e.x, e.y), fmaxf(f.x, f.y)));
          if (c < 4) *lp<v4u>(sca + j * 256) = v4u{pk2(a.x, a.y), pk2(b.x, b.y), pk2(e.x, e.y), pk2(f.x, f.y)};
        };
        v8bf sk[NSTASH][8];
        rdk(sk[0], cw);
      if (u0 == 8 && wave < 6) {
        wait_full(8 * NF + 64);
        v8bf W[16];
#pragma unroll
        for (int ks = 0; ks < 16; ++ks) W[ks] = trfrag(NF * SLOT + ks * 4096 + waddr(wave - 2));
        WAITL();
        u32 old = 0;
        if (lane == 0) old = __hip_atomic_fetch_add(lp<u32>(L_CTL + 4 * C_WRD), 1u, __ATOMIC_RELAXED, __HIP_MEMORY_SCOPE_WORKGROUP);
        if (__builtin_amdgcn_readfirstlane(old) == 3 && lane < 8) stsv(L_CTL + 4 * (C_FREE + NF + lane), NF + lane + 1);
        while (ldsv(L_CTL + 4 * C_ARDY) == 0) __builtin_amdgcn_s_sleep(1);
        FENCE();
        const u32 tag = ldsv(L_CTL + 4 * C_TAG);
        v8bf A[16];
#pragma unroll
        for (int ks = 0; ks < 16; ++ks) A[ks] = ldfrag(L_ACT + (c & 1) * ASTR + g * 16 + ks * 64);
        __builtin_amdgcn_sched_barrier(0);
        v4f d0 = {0.f, 0.f, 0.f, 0.f}, d1 = {0.f, 0.f, 0.f, 0.f};
#pragma unroll
        for (int ks = 0; ks < 16; ks += 2) { d0 = mfma(A[ks], W[ks], d0); d1 = mfma(A[ks + 1], W[ks + 1], d1); }
        const float r0 = d0[0] + d1[0], r1 = d0[1] + d1[1];
        const float n0 = dpp<0xB1>(r0), n1 = dpp<0xB1>(r1);
        const bool odd = lane & 1;
        if (lane < 16) {
          if (isM) st2(v2u{pk2(r0, r1), tag}, rws, lane * 8, W_MG + mi * 512 + (wave - 2) * 128);
          else st2(v2u{pk2(odd ? n1 : r0, odd ? r1 : n0), tag}, rws, (lane & 1) * 16384 + (lane >> 1) * 8, W_QG + qi * 256 + (wave - 2) * 64);
        }
        if (isQ && wave == 2) STAMP(5);
      }
#pragma unroll
        for (int i = 1; i < NSTASH; ++i) rdk(sk[i], cw + i * NCONS);
        while (ldsv(L_CTL + 4 * C_QRDY) == 0) __builtin_amdgcn_s_sleep(2);
        FENCE();
#pragma unroll
        for (int s = 0; s < 4; ++s) qf[s] = ldfrag(L_Q + (c & 3) * 256 + (4 * s + g) * 16);
#pragma unroll
        for (int i = 0; i < NSTASH; ++i) qk(sk[i], cw + i * NCONS);
#pragma nounroll
        for (int j = cw + NSTASH * NCONS; j < nb; j += NCONS) { v8bf kf[8]; rdk(kf, j); qk(kf, j); }
        mx = swap32_max(swap16_max(rb(mx)));
        if (lane < 4) *lp<float>(L_ML + cw * 16 + lane * 4) = mx;
        FENCE();
        if (lane == 0) __hip_atomic_fetch_add(lp<u32>(L_CTL + 4 * C_MX), 1u, __ATOMIC_RELAXED, __HIP_MEMORY_SCOPE_WORKGROUP);
      }
      float M2;
      {
        while (ldsv(L_CTL + 4 * C_MX) < NCONS) __builtin_amdgcn_s_sleep(1);
        FENCE();
        float M = *lp<float>(L_ML + (c & 3) * 4);
#pragma unroll
        for (int w = 1; w < NCONS; ++w) M = fmaxf(M, *lp<float>(L_ML + w * 16 + (c & 3) * 4));
        M2 = M * 1.4426950408889634f;
        float l = 0.f;
#pragma nounroll
        for (int j = cw + d * NCONS; j < nb; j += 4 * NCONS) {
          const v4u s = *lp<v4u>(sca + j * 256);
          float p[8];
#pragma unroll
          for (int i = 0; i < 4; ++i) { p[2 * i] = ex2(fmaf(lo16(s[i]), 1.4426950408889634f, -M2)); p[2 * i + 1] = ex2(fmaf(bfhi(s[i]), 1.4426950408889634f, -M2)); }
          l += ((p[0] + p[1]) + (p[2] + p[3])) + ((p[4] + p[5]) + (p[6] + p[7]));
          *lp<v4u>(sca + j * 256) = v4u{pk2(p[0], p[1]), pk2(p[2], p[3]), pk2(p[4], p[5]), pk2(p[6], p[7])};
        }
        l += dpp<0x124>(l); l += dpp<0x128>(l);
        l = swap32_add(swap16_add(l));
        if (lane < 4) *lp<float>(L_ML + 128 + cw * 16 + lane * 4) = l;
        FENCE();
        if (lane == 0) __hip_atomic_fetch_add(lp<u32>(L_CTL + 4 * C_SM), 1u, __ATOMIC_RELAXED, __HIP_MEMORY_SCOPE_WORKGROUP);
      }
      {
        u32 va[8];
#pragma unroll
        for (int t = 0; t < 8; ++t) va[t] = trow * 256 + ((((u32)(t ^ (trow & 7)) << 1) | ((c >> 1) & 1)) << 4) + (c & 1) * 8;
        v4f acc[8];
#pragma unroll
        for (int t = 0; t < 8; ++t) acc[t] = v4f{0.f, 0.f, 0.f, 0.f};
        int j = cw;
#pragma nounroll
        for (; j + NCONS < nb; j += NCONS) {
          const int u = uv + j;
          const u32 lb = (u & (NSLOT - 1)) * SLOT;
          wait_full(8 * u + 8);
          const v8bf pf = ldfrag(sca + j * 256);
          v8bf vf[8];
#pragma unroll
          for (int t = 0; t < 8; ++t) {
            v4s lo = __builtin_amdgcn_ds_read_tr16_b64_v4i16(lp<v4s>(lb + va[t])), hi = __builtin_amdgcn_ds_read_tr16_b64_v4i16(lp<v4s>(lb + va[t] + 4096));
            vf[t] = __builtin_bit_cast(v8bf, __builtin_shufflevector(lo, hi, 0, 1, 2, 3, 4, 5, 6, 7));
          }
          FENCE(); stsv(L_CTL + 4 * (C_FREE + (u & (NSLOT - 1))), u + 1);
#pragma unroll
          for (int t = 0; t < 8; ++t) acc[t] = mfma(vf[t], pf, acc[t]);
        }
        const int link = j - (nb - NCONS), u = uv + j;
        const u32 lb = (u & (NSLOT - 1)) * SLOT;
        v4f e0 = d == 0 ? acc[0] : d == 1 ? acc[2] : d == 2 ? acc[4] : acc[6];
        v4f e1 = d == 0 ? acc[1] : d == 1 ? acc[3] : d == 2 ? acc[5] : acc[7];
        const v4u pf = *lp<v4u>(sca + j * 256);
        v4u pm[4];
#pragma unroll
        for (int i = 0; i < 4; ++i) { pm[i] = d == i ? pf : v4u{0u, 0u, 0u, 0u}; PIN(pm[i]); }
        float inv = 1.f;
        if (link == NCONS - 1) {
          while (ldsv(L_CTL + 4 * C_SM) < NCONS) { }
          FENCE();
          float L = *lp<float>(L_ML + 128 + (c & 3) * 4);
#pragma unroll
          for (int w = 1; w < NCONS; ++w) L += *lp<float>(L_ML + 128 + w * 16 + (c & 3) * 4);
          inv = rcp(L);
          M2 += __builtin_amdgcn_logf(L);
        }
        u32 lse = canon(M2);
        PIN(lse);
        u32 oa = (c & 3) * 4096 + d * 64 + g * 16, ra = L_MRG + lane * 16;
        u32 vb[8];
#pragma unroll
        for (int t = 0; t < 8; ++t) { vb[t] = lb + va[t]; PIN(vb[t]); }
        PIN(e0); PIN(e1); PIN(oa); PIN(ra); PIN(inv);
        while (ldsv(L_CTL + 4 * C_FULL) < (u32)(8 * u + 8)) { }
        FENCE();
        v8bf vf[8];
#pragma unroll
        for (int t = 0; t < 8; ++t) {
          v4s lo = __builtin_amdgcn_ds_read_tr16_b64_v4i16(lp<v4s>(vb[t])), hi = __builtin_amdgcn_ds_read_tr16_b64_v4i16(lp<v4s>(vb[t] + 4096));
          vf[t] = __builtin_bit_cast(v8bf, __builtin_shufflevector(lo, hi, 0, 1, 2, 3, 4, 5, 6, 7));
        }
        __builtin_amdgcn_sched_barrier(0);
#pragma unroll
        for (int i = 0; i < 4; ++i) { e0 = mfma(vf[2 * i], __builtin_bit_cast(v8bf, pm[i]), e0); e1 = mfma(vf[2 * i + 1], __builtin_bit_cast(v8bf, pm[i]), e1); }
        __builtin_amdgcn_sched_barrier(0);
        v4f r0, r1;
        for (;;) {
          const u32 cnt = ldsv(L_CTL + 4 * C_MCNT);
          FENCE();
          r0 = *lp<v4f>(ra); r1 = *lp<v4f>(ra + 1024);
          FENCE();
          if (cnt >= (u32)link) break;
        }
        e0 = (e0 + r0) * inv; e1 = (e1 + r1) * inv;
        if (link == NCONS - 1) {
          st4(v4u{min(pk2(e0.x, e0.y), SENT - 1), min(pk2(e0.z, e0.w), SENT - 1), min(pk2(e1.x, e1.y), SENT - 1), min(pk2(e1.z, e1.w), SENT - 1)}, rws, oa, W_PART + grp * 16384 + mem * 256);
          if (lane < 4) st1(lse, rws, lane * 4, W_LSE + bid * 128);
          STAMP(8);
        } else {
          *lp<v4f>(ra) = e0; *lp<v4f>(ra + 1024) = e1;
          FENCE(); stsv(L_CTL + 4 * C_MCNT, link + 1);
        }
        stsv(L_CTL + 4 * (C_FREE + (u & (NSLOT - 1))), u + 1);
      }
    }
  }
  if (!(isX || isY)) { FIN(); return; }
#define TS(i) do { } while (0)
  const u32 trow = 4 * g + (c >> 2);
  auto waddr = [&](int t) -> u32 { return trow * 128 + ((((u32)(t ^ ((trow >> 1) & 3)) << 1) | ((c >> 1) & 1)) << 4) + (c & 1) * 8; };
  auto wslot = [&](int t) -> u32 { return ((uw + t) & (NSLOT - 1)) * SLOT; };
  const int half = lane >> 5, i32 = lane & 31;
  const u32 fa = L_ACT + (c & 1) * ASTR + g * 16;
  {
    constexpr int cap = SPIN_CAP;
    const rsrc_t rws = __builtin_amdgcn_make_buffer_rsrc(ws, 0, W_END, 0x00020000);
    const v4i sws = mksrd(ws, W_END);
    auto tbar = [&]() { bar(); };
    while (ldsv(L_CTL + 4 * C_FULL) < (u32)(8 * (uw + ntail))) __builtin_amdgcn_s_sleep(1);
    FENCE();
    TS(0);
    if (isX) {
      const int kvh = xi >> 3, ng = xi & 7;
      const int b = wave >> 2, h = wave & 3;
      v8bf W[16];
      if (wave >= 4) {
#pragma unroll
        for (int ks = 0; ks < 16; ++ks) W[ks] = trfrag(wslot(ks >> 1) + (ks & 1) * 4096 + waddr(wave & 3));
      }
      u32 wa = L_ACT + b * ASTR + (h * 128 + c * 8 + g * 2) * 2;
      PIN(wa);
      v4u a[4]; u32 ls = 0;
#pragma unroll
      for (int j = 0; j < 4; ++j) a[j] = v4u{0u, 0u, 0u, 0u};
      {
        const u32 vo = lane * 16, s0 = W_PART + (b * 8 + kvh) * 16384 + h * 4096;
        const u32 vl = ((c & 3) * 4 + ((g + (c >> 2)) & 3)) * 128, s2 = W_LSE + (b * 8 + kvh) * 2048 + h * 4;
        u32 pend = 0xF;
#pragma nounroll
        for (int sp = 0; sp < cap; ++sp) {
#define SEL(j, off) "s_bitcmp1_b32 %5, " #j "\n\ts_cbranch_scc0 .Lsk%=_" #j "\n\tbuffer_load_dwordx4 %" #j ", %6, %7, %8 offen offset:" #off " sc1\n.Lsk%=_" #j ":\n\t"
          asm volatile("buffer_load_dword %4, %9, %7, %10 offen sc1\n\t" SEL(0, 0) SEL(1, 1024) SEL(2, 2048) SEL(3, 3072) "s_waitcnt vmcnt(0)"
                       : "+v"(a[0]), "+v"(a[1]), "+v"(a[2]), "+v"(a[3]), "+v"(ls)
                       : "s"(pend), "v"(vo), "s"(sws), "s"(s0), "v"(vl), "s"(s2) : "memory", "scc");
#pragma unroll
          for (int j = 0; j < 4; ++j)
            if (pend & (1u << j)) { if (__all(max(max(a[j].x, a[j].y), max(a[j].z, a[j].w)) != SENT)) pend &= ~(1u << j); }
          if (pend == 0 && __all(ls != SENT)) break;
        }
      }
      TS(1);
      float M = __uint_as_float(ls);
      M = fmaxf(M, dpp<0xB1>(M)); M = fmaxf(M, dpp<0x4E>(M)); M = fmaxf(M, dpp<0x141>(M)); M = fmaxf(M, dpp<0x128>(M));
      const float cf = ex2(__uint_as_float(ls) - M);
      const float inv = rcp(row_sum(cf));
      const float cj[4] = {dpp<0x150>(cf), dpp<0x151>(cf), dpp<0x152>(cf), dpp<0x153>(cf)};
      v2f o[4];
#pragma unroll
      for (int i = 0; i < 4; ++i) {
        o[i] = v2f{lo16(a[0][i]), bfhi(a[0][i])} * cj[0];
#pragma unroll
        for (int j = 1; j < 4; ++j) o[i] += v2f{lo16(a[j][i]), bfhi(a[j][i])} * cj[j];
      }
      *lp<u32>(wa) = pk2(fold16(fold32(o[0].x, o[2].x), fold32(o[1].x, o[3].x)) * inv, fold16(fold32(o[0].y, o[2].y), fold32(o[1].y, o[3].y)) * inv);
      if (wave == 4) STAMP(9);
      TS(2);
      tbar();
      TS(3);
      if (wave >= 4) {
        v8bf A[16];
#pragma unroll
        for (int ks = 0; ks < 16; ++ks) A[ks] = ldfrag(fa + ks * 64);
        __builtin_amdgcn_sched_barrier(0);
        v4f d0 = {0.f, 0.f, 0.f, 0.f}, d1 = {0.f, 0.f, 0.f, 0.f};
#pragma unroll
        for (int ks = 0; ks < 16; ks += 2) { d0 = mfma(W[ks], A[ks], d0); d1 = mfma(W[ks + 1], A[ks + 1], d1); }
        d0 += d1;
        v4u ov = {canon(d0.x), canon(d0.y), canon(d0.z), canon(d0.w)};
        if (c < 2) st4(ov, rws, c * 2048 + g * 16, W_AP + kvh * 4096 + ng * 256 + (wave & 3) * 64);
        if (wave == 4) STAMP(10);
        TS(4);
      }
    } else {
      const int b = wave >> 2, col = (wave & 3) * 128 + i32 * 4 + half * 2;
      const u32 xv = *(const u32*)(xin + b * 512 + col);
      v2f x0 = {lo16(xv), bfhi(xv)};
      v2f ga, sb, cb1;
      {
        const u32 tag = ldsv(L_CTL + 4 * C_TAG);
        v4u mg[3];
#pragma nounroll
        for (int sp = 0; sp < cap; ++sp) {
#pragma unroll
          for (int i = 0; i < 3; ++i) mg[i] = ld4(rws, col * 8, W_MG + i * 4096);
          if (__all(((mg[0].y ^ tag) | (mg[0].w ^ tag) | (mg[1].y ^ tag) | (mg[1].w ^ tag) | (mg[2].y ^ tag) | (mg[2].w ^ tag)) == 0)) break;
          FENCE();
        }
        const int sh = 16 - 16 * b;
        ga = v2f{bfhi(mg[0].x << sh), bfhi(mg[0].z << sh)}; sb = v2f{bfhi(mg[1].x << sh), bfhi(mg[1].z << sh)};
        cb1 = rb2(v2f{bfhi(mg[2].x << sh), bfhi(mg[2].z << sh)} + 1.f);
      }
      v8bf W[16];
#pragma unroll
      for (int j = 0; j < 16; ++j)
        W[j] = trfrag((wave >= 4 ? wslot(j >> 1) + waddr(wave & 3) : wslot(8 + 2 * wave + (j >> 3)) + waddr((j >> 1) & 3)) + (j & 1) * 4096);
      u32 wa = L_ACT + b * ASTR + fpos(col) * 2, ra = L_RED + wave * 8 + half * 4;
      u32 ha = L_H1 + (c & 1) * 128 + fpos((wave & 3) * 16 + g * 4 + ((c >> 1) & 3)) * 2;
      PIN(x0); PIN(ga); PIN(sb); PIN(cb1); PIN(wa); PIN(ra); PIN(ha);
      v4u a[4];
#pragma nounroll
      for (int sp = 0; sp < cap; ++sp) {
#pragma unroll
        for (int j = 0; j < 4; ++j) a[j] = ld4(rws, half * 16384 + i32 * 16, W_AP + j * 4096 + b * 2048 + (wave & 3) * 512);
        u32 mx = 0;
#pragma unroll
        for (int j = 0; j < 4; ++j) mx = max(mx, max(max(a[j].x, a[j].y), max(a[j].z, a[j].w)));
        if (__all(mx != SENT)) break;
        FENCE();
      }
      if (wave == 4) STAMP(11);
      TS(1);
      const v4f at = (__builtin_bit_cast(v4f, a[0]) + __builtin_bit_cast(v4f, a[1])) + (__builtin_bit_cast(v4f, a[2]) + __builtin_bit_cast(v4f, a[3]));
      const v2f x1 = rb2(x0 + rb2(ga * rb2(v2f{fold32(at.x, at.z), fold32(at.y, at.w)})));
      const float st = half_sum(fold32(x1.x + x1.y, fmaf(x1.x, x1.x, x1.y * x1.y)));
      if (i32 == 0) *lp<float>(ra) = st;
      tbar();
      TS(2);
      {
        const v4f r0 = *lp<v4f>(L_RED + b * 32), r1 = *lp<v4f>(L_RED + b * 32 + 16);
        const float mean = ((r0.x + r0.z) + (r1.x + r1.z)) * (1.f / 512.f);
        const float var = fmaxf(fmaf(-mean, mean, ((r0.y + r0.w) + (r1.y + r1.w)) * (1.f / 512.f)), 0.f);
        const v2f mv = rb2(v2f{mean, var});
        const float ri = rcp(rb(__builtin_amdgcn_sqrtf(rb(mv.y + 1e-5f))));
        const v2f hh = rb2(rb2(rb2(x1 - mv.x) * ri) * cb1) + sb;
        *lp<u32>(wa) = pk2(hh.x, hh.y);
      }
      tbar();
      TS(3);
      if (wave >= 4) {
        v8bf A[16];
#pragma unroll
        for (int ks = 0; ks < 16; ++ks) A[ks] = ldfrag(fa + ks * 64);
        __builtin_amdgcn_sched_barrier(0);
        v4f d0 = {0.f, 0.f, 0.f, 0.f}, d1 = {0.f, 0.f, 0.f, 0.f};
#pragma unroll
        for (int ks = 0; ks < 16; ks += 2) { d0 = mfma(W[ks], A[ks], d0); d1 = mfma(W[ks + 1], A[ks + 1], d1); }
        d0 += d1;
        const int rr = (c >> 1) & 3;
        const float v = silu(rb(rr == 0 ? d0.x : rr == 1 ? d0.y : rr == 2 ? d0.z : d0.w));
        *lp<u16>(ha) = (u16)pk2(v, v);
      }
      tbar();
      TS(4);
      if (wave < 4) {
        const v8bf a0 = ldfrag(L_H1 + (c & 1) * 128 + g * 16), a1 = ldfrag(L_H1 + (c & 1) * 128 + 64 + g * 16);
        v4f dd[8];
#pragma unroll
        for (int t = 0; t < 8; ++t) dd[t] = mfma(W[2 * t + 1], a1, mfma(W[2 * t], a0, v4f{0.f, 0.f, 0.f, 0.f}));
        v4f rv = dd[0];
#pragma unroll
        for (int t = 1; t < 8; ++t) if ((c >> 1) == t) rv = dd[t];
        v4u ov = {canon(rv.x), canon(rv.y), canon(rv.z), canon(rv.w)};
        st4(ov, rws, (c & 1) * 2048 + (c >> 1) * 64 + g * 16, W_OP + yi * 4096 + wave * 512);
        if (wave == 1 && yi == 27 && lane == 0) st2(v2u{(u32)E, (u32)(E >> 32)}, rws, 0, W_EPOCH);
        if (wave == 0) STAMP(12);
        TS(5);
      }
    }
  }
  FIN();
}

extern "C" void kernel_launch(void* const* d_in, const int* in_sizes, int n_in, void* d_out, int out_size, void* d_ws, size_t ws_size, hipStream_t stream) {
  hipLaunchKernelGGL(fused_block, dim3(256), dim3(512), 0, stream, (const u16*)d_in[1], (const u16*)d_in[2], (const u16*)d_in[9], (const u16*)d_in[3],
                     (const u16*)d_in[4], (const u16*)d_in[6], (const u16*)d_in[7], (const u16*)d_in[0], (const u16*)d_in[8], (u16*)d_out, (unsigned char*)d_ws);
}
```

```cpp
#define DIAG 20
#include <hip/hip_runtime.h>
#include <hip/hip_bf16.h>

#define STAMPS 0

typedef unsigned int u32;
typedef unsigned long long u64;
typedef unsigned short u16;
typedef u32 v4u __attribute__((ext_vector_type(4)));
typedef u32 v2u __attribute__((ext_vector_type(2)));
typedef int v4i __attribute__((ext_vector_type(4)));
typedef float v4f __attribute__((ext_vector_type(4)));
typedef float v2f __attribute__((ext_vector_type(2)));
typedef short v4s __attribute__((ext_vector_type(4)));
typedef __bf16 v8bf __attribute__((ext_vector_type(8)));
typedef __bf16 v2bf __attribute__((ext_vector_type(2)));
#define LAS __attribute__((address_space(3)))
#define GAS __attribute__((address_space(1)))
#define DEVI __device__ __forceinline__

constexpr int NSLOT = 16, SLOT = 8192, NCONS = 6, NSTASH = 4;
#define WIN 28
#define QAT 12
#define HDELAY 400
#define MAP 2
#define TOUCH 1
#define TAU16 443
#define TOFF 0
#define NF 2
#define CODEPF 1
#define CODE_BYTES 22784
#define PSTART 6
#define TMODE 0
#define PEND 0
#define ZR 8
#define WA 16
#define WB 20
#define LA 25
#define LB 50
#define COFF -45
#define COMMON 1
#define POLLS 16
#define NX_A 64
#define NQ_A 63
#define NY_A 63
#define NS_A 66
constexpr int NX_B = 64, NQ_B = 63, NY_B = 65, NS_B = 65, NMAX = 72;
static_assert(4 * NX_A + 6 * NQ_A + 2 * NY_A + 4 * NS_A == 1024 && 4 * NX_B + 6 * NQ_B + 2 * NY_B + 4 * NS_B == 1024, "blocks");
#define BIAS 8
constexpr u64 start_table(int nx, int nq, int ny, int ns) {
  u64 t = 0; int s = 0;
  for (int r = 0; r < 16; ++r) { const int dl = s - 64 * r + BIAS; if (dl < 0 || dl > 15) return ~0ull; t |= (u64)dl << (4 * r); s += r < 4 ? nx : r < 10 ? nq : r < 12 ? ny : ns; }
  return t;
}
constexpr u64 ST_A = start_table(NX_A, NQ_A, NY_A, NS_A), ST_B = start_table(NX_B, NQ_B, NY_B, NS_B);
static_assert(ST_A != ~0ull && ST_B != ~0ull, "start table");
constexpr int L_CTL = NSLOT * SLOT;
constexpr int L_Q = L_CTL + 256;
constexpr int L_ACT = L_Q + 1024;
constexpr int ASTR = 1024 + 64;
constexpr int L_MRG = L_ACT + 2 * ASTR;
constexpr int L_ML = L_MRG + 2048;
constexpr int L_SC = L_ML + 256;
constexpr int L_JUNK = L_SC + NMAX * 256;
constexpr int L_END = L_JUNK + 256;
constexpr int L_H1 = L_SC;
constexpr int L_RED = L_H1 + 256;
static_assert(L_END <= 160 * 1024, "LDS");
constexpr int C_FULL = 0, C_QRDY = 1, C_MCNT = 2, C_CLR = 3, C_ARDY = 4, C_TAG = 5, C_WRD = 6, C_MX = 7, C_SM = 8, C_T0 = 9, C_FREE = 16;
constexpr u32 W_EPOCH = 0, W_T0 = 4096, W_SP = 8192, W_HG = W_SP + 4 * 1024 * 16, W_QG = W_HG + 512 * 8, W_MG = W_QG + 4096 * 8, W_PART = W_MG + 2048 * 8;
constexpr u32 W_LSE = W_PART + 64 * 4096;
constexpr u32 W_AP = W_LSE + 256 * 128, W_OP = W_AP + 8 * 4096, W_END = W_OP + 28 * 4096;
constexpr u32 CLRB = (W_END - W_PART) / 64;
static_assert(CLRB * 64 == W_END - W_PART && CLRB % 16 == 0, "clear split");
constexpr u32 W_STAMP = 2u << 20;
constexpr u32 SENT = 0xFFFFFFFFu;
constexpr int SPIN_CAP = 1 << 16;

DEVI float bf2f(u32 v) { return __uint_as_float(v << 16); }
DEVI float bfhi(u32 v) { return __uint_as_float(v & 0xffff0000u); }
DEVI u32 pk2(float a, float b) { v2f x = {a, b}; return __builtin_bit_cast(u32, __builtin_convertvector(x, v2bf)); }
DEVI float rb(float a) { return bf2f(pk2(a, a) & 0xffffu); }
DEVI float ex2(float x) { return __builtin_amdgcn_exp2f(x); }
DEVI float rcp(float x) { return __builtin_amdgcn_rcpf(x); }
DEVI float silu(float v) { return v * rcp(1.f + ex2(v * -1.4426950408889634f)); }
DEVI float silu_exact(float v) {
  const float t = fminf(v * -1.4426950216293335f, 126.f);
  const float r = fmaf(v, -1.92596299112662e-08f, fmaf(v, -1.4426950216293335f, -t));
  float e = ex2(t);
  e = fmaf(e, fminf(r, 1.f) * 0.6931471805599453f, e);
  const float d = 1.f + e, rc = rcp(d), q = v * rc;
  return fmaf(fmaf(-q, d, v), rc, q);
}
DEVI float lo16(u32 v) { return __uint_as_float(v << 16); }
DEVI v2f rb2(v2f a) { const u32 p = pk2(a.x, a.y); return v2f{lo16(p), bfhi(p)}; }
DEVI float fold32(float a, float b) { auto s = __builtin_amdgcn_permlane32_swap(__float_as_uint(a), __float_as_uint(b), false, false); return __uint_as_float(s[0]) + __uint_as_float(s[1]); }
DEVI float fold16(float a, float b) { auto s = __builtin_amdgcn_permlane16_swap(__float_as_uint(a), __float_as_uint(b), false, false); return __uint_as_float(s[0]) + __uint_as_float(s[1]); }
#define PIN(x) asm volatile("" : "+v"(x))
DEVI float swap16_add(float x) { auto s = __builtin_amdgcn_permlane16_swap(__float_as_uint(x), __float_as_uint(x), false, false); return __uint_as_float(s[0]) + __uint_as_float(s[1]); }
DEVI float swap32_add(float x) { auto s = __builtin_amdgcn_permlane32_swap(__float_as_uint(x), __float_as_uint(x), false, false); return __uint_as_float(s[0]) + __uint_as_float(s[1]); }
DEVI float swap16_max(float x) { auto s = __builtin_amdgcn_permlane16_swap(__float_as_uint(x), __float_as_uint(x), false, false); return fmaxf(__uint_as_float(s[0]), __uint_as_float(s[1])); }
DEVI float swap32_max(float x) { auto s = __builtin_amdgcn_permlane32_swap(__float_as_uint(x), __float_as_uint(x), false, false); return fmaxf(__uint_as_float(s[0]), __uint_as_float(s[1])); }
template <int CTRL> DEVI float dpp(float x) { return __builtin_bit_cast(float, __builtin_amdgcn_mov_dpp(__builtin_bit_cast(int, x), CTRL, 0xf, 0xf, true)); }
DEVI float row_sum(float x) { x += dpp<0xB1>(x); x += dpp<0x4E>(x); x += dpp<0x141>(x); x += dpp<0x128>(x); return x; }
DEVI float half_sum(float x) { return swap16_add(row_sum(x)); }
DEVI float wave_sum(float x) { return swap32_add(half_sum(x)); }
template <typename T> DEVI LAS T* lp(u32 a) { return (LAS T*)(size_t)a; }
DEVI u32 ldsv(u32 a) { return *(volatile LAS u32*)(size_t)a; }
DEVI void stsv(u32 a, u32 v) { *(volatile LAS u32*)(size_t)a = v; }
#define FENCE() asm volatile("" ::: "memory")
#define WAITV(n) asm volatile("s_waitcnt vmcnt(%0)" ::"i"(n) : "memory")
#define WAITL() asm volatile("s_waitcnt lgkmcnt(0)" ::: "memory")
DEVI void bar() { WAITL(); __builtin_amdgcn_s_barrier(); FENCE(); }
typedef __amdgpu_buffer_rsrc_t rsrc_t;
DEVI v4u ld4(rsrc_t r, u32 vo, u32 so) { return __builtin_bit_cast(v4u, __builtin_amdgcn_raw_buffer_load_b128(r, vo, so, 16)); }
DEVI v2u ld2(rsrc_t r, u32 vo, u32 so) { return __builtin_bit_cast(v2u, __builtin_amdgcn_raw_buffer_load_b64(r, vo, so, 16)); }
DEVI u32 ld1(rsrc_t r, u32 vo, u32 so) { return __builtin_amdgcn_raw_buffer_load_b32(r, vo, so, 16); }
DEVI void st4(v4u v, rsrc_t r, u32 vo, u32 so) { __builtin_amdgcn_raw_buffer_store_b128(v, r, vo, so, 16); }
DEVI void st2(v2u v, rsrc_t r, u32 vo, u32 so) { __builtin_amdgcn_raw_buffer_store_b64(v, r, vo, so, 16); }
DEVI void st1(u32 v, rsrc_t r, u32 vo, u32 so) { __builtin_amdgcn_raw_buffer_store_b32(v, r, vo, so, 16); }
DEVI u32 canon(float f) { u32 b = __float_as_uint(f); return b < 0xFFFFFFFEu ? b : 0xFFFFFFFEu; }
DEVI v8bf trfrag(u32 a) {
  v4s lo = __builtin_amdgcn_ds_read_tr16_b64_v4i16(lp<v4s>(a)), hi = __builtin_amdgcn_ds_read_tr16_b64_v4i16(lp<v4s>(a + 2048));
  return __builtin_bit_cast(v8bf, __builtin_shufflevector(lo, hi, 0, 1, 2, 3, 4, 5, 6, 7));
}
DEVI v4f mfma(v8bf a, v8bf b, v4f c) { return __builtin_amdgcn_mfma_f32_16x16x32_bf16(a, b, c, 0, 0, 0); }
DEVI v8bf ldfrag(u32 a) { return __builtin_bit_cast(v8bf, *lp<v4u>(a)); }
DEVI int fpos(int k) { return (k & ~31) + ((k >> 2) & 3) * 8 + ((k >> 4) & 1) * 4 + (k & 3); }
DEVI v4i mksrd(const void* p, u32 bytes) { u64 a = (u64)p; return v4i{(int)(u32)a, (int)(u32)(a >> 32), (int)bytes, 0x00020000}; }

#define DMA4(m, v0, v1, v2, v3, srd, so)                                                                                   \
  asm volatile("s_mov_b32 m0, %0\n\ts_nop 0\n\tbuffer_load_dwordx4 %1, %5, %6 offen nt lds\n\t"                           \
               "buffer_load_dwordx4 %2, %5, %6 offen offset:1024 nt lds\n\tbuffer_load_dwordx4 %3, %5, %6 offen offset:2048 nt lds\n\t" \
               "buffer_load_dwordx4 %4, %5, %6 offen offset:3072 nt lds" ::"s"(m), "v"(v0), "v"(v1), "v"(v2), "v"(v3), "s"(srd), "s"(so) : "memory")
#define DMA1(m, v0, srd, so) \
  asm volatile("s_mov_b32 m0, %0\n\ts_nop 0\n\tbuffer_load_dwordx4 %1, %2, %3 offen nt lds" ::"s"(m), "v"(v0), "s"(srd), "s"(so) : "memory")

#define STAMP(i) do { if ((i) == DIAG) tdiag = __builtin_amdgcn_s_memrealtime(); } while (0)
#define FIN() do { if (tdiag) while (__builtin_amdgcn_s_memrealtime() < tdiag + 10000) __builtin_amdgcn_s_sleep(8); } while (0)

__global__ void __launch_bounds__(512, 2)
fused_block(const u16* __restrict__ Kc, const u16* __restrict__ Vc, const u16* __restrict__ wmod, const u16* __restrict__ wq,
            const u16* __restrict__ wo, const u16* __restrict__ w1, const u16* __restrict__ w2, const u16* __restrict__ xin,
            const u16* __restrict__ cin, u16* __restrict__ out, unsigned char* ws) {
  __shared__ __attribute__((aligned(16))) unsigned char smem[L_END + 256 * STAMPS];
  asm volatile("" ::"s"((u32)(size_t)(LAS unsigned char*)smem));
  u64 tdiag = 0;
  if (DIAG == 20) { asm volatile("s_memrealtime %0\n\ts_waitcnt lgkmcnt(0)" : "=s"(tdiag)); }
  const int tid = threadIdx.x, lane = tid & 63, wave = __builtin_amdgcn_readfirstlane(tid >> 6);
  if (wave == 1 && blockIdx.x < 32) {
    u64 pc; asm volatile("s_getpc_b64 %0" : "=s"(pc));
    const v4i rc = mksrd((const void*)(pc & ~127ull), CODE_BYTES);
    const u32 vo = lane * 128, m = L_JUNK, s1 = 8192, s2 = 16384;
    asm volatile("s_mov_b32 m0, %0\n\ts_nop 0\n\tbuffer_load_dword %1, %2, 0 offen lds\n\tbuffer_load_dword %1, %2, %3 offen lds\n\tbuffer_load_dword %1, %2, %4 offen lds"
                 ::"s"(m), "v"(vo), "s"(rc), "s"(s1), "s"(s2) : "memory");
  }
  const int bid = (blockIdx.x & ~31) | ((blockIdx.x & 7) << 2) | ((blockIdx.x >> 3) & 3);
  const int grp = bid >> 4, mem = bid & 15;
  const int g = lane >> 4, c = lane & 15;
  const int r = MAP ? mem : (mem + (grp >> 1)) & 15;
  const u64 stt = (grp < 14 ? ST_A : ST_B) >> (4 * r);
  const int kv0 = 64 * r + ((int)stt & 15) - BIAS;
  const bool isS = r >= 12;
  if (wave == 0) {
    const v4i srd = isS ? mksrd(wmod, 512 * 6144) : mksrd(Kc, 1u << 27);
    const u32 k0 = g * 256 + ((c ^ g) << 4), w0 = (lane >> 3) * 6144 + (((lane & 7) ^ ((lane >> 3) & 6)) << 4);
    const u32 a0 = isS ? w0 : k0, a1 = isS ? w0 + 48128 : k0 ^ 64, a2 = isS ? w0 + 2 * 48128 : k0 ^ 128, a3 = isS ? w0 + 3 * 48128 : k0 ^ 192;
    const u32 so = isS ? (r - 12) * 128 * 6144 + grp * 128 : (u32)(grp * 1024 + kv0) << 13, hs = isS ? 32 * 6144 : 4096;
#pragma unroll
    for (int i = 0; i < 4; ++i) { const u32 m = i * 4096, s1 = so + i * hs; DMA4(m, a0, a1, a2, a3, srd, s1); }
  }
  const bool isX = r < 4, isQ = r >= 4 && r < 8, isM = r == 8 || r == 9, isY = (r == 10 || r == 11) && grp < 14, isR = r == 0 && (grp & 1);
  const int xi = grp * 4 + r, qi = grp * 4 + r - 4, mi = grp * 2 + r - 8, yi = grp * 2 + r - 10;
  const int u0 = (int)(0x2222008888880000ull >> (4 * r)) & 15;
  const int nb = 64 + (r == 15 ? BIAS : (int)(stt >> 4) & 15) - ((int)stt & 15);
  const int nf = isS ? 0 : NF;
  const int ntail = isX ? 8 : isY ? 16 : 0;
  const int uv = u0 + nb, uw = uv + nb;
  u64 E = 0;
  if (__builtin_expect(wave == 0, 1)) {
    __builtin_amdgcn_s_setprio(3);
    STAMP(0);
    int iss, pub = 0;
#define TPS()
    auto publish = [&](int v) { pub = max(pub, v); stsv(L_CTL + 4 * C_FULL, pub); };
#define PACE(W_) do { WAITV(W_); publish(iss - (W_)); } while (0)
    u32 due = ((u32)__builtin_amdgcn_s_memrealtime() + TOFF) * 16 + NF * TAU16;
    __builtin_amdgcn_s_barrier(); FENCE();
    const u32 wrow = lane >> 3, wlc = ((lane & 7) ^ (((wrow >> 1) & 3) << 1)) << 4;
    int u = NF;
#pragma nounroll
    for (int seg = u0 == 8 ? 0 : 1; seg < 3 + (ntail >> 3); ++seg) {
      const bool kv = seg == 1 || seg == 2;
      const u16* base = seg == 0 ? (isQ ? wq : wmod) : seg == 1 ? Kc : seg == 2 ? Vc : seg == 4 ? w2 : isX ? wo : w1;
      const u32 rs = seg == 0 ? (isQ ? 8192 : 6144) : (seg == 3 && !isX) ? 3584 : 1024;
      const v4i srd = mksrd(base, kv ? 1u << 27 : seg == 4 ? 1792 * 1024 : (seg == 3 && isX) ? 4096 * 1024 : 512 * rs);
      u32 so = kv ? (u32)(grp * 1024 + kv0 + (seg == 1 ? nf : 0)) << 13
                  : seg == 0 ? (isQ ? qi : 16 + mi) * 128
                             : seg == 4 ? yi << 16 : isX ? ((xi >> 3) << 19) + (xi & 7) * 128 : yi * 128;
      const u32 hstep = kv ? 4096 : 32 * rs, ustep = seg == 4 ? 128 : 2 * hstep;
      const u32 sw = g << (seg - 1), sx = seg == 1 ? 4 : 8;
      const u32 w0 = wrow * rs + wlc, wd = 8 * rs - 1024;
      const u32 a0 = kv ? g * 256 + ((c ^ sw) << 4) : w0, a1 = kv ? g * 256 + ((c ^ sw ^ sx) << 4) : w0 + wd;
      const u32 a2 = kv ? g * 256 + ((c ^ sw ^ ((2 * sx) & 15)) << 4) : w0 + 2 * wd, a3 = kv ? g * 256 + ((c ^ sw ^ ((3 * sx) & 15)) << 4) : w0 + 3 * wd;
      const int ue = seg == 0 ? NF + u0 : seg == 1 ? uv : seg == 2 ? uw : u + 8;
#pragma nounroll
      while (u < ue) {
        const bool paced = u >= 4 && (seg < 3 || u < PEND);
        const int stop = min(ue, u < 4 ? 4 : (COMMON && u < 48) ? 48 : (seg >= 3 && u < PEND) ? PEND : 1 << 20);
        u32 dd = paced ? due : due - (1u << 28);
        const int u_in = u;
        u32 t0, t1, lag; u32 vf, vt;
#define DMAS(so) "buffer_load_dwordx4 %[a0], %[srd], " so " offen nt lds\n\tbuffer_load_dwordx4 %[a1], %[srd], " so " offen offset:1024 nt lds\n\t" \
                 "buffer_load_dwordx4 %[a2], %[srd], " so " offen offset:2048 nt lds\n\tbuffer_load_dwordx4 %[a3], %[srd], " so " offen offset:3072 nt lds\n\t"
#define RDFREE "s_lshl_b32 %[t1], %[u], 2\n\ts_and_b32 %[t1], %[t1], 60\n\ts_or_b32 %[t1], %[t1], %[fb]\n\tv_mov_b32 %[vf], %[t1]\n\tds_read_b32 %[vf], %[vf]\n\t"
#define REST(W) "s_waitcnt vmcnt(" #W ")\n\ts_mov_b32 m0, %[t0]\n\ts_lshl_b32 %[t0], %[u], 3\n\t" DMAS("%[t1]")                                   \
                "s_sub_i32 %[t0], %[t0], " #W " - 4\n\ts_max_i32 %[pub], %[pub], %[t0]\n\tv_mov_b32 %[vt], %[pub]\n\tds_write_b32 %[va], %[vt]\n\t"                                      \
                "s_add_i32 %[u], %[u], 1\n\ts_add_u32 %[so], %[so], %[us]\n\ts_add_u32 %[dd], %[dd], %[tau]\n\t" RDFREE                             \
                "s_memrealtime s[98:99]\n\ts_cmp_lt_i32 %[u], %[stop]\n\ts_waitcnt vmcnt(" #W ")\n\ts_cbranch_scc1 1b\n\ts_branch 9f\n"
#define XREST(W) REST(W)
        asm volatile(RDFREE "s_memrealtime s[98:99]\n"
                     "1:\n\ts_sub_i32 %[t1], %[u], 15\n\ts_waitcnt lgkmcnt(0)\n\tv_cmp_gt_i32 vcc, %[t1], %[vf]\n\ts_lshl_b32 s98, s98, 4\n\t"
                     "s_cbranch_vccnz 8f\n"
                     "2:\n\ts_sub_i32 %[lag], s98, %[dd]\n\ts_cmp_lt_i32 %[lag], 0\n\ts_cbranch_scc1 7f\n\t"
                     "s_lshl_b32 %[t0], %[u], 13\n\ts_and_b32 %[t0], %[t0], 0x1e000\n\ts_mov_b32 m0, %[t0]\n\ts_add_u32 %[t1], %[so], %[hs]\n\t" DMAS("%[so]")
                     "s_or_b32 %[t0], %[t0], 0x1000\n\ts_cmp_lt_i32 %[lag], %[la]\n\ts_cbranch_scc1 4f\n\ts_cmp_lt_i32 %[lag], %[lb]\n\ts_cbranch_scc1 5f\n\t"
                     XREST(WIN) "4:\n\t" XREST(WA) "5:\n\t" XREST(WB)
                     "8:\n\t" RDFREE "s_sub_i32 %[t1], %[u], 15\n\ts_waitcnt lgkmcnt(0)\n\tv_cmp_gt_i32 vcc, %[t1], %[vf]\n\ts_nop 0\n\ts_cbranch_vccz 7f\n\t"
                     "s_waitcnt vmcnt(0)\n\ts_lshl_b32 %[pub], %[u], 3\n\tv_mov_b32 %[vt], %[pub]\n\tds_write_b32 %[va], %[vt]\n"
                     "6:\n\t" RDFREE "s_sub_i32 %[t1], %[u], 15\n\ts_waitcnt lgkmcnt(0)\n\tv_cmp_gt_i32 vcc, %[t1], %[vf]\n\ts_sleep 1\n\ts_cbranch_vccnz 6b\n"
                     "7:\n\ts_memrealtime s[98:99]\n\ts_waitcnt lgkmcnt(0)\n\ts_lshl_b32 s98, s98, 4\n\ts_branch 2b\n"
                     "9:\n\ts_waitcnt lgkmcnt(0)"
                     : [u] "+s"(u), [so] "+s"(so), [dd] "+s"(dd), [pub] "+s"(pub), [t0] "=&s"(t0), [t1] "=&s"(t1), [lag] "=&s"(lag), [vf] "=&v"(vf), [vt] "=&v"(vt)
                     : [a0] "v"(a0), [a1] "v"(a1), [a2] "v"(a2), [a3] "v"(a3), [srd] "s"(srd), [hs] "s"(hstep), [us] "s"(ustep), [stop] "s"(stop),
                       [va] "v"(L_CTL + 4 * C_FULL), [fb] "i"(L_CTL + 4 * C_FREE), [tau] "i"(TAU16), [la] "i"(LA * 16), [lb] "i"(LB * 16)
                     : "memory", "vcc", "scc", "s98", "s99");
        due += (u - u_in) * TAU16;
        if (u == 4) STAMP(14);
        if (u == 48) STAMP(15);
        if (u == 4 && isS) { WAITV(16); publish(16); }
        if (COMMON && u == 48) { const u32 t = __builtin_amdgcn_readfirstlane(ldsv(L_CTL + 4 * C_T0)); if (t) due = t * 16 + u * TAU16; }
      }
    }
    iss = 8 * u;
    PACE(24); PACE(16); PACE(8); PACE(0);
    STAMP(1);
    __builtin_amdgcn_s_setprio(0);
  } else {
    if (wave == 2) { stsv(L_CTL + 4 * lane, 0); WAITL(); }
    __builtin_amdgcn_s_barrier(); FENCE();
    const rsrc_t rws = __builtin_amdgcn_make_buffer_rsrc(ws, 0, W_END, 0x00020000);
    auto wait_full = [&](u32 need) { while (ldsv(L_CTL + 4 * C_FULL) < need) __builtin_amdgcn_s_sleep(1); FENCE(); };
    const u32 trow = 4 * g + (c >> 2);
    auto waddr = [&](int t) -> u32 { return trow * 128 + ((((u32)(t ^ ((trow >> 1) & 3)) << 1) | ((c >> 1) & 1)) << 4) + (c & 1) * 8; };

    if (wave == 1) {
      const u64 tstart = __builtin_amdgcn_s_memrealtime();
      u64 E0 = __hip_atomic_load((GAS u64*)(ws + W_EPOCH), __ATOMIC_RELAXED, __HIP_MEMORY_SCOPE_AGENT);
      u32 tag;
#define CHAIN_START() do { E = E0 + 1; tag = (u32)E; } while (0)
      auto silu4 = [&](u32 x, u32 y) { return v2u{pk2(silu_exact(lo16(x)), silu_exact(bfhi(x))), pk2(silu_exact(lo16(y)), silu_exact(bfhi(y)))}; };
      if (isS) {
        const v2u cv = *(const v2u*)(cin + (lane >> 5) * 512 + (r - 12) * 128 + (lane & 31) * 4);
        CHAIN_START();
        *lp<v2u>(L_ACT + (lane >> 5) * ASTR + fpos((lane & 31) * 4) * 2) = silu4(cv.x, cv.y);
        FENCE();
        v8bf act[4];
#pragma unroll
        for (int ks = 0; ks < 4; ++ks) { act[ks] = ldfrag(L_ACT + (c & 1) * ASTR + g * 16 + ks * 64); PIN(act[ks]); }
        wait_full(16);
        v8bf W[16];
#pragma unroll
        for (int j = 0; j < 16; ++j) W[j] = trfrag(waddr(j & 3) + (j >> 2) * 4096);
        FENCE(); stsv(L_CTL + 4 * C_FREE, 1); stsv(L_CTL + 4 * (C_FREE + 1), 2);
        v4f acc[4];
#pragma unroll
        for (int t = 0; t < 4; ++t) acc[t] = v4f{0.f, 0.f, 0.f, 0.f};
#pragma unroll
        for (int j = 0; j < 16; ++j) acc[j & 3] = mfma(act[j >> 2], W[j], acc[j & 3]);
        const float r0 = g == 0 ? acc[0][0] : g == 1 ? acc[1][0] : g == 2 ? acc[2][0] : acc[3][0];
        const float r1 = g == 0 ? acc[0][1] : g == 1 ? acc[1][1] : g == 2 ? acc[2][1] : acc[3][1];
        while (ldsv(L_CTL + 4 * C_CLR) == 0) { }
        st4(v4u{__float_as_uint(r0), tag, __float_as_uint(r1), tag}, rws, lane * 16, W_SP + ((r - 12) * 1024 + grp * 64) * 16);
        STAMP(3);
      } else if (isR) {
        const v4u in0 = *(const v4u*)(xin + lane * 8), in1 = *(const v4u*)(xin + 512 + lane * 8);
        u32 xs0 = *(const u32*)(xin + (grp >> 1) * 64 + (lane & ~1)), xs1 = *(const u32*)(xin + 512 + (grp >> 1) * 64 + (lane & ~1));
        CHAIN_START();
        PIN(xs0); PIN(xs1);
        xs0 >>= 16 * (lane & 1); xs1 >>= 16 * (lane & 1);
        float mr[2], ri[2];
#pragma unroll
        for (int b = 0; b < 2; ++b) {
          const v4u xv = b ? in1 : in0;
          const float x[8] = {lo16(xv.x), bfhi(xv.x), lo16(xv.y), bfhi(xv.y), lo16(xv.z), bfhi(xv.z), lo16(xv.w), bfhi(xv.w)};
          float s = 0.f;
#pragma unroll
          for (int i = 0; i < 8; ++i) s += x[i];
          const float mean = wave_sum(s) * (1.f / 512.f);
          float q2 = 0.f;
#pragma unroll
          for (int i = 0; i < 8; ++i) q2 += (x[i] - mean) * (x[i] - mean);
          const float var = wave_sum(q2) * (1.f / 512.f);
          mr[b] = rb(mean); ri[b] = rcp(rb(__builtin_amdgcn_sqrtf(rb(rb(var) + 1e-5f))));
        }
        v2f y = rb2(rb2(v2f{lo16(xs0) - mr[0], lo16(xs1) - mr[1]}) * v2f{ri[0], ri[1]});
        PIN(y);
        v4u a[8];
#pragma nounroll
        for (int sp = 0; sp < SPIN_CAP; ++sp) {
          u32 bad = 0;
#pragma unroll
          for (int i = 0; i < 8; ++i) { a[i] = ld4(rws, lane * 16, W_SP + ((i & 3) * 1024 + (i >> 2) * 512 + (grp >> 1) * 64) * 16); bad |= (a[i].y ^ tag) | (a[i].w ^ tag); }
          if (__all(bad == 0)) break;
          FENCE();
        }
        v2f sc[2];
#pragma unroll
        for (int pt = 0; pt < 2; ++pt) {
          sc[pt] = v2f{0.f, 0.f};
#pragma unroll
          for (int k4 = 0; k4 < 4; ++k4) sc[pt] += v2f{__uint_as_float(a[pt * 4 + k4].x), __uint_as_float(a[pt * 4 + k4].z)};
        }
        const v2f h = rb2(y * rb2(rb2(sc[1]) + 1.f)) + rb2(sc[0]);
        st2(v2u{pk2(h.x, h.y), tag}, rws, lane * 8, W_HG + (grp >> 1) * 512);
        STAMP(23);
      } else if (isM || isQ) {
        if (isM) {
          const v4u in0 = *(const v4u*)(cin + lane * 8), in1 = *(const v4u*)(cin + 512 + lane * 8);
          CHAIN_START();
          *lp<v2u>(L_ACT + fpos(lane * 8) * 2) = silu4(in0.x, in0.y); *lp<v2u>(L_ACT + fpos(lane * 8 + 4) * 2) = silu4(in0.z, in0.w);
          *lp<v2u>(L_ACT + ASTR + fpos(lane * 8) * 2) = silu4(in1.x, in1.y); *lp<v2u>(L_ACT + ASTR + fpos(lane * 8 + 4) * 2) = silu4(in1.z, in1.w);
        } else {
          CHAIN_START();
          while (__builtin_amdgcn_s_memrealtime() < tstart + HDELAY) __builtin_amdgcn_s_sleep(4);
          v4u a[4];
#pragma nounroll
          for (int sp = 0; sp < SPIN_CAP; ++sp) {
            u32 bad = 0;
#pragma unroll
            for (int i = 0; i < 4; ++i) { a[i] = ld4(rws, lane * 16 + i * 1024, W_HG); bad |= (a[i].y ^ tag) | (a[i].w ^ tag); }
            if (__all(bad == 0)) break;
            FENCE();
          }
#pragma unroll
          for (int i = 0; i < 4; ++i) {
            const u32 wa = L_ACT + fpos(i * 128 + lane * 2) * 2;
            *lp<u32>(wa) = (a[i].x & 0xffffu) | (a[i].z << 16); *lp<u32>(wa + ASTR) = (a[i].x >> 16) | (a[i].z & 0xffff0000u);
          }
          STAMP(4);
        }
        FENCE(); stsv(L_CTL + 4 * C_TAG, tag); stsv(L_CTL + 4 * C_ARDY, 1);
      } else CHAIN_START();
      if (COMMON && blockIdx.x < 8 && lane == 0) st2(v2u{(u32)tstart, tag}, rws, blockIdx.x * 8, W_T0);
      {
        wait_full(8 * u0 + 16 * QAT);
        v4u a[2];
#pragma nounroll
        for (int sp = 0; sp < SPIN_CAP; ++sp) {
          a[0] = ld4(rws, lane * 16, W_QG + grp * 2048); a[1] = ld4(rws, lane * 16 + 1024, W_QG + grp * 2048);
          if (__all(((a[0].y ^ tag) | (a[0].w ^ tag) | (a[1].y ^ tag) | (a[1].w ^ tag)) == 0)) break;
          __builtin_amdgcn_s_sleep(POLLS); FENCE();
        }
        *lp<v2u>(L_Q + lane * 8) = v2u{a[0].x, a[0].z}; *lp<v2u>(L_Q + 512 + lane * 8) = v2u{a[1].x, a[1].z};
        FENCE(); stsv(L_CTL + 4 * C_TAG, tag); stsv(L_CTL + 4 * C_QRDY, 1);
        STAMP(6);
        if (COMMON) {
          const v2u t = ld2(rws, (lane & 7) * 8, W_T0);
          if (__all(t.y == tag)) {
            int dt = (int)(t.x - (u32)tstart);
            dt += __builtin_amdgcn_mov_dpp(dt, 0xB1, 0xf, 0xf, true); dt += __builtin_amdgcn_mov_dpp(dt, 0x4E, 0xf, 0xf, true); dt += __builtin_amdgcn_mov_dpp(dt, 0x141, 0xf, 0xf, true);
            stsv(L_CTL + 4 * C_T0, ((u32)tstart + (dt >> 3) + COFF) | 1);
          }
        }
      }
      {
        const u16* p = Vc + ((size_t)grp * 32768 + (size_t)(kv0 + (lane & 1) * (nb - 1)) * 32) * 128;
        if (lane >= 2) p = isX ? wo + (xi >> 3) * 512 * 512 : lane == 2 ? out : lane == 3 ? xin : lane == 4 ? w2 + max(yi, 0) % 28 * 64 * 512 : w1 + (lane & 7) * 64 * 1792;
        if (lane < 2 || ((isX || r == ZR) && lane == 2) || (isY && lane < 16)) { u32 t = __builtin_nontemporal_load((const u32*)p); PIN(t); }
      }
      STAMP(2);
      if (r == ZR) {
        const int zb = grp >> 3, zc = (grp & 7) * 64 + c * 4 + g;
        wait_full(8 * uw);
        const u32 xv = xin[zb * 512 + zc];
        v2u m0, m1;
#pragma nounroll
        for (int sp = 0; sp < SPIN_CAP; ++sp) {
          m0 = ld2(rws, zc * 8, W_MG); m1 = ld2(rws, zc * 8, W_MG + 3 * 4096);
          if (__all(((m0.y ^ tag) | (m1.y ^ tag)) == 0)) break;
          FENCE();
        }
        v4u z[7];
#pragma nounroll
        for (int sp = 0; sp < SPIN_CAP; ++sp) {
          z[0] = ld4(rws, g * 4096 + c * 16, W_AP + grp * 256); z[1] = ld4(rws, g * 4096 + c * 16, W_AP + 16384 + grp * 256);
          if (__all(max(max(max(z[0].x, z[0].y), max(z[0].z, z[0].w)), max(max(z[1].x, z[1].y), max(z[1].z, z[1].w))) != SENT)) break;
          __builtin_amdgcn_s_sleep(2); FENCE();
        }
        v4f s0 = __builtin_bit_cast(v4f, z[0]), s1 = __builtin_bit_cast(v4f, z[1]);
        const float at = fold32(fold16(s0.x, s0.y), fold16(s0.z, s0.w)) + fold32(fold16(s1.x, s1.y), fold16(s1.z, s1.w));
        float x1 = rb(lo16(xv) + rb(bfhi(m0.x << (16 - 16 * zb)) * rb(at))), gb = bfhi(m1.x << (16 - 16 * zb));
        GAS u16* op = (GAS u16*)out + zb * 512 + zc;
        PIN(x1); PIN(gb); PIN(op);
#pragma nounroll
        for (int sp = 0; sp < SPIN_CAP; ++sp) {
#pragma unroll
          for (int j = 0; j < 7; ++j) z[j] = ld4(rws, g * 4096 + c * 16, W_OP + j * 16384 + grp * 256);
          u32 mx = 0;
#pragma unroll
          for (int j = 0; j < 7; ++j) mx = max(mx, max(max(z[j].x, z[j].y), max(z[j].z, z[j].w)));
          if (__all(mx != SENT)) break;
          FENCE();
        }
        const v4f s = ((__builtin_bit_cast(v4f, z[0]) + __builtin_bit_cast(v4f, z[1])) + (__builtin_bit_cast(v4f, z[2]) + __builtin_bit_cast(v4f, z[3]))) +
                      ((__builtin_bit_cast(v4f, z[4]) + __builtin_bit_cast(v4f, z[5])) + __builtin_bit_cast(v4f, z[6]));
        const float rr = x1 + rb(gb * rb(fold16(fold32(s.x, s.z), fold32(s.y, s.w))));
        *op = (u16)pk2(rr, rr);
        STAMP(13);
      }
    } else {
      if (isS && wave == 7) {
        const v4u s4 = {SENT, SENT, SENT, SENT};
        const u32 so = W_PART + (grp * 4 + r - 12) * CLRB;
#pragma unroll
        for (int i = 0; i < CLRB / 1024; ++i) st4(s4, rws, lane * 16 + (i & 3) * 1024, so + (i >> 2) * 4096);
        if (lane < (CLRB % 1024) / 16) st4(s4, rws, lane * 16, so + CLRB / 1024 * 1024);
        WAITV(0); stsv(L_CTL + 4 * C_CLR, 1);
      }

      if (wave == 6) { *lp<v4f>(L_MRG + lane * 16) = v4f{0.f, 0.f, 0.f, 0.f}; *lp<v4f>(L_MRG + 1024 + lane * 16) = v4f{0.f, 0.f, 0.f, 0.f}; }
      const int d = c >> 2, cw = wave - 2;
      const u32 sca = L_SC + (g * 4 + (c & 3)) * 16;
      {
        u32 ka[4];
#pragma unroll
        for (int s = 0; s < 4; ++s) ka[s] = c * 256 + (((4 * s + g) ^ c) << 4);
        v8bf qf[4];
        float mx = -INFINITY;
        auto rdk = [&](v8bf (&kf)[8], int j) {
          const int u = j < nf ? j : u0 + j;
          const u32 lb = (u & (NSLOT - 1)) * SLOT;
          wait_full(8 * u + 8);
#pragma unroll
          for (int s = 0; s < 4; ++s) { kf[s] = ldfrag(lb + ka[s]); kf[4 + s] = ldfrag(lb + ka[s] + 4096); }
          FENCE(); stsv(L_CTL + 4 * (C_FREE + (u & (NSLOT - 1))), u + 1);
        };
        auto qk = [&](v8bf (&kf)[8], int j) {
          v4f S0 = {0.f, 0.f, 0.f, 0.f}, S1 = {0.f, 0.f, 0.f, 0.f};
#pragma unroll
          for (int s = 0; s < 4; ++s) { S0 = mfma(kf[s], qf[s], S0); S1 = mfma(kf[4 + s], qf[s], S1); }
          const v2f a = rb2(v2f{S0.x, S0.y}) * 0.08838834764831845f, b = rb2(v2f{S0.z, S0.w}) * 0.08838834764831845f;
          const v2f e = rb2(v2f{S1.x, S1.y}) * 0.08838834764831845f, f = rb2(v2f{S1.z, S1.w}) * 0.08838834764831845f;
          mx = fmaxf(fmaxf(fmaxf(mx, fmaxf(a.x, a.y)), fmaxf(b.x, b.y)), fmaxf(fmaxf(e.x, e.y), fmaxf(f.x, f.y)));
          if (c < 4) *lp<v4u>(sca + j * 256) = v4u{pk2(a.x, a.y), pk2(b.x, b.y), pk2(e.x, e.y), pk2(f.x, f.y)};
        };
        v8bf sk[NSTASH][8];
        rdk(sk[0], cw);
      if (u0 == 8 && wave < 6) {
        wait_full(8 * NF + 64);
        v8bf W[16];
#pragma unroll
        for (int ks = 0; ks < 16; ++ks) W[ks] = trfrag(NF * SLOT + ks * 4096 + waddr(wave - 2));
        WAITL();
        u32 old = 0;
        if (lane == 0) old = __hip_atomic_fetch_add(lp<u32>(L_CTL + 4 * C_WRD), 1u, __ATOMIC_RELAXED, __HIP_MEMORY_SCOPE_WORKGROUP);
        if (__builtin_amdgcn_readfirstlane(old) == 3 && lane < 8) stsv(L_CTL + 4 * (C_FREE + NF + lane), NF + lane + 1);
        while (ldsv(L_CTL + 4 * C_ARDY) == 0) __builtin_amdgcn_s_sleep(1);
        FENCE();
        const u32 tag = ldsv(L_CTL + 4 * C_TAG);
        v8bf A[16];
#pragma unroll
        for (int ks = 0; ks < 16; ++ks) A[ks] = ldfrag(L_ACT + (c & 1) * ASTR + g * 16 + ks * 64);
        __builtin_amdgcn_sched_barrier(0);
        v4f d0 = {0.f, 0.f, 0.f, 0.f}, d1 = {0.f, 0.f, 0.f, 0.f};
#pragma unroll
        for (int ks = 0; ks < 16; ks += 2) { d0 = mfma(A[ks], W[ks], d0); d1 = mfma(A[ks + 1], W[ks + 1], d1); }
        const float r0 = d0[0] + d1[0], r1 = d0[1] + d1[1];
        const float n0 = dpp<0xB1>(r0), n1 = dpp<0xB1>(r1);
        const bool odd = lane & 1;
        if (lane < 16) {
          if (isM) st2(v2u{pk2(r0, r1), tag}, rws, lane * 8, W_MG + mi * 512 + (wave - 2) * 128);
          else st2(v2u{pk2(odd ? n1 : r0, odd ? r1 : n0), tag}, rws, (lane & 1) * 16384 + (lane >> 1) * 8, W_QG + qi * 256 + (wave - 2) * 64);
        }
        if (isQ && wave == 2) STAMP(5);
      }
#pragma unroll
        for (int i = 1; i < NSTASH; ++i) rdk(sk[i], cw + i * NCONS);
        while (ldsv(L_CTL + 4 * C_QRDY) == 0) __builtin_amdgcn_s_sleep(2);
        FENCE();
#pragma unroll
        for (int s = 0; s < 4; ++s) qf[s] = ldfrag(L_Q + (c & 3) * 256 + (4 * s + g) * 16);
#pragma unroll
        for (int i = 0; i < NSTASH; ++i) qk(sk[i], cw + i * NCONS);
#pragma nounroll
        for (int j = cw + NSTASH * NCONS; j < nb; j += NCONS) { v8bf kf[8]; rdk(kf, j); qk(kf, j); }
        mx = swap32_max(swap16_max(rb(mx)));
        if (lane < 4) *lp<float>(L_ML + cw * 16 + lane * 4) = mx;
        FENCE();
        if (lane == 0) __hip_atomic_fetch_add(lp<u32>(L_CTL + 4 * C_MX), 1u, __ATOMIC_RELAXED, __HIP_MEMORY_SCOPE_WORKGROUP);
      }
      float M2;
      {
        while (ldsv(L_CTL + 4 * C_MX) < NCONS) __builtin_amdgcn_s_sleep(1);
        FENCE();
        float M = *lp<float>(L_ML + (c & 3) * 4);
#pragma unroll
        for (int w = 1; w < NCONS; ++w) M = fmaxf(M, *lp<float>(L_ML + w * 16 + (c & 3) * 4));
        M2 = M * 1.4426950408889634f;
        float l = 0.f;
#pragma nounroll
        for (int j = cw + d * NCONS; j < nb; j += 4 * NCONS) {
          const v4u s = *lp<v4u>(sca + j * 256);
          float p[8];
#pragma unroll
          for (int i = 0; i < 4; ++i) { p[2 * i] = ex2(fmaf(lo16(s[i]), 1.4426950408889634f, -M2)); p[2 * i + 1] = ex2(fmaf(bfhi(s[i]), 1.4426950408889634f, -M2)); }
          l += ((p[0] + p[1]) + (p[2] + p[3])) + ((p[4] + p[5]) + (p[6] + p[7]));
          *lp<v4u>(sca + j * 256) = v4u{pk2(p[0], p[1]), pk2(p[2], p[3]), pk2(p[4], p[5]), pk2(p[6], p[7])};
        }
        l += dpp<0x124>(l); l += dpp<0x128>(l);
        l = swap32_add(swap16_add(l));
        if (lane < 4) *lp<float>(L_ML + 128 + cw * 16 + lane * 4) = l;
        FENCE();
        if (lane == 0) __hip_atomic_fetch_add(lp<u32>(L_CTL + 4 * C_SM), 1u, __ATOMIC_RELAXED, __HIP_MEMORY_SCOPE_WORKGROUP);
      }
      {
        u32 va[8];
#pragma unroll
        for (int t = 0; t < 8; ++t) va[t] = trow * 256 + ((((u32)(t ^ (trow & 7)) << 1) | ((c >> 1) & 1)) << 4) + (c & 1) * 8;
        v4f acc[8];
#pragma unroll
        for (int t = 0; t < 8; ++t) acc[t] = v4f{0.f, 0.f, 0.f, 0.f};
        int j = cw;
#pragma nounroll
        for (; j + NCONS < nb; j += NCONS) {
          const int u = uv + j;
          const u32 lb = (u & (NSLOT - 1)) * SLOT;
          wait_full(8 * u + 8);
          const v8bf pf = ldfrag(sca + j * 256);
          v8bf vf[8];
#pragma unroll
          for (int t = 0; t < 8; ++t) {
            v4s lo = __builtin_amdgcn_ds_read_tr16_b64_v4i16(lp<v4s>(lb + va[t])), hi = __builtin_amdgcn_ds_read_tr16_b64_v4i16(lp<v4s>(lb + va[t] + 4096));
            vf[t] = __builtin_bit_cast(v8bf, __builtin_shufflevector(lo, hi, 0, 1, 2, 3, 4, 5, 6, 7));
          }
          FENCE(); stsv(L_CTL + 4 * (C_FREE + (u & (NSLOT - 1))), u + 1);
#pragma unroll
          for (int t = 0; t < 8; ++t) acc[t] = mfma(vf[t], pf, acc[t]);
        }
        const int link = j - (nb - NCONS), u = uv + j;
        const u32 lb = (u & (NSLOT - 1)) * SLOT;
        v4f e0 = d == 0 ? acc[0] : d == 1 ? acc[2] : d == 2 ? acc[4] : acc[6];
        v4f e1 = d == 0 ? acc[1] : d == 1 ? acc[3] : d == 2 ? acc[5] : acc[7];
        const v4u pf = *lp<v4u>(sca + j * 256);
        v4u pm[4];
#pragma unroll
        for (int i = 0; i < 4; ++i) { pm[i] = d == i ? pf : v4u{0u, 0u, 0u, 0u}; PIN(pm[i]); }
        float inv = 1.f;
        if (link == NCONS - 1) {
          while (ldsv(L_CTL + 4 * C_SM) < NCONS) { }
          FENCE();
          float L = *lp<float>(L_ML + 128 + (c & 3) * 4);
#pragma unroll
          for (int w = 1; w < NCONS; ++w) L += *lp<float>(L_ML + 128 + w * 16 + (c & 3) * 4);
          inv = rcp(L);
          M2 += __builtin_amdgcn_logf(L);
        }
        u32 lse = canon(M2);
        PIN(lse);
        u32 oa = (c & 3) * 4096 + d * 64 + g * 16, ra = L_MRG + lane * 16;
        u32 vb[8];
#pragma unroll
        for (int t = 0; t < 8; ++t) { vb[t] = lb + va[t]; PIN(vb[t]); }
        PIN(e0); PIN(e1); PIN(oa); PIN(ra); PIN(inv);
        while (ldsv(L_CTL + 4 * C_FULL) < (u32)(8 * u + 8)) { }
        FENCE();
        v8bf vf[8];
#pragma unroll
        for (int t = 0; t < 8; ++t) {
          v4s lo = __builtin_amdgcn_ds_read_tr16_b64_v4i16(lp<v4s>(vb[t])), hi = __builtin_amdgcn_ds_read_tr16_b64_v4i16(lp<v4s>(vb[t] + 4096));
          vf[t] = __builtin_bit_cast(v8bf, __builtin_shufflevector(lo, hi, 0, 1, 2, 3, 4, 5, 6, 7));
        }
        __builtin_amdgcn_sched_barrier(0);
#pragma unroll
        for (int i = 0; i < 4; ++i) { e0 = mfma(vf[2 * i], __builtin_bit_cast(v8bf, pm[i]), e0); e1 = mfma(vf[2 * i + 1], __builtin_bit_cast(v8bf, pm[i]), e1); }
        __builtin_amdgcn_sched_barrier(0);
        v4f r0, r1;
        for (;;) {
          const u32 cnt = ldsv(L_CTL + 4 * C_MCNT);
          FENCE();
          r0 = *lp<v4f>(ra); r1 = *lp<v4f>(ra + 1024);
          FENCE();
          if (cnt >= (u32)link) break;
        }
        e0 = (e0 + r0) * inv; e1 = (e1 + r1) * inv;
        if (link == NCONS - 1) {
          st4(v4u{min(pk2(e0.x, e0.y), SENT - 1), min(pk2(e0.z, e0.w), SENT - 1), min(pk2(e1.x, e1.y), SENT - 1), min(pk2(e1.z, e1.w), SENT - 1)}, rws, oa, W_PART + grp * 16384 + mem * 256);
          if (lane < 4) st1(lse, rws, lane * 4, W_LSE + bid * 128);
          STAMP(8);
        } else {
          *lp<v4f>(ra) = e0; *lp<v4f>(ra + 1024) = e1;
          FENCE(); stsv(L_CTL + 4 * C_MCNT, link + 1);
        }
        stsv(L_CTL + 4 * (C_FREE + (u & (NSLOT - 1))), u + 1);
      }
    }
  }
  if (!(isX || isY)) { FIN(); return; }
#define TS(i) do { } while (0)
  const u32 trow = 4 * g + (c >> 2);
  auto waddr = [&](int t) -> u32 { return trow * 128 + ((((u32)(t ^ ((trow >> 1) & 3)) << 1) | ((c >> 1) & 1)) << 4) + (c & 1) * 8; };
  auto wslot = [&](int t) -> u32 { return ((uw + t) & (NSLOT - 1)) * SLOT; };
  const int half = lane >> 5, i32 = lane & 31;
  const u32 fa = L_ACT + (c & 1) * ASTR + g * 16;
  {
    constexpr int cap = SPIN_CAP;
    const rsrc_t rws = __builtin_amdgcn_make_buffer_rsrc(ws, 0, W_END, 0x00020000);
    const v4i sws = mksrd(ws, W_END);
    auto tbar = [&]() { bar(); };
    while (ldsv(L_CTL + 4 * C_FULL) < (u32)(8 * (uw + ntail))) __builtin_amdgcn_s_sleep(1);
    FENCE();
    TS(0);
    if (isX) {
      const int kvh = xi >> 3, ng = xi & 7;
      const int b = wave >> 2, h = wave & 3;
      v8bf W[16];
      if (wave >= 4) {
#pragma unroll
        for (int ks = 0; ks < 16; ++ks) W[ks] = trfrag(wslot(ks >> 1) + (ks & 1) * 4096 + waddr(wave & 3));
      }
      u32 wa = L_ACT + b * ASTR + (h * 128 + c * 8 + g * 2) * 2;
      PIN(wa);
      v4u a[4]; u32 ls = 0;
#pragma unroll
      for (int j = 0; j < 4; ++j) a[j] = v4u{0u, 0u, 0u, 0u};
      {
        const u32 vo = lane * 16, s0 = W_PART + (b * 8 + kvh) * 16384 + h * 4096;
        const u32 vl = ((c & 3) * 4 + ((g + (c >> 2)) & 3)) * 128, s2 = W_LSE + (b * 8 + kvh) * 2048 + h * 4;
        u32 pend = 0xF;
#pragma nounroll
        for (int sp = 0; sp < cap; ++sp) {
#define SEL(j, off) "s_bitcmp1_b32 %5, " #j "\n\ts_cbranch_scc0 .Lsk%=_" #j "\n\tbuffer_load_dwordx4 %" #j ", %6, %7, %8 offen offset:" #off " sc1\n.Lsk%=_" #j ":\n\t"
          asm volatile("buffer_load_dword %4, %9, %7, %10 offen sc1\n\t" SEL(0, 0) SEL(1, 1024) SEL(2, 2048) SEL(3, 3072) "s_waitcnt vmcnt(0)"
                       : "+v"(a[0]), "+v"(a[1]), "+v"(a[2]), "+v"(a[3]), "+v"(ls)
                       : "s"(pend), "v"(vo), "s"(sws), "s"(s0), "v"(vl), "s"(s2) : "memory", "scc");
#pragma unroll
          for (int j = 0; j < 4; ++j)
            if (pend & (1u << j)) { if (__all(max(max(a[j].x, a[j].y), max(a[j].z, a[j].w)) != SENT)) pend &= ~(1u << j); }
          if (pend == 0 && __all(ls != SENT)) break;
        }
      }
      TS(1);
      float M = __uint_as_float(ls);
      M = fmaxf(M, dpp<0xB1>(M)); M = fmaxf(M, dpp<0x4E>(M)); M = fmaxf(M, dpp<0x141>(M)); M = fmaxf(M, dpp<0x128>(M));
      const float cf = ex2(__uint_as_float(ls) - M);
      const float inv = rcp(row_sum(cf));
      const float cj[4] = {dpp<0x150>(cf), dpp<0x151>(cf), dpp<0x152>(cf), dpp<0x153>(cf)};
      v2f o[4];
#pragma unroll
      for (int i = 0; i < 4; ++i) {
        o[i] = v2f{lo16(a[0][i]), bfhi(a[0][i])} * cj[0];
#pragma unroll
        for (int j = 1; j < 4; ++j) o[i] += v2f{lo16(a[j][i]), bfhi(a[j][i])} * cj[j];
      }
      *lp<u32>(wa) = pk2(fold16(fold32(o[0].x, o[2].x), fold32(o[1].x, o[3].x)) * inv, fold16(fold32(o[0].y, o[2].y), fold32(o[1].y, o[3].y)) * inv);
      if (wave == 4) STAMP(9);
      TS(2);
      tbar();
      TS(3);
      if (wave >= 4) {
        v8bf A[16];
#pragma unroll
        for (int ks = 0; ks < 16; ++ks) A[ks] = ldfrag(fa + ks * 64);
        __builtin_amdgcn_sched_barrier(0);
        v4f d0 = {0.f, 0.f, 0.f, 0.f}, d1 = {0.f, 0.f, 0.f, 0.f};
#pragma unroll
        for (int ks = 0; ks < 16; ks += 2) { d0 = mfma(W[ks], A[ks], d0); d1 = mfma(W[ks + 1], A[ks + 1], d1); }
        d0 += d1;
        v4u ov = {canon(d0.x), canon(d0.y), canon(d0.z), canon(d0.w)};
        if (c < 2) st4(ov, rws, c * 2048 + g * 16, W_AP + kvh * 4096 + ng * 256 + (wave & 3) * 64);
        if (wave == 4) STAMP(10);
        TS(4);
      }
    } else {
      const int b = wave >> 2, col = (wave & 3) * 128 + i32 * 4 + half * 2;
      const u32 xv = *(const u32*)(xin + b * 512 + col);
      v2f x0 = {lo16(xv), bfhi(xv)};
      v2f ga, sb, cb1;
      {
        const u32 tag = ldsv(L_CTL + 4 * C_TAG);
        v4u mg[3];
#pragma nounroll
        for (int sp = 0; sp < cap; ++sp) {
#pragma unroll
          for (int i = 0; i < 3; ++i) mg[i] = ld4(rws, col * 8, W_MG + i * 4096);
          if (__all(((mg[0].y ^ tag) | (mg[0].w ^ tag) | (mg[1].y ^ tag) | (mg[1].w ^ tag) | (mg[2].y ^ tag) | (mg[2].w ^ tag)) == 0)) break;
          FENCE();
        }
        const int sh = 16 - 16 * b;
        ga = v2f{bfhi(mg[0].x << sh), bfhi(mg[0].z << sh)}; sb = v2f{bfhi(mg[1].x << sh), bfhi(mg[1].z << sh)};
        cb1 = rb2(v2f{bfhi(mg[2].x << sh), bfhi(mg[2].z << sh)} + 1.f);
      }
      v8bf W[16];
#pragma unroll
      for (int j = 0; j < 16; ++j)
        W[j] = trfrag((wave >= 4 ? wslot(j >> 1) + waddr(wave & 3) : wslot(8 + 2 * wave + (j >> 3)) + waddr((j >> 1) & 3)) + (j & 1) * 4096);
      u32 wa = L_ACT + b * ASTR + fpos(col) * 2, ra = L_RED + wave * 8 + half * 4;
      u32 ha = L_H1 + (c & 1) * 128 + fpos((wave & 3) * 16 + g * 4 + ((c >> 1) & 3)) * 2;
      PIN(x0); PIN(ga); PIN(sb); PIN(cb1); PIN(wa); PIN(ra); PIN(ha);
      v4u a[4];
#pragma nounroll
      for (int sp = 0; sp < cap; ++sp) {
#pragma unroll
        for (int j = 0; j < 4; ++j) a[j] = ld4(rws, half * 16384 + i32 * 16, W_AP + j * 4096 + b * 2048 + (wave & 3) * 512);
        u32 mx = 0;
#pragma unroll
        for (int j = 0; j < 4; ++j) mx = max(mx, max(max(a[j].x, a[j].y), max(a[j].z, a[j].w)));
        if (__all(mx != SENT)) break;
        FENCE();
      }
      if (wave == 4) STAMP(11);
      TS(1);
      const v4f at = (__builtin_bit_cast(v4f, a[0]) + __builtin_bit_cast(v4f, a[1])) + (__builtin_bit_cast(v4f, a[2]) + __builtin_bit_cast(v4f, a[3]));
      const v2f x1 = rb2(x0 + rb2(ga * rb2(v2f{fold32(at.x, at.z), fold32(at.y, at.w)})));
      const float st = half_sum(fold32(x1.x + x1.y, fmaf(x1.x, x1.x, x1.y * x1.y)));
      if (i32 == 0) *lp<float>(ra) = st;
      tbar();
      TS(2);
      {
        const v4f r0 = *lp<v4f>(L_RED + b * 32), r1 = *lp<v4f>(L_RED + b * 32 + 16);
        const float mean = ((r0.x + r0.z) + (r1.x + r1.z)) * (1.f / 512.f);
        const float var = fmaxf(fmaf(-mean, mean, ((r0.y + r0.w) + (r1.y + r1.w)) * (1.f / 512.f)), 0.f);
        const v2f mv = rb2(v2f{mean, var});
        const float ri = rcp(rb(__builtin_amdgcn_sqrtf(rb(mv.y + 1e-5f))));
        const v2f hh = rb2(rb2(rb2(x1 - mv.x) * ri) * cb1) + sb;
        *lp<u32>(wa) = pk2(hh.x, hh.y);
      }
      tbar();
      TS(3);
      if (wave >= 4) {
        v8bf A[16];
#pragma unroll
        for (int ks = 0; ks < 16; ++ks) A[ks] = ldfrag(fa + ks * 64);
        __builtin_amdgcn_sched_barrier(0);
        v4f d0 = {0.f, 0.f, 0.f, 0.f}, d1 = {0.f, 0.f, 0.f, 0.f};
#pragma unroll
        for (int ks = 0; ks < 16; ks += 2) { d0 = mfma(W[ks], A[ks], d0); d1 = mfma(W[ks + 1], A[ks + 1], d1); }
        d0 += d1;
        const int rr = (c >> 1) & 3;
        const float v = silu(rb(rr == 0 ? d0.x : rr == 1 ? d0.y : rr == 2 ? d0.z : d0.w));
        *lp<u16>(ha) = (u16)pk2(v, v);
      }
      tbar();
      TS(4);
      if (wave < 4) {
        const v8bf a0 = ldfrag(L_H1 + (c & 1) * 128 + g * 16), a1 = ldfrag(L_H1 + (c & 1) * 128 + 64 + g * 16);
        v4f dd[8];
#pragma unroll
        for (int t = 0; t < 8; ++t) dd[t] = mfma(W[2 * t + 1], a1, mfma(W[2 * t], a0, v4f{0.f, 0.f, 0.f, 0.f}));
        v4f rv = dd[0];
#pragma unroll
        for (int t = 1; t < 8; ++t) if ((c >> 1) == t) rv = dd[t];
        v4u ov = {canon(rv.x), canon(rv.y), canon(rv.z), canon(rv.w)};
        st4(ov, rws, (c & 1) * 2048 + (c >> 1) * 64 + g * 16, W_OP + yi * 4096 + wave * 512);
        if (wave == 1 && yi == 27 && lane == 0) st2(v2u{(u32)E, (u32)(E >> 32)}, rws, 0, W_EPOCH);
        if (wave == 0) STAMP(12);
        TS(5);
      }
    }
  }
  FIN();
}

extern "C" void kernel_launch(void* const* d_in, const int* in_sizes, int n_in, void* d_out, int out_size, void* d_ws, size_t ws_size, hipStream_t stream) {
  hipLaunchKernelGGL(fused_block, dim3(256), dim3(512), 0, stream, (const u16*)d_in[1], (const u16*)d_in[2], (const u16*)d_in[9], (const u16*)d_in[3],
                     (const u16*)d_in[4], (const u16*)d_in[6], (const u16*)d_in[7], (const u16*)d_in[0], (const u16*)d_in[8], (u16*)d_out, (unsigned char*)d_ws);
}
```

```cpp
#include <hip/hip_runtime.h>

typedef unsigned int u32;
typedef unsigned long long u64;
typedef unsigned short u16;
typedef u32 v4u __attribute__((ext_vector_type(4)));
typedef u32 v2u __attribute__((ext_vector_type(2)));
typedef int v4i __attribute__((ext_vector_type(4)));
typedef float v4f __attribute__((ext_vector_type(4)));
typedef float v2f __attribute__((ext_vector_type(2)));
typedef short v4s __attribute__((ext_vector_type(4)));
typedef __bf16 v8bf __attribute__((ext_vector_type(8)));
typedef __bf16 v2bf __attribute__((ext_vector_type(2)));
#define LAS __attribute__((address_space(3)))
#define GAS __attribute__((address_space(1)))
#define DEVI __device__ __forceinline__

constexpr int NSLOT = 16, SLOT = 8192;
constexpr int NCONS = 6, NSTASH = 4, NF = 2, NMAX = 72;
#define WA 16
#define WB 20
#define WC 28
#define LA 25
#define LB 50
#define TEND 3705
#define LEADX 110
#define LEADX3 190
#define LEADY 190
#define COFF -20
constexpr int QAT = 12, HDELAY = 440, CODE_BYTES = 22272;
#define BLK_A 65, 65, 65, 63, 63, 63, 63, 63, 63, 63, 64, 64, 65, 65, 65, 65
constexpr int NBLK[2][16] = {{BLK_A}, {65, 65, 65, 63, 62, 62, 62, 62, 62, 62, 67, 67, 65, 65, 65, 65}};
constexpr int BIAS = 11;
constexpr u64 start_table(int k) {
  u64 t = 0; int s = 0;
  for (int r = 0; r < 16; ++r) { const int dl = s - 64 * r + BIAS; if (dl < 0 || dl > 15 || NBLK[k][r] > NMAX) return ~0ull; t |= (u64)dl << (4 * r); s += NBLK[k][r]; }
  return s == 1024 ? t : ~0ull;
}
constexpr u64 ST_A = start_table(0), ST_B = start_table(1);
static_assert(ST_A != ~0ull && ST_B != ~0ull, "block table");
constexpr u64 pace(int k, int r, int extra, int lead) { return ((TEND - lead) * 16 + NBLK[k][r]) / (2 * NBLK[k][r] + extra) - 384; }
constexpr u64 pace_table(int k) {
  return pace(k, 0, 0, LEADX) | pace(k, 3, 0, LEADX3) << 8 | pace(k, 3, 0, LEADX) << 16 | pace(k, 4, 8, 0) << 24 | pace(k, 8, 8, 0) << 32 |
         pace(k, 10, 0, k ? 0 : LEADY) << 40 | pace(k, 12, 2, 0) << 48;
}
constexpr u64 PACE_A = pace_table(0), PACE_B = pace_table(1);
constexpr int L_CTL = NSLOT * SLOT;
constexpr int L_Q = L_CTL + 256;
constexpr int L_ACT = L_Q + 1024;
constexpr int ASTR = 1024 + 64;
constexpr int L_MRG = L_ACT + 2 * ASTR;
constexpr int L_ML = L_MRG + 2048;
constexpr int L_SC = L_ML + 256;
constexpr int L_JUNK = L_SC + NMAX * 256;
constexpr int L_END = L_JUNK + 256;
constexpr int L_H1 = L_SC;
constexpr int L_RED = L_H1 + 256;
static_assert(L_END <= 160 * 1024, "LDS");
constexpr int C_FULL = 0, C_QRDY = 1, C_MCNT = 2, C_CLR = 3, C_ARDY = 4, C_TAG = 5, C_WRD = 6, C_MX = 7, C_SM = 8, C_T0 = 9, C_FREE = 16;
constexpr u32 W_EPOCH = 0, W_T0 = 4096, W_SP = 8192, W_HG = W_SP + 4 * 1024 * 16, W_QG = W_HG + 512 * 8, W_MG = W_QG + 4096 * 8, W_PART = W_MG + 2048 * 8;
constexpr u32 W_LSE = W_PART + 64 * 4096;
constexpr u32 W_AP = W_LSE + 256 * 128, W_OP = W_AP + 8 * 4096, W_END = W_OP + 28 * 4096;
constexpr u32 CLRB = (W_END - W_PART) / 64;
static_assert(CLRB * 64 == W_END - W_PART && CLRB % 16 == 0, "clear split");
constexpr u32 SENT = 0xFFFFFFFFu;
constexpr int SPIN_CAP = 1 << 16;

DEVI float lo16(u32 v) { return __uint_as_float(v << 16); }
DEVI float bfhi(u32 v) { return __uint_as_float(v & 0xffff0000u); }
DEVI u32 pk2(float a, float b) { v2f x = {a, b}; return __builtin_bit_cast(u32, __builtin_convertvector(x, v2bf)); }
DEVI float rb(float a) { return lo16(pk2(a, a)); }
DEVI float ex2(float x) { return __builtin_amdgcn_exp2f(x); }
DEVI float rcp(float x) { return __builtin_amdgcn_rcpf(x); }
DEVI float silu(float v) { return v * rcp(1.f + ex2(v * -1.4426950408889634f)); }
DEVI float silu_exact(float v) {
  const float t = fminf(v * -1.4426950216293335f, 126.f);
  const float r = fmaf(v, -1.92596299112662e-08f, fmaf(v, -1.4426950216293335f, -t));
  float e = ex2(t);
  e = fmaf(e, fminf(r, 1.f) * 0.6931471805599453f, e);
  const float d = 1.f + e, rc = rcp(d), q = v * rc;
  return fmaf(fmaf(-q, d, v), rc, q);
}
DEVI v2f rb2(v2f a) { const u32 p = pk2(a.x, a.y); return v2f{lo16(p), bfhi(p)}; }
DEVI float fold32(float a, float b) { auto s = __builtin_amdgcn_permlane32_swap(__float_as_uint(a), __float_as_uint(b), false, false); return __uint_as_float(s[0]) + __uint_as_float(s[1]); }
DEVI float fold16(float a, float b) { auto s = __builtin_amdgcn_permlane16_swap(__float_as_uint(a), __float_as_uint(b), false, false); return __uint_as_float(s[0]) + __uint_as_float(s[1]); }
#define PIN(x) asm volatile("" : "+v"(x))
DEVI float swap16_add(float x) { auto s = __builtin_amdgcn_permlane16_swap(__float_as_uint(x), __float_as_uint(x), false, false); return __uint_as_float(s[0]) + __uint_as_float(s[1]); }
DEVI float swap32_add(float x) { auto s = __builtin_amdgcn_permlane32_swap(__float_as_uint(x), __float_as_uint(x), false, false); return __uint_as_float(s[0]) + __uint_as_float(s[1]); }
DEVI float swap16_max(float x) { auto s = __builtin_amdgcn_permlane16_swap(__float_as_uint(x), __float_as_uint(x), false, false); return fmaxf(__uint_as_float(s[0]), __uint_as_float(s[1])); }
DEVI float swap32_max(float x) { auto s = __builtin_amdgcn_permlane32_swap(__float_as_uint(x), __float_as_uint(x), false, false); return fmaxf(__uint_as_float(s[0]), __uint_as_float(s[1])); }
template <int CTRL> DEVI float dpp(float x) { return __builtin_bit_cast(float, __builtin_amdgcn_mov_dpp(__builtin_bit_cast(int, x), CTRL, 0xf, 0xf, true)); }
DEVI float row_sum(float x) { x += dpp<0xB1>(x); x += dpp<0x4E>(x); x += dpp<0x141>(x); x += dpp<0x128>(x); return x; }
DEVI float half_sum(float x) { return swap16_add(row_sum(x)); }
DEVI float wave_sum(float x) { return swap32_add(half_sum(x)); }
template <typename T> DEVI LAS T* lp(u32 a) { return (LAS T*)(size_t)a; }
DEVI u32 ldsv(u32 a) { return *(volatile LAS u32*)(size_t)a; }
DEVI void stsv(u32 a, u32 v) { *(volatile LAS u32*)(size_t)a = v; }
#define FENCE() asm volatile("" ::: "memory")
#define WAITV(n) asm volatile("s_waitcnt vmcnt(%0)" ::"i"(n) : "memory")
#define WAITL() asm volatile("s_waitcnt lgkmcnt(0)" ::: "memory")
DEVI void bar() { WAITL(); __builtin_amdgcn_s_barrier(); FENCE(); }
typedef __amdgpu_buffer_rsrc_t rsrc_t;
DEVI v4u ld4(rsrc_t r, u32 vo, u32 so) { return __builtin_bit_cast(v4u, __builtin_amdgcn_raw_buffer_load_b128(r, vo, so, 16)); }
DEVI v2u ld2(rsrc_t r, u32 vo, u32 so) { return __builtin_bit_cast(v2u, __builtin_amdgcn_raw_buffer_load_b64(r, vo, so, 16)); }
DEVI u32 ld1(rsrc_t r, u32 vo, u32 so) { return __builtin_amdgcn_raw_buffer_load_b32(r, vo, so, 16); }
DEVI void st4(v4u v, rsrc_t r, u32 vo, u32 so) { __builtin_amdgcn_raw_buffer_store_b128(v, r, vo, so, 16); }
DEVI void st2(v2u v, rsrc_t r, u32 vo, u32 so) { __builtin_amdgcn_raw_buffer_store_b64(v, r, vo, so, 16); }
DEVI void st1(u32 v, rsrc_t r, u32 vo, u32 so) { __builtin_amdgcn_raw_buffer_store_b32(v, r, vo, so, 16); }
DEVI u32 canon(float f) { u32 b = __float_as_uint(f); return b < 0xFFFFFFFEu ? b : 0xFFFFFFFEu; }
DEVI v8bf trfrag(u32 a) {
  v4s lo = __builtin_amdgcn_ds_read_tr16_b64_v4i16(lp<v4s>(a)), hi = __builtin_amdgcn_ds_read_tr16_b64_v4i16(lp<v4s>(a + 2048));
  return __builtin_bit_cast(v8bf, __builtin_shufflevector(lo, hi, 0, 1, 2, 3, 4, 5, 6, 7));
}
DEVI v4f mfma(v8bf a, v8bf b, v4f c) { return __builtin_amdgcn_mfma_f32_16x16x32_bf16(a, b, c, 0, 0, 0); }
DEVI v8bf ldfrag(u32 a) { return __builtin_bit_cast(v8bf, *lp<v4u>(a)); }
DEVI int fpos(int k) { return (k & ~31) + ((k >> 2) & 3) * 8 + ((k >> 4) & 1) * 4 + (k & 3); }
DEVI v4i mksrd(const void* p, u32 bytes) { u64 a = (u64)p; return v4i{(int)(u32)a, (int)(u32)(a >> 32), (int)bytes, 0x00020000}; }

#define DMA4(m, v0, v1, v2, v3, srd, so)                                                                                   \
  asm volatile("s_mov_b32 m0, %0\n\ts_nop 0\n\tbuffer_load_dwordx4 %1, %5, %6 offen nt lds\n\t"                           \
               "buffer_load_dwordx4 %2, %5, %6 offen offset:1024 nt lds\n\tbuffer_load_dwordx4 %3, %5, %6 offen offset:2048 nt lds\n\t" \
               "buffer_load_dwordx4 %4, %5, %6 offen offset:3072 nt lds" ::"s"(m), "v"(v0), "v"(v1), "v"(v2), "v"(v3), "s"(srd), "s"(so) : "memory")

__global__ void __launch_bounds__(512, 2)
fused_block(const u16* __restrict__ Kc, const u16* __restrict__ Vc, const u16* __restrict__ wmod, const u16* __restrict__ wq,
            const u16* __restrict__ wo, const u16* __restrict__ w1, const u16* __restrict__ w2, const u16* __restrict__ xin,
            const u16* __restrict__ cin, u16* __restrict__ out, unsigned char* ws) {
  __shared__ __attribute__((aligned(16))) unsigned char smem[L_END];
  asm volatile("" ::"s"((u32)(size_t)(LAS unsigned char*)smem));
  const int tid = threadIdx.x, lane = tid & 63, wave = __builtin_amdgcn_readfirstlane(tid >> 6);
  if (wave == 1 && blockIdx.x < 32) {
    u64 pc; asm volatile("s_getpc_b64 %0" : "=s"(pc));
    const v4i rc = mksrd((const void*)(pc & ~127ull), CODE_BYTES);
    const u32 vo = lane * 128, m = L_JUNK, s1 = 8192, s2 = 16384;
    asm volatile("s_mov_b32 m0, %0\n\ts_nop 0\n\tbuffer_load_dword %1, %2, 0 offen lds\n\tbuffer_load_dword %1, %2, %3 offen lds\n\tbuffer_load_dword %1, %2, %4 offen lds"
                 ::"s"(m), "v"(vo), "s"(rc), "s"(s1), "s"(s2) : "memory");
  }
  const int bid = (blockIdx.x & ~31) | ((blockIdx.x & 7) << 2) | ((blockIdx.x >> 3) & 3);
  const int grp = bid >> 4, r = bid & 15;
  const int g = lane >> 4, c = lane & 15;
  const u64 stt = (grp < 14 ? ST_A : ST_B) >> (4 * r);
  const int kv0 = 64 * r + ((int)stt & 15) - BIAS;
  const bool isS = r >= 12;
  if (__builtin_expect(wave == 0, 1)) {
    const v4i srd = isS ? mksrd(wmod, 512 * 6144) : mksrd(Kc, 1u << 27);
    const u32 k0 = g * 256 + ((c ^ g) << 4), w0 = (lane >> 3) * 6144 + (((lane & 7) ^ ((lane >> 3) & 6)) << 4);
    const u32 a0 = isS ? w0 : k0, a1 = isS ? w0 + 48128 : k0 ^ 64, a2 = isS ? w0 + 2 * 48128 : k0 ^ 128, a3 = isS ? w0 + 3 * 48128 : k0 ^ 192;
    const u32 so = isS ? (r - 12) * 128 * 6144 + grp * 128 : (u32)(grp * 1024 + kv0) << 13, hs = isS ? 32 * 6144 : 4096;
#pragma unroll
    for (int i = 0; i < 4; ++i) { const u32 m = i * 4096, s1 = so + i * hs; DMA4(m, a0, a1, a2, a3, srd, s1); }
  }
  if (wave == 2) { stsv(L_CTL + 4 * lane, 0); WAITL(); }
  __builtin_amdgcn_s_barrier(); FENCE();
  const u64 tstart = __builtin_amdgcn_s_memrealtime();
  FENCE();
  const bool isX = r < 4, isQ = r >= 4 && r < 8, isM = r == 8 || r == 9, isY = (r == 10 || r == 11) && grp < 14, isR = r == 0 && (grp & 1);
  const int xi = grp * 4 + r, qi = grp * 4 + r - 4, mi = grp * 2 + r - 8, yi = grp * 2 + r - 10;
  const int u0 = (int)(0x2222008888880000ull >> (4 * r)) & 15;
  const int nb = 64 + (r == 15 ? BIAS : (int)(stt >> 4) & 15) - ((int)stt & 15);
  const int nf = isS ? 0 : NF;
  const int ntail = isX ? 8 : isY ? 16 : 0;
  const int uv = u0 + nb, uw = uv + nb;
  u64 E = 0;
  int wv = wave;
  asm volatile("" : "+s"(wv));
  if (__builtin_expect(wv == 0, 1)) {
    __builtin_amdgcn_s_setprio(3);
    int iss, pub = 0;
    auto publish = [&](int v) { pub = max(pub, v); stsv(L_CTL + 4 * C_FULL, pub); };
#define PACE(W_) do { WAITV(W_); publish(iss - (W_)); } while (0)
    const u32 cls = ((u32)(0x6666554433331000ull >> (4 * r)) & 7) + (r == 3 && (grp & 1));
    const u32 tau = 384 + ((u32)((grp < 14 ? PACE_A : PACE_B) >> (8 * cls)) & 255);
    u32 due = ((u32)tstart + COFF) * 16 + NF * tau;
    const u32 wrow = lane >> 3, wlc = ((lane & 7) ^ (((wrow >> 1) & 3) << 1)) << 4;
    int u = NF;
#pragma nounroll
    for (int seg = u0 == 8 ? 0 : 1; seg < 3 + (ntail >> 3); ++seg) {
      const bool kv = seg == 1 || seg == 2;
      const u16* base = seg == 0 ? (isQ ? wq : wmod) : seg == 1 ? Kc : seg == 2 ? Vc : seg == 4 ? w2 : isX ? wo : w1;
      const u32 rs = seg == 0 ? (isQ ? 8192 : 6144) : (seg == 3 && !isX) ? 3584 : 1024;
      const v4i srd = mksrd(base, kv ? 1u << 27 : seg == 4 ? 1792 * 1024 : (seg == 3 && isX) ? 4096 * 1024 : 512 * rs);
      u32 so = kv ? (u32)(grp * 1024 + kv0 + (seg == 1 ? nf : 0)) << 13
                  : seg == 0 ? (isQ ? qi : 16 + mi) * 128
                             : seg == 4 ? yi << 16 : isX ? ((xi >> 3) << 19) + (xi & 7) * 128 : yi * 128;
      const u32 hstep = kv ? 4096 : 32 * rs, ustep = seg == 4 ? 128 : 2 * hstep;
      const u32 sw = g << (seg - 1), sx = seg == 1 ? 4 : 8;
      const u32 w0 = wrow * rs + wlc, wd = 8 * rs - 1024;
      const u32 a0 = kv ? g * 256 + ((c ^ sw) << 4) : w0, a1 = kv ? g * 256 + ((c ^ sw ^ sx) << 4) : w0 + wd;
      const u32 a2 = kv ? g * 256 + ((c ^ sw ^ ((2 * sx) & 15)) << 4) : w0 + 2 * wd, a3 = kv ? g * 256 + ((c ^ sw ^ ((3 * sx) & 15)) << 4) : w0 + 3 * wd;
      const int ue = seg == 0 ? NF + u0 : seg == 1 ? uv : seg == 2 ? uw : u + 8;
#pragma nounroll
      while (u < ue) {
        const bool paced = u >= 4 && seg < 3;
        const int stop = min(ue, u < 4 ? 4 : u < 48 ? 48 : 1 << 20);
        u32 dd = paced ? due : due - (1u << 28);
        const int u_in = u;
        u32 t0, t1, lag; u32 vf, vt;
#define DMAS(so) "buffer_load_dwordx4 %[a0], %[srd], " so " offen nt lds\n\tbuffer_load_dwordx4 %[a1], %[srd], " so " offen offset:1024 nt lds\n\t" \
                 "buffer_load_dwordx4 %[a2], %[srd], " so " offen offset:2048 nt lds\n\tbuffer_load_dwordx4 %[a3], %[srd], " so " offen offset:3072 nt lds\n\t"
#define RDFREE "s_lshl_b32 %[t1], %[u], 2\n\ts_and_b32 %[t1], %[t1], 60\n\ts_or_b32 %[t1], %[t1], %[fb]\n\tv_mov_b32 %[vf], %[t1]\n\tds_read_b32 %[vf], %[vf]\n\t"
#define REST(W) "s_waitcnt vmcnt(" #W ")\n\ts_mov_b32 m0, %[t0]\n\ts_lshl_b32 %[t0], %[u], 3\n\t" DMAS("%[t1]")                                   \
                "s_sub_i32 %[t0], %[t0], " #W " - 4\n\ts_max_i32 %[pub], %[pub], %[t0]\n\tv_mov_b32 %[vt], %[pub]\n\tds_write_b32 %[va], %[vt]\n\t"                                      \
                "s_add_i32 %[u], %[u], 1\n\ts_add_u32 %[so], %[so], %[us]\n\ts_add_u32 %[dd], %[dd], %[tau]\n\t" RDFREE                             \
                "s_memrealtime s[98:99]\n\ts_cmp_lt_i32 %[u], %[stop]\n\ts_waitcnt vmcnt(" #W ")\n\ts_cbranch_scc1 1b\n\ts_branch 9f\n"
#define XREST(W) REST(W)
        asm volatile(RDFREE "s_memrealtime s[98:99]\n"
                     "1:\n\ts_sub_i32 %[t1], %[u], 15\n\ts_waitcnt lgkmcnt(0)\n\tv_cmp_gt_i32 vcc, %[t1], %[vf]\n\ts_lshl_b32 s98, s98, 4\n\t"
                     "s_cbranch_vccnz 8f\n"
                     "2:\n\ts_sub_i32 %[lag], s98, %[dd]\n\ts_cmp_lt_i32 %[lag], 0\n\ts_cbranch_scc1 7f\n\t"
                     "s_lshl_b32 %[t0], %[u], 13\n\ts_and_b32 %[t0], %[t0], 0x1e000\n\ts_mov_b32 m0, %[t0]\n\ts_add_u32 %[t1], %[so], %[hs]\n\t" DMAS("%[so]")
                     "s_or_b32 %[t0], %[t0], 0x1000\n\ts_cmp_lt_i32 %[lag], %[la]\n\ts_cbranch_scc1 4f\n\ts_cmp_lt_i32 %[lag], %[lb]\n\ts_cbranch_scc1 5f\n\t"
                     XREST(WC) "4:\n\t" XREST(WA) "5:\n\t" XREST(WB)
                     "8:\n\t" RDFREE "s_sub_i32 %[t1], %[u], 15\n\ts_waitcnt lgkmcnt(0)\n\tv_cmp_gt_i32 vcc, %[t1], %[vf]\n\ts_nop 0\n\ts_cbranch_vccz 7f\n\t"
                     "s_waitcnt vmcnt(0)\n\ts_lshl_b32 %[pub], %[u], 3\n\tv_mov_b32 %[vt], %[pub]\n\tds_write_b32 %[va], %[vt]\n"
                     "6:\n\t" RDFREE "s_sub_i32 %[t1], %[u], 15\n\ts_waitcnt lgkmcnt(0)\n\tv_cmp_gt_i32 vcc, %[t1], %[vf]\n\ts_sleep 1\n\ts_cbranch_vccnz 6b\n"
                     "7:\n\ts_memrealtime s[98:99]\n\ts_waitcnt lgkmcnt(0)\n\ts_lshl_b32 s98, s98, 4\n\ts_branch 2b\n"
                     "9:\n\ts_waitcnt lgkmcnt(0)"
                     : [u] "+s"(u), [so] "+s"(so), [dd] "+s"(dd), [pub] "+s"(pub), [t0] "=&s"(t0), [t1] "=&s"(t1), [lag] "=&s"(lag), [vf] "=&v"(vf), [vt] "=&v"(vt)
                     : [a0] "v"(a0), [a1] "v"(a1), [a2] "v"(a2), [a3] "v"(a3), [srd] "s"(srd), [hs] "s"(hstep), [us] "s"(ustep), [stop] "s"(stop),
                       [va] "v"(L_CTL + 4 * C_FULL), [fb] "i"(L_CTL + 4 * C_FREE), [tau] "s"(tau), [la] "i"(LA * 16), [lb] "i"(LB * 16)
                     : "memory", "vcc", "scc", "s98", "s99");
        due += (u - u_in) * tau;
        if (u == 4 && isS) { WAITV(16); publish(16); }
        if (u == 48) { const u32 t = __builtin_amdgcn_readfirstlane(ldsv(L_CTL + 4 * C_T0)); if (t) due = t * 16 + u * tau; }
      }
    }
    iss = 8 * u;
    if (!ntail) { PACE(24); PACE(16); PACE(8); PACE(0); }
    __builtin_amdgcn_s_setprio(0);
  } else {
    const rsrc_t rws = __builtin_amdgcn_make_buffer_rsrc(ws, 0, W_END, 0x00020000);
    auto wait_full = [&](u32 need) { while (ldsv(L_CTL + 4 * C_FULL) < need) __builtin_amdgcn_s_sleep(1); FENCE(); };
    const u32 trow = 4 * g + (c >> 2);
    auto waddr = [&](int t) -> u32 { return trow * 128 + ((((u32)(t ^ ((trow >> 1) & 3)) << 1) | ((c >> 1) & 1)) << 4) + (c & 1) * 8; };

    if (wave == 1) {
      const u64 E0 = __hip_atomic_load((GAS u64*)(ws + W_EPOCH), __ATOMIC_RELAXED, __HIP_MEMORY_SCOPE_AGENT);
      u32 tag;
#define CHAIN_START() do { E = E0 + 1; tag = (u32)E; } while (0)
      auto silu4 = [&](u32 x, u32 y) { return v2u{pk2(silu_exact(lo16(x)), silu_exact(bfhi(x))), pk2(silu_exact(lo16(y)), silu_exact(bfhi(y)))}; };
      if (isS) {
        const v2u cv = *(const v2u*)(cin + (lane >> 5) * 512 + (r - 12) * 128 + (lane & 31) * 4);
        CHAIN_START();
        *lp<v2u>(L_ACT + (lane >> 5) * ASTR + fpos((lane & 31) * 4) * 2) = silu4(cv.x, cv.y);
        FENCE();
        v8bf act[4];
#pragma unroll
        for (int ks = 0; ks < 4; ++ks) { act[ks] = ldfrag(L_ACT + (c & 1) * ASTR + g * 16 + ks * 64); PIN(act[ks]); }
        wait_full(16);
        v8bf W[16];
#pragma unroll
        for (int j = 0; j < 16; ++j) W[j] = trfrag(waddr(j & 3) + (j >> 2) * 4096);
        FENCE(); stsv(L_CTL + 4 * C_FREE, 1); stsv(L_CTL + 4 * (C_FREE + 1), 2);
        v4f acc[4];
#pragma unroll
        for (int t = 0; t < 4; ++t) acc[t] = v4f{0.f, 0.f, 0.f, 0.f};
#pragma unroll
        for (int j = 0; j < 16; ++j) acc[j & 3] = mfma(act[j >> 2], W[j], acc[j & 3]);
        const float r0 = g == 0 ? acc[0][0] : g == 1 ? acc[1][0] : g == 2 ? acc[2][0] : acc[3][0];
        const float r1 = g == 0 ? acc[0][1] : g == 1 ? acc[1][1] : g == 2 ? acc[2][1] : acc[3][1];
        while (ldsv(L_CTL + 4 * C_CLR) == 0) { }
        FENCE();
        st4(v4u{__float_as_uint(r0), tag, __float_as_uint(r1), tag}, rws, lane * 16, W_SP + ((r - 12) * 1024 + grp * 64) * 16);
      } else if (isR) {
        const v4u in0 = *(const v4u*)(xin + lane * 8), in1 = *(const v4u*)(xin + 512 + lane * 8);
        u32 xs0 = *(const u32*)(xin + (grp >> 1) * 64 + (lane & ~1)), xs1 = *(const u32*)(xin + 512 + (grp >> 1) * 64 + (lane & ~1));
        CHAIN_START();
        PIN(xs0); PIN(xs1);
        xs0 >>= 16 * (lane & 1); xs1 >>= 16 * (lane & 1);
        float mr[2], ri[2];
#pragma unroll
        for (int b = 0; b < 2; ++b) {
          const v4u xv = b ? in1 : in0;
          const float x[8] = {lo16(xv.x), bfhi(xv.x), lo16(xv.y), bfhi(xv.y), lo16(xv.z), bfhi(xv.z), lo16(xv.w), bfhi(xv.w)};
          float s = 0.f;
#pragma unroll
          for (int i = 0; i < 8; ++i) s += x[i];
          const float mean = wave_sum(s) * (1.f / 512.f);
          float q2 = 0.f;
#pragma unroll
          for (int i = 0; i < 8; ++i) q2 += (x[i] - mean) * (x[i] - mean);
          const float var = wave_sum(q2) * (1.f / 512.f);
          mr[b] = rb(mean); ri[b] = rcp(rb(__builtin_amdgcn_sqrtf(rb(rb(var) + 1e-5f))));
        }
        v2f y = rb2(rb2(v2f{lo16(xs0) - mr[0], lo16(xs1) - mr[1]}) * v2f{ri[0], ri[1]});
        PIN(y);
        v4u a[8];
#pragma nounroll
        for (int sp = 0; sp < SPIN_CAP; ++sp) {
          u32 bad = 0;
#pragma unroll
          for (int i = 0; i < 8; ++i) { a[i] = ld4(rws, lane * 16, W_SP + ((i & 3) * 1024 + (i >> 2) * 512 + (grp >> 1) * 64) * 16); bad |= (a[i].y ^ tag) | (a[i].w ^ tag); }
          if (__all(bad == 0)) break;
          FENCE();
        }
        v2f sc[2];
#pragma unroll
        for (int pt = 0; pt < 2; ++pt) {
          sc[pt] = v2f{0.f, 0.f};
#pragma unroll
          for (int k4 = 0; k4 < 4; ++k4) sc[pt] += v2f{__uint_as_float(a[pt * 4 + k4].x), __uint_as_float(a[pt * 4 + k4].z)};
        }
        const v2f h = rb2(y * rb2(rb2(sc[1]) + 1.f)) + rb2(sc[0]);
        st2(v2u{pk2(h.x, h.y), tag}, rws, lane * 8, W_HG + (grp >> 1) * 512);
      } else if (isM || isQ) {
        if (isM) {
          const v4u in0 = *(const v4u*)(cin + lane * 8), in1 = *(const v4u*)(cin + 512 + lane * 8);
          CHAIN_START();
          *lp<v2u>(L_ACT + fpos(lane * 8) * 2) = silu4(in0.x, in0.y); *lp<v2u>(L_ACT + fpos(lane * 8 + 4) * 2) = silu4(in0.z, in0.w);
          *lp<v2u>(L_ACT + ASTR + fpos(lane * 8) * 2) = silu4(in1.x, in1.y); *lp<v2u>(L_ACT + ASTR + fpos(lane * 8 + 4) * 2) = silu4(in1.z, in1.w);
        } else {
          CHAIN_START();
          while (__builtin_amdgcn_s_memrealtime() < tstart + HDELAY) __builtin_amdgcn_s_sleep(4);
          v4u a[4];
#pragma nounroll
          for (int sp = 0; sp < SPIN_CAP; ++sp) {
            u32 bad = 0;
#pragma unroll
            for (int i = 0; i < 4; ++i) { a[i] = ld4(rws, lane * 16 + i * 1024, W_HG); bad |= (a[i].y ^ tag) | (a[i].w ^ tag); }
            if (__all(bad == 0)) break;
            FENCE();
          }
#pragma unroll
          for (int i = 0; i < 4; ++i) {
            const u32 wa = L_ACT + fpos(i * 128 + lane * 2) * 2;
            *lp<u32>(wa) = (a[i].x & 0xffffu) | (a[i].z << 16); *lp<u32>(wa + ASTR) = (a[i].x >> 16) | (a[i].z & 0xffff0000u);
          }
        }
        FENCE(); stsv(L_CTL + 4 * C_TAG, tag); stsv(L_CTL + 4 * C_ARDY, 1);
      } else CHAIN_START();
      if (blockIdx.x < 8 && lane == 0) st2(v2u{(u32)tstart, tag}, rws, blockIdx.x * 8, W_T0);
      {
        wait_full(8 * u0 + 16 * QAT);
        v4u a[2];
#pragma nounroll
        for (int sp = 0; sp < SPIN_CAP; ++sp) {
          a[0] = ld4(rws, lane * 16, W_QG + grp * 2048); a[1] = ld4(rws, lane * 16 + 1024, W_QG + grp * 2048);
          if (__all(((a[0].y ^ tag) | (a[0].w ^ tag) | (a[1].y ^ tag) | (a[1].w ^ tag)) == 0)) break;
          __builtin_amdgcn_s_sleep(16); FENCE();
        }
        *lp<v2u>(L_Q + lane * 8) = v2u{a[0].x, a[0].z}; *lp<v2u>(L_Q + 512 + lane * 8) = v2u{a[1].x, a[1].z};
        FENCE(); stsv(L_CTL + 4 * C_TAG, tag); stsv(L_CTL + 4 * C_QRDY, 1);
        {
          const v2u t = ld2(rws, (lane & 7) * 8, W_T0);
          if (__all(t.y == tag)) {
            int dt = (int)(t.x - (u32)tstart);
            dt += __builtin_amdgcn_mov_dpp(dt, 0xB1, 0xf, 0xf, true); dt += __builtin_amdgcn_mov_dpp(dt, 0x4E, 0xf, 0xf, true); dt += __builtin_amdgcn_mov_dpp(dt, 0x141, 0xf, 0xf, true);
            if (dt > -2400 && dt < 2400) stsv(L_CTL + 4 * C_T0, ((u32)tstart + (dt >> 3) + COFF) | 1);
          }
        }
      }
      if (ntail) while (ldsv(L_CTL + 4 * C_FULL) < (u32)(8 * uw)) __builtin_amdgcn_s_sleep(8);
      if (r == 8) {
        wait_full(8 * uw);
        const int zb = grp >> 3, zc = (grp & 7) * 64 + c * 4 + g;
        const u32 xv = xin[zb * 512 + zc];
        v2u m0, m1;
#pragma nounroll
        for (int sp = 0; sp < SPIN_CAP; ++sp) {
          m0 = ld2(rws, zc * 8, W_MG); m1 = ld2(rws, zc * 8, W_MG + 3 * 4096);
          if (__all(((m0.y ^ tag) | (m1.y ^ tag)) == 0)) break;
          FENCE();
        }
        v4u z[7];
#pragma nounroll
        for (int sp = 0; sp < SPIN_CAP; ++sp) {
          z[0] = ld4(rws, g * 4096 + c * 16, W_AP + grp * 256); z[1] = ld4(rws, g * 4096 + c * 16, W_AP + 16384 + grp * 256);
          if (__all(max(max(max(z[0].x, z[0].y), max(z[0].z, z[0].w)), max(max(z[1].x, z[1].y), max(z[1].z, z[1].w))) != SENT)) break;
          __builtin_amdgcn_s_sleep(2); FENCE();
        }
        v4f s0 = __builtin_bit_cast(v4f, z[0]), s1 = __builtin_bit_cast(v4f, z[1]);
        const float at = fold32(fold16(s0.x, s0.y), fold16(s0.z, s0.w)) + fold32(fold16(s1.x, s1.y), fold16(s1.z, s1.w));
        float x1 = rb(lo16(xv) + rb(bfhi(m0.x << (16 - 16 * zb)) * rb(at))), gb = bfhi(m1.x << (16 - 16 * zb));
        GAS u16* op = (GAS u16*)out + zb * 512 + zc;
        PIN(x1); PIN(gb); PIN(op);
#pragma nounroll
        for (int sp = 0; sp < SPIN_CAP; ++sp) {
#pragma unroll
          for (int j = 0; j < 7; ++j) z[j] = ld4(rws, g * 4096 + c * 16, W_OP + j * 16384 + grp * 256);
          u32 mx = 0;
#pragma unroll
          for (int j = 0; j < 7; ++j) mx = max(mx, max(max(z[j].x, z[j].y), max(z[j].z, z[j].w)));
          if (__all(mx != SENT)) break;
          FENCE();
        }
        const v4f s = ((__builtin_bit_cast(v4f, z[0]) + __builtin_bit_cast(v4f, z[1])) + (__builtin_bit_cast(v4f, z[2]) + __builtin_bit_cast(v4f, z[3]))) +
                      ((__builtin_bit_cast(v4f, z[4]) + __builtin_bit_cast(v4f, z[5])) + __builtin_bit_cast(v4f, z[6]));
        const float rr = x1 + rb(gb * rb(fold16(fold32(s.x, s.z), fold32(s.y, s.w))));
        *op = (u16)pk2(rr, rr);
        __builtin_amdgcn_endpgm();
      }
    } else {
      if (isS && wave == 7) {
        const v4u s4 = {SENT, SENT, SENT, SENT};
        const u32 so = W_PART + (grp * 4 + r - 12) * CLRB;
#pragma unroll
        for (int i = 0; i < CLRB / 1024; ++i) st4(s4, rws, lane * 16 + (i & 3) * 1024, so + (i >> 2) * 4096);
        if (lane < (CLRB % 1024) / 16) st4(s4, rws, lane * 16, so + CLRB / 1024 * 1024);
        WAITV(0); stsv(L_CTL + 4 * C_CLR, 1);
      }

      if (wave == 6) { *lp<v4f>(L_MRG + lane * 16) = v4f{0.f, 0.f, 0.f, 0.f}; *lp<v4f>(L_MRG + 1024 + lane * 16) = v4f{0.f, 0.f, 0.f, 0.f}; }
      const int d = c >> 2, cw = wave - 2;
      const u32 sca = L_SC + (g * 4 + (c & 3)) * 16;
      {
        u32 ka[4];
#pragma unroll
        for (int s = 0; s < 4; ++s) ka[s] = c * 256 + (((4 * s + g) ^ c) << 4);
        v8bf qf[4];
        float mx = -INFINITY;
        auto rdk = [&](v8bf (&kf)[8], int j) {
          const int u = j < nf ? j : u0 + j;
          const u32 lb = (u & (NSLOT - 1)) * SLOT;
          wait_full(8 * u + 8);
#pragma unroll
          for (int s = 0; s < 4; ++s) { kf[s] = ldfrag(lb + ka[s]); kf[4 + s] = ldfrag(lb + ka[s] + 4096); }
          FENCE(); stsv(L_CTL + 4 * (C_FREE + (u & (NSLOT - 1))), u + 1);
        };
        auto qk = [&](v8bf (&kf)[8], int j) {
          v4f S0 = {0.f, 0.f, 0.f, 0.f}, S1 = {0.f, 0.f, 0.f, 0.f};
#pragma unroll
          for (int s = 0; s < 4; ++s) { S0 = mfma(kf[s], qf[s], S0); S1 = mfma(kf[4 + s], qf[s], S1); }
          const v2f a = rb2(v2f{S0.x, S0.y}) * 0.08838834764831845f, b = rb2(v2f{S0.z, S0.w}) * 0.08838834764831845f;
          const v2f e = rb2(v2f{S1.x, S1.y}) * 0.08838834764831845f, f = rb2(v2f{S1.z, S1.w}) * 0.08838834764831845f;
          mx = fmaxf(fmaxf(fmaxf(mx, fmaxf(a.x, a.y)), fmaxf(b.x, b.y)), fmaxf(fmaxf(e.x, e.y), fmaxf(f.x, f.y)));
          if (c < 4) *lp<v4u>(sca + j * 256) = v4u{pk2(a.x, a.y), pk2(b.x, b.y), pk2(e.x, e.y), pk2(f.x, f.y)};
        };
        v8bf sk[NSTASH][8];
        rdk(sk[0], cw);
      if (u0 == 8 && wave < 6) {
        wait_full(8 * NF + 64);
        v8bf W[16];
#pragma unroll
        for (int ks = 0; ks < 16; ++ks) W[ks] = trfrag(NF * SLOT + ks * 4096 + waddr(wave - 2));
        WAITL();
        u32 old = 0;
        if (lane == 0) old = __hip_atomic_fetch_add(lp<u32>(L_CTL + 4 * C_WRD), 1u, __ATOMIC_RELAXED, __HIP_MEMORY_SCOPE_WORKGROUP);
        if (__builtin_amdgcn_readfirstlane(old) == 3 && lane < 8) stsv(L_CTL + 4 * (C_FREE + NF + lane), NF + lane + 1);
        while (ldsv(L_CTL + 4 * C_ARDY) == 0) __builtin_amdgcn_s_sleep(1);
        FENCE();
        const u32 tag = ldsv(L_CTL + 4 * C_TAG);
        v8bf A[16];
#pragma unroll
        for (int ks = 0; ks < 16; ++ks) A[ks] = ldfrag(L_ACT + (c & 1) * ASTR + g * 16 + ks * 64);
        __builtin_amdgcn_sched_barrier(0);
        v4f d0 = {0.f, 0.f, 0.f, 0.f}, d1 = {0.f, 0.f, 0.f, 0.f};
#pragma unroll
        for (int ks = 0; ks < 16; ks += 2) { d0 = mfma(A[ks], W[ks], d0); d1 = mfma(A[ks + 1], W[ks + 1], d1); }
        const float r0 = d0[0] + d1[0], r1 = d0[1] + d1[1];
        const float n0 = dpp<0xB1>(r0), n1 = dpp<0xB1>(r1);
        const bool odd = lane & 1;
        if (lane < 16) {
          if (isM) st2(v2u{pk2(r0, r1), tag}, rws, lane * 8, W_MG + mi * 512 + (wave - 2) * 128);
          else st2(v2u{pk2(odd ? n1 : r0, odd ? r1 : n0), tag}, rws, (lane & 1) * 16384 + (lane >> 1) * 8, W_QG + qi * 256 + (wave - 2) * 64);
        }
      }
#pragma unroll
        for (int i = 1; i < NSTASH; ++i) rdk(sk[i], cw + i * NCONS);
        while (ldsv(L_CTL + 4 * C_QRDY) == 0) __builtin_amdgcn_s_sleep(2);
        FENCE();
#pragma unroll
        for (int s = 0; s < 4; ++s) qf[s] = ldfrag(L_Q + (c & 3) * 256 + (4 * s + g) * 16);
#pragma unroll
        for (int i = 0; i < NSTASH; ++i) qk(sk[i], cw + i * NCONS);
#pragma nounroll
        for (int j = cw + NSTASH * NCONS; j < nb; j += NCONS) { v8bf kf[8]; rdk(kf, j); qk(kf, j); }
        mx = swap32_max(swap16_max(rb(mx)));
        if (lane < 4) *lp<float>(L_ML + cw * 16 + lane * 4) = mx;
        FENCE();
        if (lane == 0) __hip_atomic_fetch_add(lp<u32>(L_CTL + 4 * C_MX), 1u, __ATOMIC_RELAXED, __HIP_MEMORY_SCOPE_WORKGROUP);
      }
      float M2;
      {
        while (ldsv(L_CTL + 4 * C_MX) < NCONS) __builtin_amdgcn_s_sleep(1);
        FENCE();
        float M = *lp<float>(L_ML + (c & 3) * 4);
#pragma unroll
        for (int w = 1; w < NCONS; ++w) M = fmaxf(M, *lp<float>(L_ML + w * 16 + (c & 3) * 4));
        M2 = M * 1.4426950408889634f;
        float l = 0.f;
#pragma nounroll
        for (int j = cw + d * NCONS; j < nb; j += 4 * NCONS) {
          const v4u s = *lp<v4u>(sca + j * 256);
          float p[8];
#pragma unroll
          for (int i = 0; i < 4; ++i) { p[2 * i] = ex2(fmaf(lo16(s[i]), 1.4426950408889634f, -M2)); p[2 * i + 1] = ex2(fmaf(bfhi(s[i]), 1.4426950408889634f, -M2)); }
          l += ((p[0] + p[1]) + (p[2] + p[3])) + ((p[4] + p[5]) + (p[6] + p[7]));
          *lp<v4u>(sca + j * 256) = v4u{pk2(p[0], p[1]), pk2(p[2], p[3]), pk2(p[4], p[5]), pk2(p[6], p[7])};
        }
        l += dpp<0x124>(l); l += dpp<0x128>(l);
        l = swap32_add(swap16_add(l));
        if (lane < 4) *lp<float>(L_ML + 128 + cw * 16 + lane * 4) = l;
        FENCE();
        if (lane == 0) __hip_atomic_fetch_add(lp<u32>(L_CTL + 4 * C_SM), 1u, __ATOMIC_RELAXED, __HIP_MEMORY_SCOPE_WORKGROUP);
      }
      {
        u32 va[8];
#pragma unroll
        for (int t = 0; t < 8; ++t) va[t] = trow * 256 + ((((u32)(t ^ (trow & 7)) << 1) | ((c >> 1) & 1)) << 4) + (c & 1) * 8;
        v4f acc[8];
#pragma unroll
        for (int t = 0; t < 8; ++t) acc[t] = v4f{0.f, 0.f, 0.f, 0.f};
        int j = cw;
#pragma nounroll
        for (; j + NCONS < nb; j += NCONS) {
          const int u = uv + j;
          const u32 lb = (u & (NSLOT - 1)) * SLOT;
          wait_full(8 * u + 8);
          const v8bf pf = ldfrag(sca + j * 256);
          v8bf vf[8];
#pragma unroll
          for (int t = 0; t < 8; ++t) {
            v4s lo = __builtin_amdgcn_ds_read_tr16_b64_v4i16(lp<v4s>(lb + va[t])), hi = __builtin_amdgcn_ds_read_tr16_b64_v4i16(lp<v4s>(lb + va[t] + 4096));
            vf[t] = __builtin_bit_cast(v8bf, __builtin_shufflevector(lo, hi, 0, 1, 2, 3, 4, 5, 6, 7));
          }
          FENCE(); stsv(L_CTL + 4 * (C_FREE + (u & (NSLOT - 1))), u + 1);
#pragma unroll
          for (int t = 0; t < 8; ++t) acc[t] = mfma(vf[t], pf, acc[t]);
        }
        const int link = j - (nb - NCONS), u = uv + j;
        const u32 lb = (u & (NSLOT - 1)) * SLOT;
        v4f e0 = d == 0 ? acc[0] : d == 1 ? acc[2] : d == 2 ? acc[4] : acc[6];
        v4f e1 = d == 0 ? acc[1] : d == 1 ? acc[3] : d == 2 ? acc[5] : acc[7];
        const v4u pf = *lp<v4u>(sca + j * 256);
        v4u pm[4];
#pragma unroll
        for (int i = 0; i < 4; ++i) { pm[i] = d == i ? pf : v4u{0u, 0u, 0u, 0u}; PIN(pm[i]); }
        float inv = 1.f;
        if (link == NCONS - 1) {
          while (ldsv(L_CTL + 4 * C_SM) < NCONS) { }
          FENCE();
          float L = *lp<float>(L_ML + 128 + (c & 3) * 4);
#pragma unroll
          for (int w = 1; w < NCONS; ++w) L += *lp<float>(L_ML + 128 + w * 16 + (c & 3) * 4);
          inv = rcp(L);
          M2 += __builtin_amdgcn_logf(L);
        }
        u32 lse = canon(M2);
        PIN(lse);
        u32 oa = (c & 3) * 4096 + d * 64 + g * 16, ra = L_MRG + lane * 16;
        u32 vb[8];
#pragma unroll
        for (int t = 0; t < 8; ++t) { vb[t] = lb + va[t]; PIN(vb[t]); }
        PIN(e0); PIN(e1); PIN(oa); PIN(ra); PIN(inv);
        while (ldsv(L_CTL + 4 * C_FULL) < (u32)(8 * u + 8)) { }
        FENCE();
        v8bf vf[8];
#pragma unroll
        for (int t = 0; t < 8; ++t) {
          v4s lo = __builtin_amdgcn_ds_read_tr16_b64_v4i16(lp<v4s>(vb[t])), hi = __builtin_amdgcn_ds_read_tr16_b64_v4i16(lp<v4s>(vb[t] + 4096));
          vf[t] = __builtin_bit_cast(v8bf, __builtin_shufflevector(lo, hi, 0, 1, 2, 3, 4, 5, 6, 7));
        }
        __builtin_amdgcn_sched_barrier(0);
#pragma unroll
        for (int i = 0; i < 4; ++i) { e0 = mfma(vf[2 * i], __builtin_bit_cast(v8bf, pm[i]), e0); e1 = mfma(vf[2 * i + 1], __builtin_bit_cast(v8bf, pm[i]), e1); }
        __builtin_amdgcn_sched_barrier(0);
        v4f r0, r1;
        for (;;) {
          const u32 cnt = ldsv(L_CTL + 4 * C_MCNT);
          FENCE();
          r0 = *lp<v4f>(ra); r1 = *lp<v4f>(ra + 1024);
          FENCE();
          if (cnt >= (u32)link) break;
        }
        e0 = (e0 + r0) * inv; e1 = (e1 + r1) * inv;
        if (link == NCONS - 1) {
          st4(v4u{min(pk2(e0.x, e0.y), SENT - 1), min(pk2(e0.z, e0.w), SENT - 1), min(pk2(e1.x, e1.y), SENT - 1), min(pk2(e1.z, e1.w), SENT - 1)}, rws, oa, W_PART + grp * 16384 + r * 256);
          if (lane < 4) st1(lse, rws, lane * 4, W_LSE + bid * 128);
        } else {
          *lp<v4f>(ra) = e0; *lp<v4f>(ra + 1024) = e1;
          FENCE(); stsv(L_CTL + 4 * C_MCNT, link + 1);
        }
        stsv(L_CTL + 4 * (C_FREE + (u & (NSLOT - 1))), u + 1);
      }
    }
  }
  if (!(isX || isY)) return;
  const u32 trow = 4 * g + (c >> 2);
  auto waddr = [&](int t) -> u32 { return trow * 128 + ((((u32)(t ^ ((trow >> 1) & 3)) << 1) | ((c >> 1) & 1)) << 4) + (c & 1) * 8; };
  auto wslot = [&](int t) -> u32 { return ((uw + t) & (NSLOT - 1)) * SLOT; };
  const int half = lane >> 5, i32 = lane & 31;
  const u32 fa = L_ACT + (c & 1) * ASTR + g * 16;
  {
    const rsrc_t rws = __builtin_amdgcn_make_buffer_rsrc(ws, 0, W_END, 0x00020000);
    auto weights_in = [&]() {
      if (wave == 0) { WAITV(0); stsv(L_CTL + 4 * C_FULL, 8 * (uw + ntail)); }
      else while (ldsv(L_CTL + 4 * C_FULL) < (u32)(8 * (uw + ntail))) __builtin_amdgcn_s_sleep(1);
      FENCE();
    };
    if (isX) {
      const int kvh = xi >> 3, ng = xi & 7;
      const int b = wave >> 2, h = wave & 3;
      u32 wa = L_ACT + b * ASTR + (h * 128 + c * 8 + g * 2) * 2, wb[8];
#pragma unroll
      for (int i = 0; i < 8; ++i) { wb[i] = wslot(i) + waddr(wave & 3); PIN(wb[i]); }
      u32 vo = lane * 16 + W_PART + (b * 8 + kvh) * 16384 + h * 4096;
      u32 vl = ((c & 3) * 4 + ((g + (c >> 2)) & 3)) * 128 + W_LSE + (b * 8 + kvh) * 2048 + h * 4;
      u32 vs = c * 2048 + g * 16 + W_AP + kvh * 4096 + ng * 256 + (wave & 3) * 64, fb = fa;
      PIN(wa); PIN(vo); PIN(vl); PIN(vs); PIN(fb);
      weights_in();
      v4u a[4]; u32 ls;
      ls = ld1(rws, vl, 0);
#pragma unroll
      for (int j = 0; j < 4; ++j) a[j] = ld4(rws, vo + j * 1024, 0);
      __builtin_amdgcn_sched_barrier(0);
      v8bf W[16];
      if (wave >= 4) {
#pragma unroll
        for (int ks = 0; ks < 16; ++ks) W[ks] = trfrag(wb[ks >> 1] + (ks & 1) * 4096);
      }
#pragma nounroll
      for (int sp = 0; sp < SPIN_CAP; ++sp) {
        u32 mx = ls;
#pragma unroll
        for (int j = 0; j < 4; ++j) mx = max(max(mx, max(a[j].x, a[j].y)), max(a[j].z, a[j].w));
        if (__all(mx != SENT)) break;
        FENCE();
        ls = ld1(rws, vl, 0);
#pragma unroll
        for (int j = 0; j < 4; ++j) a[j] = ld4(rws, vo + j * 1024, 0);
      }
      float M = __uint_as_float(ls);
      M = fmaxf(M, dpp<0xB1>(M)); M = fmaxf(M, dpp<0x4E>(M)); M = fmaxf(M, dpp<0x141>(M)); M = fmaxf(M, dpp<0x128>(M));
      const float cf = ex2(__uint_as_float(ls) - M);
      const float inv = rcp(row_sum(cf));
      const float cj[4] = {dpp<0x150>(cf), dpp<0x151>(cf), dpp<0x152>(cf), dpp<0x153>(cf)};
      v2f o[4];
#pragma unroll
      for (int i = 0; i < 4; ++i) {
        o[i] = v2f{lo16(a[0][i]), bfhi(a[0][i])} * cj[0];
#pragma unroll
        for (int j = 1; j < 4; ++j) o[i] += v2f{lo16(a[j][i]), bfhi(a[j][i])} * cj[j];
      }
      *lp<u32>(wa) = pk2(fold16(fold32(o[0].x, o[2].x), fold32(o[1].x, o[3].x)) * inv, fold16(fold32(o[0].y, o[2].y), fold32(o[1].y, o[3].y)) * inv);
      bar();
      if (wave >= 4) {
        v8bf A[16];
#pragma unroll
        for (int ks = 0; ks < 16; ++ks) A[ks] = ldfrag(fb + ks * 64);
        __builtin_amdgcn_sched_barrier(0);
        v4f d0 = {0.f, 0.f, 0.f, 0.f};
#pragma unroll
        for (int ks = 0; ks < 16; ++ks) d0 = mfma(W[ks], A[ks], d0);
        v4u ov = {canon(d0.x), canon(d0.y), canon(d0.z), canon(d0.w)};
        if (c < 2) st4(ov, rws, vs, 0);
      }
    } else {
      const int b = wave >> 2, col = (wave & 3) * 128 + i32 * 4 + half * 2;
      u32 wb[8];
#pragma unroll
      for (int i = 0; i < 8; ++i) { wb[i] = wave >= 4 ? wslot(i) + waddr(wave & 3) : wslot(8 + 2 * wave + (i >> 2)) + waddr(i & 3); PIN(wb[i]); }
      u32 wa = L_ACT + b * ASTR + fpos(col) * 2, ra = L_RED + wave * 8 + half * 4, rb_ = L_RED + b * 32;
      u32 ha = L_H1 + (c & 1) * 128 + fpos((wave & 3) * 16 + g * 4 + ((c >> 1) & 3)) * 2, fb = wave >= 4 ? fa : L_H1 + (c & 1) * 128 + g * 16;
      u32 vo = half * 16384 + i32 * 16 + W_AP + b * 2048 + (wave & 3) * 512, vs = (c & 1) * 2048 + (c >> 1) * 64 + g * 16 + W_OP + yi * 4096 + wave * 512;
      PIN(wa); PIN(ra); PIN(rb_); PIN(ha); PIN(fb); PIN(vo); PIN(vs);
      v2f x0, ga, sb, cb1;
      {
        const u32 xv = *(const u32*)(xin + b * 512 + col);
        const u32 tag = ldsv(L_CTL + 4 * C_TAG);
        v4u mg[3];
#pragma nounroll
        for (int sp = 0; sp < SPIN_CAP; ++sp) {
#pragma unroll
          for (int i = 0; i < 3; ++i) mg[i] = ld4(rws, col * 8, W_MG + i * 4096);
          if (__all(((mg[0].y ^ tag) | (mg[0].w ^ tag) | (mg[1].y ^ tag) | (mg[1].w ^ tag) | (mg[2].y ^ tag) | (mg[2].w ^ tag)) == 0)) break;
          FENCE();
        }
        const int sh = 16 - 16 * b;
        x0 = v2f{lo16(xv), bfhi(xv)};
        ga = v2f{bfhi(mg[0].x << sh), bfhi(mg[0].z << sh)}; sb = v2f{bfhi(mg[1].x << sh), bfhi(mg[1].z << sh)};
        cb1 = rb2(v2f{bfhi(mg[2].x << sh), bfhi(mg[2].z << sh)} + 1.f);
      }
      PIN(x0); PIN(ga); PIN(sb); PIN(cb1);
      weights_in();
      v4u a[4];
#pragma unroll
      for (int j = 0; j < 4; ++j) a[j] = ld4(rws, vo, j * 4096);
      __builtin_amdgcn_sched_barrier(0);
      v8bf W[16];
#pragma unroll
      for (int j = 0; j < 16; ++j) W[j] = trfrag(wb[j >> 1] + (j & 1) * 4096);
#pragma nounroll
      for (int sp = 0; sp < SPIN_CAP; ++sp) {
        u32 mx = 0;
#pragma unroll
        for (int j = 0; j < 4; ++j) mx = max(max(mx, max(a[j].x, a[j].y)), max(a[j].z, a[j].w));
        if (__all(mx != SENT)) break;
        FENCE();
#pragma unroll
        for (int j = 0; j < 4; ++j) a[j] = ld4(rws, vo, j * 4096);
      }
      const v4f at = (__builtin_bit_cast(v4f, a[0]) + __builtin_bit_cast(v4f, a[1])) + (__builtin_bit_cast(v4f, a[2]) + __builtin_bit_cast(v4f, a[3]));
      const v2f x1 = rb2(x0 + rb2(ga * rb2(v2f{fold32(at.x, at.z), fold32(at.y, at.w)})));
      const float st = half_sum(fold32(x1.x + x1.y, fmaf(x1.x, x1.x, x1.y * x1.y)));
      if (i32 == 0) *lp<float>(ra) = st;
      bar();
      {
        const v4f r0 = *lp<v4f>(rb_), r1 = *lp<v4f>(rb_ + 16);
        const float mean = ((r0.x + r0.z) + (r1.x + r1.z)) * (1.f / 512.f);
        const float var = fmaxf(fmaf(-mean, mean, ((r0.y + r0.w) + (r1.y + r1.w)) * (1.f / 512.f)), 0.f);
        const v2f mv = rb2(v2f{mean, var});
        const float ri = rcp(rb(__builtin_amdgcn_sqrtf(rb(mv.y + 1e-5f))));
        const v2f hh = rb2(rb2(rb2(x1 - mv.x) * ri) * cb1) + sb;
        *lp<u32>(wa) = pk2(hh.x, hh.y);
      }
      bar();
      if (wave >= 4) {
        v8bf A[16];
#pragma unroll
        for (int ks = 0; ks < 16; ++ks) A[ks] = ldfrag(fb + ks * 64);
        __builtin_amdgcn_sched_barrier(0);
        v4f d0 = {0.f, 0.f, 0.f, 0.f};
#pragma unroll
        for (int ks = 0; ks < 16; ++ks) d0 = mfma(W[ks], A[ks], d0);
        float e0 = (c & 2) ? d0.y : d0.x, e1 = (c & 2) ? d0.w : d0.z;
        PIN(e0); PIN(e1);
        const float v = silu(rb((c & 4) ? e1 : e0));
        *lp<u16>(ha) = (u16)pk2(v, v);
      }
      bar();
      if (wave < 4) {
        const v8bf a0 = ldfrag(fb), a1 = ldfrag(fb + 64);
        v4f dd[8];
#pragma unroll
        for (int t = 0; t < 8; ++t) dd[t] = mfma(W[2 * t + 1], a1, mfma(W[2 * t], a0, v4f{0.f, 0.f, 0.f, 0.f}));
        v4f rv = dd[0];
#pragma unroll
        for (int t = 1; t < 8; ++t) if ((c >> 1) == t) rv = dd[t];
        v4u ov = {canon(rv.x), canon(rv.y), canon(rv.z), canon(rv.w)};
        st4(ov, rws, vs, 0);
        if (wave == 1 && yi == 27 && lane == 0) st2(v2u{(u32)E, (u32)(E >> 32)}, rws, 0, W_EPOCH);
      }
    }
  }
}

extern "C" void kernel_launch(void* const* d_in, const int* in_sizes, int n_in, void* d_out, int out_size, void* d_ws, size_t ws_size, hipStream_t stream) {
  hipLaunchKernelGGL(fused_block, dim3(256), dim3(512), 0, stream, (const u16*)d_in[1], (const u16*)d_in[2], (const u16*)d_in[9], (const u16*)d_in[3],
                     (const u16*)d_in[4], (const u16*)d_in[6], (const u16*)d_in[7], (const u16*)d_in[0], (const u16*)d_in[8], (u16*)d_out, (unsigned char*)d_ws);
}
```

```cpp
#include <hip/hip_runtime.h>

typedef unsigned int u32;
typedef unsigned long long u64;
typedef unsigned short u16;
typedef u32 v4u __attribute__((ext_vector_type(4)));
typedef u32 v2u __attribute__((ext_vector_type(2)));
typedef int v4i __attribute__((ext_vector_type(4)));
typedef float v4f __attribute__((ext_vector_type(4)));
typedef float v2f __attribute__((ext_vector_type(2)));
typedef short v4s __attribute__((ext_vector_type(4)));
typedef __bf16 v8bf __attribute__((ext_vector_type(8)));
typedef __bf16 v2bf __attribute__((ext_vector_type(2)));
#define LAS __attribute__((address_space(3)))
#define GAS __attribute__((address_space(1)))
#define DEVI __device__ __forceinline__

constexpr int NSLOT = 16, SLOT = 8192;
constexpr int NCONS = 6, NSTASH = 4, NF = 2, NMAX = 72;
#define WA 16
#define WB 20
#define WC 28
#define LA 25
#define LB 50
#define TEND 3705
#define LEADX 130
#define LEADX3 210
#define LEADY 190
#define COFF -20
#define W2W 24
#define HEAD 16
#define XW 5
constexpr int QAT = 12, HDELAY = 440, CODE_BYTES = 23296;
#define BLK_A 65, 65, 65, 63, 63, 63, 63, 63, 63, 63, 64, 64, 65, 65, 65, 65
constexpr int NBLK[2][16] = {{BLK_A}, {65, 65, 65, 63, 62, 62, 62, 62, 62, 62, 67, 67, 65, 65, 65, 65}};
constexpr int BIAS = 11;
constexpr u64 start_table(int k) {
  u64 t = 0; int s = 0;
  for (int r = 0; r < 16; ++r) { const int dl = s - 64 * r + BIAS; if (dl < 0 || dl > 15 || NBLK[k][r] > NMAX) return ~0ull; t |= (u64)dl << (4 * r); s += NBLK[k][r]; }
  return s == 1024 ? t : ~0ull;
}
constexpr u64 ST_A = start_table(0), ST_B = start_table(1);
static_assert(ST_A != ~0ull && ST_B != ~0ull, "block table");
constexpr u64 pace(int k, int r, int extra, int lead) { return ((TEND - lead) * 16 + NBLK[k][r]) / (2 * NBLK[k][r] + extra) - 384; }
constexpr u64 pace_table(int k) {
  return pace(k, 0, 0, LEADX) | pace(k, 3, 0, LEADX3) << 8 | pace(k, 3, 0, LEADX) << 16 | pace(k, 4, 8, 0) << 24 | pace(k, 8, 8, 0) << 32 |
         pace(k, 10, 0, k ? 0 : LEADY) << 40 | pace(k, 12, 2, 0) << 48;
}
constexpr u64 PACE_A = pace_table(0), PACE_B = pace_table(1);
constexpr int L_CTL = NSLOT * SLOT;
constexpr int L_Q = L_CTL + 256;
constexpr int L_ACT = L_Q + 1024;
constexpr int ASTR = 1024 + 64;
constexpr int L_MRG = L_ACT + 2 * ASTR;
constexpr int L_ML = L_MRG + 2048;
constexpr int L_SC = L_ML + 256;
constexpr int L_JUNK = L_SC + NMAX * 256;
constexpr int L_END = L_JUNK + 256;
constexpr int L_H1 = L_SC;
constexpr int L_RED = L_H1 + 256;
static_assert(L_END <= 160 * 1024, "LDS");
constexpr int C_FULL = 0, C_QRDY = 1, C_MCNT = 2, C_CLR = 3, C_ARDY = 4, C_TAG = 5, C_WRD = 6, C_MX = 7, C_SM = 8, C_T0 = 9, C_ISS = 10, C_VISS = 11, C_WIN = 12, C_FREE = 16;
constexpr u32 W_EPOCH = 0, W_T0 = 4096, W_SP = 8192, W_HG = W_SP + 4 * 1024 * 16, W_QG = W_HG + 512 * 8, W_MG = W_QG + 4096 * 8, W_PART = W_MG + 2048 * 8;
constexpr u32 W_LSE = W_PART + 64 * 4096;
constexpr u32 W_AP = W_LSE + 256 * 128, W_OP = W_AP + 8 * 4096, W_END = W_OP + 28 * 4096;
constexpr u32 CLRB = (W_END - W_PART) / 64;
static_assert(CLRB * 64 == W_END - W_PART && CLRB % 16 == 0, "clear split");
constexpr u32 SENT = 0xFFFFFFFFu;
constexpr int SPIN_CAP = 1 << 16;

DEVI float lo16(u32 v) { return __uint_as_float(v << 16); }
DEVI float bfhi(u32 v) { return __uint_as_float(v & 0xffff0000u); }
DEVI u32 pk2(float a, float b) { v2f x = {a, b}; return __builtin_bit_cast(u32, __builtin_convertvector(x, v2bf)); }
DEVI float rb(float a) { return lo16(pk2(a, a)); }
DEVI float ex2(float x) { return __builtin_amdgcn_exp2f(x); }
DEVI float rcp(float x) { return __builtin_amdgcn_rcpf(x); }
DEVI float silu(float v) { return v * rcp(1.f + ex2(v * -1.4426950408889634f)); }
DEVI float silu_exact(float v) {
  const float t = fminf(v * -1.4426950216293335f, 126.f);
  const float r = fmaf(v, -1.92596299112662e-08f, fmaf(v, -1.4426950216293335f, -t));
  float e = ex2(t);
  e = fmaf(e, fminf(r, 1.f) * 0.6931471805599453f, e);
  const float d = 1.f + e, rc = rcp(d), q = v * rc;
  return fmaf(fmaf(-q, d, v), rc, q);
}
DEVI v2f rb2(v2f a) { const u32 p = pk2(a.x, a.y); return v2f{lo16(p), bfhi(p)}; }
DEVI float fold32(float a, float b) { auto s = __builtin_amdgcn_permlane32_swap(__float_as_uint(a), __float_as_uint(b), false, false); return __uint_as_float(s[0]) + __uint_as_float(s[1]); }
DEVI float fold16(float a, float b) { auto s = __builtin_amdgcn_permlane16_swap(__float_as_uint(a), __float_as_uint(b), false, false); return __uint_as_float(s[0]) + __uint_as_float(s[1]); }
#define PIN(x) asm volatile("" : "+v"(x))
DEVI float swap16_add(float x) { auto s = __builtin_amdgcn_permlane16_swap(__float_as_uint(x), __float_as_uint(x), false, false); return __uint_as_float(s[0]) + __uint_as_float(s[1]); }
DEVI float swap32_add(float x) { auto s = __builtin_amdgcn_permlane32_swap(__float_as_uint(x), __float_as_uint(x), false, false); return __uint_as_float(s[0]) + __uint_as_float(s[1]); }
DEVI float swap16_max(float x) { auto s = __builtin_amdgcn_permlane16_swap(__float_as_uint(x), __float_as_uint(x), false, false); return fmaxf(__uint_as_float(s[0]), __uint_as_float(s[1])); }
DEVI float swap32_max(float x) { auto s = __builtin_amdgcn_permlane32_swap(__float_as_uint(x), __float_as_uint(x), false, false); return fmaxf(__uint_as_float(s[0]), __uint_as_float(s[1])); }
template <int CTRL> DEVI float dpp(float x) { return __builtin_bit_cast(float, __builtin_amdgcn_mov_dpp(__builtin_bit_cast(int, x), CTRL, 0xf, 0xf, true)); }
DEVI float row_sum(float x) { x += dpp<0xB1>(x); x += dpp<0x4E>(x); x += dpp<0x141>(x); x += dpp<0x128>(x); return x; }
DEVI float half_sum(float x) { return swap16_add(row_sum(x)); }
DEVI float wave_sum(float x) { return swap32_add(half_sum(x)); }
template <typename T> DEVI LAS T* lp(u32 a) { return (LAS T*)(size_t)a; }
DEVI u32 ldsv(u32 a) { return *(volatile LAS u32*)(size_t)a; }
DEVI void stsv(u32 a, u32 v) { *(volatile LAS u32*)(size_t)a = v; }
#define FENCE() asm volatile("" ::: "memory")
#define WAITV(n) asm volatile("s_waitcnt vmcnt(%0)" ::"i"(n) : "memory")
#define WAITL() asm volatile("s_waitcnt lgkmcnt(0)" ::: "memory")
DEVI void bar() { WAITL(); __builtin_amdgcn_s_barrier(); FENCE(); }
typedef __amdgpu_buffer_rsrc_t rsrc_t;
DEVI v4u ld4(rsrc_t r, u32 vo, u32 so) { return __builtin_bit_cast(v4u, __builtin_amdgcn_raw_buffer_load_b128(r, vo, so, 16)); }
DEVI v2u ld2(rsrc_t r, u32 vo, u32 so) { return __builtin_bit_cast(v2u, __builtin_amdgcn_raw_buffer_load_b64(r, vo, so, 16)); }
DEVI u32 ld1(rsrc_t r, u32 vo, u32 so) { return __builtin_amdgcn_raw_buffer_load_b32(r, vo, so, 16); }
DEVI void st4(v4u v, rsrc_t r, u32 vo, u32 so) { __builtin_amdgcn_raw_buffer_store_b128(v, r, vo, so, 16); }
DEVI void st2(v2u v, rsrc_t r, u32 vo, u32 so) { __builtin_amdgcn_raw_buffer_store_b64(v, r, vo, so, 16); }
DEVI void st1(u32 v, rsrc_t r, u32 vo, u32 so) { __builtin_amdgcn_raw_buffer_store_b32(v, r, vo, so, 16); }
DEVI u32 canon(float f) { u32 b = __float_as_uint(f); return b < 0xFFFFFFFEu ? b : 0xFFFFFFFEu; }
DEVI v8bf trfrag(u32 a) {
  v4s lo = __builtin_amdgcn_ds_read_tr16_b64_v4i16(lp<v4s>(a)), hi = __builtin_amdgcn_ds_read_tr16_b64_v4i16(lp<v4s>(a + 2048));
  return __builtin_bit_cast(v8bf, __builtin_shufflevector(lo, hi, 0, 1, 2, 3, 4, 5, 6, 7));
}
DEVI v4f mfma(v8bf a, v8bf b, v4f c) { return __builtin_amdgcn_mfma_f32_16x16x32_bf16(a, b, c, 0, 0, 0); }
DEVI v8bf ldfrag(u32 a) { return __builtin_bit_cast(v8bf, *lp<v4u>(a)); }
DEVI int fpos(int k) { return (k & ~31) + ((k >> 2) & 3) * 8 + ((k >> 4) & 1) * 4 + (k & 3); }
DEVI v4i mksrd(const void* p, u32 bytes) { u64 a = (u64)p; return v4i{(int)(u32)a, (int)(u32)(a >> 32), (int)bytes, 0x00020000}; }

#define DMA4(m, v0, v1, v2, v3, srd, so)                                                                                   \
  asm volatile("s_mov_b32 m0, %0\n\ts_nop 0\n\tbuffer_load_dwordx4 %1, %5, %6 offen nt lds\n\t"                           \
               "buffer_load_dwordx4 %2, %5, %6 offen offset:1024 nt lds\n\tbuffer_load_dwordx4 %3, %5, %6 offen offset:2048 nt lds\n\t" \
               "buffer_load_dwordx4 %4, %5, %6 offen offset:3072 nt lds" ::"s"(m), "v"(v0), "v"(v1), "v"(v2), "v"(v3), "s"(srd), "s"(so) : "memory")

__global__ void __launch_bounds__(512, 2)
fused_block(const u16* __restrict__ Kc, const u16* __restrict__ Vc, const u16* __restrict__ wmod, const u16* __restrict__ wq,
            const u16* __restrict__ wo, const u16* __restrict__ w1, const u16* __restrict__ w2, const u16* __restrict__ xin,
            const u16* __restrict__ cin, u16* __restrict__ out, unsigned char* ws) {
  __shared__ __attribute__((aligned(16))) unsigned char smem[L_END];
  asm volatile("" ::"s"((u32)(size_t)(LAS unsigned char*)smem));
  const int tid = threadIdx.x, lane = tid & 63, wave = __builtin_amdgcn_readfirstlane(tid >> 6);
  if (wave == 1 && blockIdx.x < 32) {
    u64 pc; asm volatile("s_getpc_b64 %0" : "=s"(pc));
    const v4i rc = mksrd((const void*)(pc & ~127ull), CODE_BYTES);
    const u32 vo = lane * 128, m = L_JUNK, s1 = 8192, s2 = 16384;
    asm volatile("s_mov_b32 m0, %0\n\ts_nop 0\n\tbuffer_load_dword %1, %2, 0 offen lds\n\tbuffer_load_dword %1, %2, %3 offen lds\n\tbuffer_load_dword %1, %2, %4 offen lds"
                 ::"s"(m), "v"(vo), "s"(rc), "s"(s1), "s"(s2) : "memory");
  }
  const int bid = (blockIdx.x & ~31) | ((blockIdx.x & 7) << 2) | ((blockIdx.x >> 3) & 3);
  const int grp = bid >> 4, r = bid & 15;
  const int g = lane >> 4, c = lane & 15;
  const u64 stt = (grp < 14 ? ST_A : ST_B) >> (4 * r);
  const int kv0 = 64 * r + ((int)stt & 15) - BIAS;
  const bool isS = r >= 12;
  if (__builtin_expect(wave == 0, 1)) {
    const v4i srd = isS ? mksrd(wmod, 512 * 6144) : mksrd(Kc, 1u << 27);
    const u32 k0 = g * 256 + ((c ^ g) << 4), w0 = (lane >> 3) * 6144 + (((lane & 7) ^ ((lane >> 3) & 6)) << 4);
    const u32 a0 = isS ? w0 : k0, a1 = isS ? w0 + 48128 : k0 ^ 64, a2 = isS ? w0 + 2 * 48128 : k0 ^ 128, a3 = isS ? w0 + 3 * 48128 : k0 ^ 192;
    const u32 so = isS ? (r - 12) * 128 * 6144 + grp * 128 : (u32)(grp * 1024 + kv0) << 13, hs = isS ? 32 * 6144 : 4096;
#pragma unroll
    for (int i = 0; i < 4; ++i) { const u32 m = i * 4096, s1 = so + i * hs; DMA4(m, a0, a1, a2, a3, srd, s1); }
  }
  if (wave == 2) { stsv(L_CTL + 4 * lane, 0); WAITL(); }
  __builtin_amdgcn_s_barrier(); FENCE();
  const u64 tstart = __builtin_amdgcn_s_memrealtime();
  FENCE();
  const bool isX = r < 4, isQ = r >= 4 && r < 8, isM = r == 8 || r == 9, isY = (r == 10 || r == 11) && grp < 14, isR = r == 0 && (grp & 1);
  const int xi = grp * 4 + r, qi = grp * 4 + r - 4, mi = grp * 2 + r - 8, yi = grp * 2 + r - 10;
  const int u0 = (int)(0x2222008888880000ull >> (4 * r)) & 15;
  const int nb = 64 + (r == 15 ? BIAS : (int)(stt >> 4) & 15) - ((int)stt & 15);
  const int nf = isS ? 0 : NF;
  const int ntail = isX ? 8 : isY ? 16 : 0;
  const int uv = u0 + nb, uw = uv + nb;
  u64 E = 0;
  auto arrive = [&](int w) { if (lane == 0) __hip_atomic_fetch_add(lp<u32>(L_CTL + 4 * w), 1u, __ATOMIC_RELAXED, __HIP_MEMORY_SCOPE_WORKGROUP); };
  int wv = wave;
  asm volatile("" : "+s"(wv));
  if (__builtin_expect(wv == 0, 1)) {
    __builtin_amdgcn_s_setprio(3);
    int iss, pub = 0;
    auto publish = [&](int v) { pub = max(pub, v); stsv(L_CTL + 4 * C_FULL, pub); };
#define PACE(W_) do { WAITV(W_); publish(iss - (W_)); } while (0)
    const u32 cls = ((u32)(0x6666554433331000ull >> (4 * r)) & 7) + (r == 3 && (grp & 1));
    const u32 tau = 384 + ((u32)((grp < 14 ? PACE_A : PACE_B) >> (8 * cls)) & 255);
    u32 due = ((u32)tstart + COFF) * 16 + NF * tau;
    const u32 wrow = lane >> 3, wlc = ((lane & 7) ^ (((wrow >> 1) & 3) << 1)) << 4;
    int u = NF;
#pragma nounroll
    for (int seg = u0 == 8 ? 0 : 1; seg < (ntail ? 4 : 3); ++seg) {
      const bool kv = seg == 1 || seg == 2;
      const u16* base = seg == 0 ? (isQ ? wq : wmod) : seg == 1 ? Kc : seg == 2 ? Vc : isX ? wo : w1;
      const u32 rs = seg == 0 ? (isQ ? 8192 : 6144) : (seg == 3 && !isX) ? 3584 : 1024;
      const v4i srd = mksrd(base, kv ? 1u << 27 : (seg == 3 && isX) ? 4096 * 1024 : 512 * rs);
      u32 so = kv ? (u32)(grp * 1024 + kv0 + (seg == 1 ? nf : 0)) << 13
                  : seg == 0 ? (isQ ? qi : 16 + mi) * 128
                             : isX ? ((xi >> 3) << 19) + (xi & 7) * 128 : yi * 128;
      const u32 hstep = kv ? 4096 : 32 * rs, ustep = 2 * hstep;
      const u32 sw = g << (seg - 1), sx = seg == 1 ? 4 : 8;
      const u32 w0 = wrow * rs + wlc, wd = 8 * rs - 1024;
      const u32 a0 = kv ? g * 256 + ((c ^ sw) << 4) : w0, a1 = kv ? g * 256 + ((c ^ sw ^ sx) << 4) : w0 + wd;
      const u32 a2 = kv ? g * 256 + ((c ^ sw ^ ((2 * sx) & 15)) << 4) : w0 + 2 * wd, a3 = kv ? g * 256 + ((c ^ sw ^ ((3 * sx) & 15)) << 4) : w0 + 3 * wd;
      const int ue = seg == 0 ? NF + u0 : seg == 1 ? uv : seg == 2 ? uw : uw + (ntail >> 1);
#pragma nounroll
      while (u < ue) {
        const bool paced = u >= 4 && seg < 3;
        const int stop = min(ue, u < 4 ? 4 : u < 48 ? 48 : 1 << 20);
        u32 dd = paced ? due : due - (1u << 28);
        const int u_in = u, la = (seg == 3 && isX) ? 0x7fffffff : LA * 16;
        u32 t0, t1, lag; u32 vf, vt;
#define DMAS(so) "buffer_load_dwordx4 %[a0], %[srd], " so " offen nt lds\n\tbuffer_load_dwordx4 %[a1], %[srd], " so " offen offset:1024 nt lds\n\t" \
                 "buffer_load_dwordx4 %[a2], %[srd], " so " offen offset:2048 nt lds\n\tbuffer_load_dwordx4 %[a3], %[srd], " so " offen offset:3072 nt lds\n\t"
#define RDFREE "s_lshl_b32 %[t1], %[u], 2\n\ts_and_b32 %[t1], %[t1], 60\n\ts_or_b32 %[t1], %[t1], %[fb]\n\tv_mov_b32 %[vf], %[t1]\n\tds_read_b32 %[vf], %[vf]\n\t"
#define REST(W) "s_waitcnt vmcnt(" #W ")\n\ts_mov_b32 m0, %[t0]\n\ts_lshl_b32 %[t0], %[u], 3\n\t" DMAS("%[t1]")                                   \
                "s_sub_i32 %[t0], %[t0], " #W " - 4\n\ts_max_i32 %[pub], %[pub], %[t0]\n\tv_mov_b32 %[vt], %[pub]\n\tds_write_b32 %[va], %[vt]\n\t"                                      \
                "s_add_i32 %[u], %[u], 1\n\ts_add_u32 %[so], %[so], %[us]\n\ts_add_u32 %[dd], %[dd], %[tau]\n\t" RDFREE                             \
                "s_memrealtime s[98:99]\n\ts_cmp_lt_i32 %[u], %[stop]\n\ts_waitcnt vmcnt(" #W ")\n\ts_cbranch_scc1 1b\n\ts_branch 9f\n"
#define XREST(W) REST(W)
        asm volatile(RDFREE "s_memrealtime s[98:99]\n"
                     "1:\n\ts_sub_i32 %[t1], %[u], 15\n\ts_waitcnt lgkmcnt(0)\n\tv_cmp_gt_i32 vcc, %[t1], %[vf]\n\ts_lshl_b32 s98, s98, 4\n\t"
                     "s_cbranch_vccnz 8f\n"
                     "2:\n\ts_sub_i32 %[lag], s98, %[dd]\n\ts_cmp_lt_i32 %[lag], 0\n\ts_cbranch_scc1 7f\n\t"
                     "s_lshl_b32 %[t0], %[u], 13\n\ts_and_b32 %[t0], %[t0], 0x1e000\n\ts_mov_b32 m0, %[t0]\n\ts_add_u32 %[t1], %[so], %[hs]\n\t" DMAS("%[so]")
                     "s_or_b32 %[t0], %[t0], 0x1000\n\ts_cmp_lt_i32 %[lag], %[la]\n\ts_cbranch_scc1 4f\n\ts_cmp_lt_i32 %[lag], %[lb]\n\ts_cbranch_scc1 5f\n\t"
                     XREST(WC) "4:\n\t" XREST(WA) "5:\n\t" XREST(WB)
                     "8:\n\t" RDFREE "s_sub_i32 %[t1], %[u], 15\n\ts_waitcnt lgkmcnt(0)\n\tv_cmp_gt_i32 vcc, %[t1], %[vf]\n\ts_nop 0\n\ts_cbranch_vccz 7f\n\t"
                     "s_waitcnt vmcnt(0)\n\ts_lshl_b32 %[pub], %[u], 3\n\tv_mov_b32 %[vt], %[pub]\n\tds_write_b32 %[va], %[vt]\n"
                     "6:\n\t" RDFREE "s_sub_i32 %[t1], %[u], 15\n\ts_waitcnt lgkmcnt(0)\n\tv_cmp_gt_i32 vcc, %[t1], %[vf]\n\ts_sleep 1\n\ts_cbranch_vccnz 6b\n"
                     "7:\n\ts_memrealtime s[98:99]\n\ts_waitcnt lgkmcnt(0)\n\ts_lshl_b32 s98, s98, 4\n\ts_branch 2b\n"
                     "9:\n\ts_waitcnt lgkmcnt(0)"
                     : [u] "+s"(u), [so] "+s"(so), [dd] "+s"(dd), [pub] "+s"(pub), [t0] "=&s"(t0), [t1] "=&s"(t1), [lag] "=&s"(lag), [vf] "=&v"(vf), [vt] "=&v"(vt)
                     : [a0] "v"(a0), [a1] "v"(a1), [a2] "v"(a2), [a3] "v"(a3), [srd] "s"(srd), [hs] "s"(hstep), [us] "s"(ustep), [stop] "s"(stop),
                       [va] "v"(L_CTL + 4 * C_FULL), [fb] "i"(L_CTL + 4 * C_FREE), [tau] "s"(tau), [la] "s"(la), [lb] "i"(LB * 16)
                     : "memory", "vcc", "scc", "s98", "s99");
        due += (u - u_in) * tau;
        if (u == 4 && isS) { WAITV(16); publish(16); }
        if (u == uw) stsv(L_CTL + 4 * C_VISS, 1);
        if (u == 48) { const u32 t = __builtin_amdgcn_readfirstlane(ldsv(L_CTL + 4 * C_T0)); if (t) due = t * 16 + u * tau; }
      }
    }
    iss = 8 * u;
    if (!ntail) { PACE(24); PACE(16); PACE(8); PACE(0); }
    else arrive(C_ISS);
    __builtin_amdgcn_s_setprio(0);
  } else {
    const rsrc_t rws = __builtin_amdgcn_make_buffer_rsrc(ws, 0, W_END, 0x00020000);
    auto wait_full = [&](u32 need) { while (ldsv(L_CTL + 4 * C_FULL) < need) __builtin_amdgcn_s_sleep(1); FENCE(); };
    const u32 trow = 4 * g + (c >> 2);
    auto waddr = [&](int t) -> u32 { return trow * 128 + ((((u32)(t ^ ((trow >> 1) & 3)) << 1) | ((c >> 1) & 1)) << 4) + (c & 1) * 8; };

    if (wave == 1) {
      const u64 E0 = __hip_atomic_load((GAS u64*)(ws + W_EPOCH), __ATOMIC_RELAXED, __HIP_MEMORY_SCOPE_AGENT);
      u32 tag;
#define CHAIN_START() do { E = E0 + 1; tag = (u32)E; } while (0)
      auto silu4 = [&](u32 x, u32 y) { return v2u{pk2(silu_exact(lo16(x)), silu_exact(bfhi(x))), pk2(silu_exact(lo16(y)), silu_exact(bfhi(y)))}; };
      if (isS) {
        const v2u cv = *(const v2u*)(cin + (lane >> 5) * 512 + (r - 12) * 128 + (lane & 31) * 4);
        CHAIN_START();
        *lp<v2u>(L_ACT + (lane >> 5) * ASTR + fpos((lane & 31) * 4) * 2) = silu4(cv.x, cv.y);
        FENCE();
        v8bf act[4];
#pragma unroll
        for (int ks = 0; ks < 4; ++ks) { act[ks] = ldfrag(L_ACT + (c & 1) * ASTR + g * 16 + ks * 64); PIN(act[ks]); }
        wait_full(16);
        v8bf W[16];
#pragma unroll
        for (int j = 0; j < 16; ++j) W[j] = trfrag(waddr(j & 3) + (j >> 2) * 4096);
        FENCE(); stsv(L_CTL + 4 * C_FREE, 1); stsv(L_CTL + 4 * (C_FREE + 1), 2);
        v4f acc[4];
#pragma unroll
        for (int t = 0; t < 4; ++t) acc[t] = v4f{0.f, 0.f, 0.f, 0.f};
#pragma unroll
        for (int j = 0; j < 16; ++j) acc[j & 3] = mfma(act[j >> 2], W[j], acc[j & 3]);
        const float r0 = g == 0 ? acc[0][0] : g == 1 ? acc[1][0] : g == 2 ? acc[2][0] : acc[3][0];
        const float r1 = g == 0 ? acc[0][1] : g == 1 ? acc[1][1] : g == 2 ? acc[2][1] : acc[3][1];
        while (ldsv(L_CTL + 4 * C_CLR) == 0) { }
        FENCE();
        st4(v4u{__float_as_uint(r0), tag, __float_as_uint(r1), tag}, rws, lane * 16, W_SP + ((r - 12) * 1024 + grp * 64) * 16);
      } else if (isR) {
        const v4u in0 = *(const v4u*)(xin + lane * 8), in1 = *(const v4u*)(xin + 512 + lane * 8);
        u32 xs0 = *(const u32*)(xin + (grp >> 1) * 64 + (lane & ~1)), xs1 = *(const u32*)(xin + 512 + (grp >> 1) * 64 + (lane & ~1));
        CHAIN_START();
        PIN(xs0); PIN(xs1);
        xs0 >>= 16 * (lane & 1); xs1 >>= 16 * (lane & 1);
        float mr[2], ri[2];
#pragma unroll
        for (int b = 0; b < 2; ++b) {
          const v4u xv = b ? in1 : in0;
          const float x[8] = {lo16(xv.x), bfhi(xv.x), lo16(xv.y), bfhi(xv.y), lo16(xv.z), bfhi(xv.z), lo16(xv.w), bfhi(xv.w)};
          float s = 0.f;
#pragma unroll
          for (int i = 0; i < 8; ++i) s += x[i];
          const float mean = wave_sum(s) * (1.f / 512.f);
          float q2 = 0.f;
#pragma unroll
          for (int i = 0; i < 8; ++i) q2 += (x[i] - mean) * (x[i] - mean);
          const float var = wave_sum(q2) * (1.f / 512.f);
          mr[b] = rb(mean); ri[b] = rcp(rb(__builtin_amdgcn_sqrtf(rb(rb(var) + 1e-5f))));
        }
        v2f y = rb2(rb2(v2f{lo16(xs0) - mr[0], lo16(xs1) - mr[1]}) * v2f{ri[0], ri[1]});
        PIN(y);
        v4u a[8];
#pragma nounroll
        for (int sp = 0; sp < SPIN_CAP; ++sp) {
          u32 bad = 0;
#pragma unroll
          for (int i = 0; i < 8; ++i) { a[i] = ld4(rws, lane * 16, W_SP + ((i & 3) * 1024 + (i >> 2) * 512 + (grp >> 1) * 64) * 16); bad |= (a[i].y ^ tag) | (a[i].w ^ tag); }
          if (__all(bad == 0)) break;
          FENCE();
        }
        v2f sc[2];
#pragma unroll
        for (int pt = 0; pt < 2; ++pt) {
          sc[pt] = v2f{0.f, 0.f};
#pragma unroll
          for (int k4 = 0; k4 < 4; ++k4) sc[pt] += v2f{__uint_as_float(a[pt * 4 + k4].x), __uint_as_float(a[pt * 4 + k4].z)};
        }
        const v2f h = rb2(y * rb2(rb2(sc[1]) + 1.f)) + rb2(sc[0]);
        st2(v2u{pk2(h.x, h.y), tag}, rws, lane * 8, W_HG + (grp >> 1) * 512);
      } else if (isM || isQ) {
        if (isM) {
          const v4u in0 = *(const v4u*)(cin + lane * 8), in1 = *(const v4u*)(cin + 512 + lane * 8);
          CHAIN_START();
          *lp<v2u>(L_ACT + fpos(lane * 8) * 2) = silu4(in0.x, in0.y); *lp<v2u>(L_ACT + fpos(lane * 8 + 4) * 2) = silu4(in0.z, in0.w);
          *lp<v2u>(L_ACT + ASTR + fpos(lane * 8) * 2) = silu4(in1.x, in1.y); *lp<v2u>(L_ACT + ASTR + fpos(lane * 8 + 4) * 2) = silu4(in1.z, in1.w);
        } else {
          CHAIN_START();
          while (__builtin_amdgcn_s_memrealtime() < tstart + HDELAY) __builtin_amdgcn_s_sleep(4);
          v4u a[4];
#pragma nounroll
          for (int sp = 0; sp < SPIN_CAP; ++sp) {
            u32 bad = 0;
#pragma unroll
            for (int i = 0; i < 4; ++i) { a[i] = ld4(rws, lane * 16 + i * 1024, W_HG); bad |= (a[i].y ^ tag) | (a[i].w ^ tag); }
            if (__all(bad == 0)) break;
            FENCE();
          }
#pragma unroll
          for (int i = 0; i < 4; ++i) {
            const u32 wa = L_ACT + fpos(i * 128 + lane * 2) * 2;
            *lp<u32>(wa) = (a[i].x & 0xffffu) | (a[i].z << 16); *lp<u32>(wa + ASTR) = (a[i].x >> 16) | (a[i].z & 0xffff0000u);
          }
        }
        FENCE(); stsv(L_CTL + 4 * C_TAG, tag); stsv(L_CTL + 4 * C_ARDY, 1);
      } else CHAIN_START();
      if (blockIdx.x < 8 && lane == 0) st2(v2u{(u32)tstart, tag}, rws, blockIdx.x * 8, W_T0);
      {
        wait_full(8 * u0 + 16 * QAT);
        v4u a[2];
#pragma nounroll
        for (int sp = 0; sp < SPIN_CAP; ++sp) {
          a[0] = ld4(rws, lane * 16, W_QG + grp * 2048); a[1] = ld4(rws, lane * 16 + 1024, W_QG + grp * 2048);
          if (__all(((a[0].y ^ tag) | (a[0].w ^ tag) | (a[1].y ^ tag) | (a[1].w ^ tag)) == 0)) break;
          __builtin_amdgcn_s_sleep(16); FENCE();
        }
        *lp<v2u>(L_Q + lane * 8) = v2u{a[0].x, a[0].z}; *lp<v2u>(L_Q + 512 + lane * 8) = v2u{a[1].x, a[1].z};
        FENCE(); stsv(L_CTL + 4 * C_TAG, tag); stsv(L_CTL + 4 * C_QRDY, 1);
        {
          const v2u t = ld2(rws, (lane & 7) * 8, W_T0);
          if (__all(t.y == tag)) {
            int dt = (int)(t.x - (u32)tstart);
            dt += __builtin_amdgcn_mov_dpp(dt, 0xB1, 0xf, 0xf, true); dt += __builtin_amdgcn_mov_dpp(dt, 0x4E, 0xf, 0xf, true); dt += __builtin_amdgcn_mov_dpp(dt, 0x141, 0xf, 0xf, true);
            if (dt > -2400 && dt < 2400) stsv(L_CTL + 4 * C_T0, ((u32)tstart + (dt >> 3) + COFF) | 1);
          }
        }
      }
      if (ntail) {
        const v4i srd = mksrd(isX ? wo : w2, isX ? 4096 * 1024 : 1792 * 1024);
        const u32 a0 = (lane >> 3) * 1024 + (((lane & 7) ^ ((lane >> 3) & 6)) << 4), a1 = a0 + 7168, a2 = a0 + 2 * 7168, a3 = a0 + 3 * 7168;
        u32 so = isX ? ((xi >> 3) << 19) + (xi & 7) * 128 + 256 * 1024 : yi << 16;
        while (ldsv(L_CTL + 4 * C_VISS) == 0) __builtin_amdgcn_s_sleep(2);
#pragma nounroll
        for (int u = uw + (ntail >> 1); u < uw + ntail; ++u, so += isX ? 65536 : 128) {
          while ((int)ldsv(L_CTL + 4 * (C_FREE + (u & (NSLOT - 1)))) < u + 1 - NSLOT) __builtin_amdgcn_s_sleep(1);
          const u32 lb = (u & (NSLOT - 1)) * SLOT, m = lb + 4096, s1 = so + 32768;
          DMA4(lb, a0, a1, a2, a3, srd, so);
          DMA4(m, a0, a1, a2, a3, srd, s1);
          WAITV(W2W);
        }
        arrive(C_ISS);
      }
      if (r == 8) {
        wait_full(8 * uw);
        const int zb = grp >> 3, zc = (grp & 7) * 64 + c * 4 + g;
        const u32 xv = xin[zb * 512 + zc];
        v2u m0, m1;
#pragma nounroll
        for (int sp = 0; sp < SPIN_CAP; ++sp) {
          m0 = ld2(rws, zc * 8, W_MG); m1 = ld2(rws, zc * 8, W_MG + 3 * 4096);
          if (__all(((m0.y ^ tag) | (m1.y ^ tag)) == 0)) break;
          FENCE();
        }
        v4u z[7];
#pragma nounroll
        for (int sp = 0; sp < SPIN_CAP; ++sp) {
          z[0] = ld4(rws, g * 4096 + c * 16, W_AP + grp * 256); z[1] = ld4(rws, g * 4096 + c * 16, W_AP + 16384 + grp * 256);
          if (__all(max(max(max(z[0].x, z[0].y), max(z[0].z, z[0].w)), max(max(z[1].x, z[1].y), max(z[1].z, z[1].w))) != SENT)) break;
          __builtin_amdgcn_s_sleep(2); FENCE();
        }
        v4f s0 = __builtin_bit_cast(v4f, z[0]), s1 = __builtin_bit_cast(v4f, z[1]);
        const float at = fold32(fold16(s0.x, s0.y), fold16(s0.z, s0.w)) + fold32(fold16(s1.x, s1.y), fold16(s1.z, s1.w));
        float x1 = rb(lo16(xv) + rb(bfhi(m0.x << (16 - 16 * zb)) * rb(at))), gb = bfhi(m1.x << (16 - 16 * zb));
        GAS u16* op = (GAS u16*)out + zb * 512 + zc;
        PIN(x1); PIN(gb); PIN(op);
#pragma nounroll
        for (int sp = 0; sp < SPIN_CAP; ++sp) {
#pragma unroll
          for (int j = 0; j < 7; ++j) z[j] = ld4(rws, g * 4096 + c * 16, W_OP + j * 16384 + grp * 256);
          u32 mx = 0;
#pragma unroll
          for (int j = 0; j < 7; ++j) mx = max(mx, max(max(z[j].x, z[j].y), max(z[j].z, z[j].w)));
          if (__all(mx != SENT)) break;
          FENCE();
        }
        const v4f s = ((__builtin_bit_cast(v4f, z[0]) + __builtin_bit_cast(v4f, z[1])) + (__builtin_bit_cast(v4f, z[2]) + __builtin_bit_cast(v4f, z[3]))) +
                      ((__builtin_bit_cast(v4f, z[4]) + __builtin_bit_cast(v4f, z[5])) + __builtin_bit_cast(v4f, z[6]));
        const float rr = x1 + rb(gb * rb(fold16(fold32(s.x, s.z), fold32(s.y, s.w))));
        *op = (u16)pk2(rr, rr);
        __builtin_amdgcn_endpgm();
      }
    } else {
      if (isS && wave == 7) {
        const v4u s4 = {SENT, SENT, SENT, SENT};
        const u32 so = W_PART + (grp * 4 + r - 12) * CLRB;
#pragma unroll
        for (int i = 0; i < CLRB / 1024; ++i) st4(s4, rws, lane * 16 + (i & 3) * 1024, so + (i >> 2) * 4096);
        if (lane < (CLRB % 1024) / 16) st4(s4, rws, lane * 16, so + CLRB / 1024 * 1024);
        WAITV(0); stsv(L_CTL + 4 * C_CLR, 1);
      }

      if (wave == 6) { *lp<v4f>(L_MRG + lane * 16) = v4f{0.f, 0.f, 0.f, 0.f}; *lp<v4f>(L_MRG + 1024 + lane * 16) = v4f{0.f, 0.f, 0.f, 0.f}; }
      const int d = c >> 2, cw = wave - 2;
      const u32 sca = L_SC + (g * 4 + (c & 3)) * 16;
      {
        u32 ka[4];
#pragma unroll
        for (int s = 0; s < 4; ++s) ka[s] = c * 256 + (((4 * s + g) ^ c) << 4);
        v8bf qf[4];
        float mx = -INFINITY;
        auto rdk = [&](v8bf (&kf)[8], int j) {
          const int u = j < nf ? j : u0 + j;
          const u32 lb = (u & (NSLOT - 1)) * SLOT;
          wait_full(8 * u + 8);
#pragma unroll
          for (int s = 0; s < 4; ++s) { kf[s] = ldfrag(lb + ka[s]); kf[4 + s] = ldfrag(lb + ka[s] + 4096); }
          FENCE(); stsv(L_CTL + 4 * (C_FREE + (u & (NSLOT - 1))), u + 1);
        };
        auto qk = [&](v8bf (&kf)[8], int j) {
          v4f S0 = {0.f, 0.f, 0.f, 0.f}, S1 = {0.f, 0.f, 0.f, 0.f};
#pragma unroll
          for (int s = 0; s < 4; ++s) { S0 = mfma(kf[s], qf[s], S0); S1 = mfma(kf[4 + s], qf[s], S1); }
          const v2f a = rb2(v2f{S0.x, S0.y}) * 0.08838834764831845f, b = rb2(v2f{S0.z, S0.w}) * 0.08838834764831845f;
          const v2f e = rb2(v2f{S1.x, S1.y}) * 0.08838834764831845f, f = rb2(v2f{S1.z, S1.w}) * 0.08838834764831845f;
          mx = fmaxf(fmaxf(fmaxf(mx, fmaxf(a.x, a.y)), fmaxf(b.x, b.y)), fmaxf(fmaxf(e.x, e.y), fmaxf(f.x, f.y)));
          if (c < 4) *lp<v4u>(sca + j * 256) = v4u{pk2(a.x, a.y), pk2(b.x, b.y), pk2(e.x, e.y), pk2(f.x, f.y)};
        };
        v8bf sk[NSTASH][8];
        rdk(sk[0], cw);
      if (u0 == 8 && wave < 6) {
        wait_full(8 * NF + 64);
        v8bf W[16];
#pragma unroll
        for (int ks = 0; ks < 16; ++ks) W[ks] = trfrag(NF * SLOT + ks * 4096 + waddr(wave - 2));
        WAITL();
        u32 old = 0;
        if (lane == 0) old = __hip_atomic_fetch_add(lp<u32>(L_CTL + 4 * C_WRD), 1u, __ATOMIC_RELAXED, __HIP_MEMORY_SCOPE_WORKGROUP);
        if (__builtin_amdgcn_readfirstlane(old) == 3 && lane < 8) stsv(L_CTL + 4 * (C_FREE + NF + lane), NF + lane + 1);
        while (ldsv(L_CTL + 4 * C_ARDY) == 0) __builtin_amdgcn_s_sleep(1);
        FENCE();
        const u32 tag = ldsv(L_CTL + 4 * C_TAG);
        v8bf A[16];
#pragma unroll
        for (int ks = 0; ks < 16; ++ks) A[ks] = ldfrag(L_ACT + (c & 1) * ASTR + g * 16 + ks * 64);
        __builtin_amdgcn_sched_barrier(0);
        v4f d0 = {0.f, 0.f, 0.f, 0.f}, d1 = {0.f, 0.f, 0.f, 0.f};
#pragma unroll
        for (int ks = 0; ks < 16; ks += 2) { d0 = mfma(A[ks], W[ks], d0); d1 = mfma(A[ks + 1], W[ks + 1], d1); }
        const float r0 = d0[0] + d1[0], r1 = d0[1] + d1[1];
        const float n0 = dpp<0xB1>(r0), n1 = dpp<0xB1>(r1);
        const bool odd = lane & 1;
        if (lane < 16) {
          if (isM) st2(v2u{pk2(r0, r1), tag}, rws, lane * 8, W_MG + mi * 512 + (wave - 2) * 128);
          else st2(v2u{pk2(odd ? n1 : r0, odd ? r1 : n0), tag}, rws, (lane & 1) * 16384 + (lane >> 1) * 8, W_QG + qi * 256 + (wave - 2) * 64);
        }
      }
#pragma unroll
        for (int i = 1; i < NSTASH; ++i) rdk(sk[i], cw + i * NCONS);
        while (ldsv(L_CTL + 4 * C_QRDY) == 0) __builtin_amdgcn_s_sleep(2);
        FENCE();
#pragma unroll
        for (int s = 0; s < 4; ++s) qf[s] = ldfrag(L_Q + (c & 3) * 256 + (4 * s + g) * 16);
#pragma unroll
        for (int i = 0; i < NSTASH; ++i) qk(sk[i], cw + i * NCONS);
#pragma nounroll
        for (int j = cw + NSTASH * NCONS; j < nb; j += NCONS) { v8bf kf[8]; rdk(kf, j); qk(kf, j); }
        mx = swap32_max(swap16_max(rb(mx)));
        if (lane < 4) *lp<float>(L_ML + cw * 16 + lane * 4) = mx;
        FENCE();
        if (lane == 0) __hip_atomic_fetch_add(lp<u32>(L_CTL + 4 * C_MX), 1u, __ATOMIC_RELAXED, __HIP_MEMORY_SCOPE_WORKGROUP);
      }
      float M2;
      {
        while (ldsv(L_CTL + 4 * C_MX) < NCONS) __builtin_amdgcn_s_sleep(1);
        FENCE();
        float M = *lp<float>(L_ML + (c & 3) * 4);
#pragma unroll
        for (int w = 1; w < NCONS; ++w) M = fmaxf(M, *lp<float>(L_ML + w * 16 + (c & 3) * 4));
        M2 = M * 1.4426950408889634f;
        float l = 0.f;
#pragma nounroll
        for (int j = cw + d * NCONS; j < nb; j += 4 * NCONS) {
          const v4u s = *lp<v4u>(sca + j * 256);
          float p[8];
#pragma unroll
          for (int i = 0; i < 4; ++i) { p[2 * i] = ex2(fmaf(lo16(s[i]), 1.4426950408889634f, -M2)); p[2 * i + 1] = ex2(fmaf(bfhi(s[i]), 1.4426950408889634f, -M2)); }
          l += ((p[0] + p[1]) + (p[2] + p[3])) + ((p[4] + p[5]) + (p[6] + p[7]));
          *lp<v4u>(sca + j * 256) = v4u{pk2(p[0], p[1]), pk2(p[2], p[3]), pk2(p[4], p[5]), pk2(p[6], p[7])};
        }
        l += dpp<0x124>(l); l += dpp<0x128>(l);
        l = swap32_add(swap16_add(l));
        if (lane < 4) *lp<float>(L_ML + 128 + cw * 16 + lane * 4) = l;
        FENCE();
        if (lane == 0) __hip_atomic_fetch_add(lp<u32>(L_CTL + 4 * C_SM), 1u, __ATOMIC_RELAXED, __HIP_MEMORY_SCOPE_WORKGROUP);
      }
      {
        u32 va[8];
#pragma unroll
        for (int t = 0; t < 8; ++t) va[t] = trow * 256 + ((((u32)(t ^ (trow & 7)) << 1) | ((c >> 1) & 1)) << 4) + (c & 1) * 8;
        v4f acc[8];
#pragma unroll
        for (int t = 0; t < 8; ++t) acc[t] = v4f{0.f, 0.f, 0.f, 0.f};
        int j = cw;
#pragma nounroll
        for (; j + NCONS < nb; j += NCONS) {
          const int u = uv + j;
          const u32 lb = (u & (NSLOT - 1)) * SLOT;
          wait_full(8 * u + 8);
          const v8bf pf = ldfrag(sca + j * 256);
          v8bf vf[8];
#pragma unroll
          for (int t = 0; t < 8; ++t) {
            v4s lo = __builtin_amdgcn_ds_read_tr16_b64_v4i16(lp<v4s>(lb + va[t])), hi = __builtin_amdgcn_ds_read_tr16_b64_v4i16(lp<v4s>(lb + va[t] + 4096));
            vf[t] = __builtin_bit_cast(v8bf, __builtin_shufflevector(lo, hi, 0, 1, 2, 3, 4, 5, 6, 7));
          }
          FENCE(); stsv(L_CTL + 4 * (C_FREE + (u & (NSLOT - 1))), u + 1);
#pragma unroll
          for (int t = 0; t < 8; ++t) acc[t] = mfma(vf[t], pf, acc[t]);
        }
        const int link = j - (nb - NCONS), u = uv + j;
        const u32 lb = (u & (NSLOT - 1)) * SLOT;
        v4f e0 = d == 0 ? acc[0] : d == 1 ? acc[2] : d == 2 ? acc[4] : acc[6];
        v4f e1 = d == 0 ? acc[1] : d == 1 ? acc[3] : d == 2 ? acc[5] : acc[7];
        const v4u pf = *lp<v4u>(sca + j * 256);
        v4u pm[4];
#pragma unroll
        for (int i = 0; i < 4; ++i) { pm[i] = d == i ? pf : v4u{0u, 0u, 0u, 0u}; PIN(pm[i]); }
        float inv = 1.f;
        if (link == NCONS - 1) {
          while (ldsv(L_CTL + 4 * C_SM) < NCONS) { }
          FENCE();
          float L = *lp<float>(L_ML + 128 + (c & 3) * 4);
#pragma unroll
          for (int w = 1; w < NCONS; ++w) L += *lp<float>(L_ML + 128 + w * 16 + (c & 3) * 4);
          inv = rcp(L);
          M2 += __builtin_amdgcn_logf(L);
        }
        u32 lse = canon(M2);
        PIN(lse);
        u32 oa = (c & 3) * 4096 + d * 64 + g * 16, ra = L_MRG + lane * 16;
        u32 vb[8];
#pragma unroll
        for (int t = 0; t < 8; ++t) { vb[t] = lb + va[t]; PIN(vb[t]); }
        PIN(e0); PIN(e1); PIN(oa); PIN(ra); PIN(inv);
        if (link == NCONS - 1) {
          while (ldsv(L_CTL + 4 * C_FULL) < (u32)(8 * u + 8 - HEAD)) { }
          if (lane < 4) st1(lse, rws, lane * 4, W_LSE + bid * 128);
        }
        while (ldsv(L_CTL + 4 * C_FULL) < (u32)(8 * u + 8)) { }
        FENCE();
        v8bf vf[8];
#pragma unroll
        for (int t = 0; t < 8; ++t) {
          v4s lo = __builtin_amdgcn_ds_read_tr16_b64_v4i16(lp<v4s>(vb[t])), hi = __builtin_amdgcn_ds_read_tr16_b64_v4i16(lp<v4s>(vb[t] + 4096));
          vf[t] = __builtin_bit_cast(v8bf, __builtin_shufflevector(lo, hi, 0, 1, 2, 3, 4, 5, 6, 7));
        }
        __builtin_amdgcn_sched_barrier(0);
#pragma unroll
        for (int i = 0; i < 4; ++i) { e0 = mfma(vf[2 * i], __builtin_bit_cast(v8bf, pm[i]), e0); e1 = mfma(vf[2 * i + 1], __builtin_bit_cast(v8bf, pm[i]), e1); }
        __builtin_amdgcn_sched_barrier(0);
        v4f r0, r1;
        for (;;) {
          const u32 cnt = ldsv(L_CTL + 4 * C_MCNT);
          FENCE();
          r0 = *lp<v4f>(ra); r1 = *lp<v4f>(ra + 1024);
          FENCE();
          if (cnt >= (u32)link) break;
        }
        e0 = (e0 + r0) * inv; e1 = (e1 + r1) * inv;
        if (link == NCONS - 1) {
          st4(v4u{min(pk2(e0.x, e0.y), SENT - 1), min(pk2(e0.z, e0.w), SENT - 1), min(pk2(e1.x, e1.y), SENT - 1), min(pk2(e1.z, e1.w), SENT - 1)}, rws, oa, W_PART + grp * 16384 + r * 256);
        } else {
          *lp<v4f>(ra) = e0; *lp<v4f>(ra + 1024) = e1;
          FENCE(); stsv(L_CTL + 4 * C_MCNT, link + 1);
        }
        stsv(L_CTL + 4 * (C_FREE + (u & (NSLOT - 1))), u + 1);
      }
    }
  }
  if (!(isX || isY)) return;
  const u32 trow = 4 * g + (c >> 2);
  auto waddr = [&](int t) -> u32 { return trow * 128 + ((((u32)(t ^ ((trow >> 1) & 3)) << 1) | ((c >> 1) & 1)) << 4) + (c & 1) * 8; };
  auto wslot = [&](int t) -> u32 { return ((uw + t) & (NSLOT - 1)) * SLOT; };
  const int half = lane >> 5, i32 = lane & 31;
  const u32 fa = L_ACT + (c & 1) * ASTR + g * 16;
  {
    const rsrc_t rws = __builtin_amdgcn_make_buffer_rsrc(ws, 0, W_END, 0x00020000);
    auto weights_done = [&]() { WAITV(0); if (wave == 0) stsv(L_CTL + 4 * C_FULL, 8 * (uw + ntail)); arrive(C_WIN); };
    auto weights_in = [&]() { while (ldsv(L_CTL + 4 * C_WIN) < 2) __builtin_amdgcn_s_sleep(1); FENCE(); };
    if (isX) {
      const int kvh = xi >> 3, ng = xi & 7;
      const int b = wave >> 2, h = wave & 3;
      u32 wa = L_ACT + b * ASTR + (h * 128 + c * 8 + g * 2) * 2, wb[8];
#pragma unroll
      for (int i = 0; i < 8; ++i) { wb[i] = wslot(i) + waddr(wave & 3); PIN(wb[i]); }
      u32 vo = lane * 16 + W_PART + (b * 8 + kvh) * 16384 + h * 4096;
      u32 vl = ((c & 3) * 4 + ((g + (c >> 2)) & 3)) * 128 + W_LSE + (b * 8 + kvh) * 2048 + h * 4;
      u32 vs = c * 2048 + g * 16 + W_AP + kvh * 4096 + ng * 256 + (wave & 3) * 64, fb = fa;
      PIN(wa); PIN(vo); PIN(vl); PIN(vs); PIN(fb);
      while (ldsv(L_CTL + 4 * C_ISS) < 2) __builtin_amdgcn_s_sleep(1);
      FENCE();
      u32 ls = ld1(rws, vl, 0);
      if (wave < 2) weights_done();
      if (!__all(ls != SENT)) {
#pragma nounroll
        for (int sp = 0; sp < SPIN_CAP; ++sp) {
          __builtin_amdgcn_s_sleep(1); FENCE();
          ls = ld1(rws, vl, 0);
          if (__all(ls != SENT)) break;
        }
        __builtin_amdgcn_s_sleep(XW);
      }
      FENCE();
      v4u a[4];
#pragma unroll
      for (int j = 0; j < 4; ++j) a[j] = ld4(rws, vo + j * 1024, 0);
      __builtin_amdgcn_sched_barrier(0);
      v8bf W[16];
      if (wave >= 4) {
        weights_in();
#pragma unroll
        for (int ks = 0; ks < 16; ++ks) W[ks] = trfrag(wb[ks >> 1] + (ks & 1) * 4096);
      }
      float M = __uint_as_float(ls);
      M = fmaxf(M, dpp<0xB1>(M)); M = fmaxf(M, dpp<0x4E>(M)); M = fmaxf(M, dpp<0x141>(M)); M = fmaxf(M, dpp<0x128>(M));
      float cf = ex2(__uint_as_float(ls) - M);
      cf *= rcp(row_sum(cf));
      float cj[4] = {dpp<0x150>(cf), dpp<0x151>(cf), dpp<0x152>(cf), dpp<0x153>(cf)};
      PIN(cj[0]); PIN(cj[1]); PIN(cj[2]); PIN(cj[3]);
#pragma nounroll
      for (int sp = 0; sp < SPIN_CAP; ++sp) {
        u32 mx = 0;
#pragma unroll
        for (int j = 0; j < 4; ++j) mx = max(max(mx, max(a[j].x, a[j].y)), max(a[j].z, a[j].w));
        if (__all(mx != SENT)) break;
        FENCE();
#pragma unroll
        for (int j = 0; j < 4; ++j) a[j] = ld4(rws, vo + j * 1024, 0);
      }
      v2f o[4];
#pragma unroll
      for (int i = 0; i < 4; ++i) {
        o[i] = v2f{lo16(a[0][i]), bfhi(a[0][i])} * cj[0];
#pragma unroll
        for (int j = 1; j < 4; ++j) o[i] += v2f{lo16(a[j][i]), bfhi(a[j][i])} * cj[j];
      }
      *lp<u32>(wa) = pk2(fold16(fold32(o[0].x, o[2].x), fold32(o[1].x, o[3].x)), fold16(fold32(o[0].y, o[2].y), fold32(o[1].y, o[3].y)));
      bar();
      if (wave >= 4) {
        v8bf A[16];
#pragma unroll
        for (int ks = 0; ks < 16; ++ks) A[ks] = ldfrag(fb + ks * 64);
        __builtin_amdgcn_sched_barrier(0);
        v4f d0 = {0.f, 0.f, 0.f, 0.f};
#pragma unroll
        for (int ks = 0; ks < 16; ++ks) d0 = mfma(W[ks], A[ks], d0);
        v4u ov = {canon(d0.x), canon(d0.y), canon(d0.z), canon(d0.w)};
        if (c < 2) st4(ov, rws, vs, 0);
      }
    } else {
      const int b = wave >> 2, col = (wave & 3) * 128 + i32 * 4 + half * 2;
      u32 wb[8];
#pragma unroll
      for (int i = 0; i < 8; ++i) { wb[i] = wave >= 4 ? wslot(i) + waddr(wave & 3) : wslot(8 + 2 * wave + (i >> 2)) + waddr(i & 3); PIN(wb[i]); }
      u32 wa = L_ACT + b * ASTR + fpos(col) * 2, ra = L_RED + wave * 8 + half * 4, rb_ = L_RED + b * 32;
      u32 ha = L_H1 + (c & 1) * 128 + fpos((wave & 3) * 16 + g * 4 + ((c >> 1) & 3)) * 2, fb = wave >= 4 ? fa : L_H1 + (c & 1) * 128 + g * 16;
      u32 vo = half * 16384 + i32 * 16 + W_AP + b * 2048 + (wave & 3) * 512, vs = (c & 1) * 2048 + (c >> 1) * 64 + g * 16 + W_OP + yi * 4096 + wave * 512;
      PIN(wa); PIN(ra); PIN(rb_); PIN(ha); PIN(fb); PIN(vo); PIN(vs);
      v2f x0, ga, sb, cb1;
      {
        const u32 xv = *(const u32*)(xin + b * 512 + col);
        const u32 tag = ldsv(L_CTL + 4 * C_TAG);
        v4u mg[3];
#pragma nounroll
        for (int sp = 0; sp < SPIN_CAP; ++sp) {
#pragma unroll
          for (int i = 0; i < 3; ++i) mg[i] = ld4(rws, col * 8, W_MG + i * 4096);
          if (__all(((mg[0].y ^ tag) | (mg[0].w ^ tag) | (mg[1].y ^ tag) | (mg[1].w ^ tag) | (mg[2].y ^ tag) | (mg[2].w ^ tag)) == 0)) break;
          FENCE();
        }
        const int sh = 16 - 16 * b;
        x0 = v2f{lo16(xv), bfhi(xv)};
        ga = v2f{bfhi(mg[0].x << sh), bfhi(mg[0].z << sh)}; sb = v2f{bfhi(mg[1].x << sh), bfhi(mg[1].z << sh)};
        cb1 = rb2(v2f{bfhi(mg[2].x << sh), bfhi(mg[2].z << sh)} + 1.f);
      }
      PIN(x0); PIN(ga); PIN(sb); PIN(cb1);
      if (wave < 2) weights_done();
      weights_in();
      v4u a[4];
#pragma unroll
      for (int j = 0; j < 4; ++j) a[j] = ld4(rws, vo, j * 4096);
      __builtin_amdgcn_sched_barrier(0);
      v8bf W[16];
#pragma unroll
      for (int j = 0; j < 16; ++j) W[j] = trfrag(wb[j >> 1] + (j & 1) * 4096);
#pragma nounroll
      for (int sp = 0; sp < SPIN_CAP; ++sp) {
        u32 mx = 0;
#pragma unroll
        for (int j = 0; j < 4; ++j) mx = max(max(mx, max(a[j].x, a[j].y)), max(a[j].z, a[j].w));
        if (__all(mx != SENT)) break;
        FENCE();
#pragma unroll
        for (int j = 0; j < 4; ++j) a[j] = ld4(rws, vo, j * 4096);
      }
      const v4f at = (__builtin_bit_cast(v4f, a[0]) + __builtin_bit_cast(v4f, a[1])) + (__builtin_bit_cast(v4f, a[2]) + __builtin_bit_cast(v4f, a[3]));
      const v2f x1 = rb2(x0 + rb2(ga * rb2(v2f{fold32(at.x, at.z), fold32(at.y, at.w)})));
      const float st = half_sum(fold32(x1.x + x1.y, fmaf(x1.x, x1.x, x1.y * x1.y)));
      if (i32 == 0) *lp<float>(ra) = st;
      bar();
      {
        const v4f r0 = *lp<v4f>(rb_), r1 = *lp<v4f>(rb_ + 16);
        const float mean = ((r0.x + r0.z) + (r1.x + r1.z)) * (1.f / 512.f);
        const float var = fmaxf(fmaf(-mean, mean, ((r0.y + r0.w) + (r1.y + r1.w)) * (1.f / 512.f)), 0.f);
        const v2f mv = rb2(v2f{mean, var});
        const float ri = rcp(rb(__builtin_amdgcn_sqrtf(rb(mv.y + 1e-5f))));
        const v2f hh = rb2(rb2(rb2(x1 - mv.x) * ri) * cb1) + sb;
        *lp<u32>(wa) = pk2(hh.x, hh.y);
      }
      bar();
      if (wave >= 4) {
        v8bf A[16];
#pragma unroll
        for (int ks = 0; ks < 16; ++ks) A[ks] = ldfrag(fb + ks * 64);
        __builtin_amdgcn_sched_barrier(0);
        v4f d0 = {0.f, 0.f, 0.f, 0.f};
#pragma unroll
        for (int ks = 0; ks < 16; ++ks) d0 = mfma(W[ks], A[ks], d0);
        float e0 = (c & 2) ? d0.y : d0.x, e1 = (c & 2) ? d0.w : d0.z;
        PIN(e0); PIN(e1);
        const float v = silu(rb((c & 4) ? e1 : e0));
        *lp<u16>(ha) = (u16)pk2(v, v);
      }
      bar();
      if (wave < 4) {
        const v8bf a0 = ldfrag(fb), a1 = ldfrag(fb + 64);
        v4f dd[8];
#pragma unroll
        for (int t = 0; t < 8; ++t) dd[t] = mfma(W[2 * t + 1], a1, mfma(W[2 * t], a0, v4f{0.f, 0.f, 0.f, 0.f}));
        v4f rv = dd[0];
#pragma unroll
        for (int t = 1; t < 8; ++t) if ((c >> 1) == t) rv = dd[t];
        v4u ov = {canon(rv.x), canon(rv.y), canon(rv.z), canon(rv.w)};
        st4(ov, rws, vs, 0);
        if (wave == 1 && yi == 27 && lane == 0) st2(v2u{(u32)E, (u32)(E >> 32)}, rws, 0, W_EPOCH);
      }
    }
  }
}

extern "C" void kernel_launch(void* const* d_in, const int* in_sizes, int n_in, void* d_out, int out_size, void* d_ws, size_t ws_size, hipStream_t stream) {
  hipLaunchKernelGGL(fused_block, dim3(256), dim3(512), 0, stream, (const u16*)d_in[1], (const u16*)d_in[2], (const u16*)d_in[9], (const u16*)d_in[3],
                     (const u16*)d_in[4], (const u16*)d_in[6], (const u16*)d_in[7], (const u16*)d_in[0], (const u16*)d_in[8], (u16*)d_out, (unsigned char*)d_ws);
}
```

```cpp
#include <hip/hip_runtime.h>

typedef unsigned int u32;
typedef unsigned long long u64;
typedef unsigned short u16;
typedef u32 v4u __attribute__((ext_vector_type(4)));
typedef u32 v2u __attribute__((ext_vector_type(2)));
typedef int v4i __attribute__((ext_vector_type(4)));
typedef float v4f __attribute__((ext_vector_type(4)));
typedef float v2f __attribute__((ext_vector_type(2)));
typedef short v4s __attribute__((ext_vector_type(4)));
typedef __bf16 v8bf __attribute__((ext_vector_type(8)));
typedef __bf16 v2bf __attribute__((ext_vector_type(2)));
#define LAS __attribute__((address_space(3)))
#define GAS __attribute__((address_space(1)))
#define DEVI __device__ __forceinline__

constexpr int NSLOT = 16, SLOT = 8192;
constexpr int NCONS = 6, NSTASH = 4, NF = 2, NMAX = 72;
#define WA 16
#define WB 20
#define WC 24
#define WD 28
#define LA 25
#define LB 60
#define LC 120
#define TEND 3705
#define LEADX 130
#define LEADX3 210
#define LEADY 190
#define COFF -20
#define W2W 24
#define HEAD 16
#define XW 5
constexpr int QAT = 12, HDELAY = 440, CODE_BYTES = 23552;
#define BLK_A 65, 65, 65, 63, 63, 63, 63, 63, 63, 63, 64, 64, 65, 65, 65, 65
constexpr int NBLK[2][16] = {{BLK_A}, {65, 65, 65, 63, 62, 62, 62, 62, 62, 62, 67, 67, 65, 65, 65, 65}};
constexpr int BIAS = 11;
constexpr u64 start_table(int k) {
  u64 t = 0; int s = 0;
  for (int r = 0; r < 16; ++r) { const int dl = s - 64 * r + BIAS; if (dl < 0 || dl > 15 || NBLK[k][r] > NMAX) return ~0ull; t |= (u64)dl << (4 * r); s += NBLK[k][r]; }
  return s == 1024 ? t : ~0ull;
}
constexpr u64 ST_A = start_table(0), ST_B = start_table(1);
static_assert(ST_A != ~0ull && ST_B != ~0ull, "block table");
constexpr u64 pace(int k, int r, int extra, int lead) { return ((TEND - lead) * 16 + NBLK[k][r]) / (2 * NBLK[k][r] + extra) - 384; }
constexpr u64 pace_table(int k) {
  return pace(k, 0, 0, LEADX) | pace(k, 3, 0, LEADX3) << 8 | pace(k, 3, 0, LEADX) << 16 | pace(k, 4, 8, 0) << 24 | pace(k, 8, 8, 0) << 32 |
         pace(k, 10, 0, k ? 0 : LEADY) << 40 | pace(k, 12, 2, 0) << 48;
}
constexpr u64 PACE_A = pace_table(0), PACE_B = pace_table(1);
constexpr int L_CTL = NSLOT * SLOT;
constexpr int L_Q = L_CTL + 256;
constexpr int L_ACT = L_Q + 1024;
constexpr int ASTR = 1024 + 64;
constexpr int L_MRG = L_ACT + 2 * ASTR;
constexpr int L_ML = L_MRG + 2048;
constexpr int L_SC = L_ML + 256;
constexpr int L_JUNK = L_SC + NMAX * 256;
constexpr int L_END = L_JUNK + 256;
constexpr int L_H1 = L_SC;
constexpr int L_RED = L_H1 + 256;
static_assert(L_END <= 160 * 1024, "LDS");
constexpr int C_FULL = 0, C_QRDY = 1, C_MCNT = 2, C_CLR = 3, C_ARDY = 4, C_TAG = 5, C_WRD = 6, C_MX = 7, C_SM = 8, C_T0 = 9, C_ISS = 10, C_VISS = 11, C_WIN = 12, C_FREE = 16;
constexpr u32 W_EPOCH = 0, W_T0 = 4096, W_SP = 8192, W_HG = W_SP + 4 * 1024 * 16, W_QG = W_HG + 512 * 8, W_MG = W_QG + 4096 * 8, W_PART = W_MG + 2048 * 8;
constexpr u32 W_ML = W_PART + 64 * 4096;
constexpr u32 W_AP = W_ML + 256 * 128, W_OP = W_AP + 8 * 4096, W_END = W_OP + 28 * 4096;
constexpr u32 CLRB = (W_END - W_PART) / 64;
static_assert(CLRB * 64 == W_END - W_PART && CLRB % 16 == 0, "clear split");
constexpr u32 SENT = 0xFFFFFFFFu;
constexpr int SPIN_CAP = 1 << 16;

DEVI float lo16(u32 v) { return __uint_as_float(v << 16); }
DEVI float bfhi(u32 v) { return __uint_as_float(v & 0xffff0000u); }
DEVI u32 pk2(float a, float b) { v2f x = {a, b}; return __builtin_bit_cast(u32, __builtin_convertvector(x, v2bf)); }
DEVI float rb(float a) { return lo16(pk2(a, a)); }
DEVI float ex2(float x) { return __builtin_amdgcn_exp2f(x); }
DEVI float rcp(float x) { return __builtin_amdgcn_rcpf(x); }
DEVI float silu(float v) { return v * rcp(1.f + ex2(v * -1.4426950408889634f)); }
DEVI float silu_exact(float v) {
  const float t = fminf(v * -1.4426950216293335f, 126.f);
  const float r = fmaf(v, -1.92596299112662e-08f, fmaf(v, -1.4426950216293335f, -t));
  float e = ex2(t);
  e = fmaf(e, fminf(r, 1.f) * 0.6931471805599453f, e);
  const float d = 1.f + e, rc = rcp(d), q = v * rc;
  return fmaf(fmaf(-q, d, v), rc, q);
}
DEVI v2f rb2(v2f a) { const u32 p = pk2(a.x, a.y); return v2f{lo16(p), bfhi(p)}; }
DEVI float fold32(float a, float b) { auto s = __builtin_amdgcn_permlane32_swap(__float_as_uint(a), __float_as_uint(b), false, false); return __uint_as_float(s[0]) + __uint_as_float(s[1]); }
DEVI float fold16(float a, float b) { auto s = __builtin_amdgcn_permlane16_swap(__float_as_uint(a), __float_as_uint(b), false, false); return __uint_as_float(s[0]) + __uint_as_float(s[1]); }
#define PIN(x) asm volatile("" : "+v"(x))
DEVI float swap16_add(float x) { auto s = __builtin_amdgcn_permlane16_swap(__float_as_uint(x), __float_as_uint(x), false, false); return __uint_as_float(s[0]) + __uint_as_float(s[1]); }
DEVI float swap32_add(float x) { auto s = __builtin_amdgcn_permlane32_swap(__float_as_uint(x), __float_as_uint(x), false, false); return __uint_as_float(s[0]) + __uint_as_float(s[1]); }
DEVI float swap16_max(float x) { auto s = __builtin_amdgcn_permlane16_swap(__float_as_uint(x), __float_as_uint(x), false, false); return fmaxf(__uint_as_float(s[0]), __uint_as_float(s[1])); }
DEVI float swap32_max(float x) { auto s = __builtin_amdgcn_permlane32_swap(__float_as_uint(x), __float_as_uint(x), false, false); return fmaxf(__uint_as_float(s[0]), __uint_as_float(s[1])); }
template <int CTRL> DEVI float dpp(float x) { return __builtin_bit_cast(float, __builtin_amdgcn_mov_dpp(__builtin_bit_cast(int, x), CTRL, 0xf, 0xf, true)); }
DEVI float row_sum(float x) { x += dpp<0xB1>(x); x += dpp<0x4E>(x); x += dpp<0x141>(x); x += dpp<0x128>(x); return x; }
DEVI float half_sum(float x) { return swap16_add(row_sum(x)); }
DEVI float wave_sum(float x) { return swap32_add(half_sum(x)); }
template <typename T> DEVI LAS T* lp(u32 a) { return (LAS T*)(size_t)a; }
DEVI u32 ldsv(u32 a) { return *(volatile LAS u32*)(size_t)a; }
DEVI void stsv(u32 a, u32 v) { *(volatile LAS u32*)(size_t)a = v; }
#define FENCE() asm volatile("" ::: "memory")
#define WAITV(n) asm volatile("s_waitcnt vmcnt(%0)" ::"i"(n) : "memory")
#define WAITL() asm volatile("s_waitcnt lgkmcnt(0)" ::: "memory")
DEVI void bar() { WAITL(); __builtin_amdgcn_s_barrier(); FENCE(); }
typedef __amdgpu_buffer_rsrc_t rsrc_t;
DEVI v4u ld4(rsrc_t r, u32 vo, u32 so) { return __builtin_bit_cast(v4u, __builtin_amdgcn_raw_buffer_load_b128(r, vo, so, 16)); }
DEVI v2u ld2(rsrc_t r, u32 vo, u32 so) { return __builtin_bit_cast(v2u, __builtin_amdgcn_raw_buffer_load_b64(r, vo, so, 16)); }
DEVI void st4(v4u v, rsrc_t r, u32 vo, u32 so) { __builtin_amdgcn_raw_buffer_store_b128(v, r, vo, so, 16); }
DEVI void st2(v2u v, rsrc_t r, u32 vo, u32 so) { __builtin_amdgcn_raw_buffer_store_b64(v, r, vo, so, 16); }
DEVI u32 canon(float f) { u32 b = __float_as_uint(f); return b < 0xFFFFFFFEu ? b : 0xFFFFFFFEu; }
DEVI v8bf trfrag(u32 a) {
  v4s lo = __builtin_amdgcn_ds_read_tr16_b64_v4i16(lp<v4s>(a)), hi = __builtin_amdgcn_ds_read_tr16_b64_v4i16(lp<v4s>(a + 2048));
  return __builtin_bit_cast(v8bf, __builtin_shufflevector(lo, hi, 0, 1, 2, 3, 4, 5, 6, 7));
}
DEVI v4f mfma(v8bf a, v8bf b, v4f c) { return __builtin_amdgcn_mfma_f32_16x16x32_bf16(a, b, c, 0, 0, 0); }
DEVI v8bf ldfrag(u32 a) { return __builtin_bit_cast(v8bf, *lp<v4u>(a)); }
DEVI int fpos(int k) { return (k & ~31) + ((k >> 2) & 3) * 8 + ((k >> 4) & 1) * 4 + (k & 3); }
DEVI v4i mksrd(const void* p, u32 bytes) { u64 a = (u64)p; return v4i{(int)(u32)a, (int)(u32)(a >> 32), (int)bytes, 0x00020000}; }

#define DMA4(m, v0, v1, v2, v3, srd, so)                                                                                   \
  asm volatile("s_mov_b32 m0, %0\n\ts_nop 0\n\tbuffer_load_dwordx4 %1, %5, %6 offen nt lds\n\t"                           \
               "buffer_load_dwordx4 %2, %5, %6 offen offset:1024 nt lds\n\tbuffer_load_dwordx4 %3, %5, %6 offen offset:2048 nt lds\n\t" \
               "buffer_load_dwordx4 %4, %5, %6 offen offset:3072 nt lds" ::"s"(m), "v"(v0), "v"(v1), "v"(v2), "v"(v3), "s"(srd), "s"(so) : "memory")

__global__ void __launch_bounds__(512, 2) __attribute__((aligned(4096)))
fused_block(const u16* __restrict__ Kc, const u16* __restrict__ Vc, const u16* __restrict__ wmod, const u16* __restrict__ wq,
            const u16* __restrict__ wo, const u16* __restrict__ w1, const u16* __restrict__ w2, const u16* __restrict__ xin,
            const u16* __restrict__ cin, u16* __restrict__ out, unsigned char* ws) {
  __shared__ __attribute__((aligned(16))) unsigned char smem[L_END];
  asm volatile("" ::"s"((u32)(size_t)(LAS unsigned char*)smem));
  const int tid = threadIdx.x, lane = tid & 63, wave = __builtin_amdgcn_readfirstlane(tid >> 6);
  if (wave == 1 && blockIdx.x < 32) {
    u64 pc; asm volatile("s_getpc_b64 %0" : "=s"(pc));
    const v4i rc = mksrd((const void*)(pc & ~127ull), CODE_BYTES);
    const u32 vo = lane * 128, m = L_JUNK, s1 = 8192, s2 = 16384;
    asm volatile("s_mov_b32 m0, %0\n\ts_nop 0\n\tbuffer_load_dword %1, %2, 0 offen lds\n\tbuffer_load_dword %1, %2, %3 offen lds\n\tbuffer_load_dword %1, %2, %4 offen lds"
                 ::"s"(m), "v"(vo), "s"(rc), "s"(s1), "s"(s2) : "memory");
  }
  const int bid = (blockIdx.x & ~31) | ((blockIdx.x & 7) << 2) | ((blockIdx.x >> 3) & 3);
  const int grp = bid >> 4, r = bid & 15;
  const int g = lane >> 4, c = lane & 15;
  const u64 stt = (grp < 14 ? ST_A : ST_B) >> (4 * r);
  const int kv0 = 64 * r + ((int)stt & 15) - BIAS;
  const bool isS = r >= 12;
  if (__builtin_expect(wave == 0, 1)) {
    const v4i srd = isS ? mksrd(wmod, 512 * 6144) : mksrd(Kc, 1u << 27);
    const u32 k0 = g * 256 + ((c ^ g) << 4), w0 = (lane >> 3) * 6144 + (((lane & 7) ^ ((lane >> 3) & 6)) << 4);
    const u32 a0 = isS ? w0 : k0, a1 = isS ? w0 + 48128 : k0 ^ 64, a2 = isS ? w0 + 2 * 48128 : k0 ^ 128, a3 = isS ? w0 + 3 * 48128 : k0 ^ 192;
    const u32 so = isS ? (r - 12) * 128 * 6144 + grp * 128 : (u32)(grp * 1024 + kv0) << 13, hs = isS ? 32 * 6144 : 4096;
#pragma unroll
    for (int i = 0; i < 4; ++i) { const u32 m = i * 4096, s1 = so + i * hs; DMA4(m, a0, a1, a2, a3, srd, s1); }
  }
  if (wave == 2) { stsv(L_CTL + 4 * lane, 0); WAITL(); }
  __builtin_amdgcn_s_barrier(); FENCE();
  const u64 tstart = __builtin_amdgcn_s_memrealtime();
  FENCE();
  const bool isX = r < 4, isQ = r >= 4 && r < 8, isM = r == 8 || r == 9, isY = (r == 10 || r == 11) && grp < 14, isR = r == 0 && (grp & 1);
  const int xi = grp * 4 + r, qi = grp * 4 + r - 4, mi = grp * 2 + r - 8, yi = grp * 2 + r - 10;
  const int u0 = (int)(0x2222008888880000ull >> (4 * r)) & 15;
  const int nb = 64 + (r == 15 ? BIAS : (int)(stt >> 4) & 15) - ((int)stt & 15);
  const int nf = isS ? 0 : NF;
  const int ntail = isX ? 8 : isY ? 16 : 0;
  const int uv = u0 + nb, uw = uv + nb;
  u64 E = 0;
  auto arrive = [&](int w) { if (lane == 0) __hip_atomic_fetch_add(lp<u32>(L_CTL + 4 * w), 1u, __ATOMIC_RELAXED, __HIP_MEMORY_SCOPE_WORKGROUP); };
  int wv = wave;
  asm volatile("" : "+s"(wv));
  if (__builtin_expect(wv == 0, 1)) {
    __builtin_amdgcn_s_setprio(3);
    int iss, pub = 0;
    auto publish = [&](int v) { pub = max(pub, v); stsv(L_CTL + 4 * C_FULL, pub); };
#define PACE(W_) do { WAITV(W_); publish(iss - (W_)); } while (0)
    const u32 cls = ((u32)(0x6666554433331000ull >> (4 * r)) & 7) + (r == 3 && (grp & 1));
    const u32 tau = 384 + ((u32)((grp < 14 ? PACE_A : PACE_B) >> (8 * cls)) & 255);
    u32 due = ((u32)tstart + COFF) * 16 + NF * tau;
    const u32 wrow = lane >> 3, wlc = ((lane & 7) ^ (((wrow >> 1) & 3) << 1)) << 4;
    int u = NF;
#pragma nounroll
    for (int seg = u0 == 8 ? 0 : 1; seg < (ntail ? 4 : 3); ++seg) {
      const bool kv = seg == 1 || seg == 2;
      const u16* base = seg == 0 ? (isQ ? wq : wmod) : seg == 1 ? Kc : seg == 2 ? Vc : isX ? wo : w1;
      const u32 rs = seg == 0 ? (isQ ? 8192 : 6144) : (seg == 3 && !isX) ? 3584 : 1024;
      const v4i srd = mksrd(base, kv ? 1u << 27 : (seg == 3 && isX) ? 4096 * 1024 : 512 * rs);
      u32 so = kv ? (u32)(grp * 1024 + kv0 + (seg == 1 ? nf : 0)) << 13
                  : seg == 0 ? (isQ ? qi : 16 + mi) * 128
                             : isX ? ((xi >> 3) << 19) + (xi & 7) * 128 : yi * 128;
      const u32 hstep = kv ? 4096 : 32 * rs, ustep = 2 * hstep;
      const u32 sw = seg == 2 ? 2 * g : g, sx = seg == 1 ? 4 : 8;
      const u32 w0 = wrow * rs + wlc, wd = 8 * rs - 1024;
      const u32 a0 = kv ? g * 256 + ((c ^ sw) << 4) : w0, a1 = kv ? g * 256 + ((c ^ sw ^ sx) << 4) : w0 + wd;
      const u32 a2 = kv ? g * 256 + ((c ^ sw ^ ((2 * sx) & 15)) << 4) : w0 + 2 * wd, a3 = kv ? g * 256 + ((c ^ sw ^ ((3 * sx) & 15)) << 4) : w0 + 3 * wd;
      const int ue = seg == 0 ? NF + u0 : seg == 1 ? uv : seg == 2 ? uw : uw + (ntail >> 1);
#pragma nounroll
      while (u < ue) {
        const bool paced = u >= 4 && seg < 3;
        const int stop = min(ue, u < 4 ? 4 : u < 48 ? 48 : 1 << 20);
        u32 dd = paced ? due : due - (1u << 28);
        const int u_in = u, la = (seg == 3 && isX) ? 0x7fffffff : LA * 16;
        u32 t0, t1, lag; u32 vf, vt;
#define DMAS(so) "buffer_load_dwordx4 %[a0], %[srd], " so " offen nt lds\n\tbuffer_load_dwordx4 %[a1], %[srd], " so " offen offset:1024 nt lds\n\t" \
                 "buffer_load_dwordx4 %[a2], %[srd], " so " offen offset:2048 nt lds\n\tbuffer_load_dwordx4 %[a3], %[srd], " so " offen offset:3072 nt lds\n\t"
#define RDFREE "s_lshl_b32 %[t1], %[u], 2\n\ts_and_b32 %[t1], %[t1], 60\n\ts_or_b32 %[t1], %[t1], %[fb]\n\tv_mov_b32 %[vf], %[t1]\n\tds_read_b32 %[vf], %[vf]\n\t"
#define REST(W) "s_waitcnt vmcnt(" #W ")\n\ts_mov_b32 m0, %[t0]\n\ts_lshl_b32 %[t0], %[u], 3\n\t" DMAS("%[t1]")                                   \
                "s_sub_i32 %[t0], %[t0], " #W " - 4\n\ts_max_i32 %[pub], %[pub], %[t0]\n\tv_mov_b32 %[vt], %[pub]\n\tds_write_b32 %[va], %[vt]\n\t"                                      \
                "s_add_i32 %[u], %[u], 1\n\ts_add_u32 %[so], %[so], %[us]\n\ts_add_u32 %[dd], %[dd], %[tau]\n\t" RDFREE                             \
                "s_memrealtime s[98:99]\n\ts_cmp_lt_i32 %[u], %[stop]\n\ts_waitcnt vmcnt(" #W ")\n\ts_cbranch_scc1 1b\n\ts_branch 9f\n"
#define XREST(W) REST(W)
        asm volatile(RDFREE "s_memrealtime s[98:99]\n"
                     "1:\n\ts_sub_i32 %[t1], %[u], 15\n\ts_waitcnt lgkmcnt(0)\n\tv_cmp_gt_i32 vcc, %[t1], %[vf]\n\ts_lshl_b32 s98, s98, 4\n\t"
                     "s_cbranch_vccnz 8f\n"
                     "2:\n\ts_sub_i32 %[lag], s98, %[dd]\n\ts_cmp_lt_i32 %[lag], 0\n\ts_cbranch_scc1 7f\n\t"
                     "s_lshl_b32 %[t0], %[u], 13\n\ts_and_b32 %[t0], %[t0], 0x1e000\n\ts_mov_b32 m0, %[t0]\n\ts_add_u32 %[t1], %[so], %[hs]\n\t" DMAS("%[so]")
                     "s_or_b32 %[t0], %[t0], 0x1000\n\ts_cmp_lt_i32 %[lag], %[la]\n\ts_cbranch_scc1 4f\n\ts_cmp_lt_i32 %[lag], %[lb]\n\ts_cbranch_scc1 5f\n\t"
                     "s_cmp_lt_i32 %[lag], %[lc]\n\ts_cbranch_scc1 3f\n\t"
                     XREST(WD) "3:\n\t" XREST(WC) "4:\n\t" XREST(WA) "5:\n\t" XREST(WB)
                     "8:\n\t" RDFREE "s_sub_i32 %[t1], %[u], 15\n\ts_waitcnt lgkmcnt(0)\n\tv_cmp_gt_i32 vcc, %[t1], %[vf]\n\ts_nop 0\n\ts_cbranch_vccz 7f\n\t"
                     "s_waitcnt vmcnt(0)\n\ts_lshl_b32 %[pub], %[u], 3\n\tv_mov_b32 %[vt], %[pub]\n\tds_write_b32 %[va], %[vt]\n"
                     "6:\n\t" RDFREE "s_sub_i32 %[t1], %[u], 15\n\ts_waitcnt lgkmcnt(0)\n\tv_cmp_gt_i32 vcc, %[t1], %[vf]\n\ts_sleep 1\n\ts_cbranch_vccnz 6b\n"
                     "7:\n\ts_memrealtime s[98:99]\n\ts_waitcnt lgkmcnt(0)\n\ts_lshl_b32 s98, s98, 4\n\ts_branch 2b\n"
                     "9:\n\ts_waitcnt lgkmcnt(0)"
                     : [u] "+s"(u), [so] "+s"(so), [dd] "+s"(dd), [pub] "+s"(pub), [t0] "=&s"(t0), [t1] "=&s"(t1), [lag] "=&s"(lag), [vf] "=&v"(vf), [vt] "=&v"(vt)
                     : [a0] "v"(a0), [a1] "v"(a1), [a2] "v"(a2), [a3] "v"(a3), [srd] "s"(srd), [hs] "s"(hstep), [us] "s"(ustep), [stop] "s"(stop),
                       [va] "v"(L_CTL + 4 * C_FULL), [fb] "i"(L_CTL + 4 * C_FREE), [tau] "s"(tau), [la] "s"(la), [lb] "i"(LB * 16), [lc] "i"(LC * 16)
                     : "memory", "vcc", "scc", "s98", "s99");
        due += (u - u_in) * tau;
        if (u == 4 && isS) { WAITV(16); publish(16); }
        if (u == uw) stsv(L_CTL + 4 * C_VISS, 1);
        if (u == 48) { const u32 t = __builtin_amdgcn_readfirstlane(ldsv(L_CTL + 4 * C_T0)); if (t) due = t * 16 + u * tau; }
      }
    }
    iss = 8 * u;
    if (!ntail) { PACE(24); PACE(16); PACE(8); PACE(0); }
    else arrive(C_ISS);
    __builtin_amdgcn_s_setprio(0);
  } else {
    const rsrc_t rws = __builtin_amdgcn_make_buffer_rsrc(ws, 0, W_END, 0x00020000);
    auto wait_full = [&](u32 need) { while (ldsv(L_CTL + 4 * C_FULL) < need) __builtin_amdgcn_s_sleep(1); FENCE(); };
    const u32 trow = 4 * g + (c >> 2);
    auto waddr = [&](int t) -> u32 { return trow * 128 + ((((u32)(t ^ ((trow >> 1) & 3)) << 1) | ((c >> 1) & 1)) << 4) + (c & 1) * 8; };

    if (wave == 1) {
      const u64 E0 = __hip_atomic_load((GAS u64*)(ws + W_EPOCH), __ATOMIC_RELAXED, __HIP_MEMORY_SCOPE_AGENT);
      u32 tag;
#define CHAIN_START() do { E = E0 + 1; tag = (u32)E; } while (0)
      auto silu4 = [&](u32 x, u32 y) { return v2u{pk2(silu_exact(lo16(x)), silu_exact(bfhi(x))), pk2(silu_exact(lo16(y)), silu_exact(bfhi(y)))}; };
      if (isS) {
        const v2u cv = *(const v2u*)(cin + (lane >> 5) * 512 + (r - 12) * 128 + (lane & 31) * 4);
        CHAIN_START();
        *lp<v2u>(L_ACT + (lane >> 5) * ASTR + fpos((lane & 31) * 4) * 2) = silu4(cv.x, cv.y);
        FENCE();
        v8bf act[4];
#pragma unroll
        for (int ks = 0; ks < 4; ++ks) { act[ks] = ldfrag(L_ACT + (c & 1) * ASTR + g * 16 + ks * 64); PIN(act[ks]); }
        wait_full(16);
        v8bf W[16];
#pragma unroll
        for (int j = 0; j < 16; ++j) W[j] = trfrag(waddr(j & 3) + (j >> 2) * 4096);
        FENCE(); stsv(L_CTL + 4 * C_FREE, 1); stsv(L_CTL + 4 * (C_FREE + 1), 2);
        v4f acc[4];
#pragma unroll
        for (int t = 0; t < 4; ++t) acc[t] = v4f{0.f, 0.f, 0.f, 0.f};
#pragma unroll
        for (int j = 0; j < 16; ++j) acc[j & 3] = mfma(act[j >> 2], W[j], acc[j & 3]);
        const float r0 = g == 0 ? acc[0][0] : g == 1 ? acc[1][0] : g == 2 ? acc[2][0] : acc[3][0];
        const float r1 = g == 0 ? acc[0][1] : g == 1 ? acc[1][1] : g == 2 ? acc[2][1] : acc[3][1];
        while (ldsv(L_CTL + 4 * C_CLR) == 0) { }
        FENCE();
        st4(v4u{__float_as_uint(r0), tag, __float_as_uint(r1), tag}, rws, lane * 16, W_SP + ((r - 12) * 1024 + grp * 64) * 16);
      } else if (isR) {
        const v4u in0 = *(const v4u*)(xin + lane * 8), in1 = *(const v4u*)(xin + 512 + lane * 8);
        u32 xs0 = *(const u32*)(xin + (grp >> 1) * 64 + (lane & ~1)), xs1 = *(const u32*)(xin + 512 + (grp >> 1) * 64 + (lane & ~1));
        CHAIN_START();
        PIN(xs0); PIN(xs1);
        xs0 >>= 16 * (lane & 1); xs1 >>= 16 * (lane & 1);
        float mr[2], ri[2];
#pragma unroll
        for (int b = 0; b < 2; ++b) {
          const v4u xv = b ? in1 : in0;
          const float x[8] = {lo16(xv.x), bfhi(xv.x), lo16(xv.y), bfhi(xv.y), lo16(xv.z), bfhi(xv.z), lo16(xv.w), bfhi(xv.w)};
          float s = 0.f;
#pragma unroll
          for (int i = 0; i < 8; ++i) s += x[i];
          const float mean = wave_sum(s) * (1.f / 512.f);
          float q2 = 0.f;
#pragma unroll
          for (int i = 0; i < 8; ++i) q2 += (x[i] - mean) * (x[i] - mean);
          const float var = wave_sum(q2) * (1.f / 512.f);
          mr[b] = rb(mean); ri[b] = rcp(rb(__builtin_amdgcn_sqrtf(rb(rb(var) + 1e-5f))));
        }
        v2f y = rb2(rb2(v2f{lo16(xs0) - mr[0], lo16(xs1) - mr[1]}) * v2f{ri[0], ri[1]});
        PIN(y);
        v4u a[8];
#pragma nounroll
        for (int sp = 0; sp < SPIN_CAP; ++sp) {
          u32 bad = 0;
#pragma unroll
          for (int i = 0; i < 8; ++i) { a[i] = ld4(rws, lane * 16, W_SP + ((i & 3) * 1024 + (i >> 2) * 512 + (grp >> 1) * 64) * 16); bad |= (a[i].y ^ tag) | (a[i].w ^ tag); }
          if (__all(bad == 0)) break;
          FENCE();
        }
        v2f sc[2];
#pragma unroll
        for (int pt = 0; pt < 2; ++pt) {
          sc[pt] = v2f{0.f, 0.f};
#pragma unroll
          for (int k4 = 0; k4 < 4; ++k4) sc[pt] += v2f{__uint_as_float(a[pt * 4 + k4].x), __uint_as_float(a[pt * 4 + k4].z)};
        }
        const v2f h = rb2(y * rb2(rb2(sc[1]) + 1.f)) + rb2(sc[0]);
        st2(v2u{pk2(h.x, h.y), tag}, rws, lane * 8, W_HG + (grp >> 1) * 512);
      } else if (isM || isQ) {
        if (isM) {
          const v4u in0 = *(const v4u*)(cin + lane * 8), in1 = *(const v4u*)(cin + 512 + lane * 8);
          CHAIN_START();
          *lp<v2u>(L_ACT + fpos(lane * 8) * 2) = silu4(in0.x, in0.y); *lp<v2u>(L_ACT + fpos(lane * 8 + 4) * 2) = silu4(in0.z, in0.w);
          *lp<v2u>(L_ACT + ASTR + fpos(lane * 8) * 2) = silu4(in1.x, in1.y); *lp<v2u>(L_ACT + ASTR + fpos(lane * 8 + 4) * 2) = silu4(in1.z, in1.w);
        } else {
          CHAIN_START();
          while (__builtin_amdgcn_s_memrealtime() < tstart + HDELAY) __builtin_amdgcn_s_sleep(4);
          v4u a[4];
#pragma nounroll
          for (int sp = 0; sp < SPIN_CAP; ++sp) {
            u32 bad = 0;
#pragma unroll
            for (int i = 0; i < 4; ++i) { a[i] = ld4(rws, lane * 16 + i * 1024, W_HG); bad |= (a[i].y ^ tag) | (a[i].w ^ tag); }
            if (__all(bad == 0)) break;
            FENCE();
          }
#pragma unroll
          for (int i = 0; i < 4; ++i) {
            const u32 wa = L_ACT + fpos(i * 128 + lane * 2) * 2;
            *lp<u32>(wa) = (a[i].x & 0xffffu) | (a[i].z << 16); *lp<u32>(wa + ASTR) = (a[i].x >> 16) | (a[i].z & 0xffff0000u);
          }
        }
        FENCE(); stsv(L_CTL + 4 * C_TAG, tag); stsv(L_CTL + 4 * C_ARDY, 1);
      } else CHAIN_START();
      if (blockIdx.x < 8 && lane == 0) st2(v2u{(u32)tstart, tag}, rws, blockIdx.x * 8, W_T0);
      {
        wait_full(8 * u0 + 16 * QAT);
        v4u a[2];
#pragma nounroll
        for (int sp = 0; sp < SPIN_CAP; ++sp) {
          a[0] = ld4(rws, lane * 16, W_QG + grp * 2048); a[1] = ld4(rws, lane * 16 + 1024, W_QG + grp * 2048);
          if (__all(((a[0].y ^ tag) | (a[0].w ^ tag) | (a[1].y ^ tag) | (a[1].w ^ tag)) == 0)) break;
          __builtin_amdgcn_s_sleep(16); FENCE();
        }
        *lp<v2u>(L_Q + lane * 8) = v2u{a[0].x, a[0].z}; *lp<v2u>(L_Q + 512 + lane * 8) = v2u{a[1].x, a[1].z};
        FENCE(); stsv(L_CTL + 4 * C_TAG, tag); stsv(L_CTL + 4 * C_QRDY, 1);
        {
          const v2u t = ld2(rws, (lane & 7) * 8, W_T0);
          if (__all(t.y == tag)) {
            int dt = (int)(t.x - (u32)tstart);
            dt += __builtin_amdgcn_mov_dpp(dt, 0xB1, 0xf, 0xf, true); dt += __builtin_amdgcn_mov_dpp(dt, 0x4E, 0xf, 0xf, true); dt += __builtin_amdgcn_mov_dpp(dt, 0x141, 0xf, 0xf, true);
            if (dt > -2400 && dt < 2400) stsv(L_CTL + 4 * C_T0, ((u32)tstart + (dt >> 3) + COFF) | 1);
          }
        }
      }
      if (ntail) {
        const v4i srd = mksrd(isX ? wo : w2, isX ? 4096 * 1024 : 1792 * 1024);
        const u32 a0 = (lane >> 3) * 1024 + (((lane & 7) ^ ((lane >> 3) & 6)) << 4), a1 = a0 + 7168, a2 = a0 + 2 * 7168, a3 = a0 + 3 * 7168;
        u32 so = isX ? ((xi >> 3) << 19) + (xi & 7) * 128 + 256 * 1024 : yi << 16;
        while (ldsv(L_CTL + 4 * C_VISS) == 0) __builtin_amdgcn_s_sleep(2);
#pragma nounroll
        for (int u = uw + (ntail >> 1); u < uw + ntail; ++u, so += isX ? 65536 : 128) {
          while ((int)ldsv(L_CTL + 4 * (C_FREE + (u & (NSLOT - 1)))) < u + 1 - NSLOT) __builtin_amdgcn_s_sleep(1);
          const u32 lb = (u & (NSLOT - 1)) * SLOT, m = lb + 4096, s1 = so + 32768;
          DMA4(lb, a0, a1, a2, a3, srd, so);
          DMA4(m, a0, a1, a2, a3, srd, s1);
          WAITV(W2W);
        }
        arrive(C_ISS);
      }
      if (r == 8) {
        wait_full(8 * uw);
        const int zb = grp >> 3, zc = (grp & 7) * 64 + c * 4 + g;
        const u32 xv = xin[zb * 512 + zc];
        v2u m0, m1;
#pragma nounroll
        for (int sp = 0; sp < SPIN_CAP; ++sp) {
          m0 = ld2(rws, zc * 8, W_MG); m1 = ld2(rws, zc * 8, W_MG + 3 * 4096);
          if (__all(((m0.y ^ tag) | (m1.y ^ tag)) == 0)) break;
          FENCE();
        }
        v4u z[7];
#pragma nounroll
        for (int sp = 0; sp < SPIN_CAP; ++sp) {
          z[0] = ld4(rws, g * 4096 + c * 16, W_AP + grp * 256); z[1] = ld4(rws, g * 4096 + c * 16, W_AP + 16384 + grp * 256);
          if (__all(max(max(max(z[0].x, z[0].y), max(z[0].z, z[0].w)), max(max(z[1].x, z[1].y), max(z[1].z, z[1].w))) != SENT)) break;
          __builtin_amdgcn_s_sleep(2); FENCE();
        }
        v4f s0 = __builtin_bit_cast(v4f, z[0]), s1 = __builtin_bit_cast(v4f, z[1]);
        const float at = fold32(fold16(s0.x, s0.y), fold16(s0.z, s0.w)) + fold32(fold16(s1.x, s1.y), fold16(s1.z, s1.w));
        float x1 = rb(lo16(xv) + rb(bfhi(m0.x << (16 - 16 * zb)) * rb(at))), gb = bfhi(m1.x << (16 - 16 * zb));
        GAS u16* op = (GAS u16*)out + zb * 512 + zc;
        PIN(x1); PIN(gb); PIN(op);
#pragma nounroll
        for (int sp = 0; sp < SPIN_CAP; ++sp) {
#pragma unroll
          for (int j = 0; j < 7; ++j) z[j] = ld4(rws, g * 4096 + c * 16, W_OP + j * 16384 + grp * 256);
          u32 mx = 0;
#pragma unroll
          for (int j = 0; j < 7; ++j) mx = max(mx, max(max(z[j].x, z[j].y), max(z[j].z, z[j].w)));
          if (__all(mx != SENT)) break;
          FENCE();
        }
        const v4f s = ((__builtin_bit_cast(v4f, z[0]) + __builtin_bit_cast(v4f, z[1])) + (__builtin_bit_cast(v4f, z[2]) + __builtin_bit_cast(v4f, z[3]))) +
                      ((__builtin_bit_cast(v4f, z[4]) + __builtin_bit_cast(v4f, z[5])) + __builtin_bit_cast(v4f, z[6]));
        const float rr = x1 + rb(gb * rb(fold16(fold32(s.x, s.z), fold32(s.y, s.w))));
        *op = (u16)pk2(rr, rr);
        __builtin_amdgcn_endpgm();
      }
    } else {
      if (isS && wave == 7) {
        const v4u s4 = {SENT, SENT, SENT, SENT};
        const u32 so = W_PART + (grp * 4 + r - 12) * CLRB;
#pragma unroll
        for (int i = 0; i < CLRB / 1024; ++i) st4(s4, rws, lane * 16 + (i & 3) * 1024, so + (i >> 2) * 4096);
        if (lane < (CLRB % 1024) / 16) st4(s4, rws, lane * 16, so + CLRB / 1024 * 1024);
        WAITV(0); stsv(L_CTL + 4 * C_CLR, 1);
      }

      if (wave == 6) { *lp<v4f>(L_MRG + lane * 16) = v4f{0.f, 0.f, 0.f, 0.f}; *lp<v4f>(L_MRG + 1024 + lane * 16) = v4f{0.f, 0.f, 0.f, 0.f}; }
      const int d = c >> 2, cw = wave - 2;
      const u32 sca = L_SC + (g * 4 + (c & 3)) * 16;
      {
        u32 ka[4];
#pragma unroll
        for (int s = 0; s < 4; ++s) ka[s] = c * 256 + (((4 * s + g) ^ c) << 4);
        v8bf qf[4];
        float mx = -INFINITY;
        auto rdk = [&](v8bf (&kf)[8], int j) {
          const int u = j < nf ? j : u0 + j;
          const u32 lb = (u & (NSLOT - 1)) * SLOT;
          wait_full(8 * u + 8);
#pragma unroll
          for (int s = 0; s < 4; ++s) { kf[s] = ldfrag(lb + ka[s]); kf[4 + s] = ldfrag(lb + ka[s] + 4096); }
          FENCE(); stsv(L_CTL + 4 * (C_FREE + (u & (NSLOT - 1))), u + 1);
        };
        auto qk = [&](v8bf (&kf)[8], int j) {
          v4f S0 = {0.f, 0.f, 0.f, 0.f}, S1 = {0.f, 0.f, 0.f, 0.f};
#pragma unroll
          for (int s = 0; s < 4; ++s) { S0 = mfma(kf[s], qf[s], S0); S1 = mfma(kf[4 + s], qf[s], S1); }
          const v2f a = rb2(v2f{S0.x, S0.y}) * 0.08838834764831845f, b = rb2(v2f{S0.z, S0.w}) * 0.08838834764831845f;
          const v2f e = rb2(v2f{S1.x, S1.y}) * 0.08838834764831845f, f = rb2(v2f{S1.z, S1.w}) * 0.08838834764831845f;
          mx = fmaxf(fmaxf(fmaxf(mx, fmaxf(a.x, a.y)), fmaxf(b.x, b.y)), fmaxf(fmaxf(e.x, e.y), fmaxf(f.x, f.y)));
          if (c < 4) *lp<v4u>(sca + j * 256) = v4u{pk2(a.x, a.y), pk2(b.x, b.y), pk2(e.x, e.y), pk2(f.x, f.y)};
        };
        v8bf sk[NSTASH][8];
        rdk(sk[0], cw);
        if (u0 == 8 && wave < 6) {
          wait_full(8 * NF + 64);
          v8bf W[16];
#pragma unroll
          for (int ks = 0; ks < 16; ++ks) W[ks] = trfrag(NF * SLOT + ks * 4096 + waddr(wave - 2));
          WAITL();
          u32 old = 0;
          if (lane == 0) old = __hip_atomic_fetch_add(lp<u32>(L_CTL + 4 * C_WRD), 1u, __ATOMIC_RELAXED, __HIP_MEMORY_SCOPE_WORKGROUP);
          if (__builtin_amdgcn_readfirstlane(old) == 3 && lane < 8) stsv(L_CTL + 4 * (C_FREE + NF + lane), NF + lane + 1);
          while (ldsv(L_CTL + 4 * C_ARDY) == 0) __builtin_amdgcn_s_sleep(1);
          FENCE();
          const u32 tag = ldsv(L_CTL + 4 * C_TAG);
          v8bf A[16];
#pragma unroll
          for (int ks = 0; ks < 16; ++ks) A[ks] = ldfrag(L_ACT + (c & 1) * ASTR + g * 16 + ks * 64);
          __builtin_amdgcn_sched_barrier(0);
          v4f d0 = {0.f, 0.f, 0.f, 0.f}, d1 = {0.f, 0.f, 0.f, 0.f};
#pragma unroll
          for (int ks = 0; ks < 16; ks += 2) { d0 = mfma(A[ks], W[ks], d0); d1 = mfma(A[ks + 1], W[ks + 1], d1); }
          const float r0 = d0[0] + d1[0], r1 = d0[1] + d1[1];
          const float n0 = dpp<0xB1>(r0), n1 = dpp<0xB1>(r1);
          const bool odd = lane & 1;
          if (lane < 16) {
            if (isM) st2(v2u{pk2(r0, r1), tag}, rws, lane * 8, W_MG + mi * 512 + (wave - 2) * 128);
            else st2(v2u{pk2(odd ? n1 : r0, odd ? r1 : n0), tag}, rws, (lane & 1) * 16384 + (lane >> 1) * 8, W_QG + qi * 256 + (wave - 2) * 64);
          }
        }
#pragma unroll
        for (int i = 1; i < NSTASH; ++i) rdk(sk[i], cw + i * NCONS);
        while (ldsv(L_CTL + 4 * C_QRDY) == 0) __builtin_amdgcn_s_sleep(2);
        FENCE();
#pragma unroll
        for (int s = 0; s < 4; ++s) qf[s] = ldfrag(L_Q + (c & 3) * 256 + (4 * s + g) * 16);
#pragma unroll
        for (int i = 0; i < NSTASH; ++i) qk(sk[i], cw + i * NCONS);
#pragma nounroll
        for (int j = cw + NSTASH * NCONS; j < nb; j += NCONS) { v8bf kf[8]; rdk(kf, j); qk(kf, j); }
        mx = swap32_max(swap16_max(rb(mx)));
        if (lane < 4) *lp<float>(L_ML + cw * 16 + lane * 4) = mx;
        FENCE();
        arrive(C_MX);
      }
      float M;
      {
        while (ldsv(L_CTL + 4 * C_MX) < NCONS) __builtin_amdgcn_s_sleep(1);
        FENCE();
        M = *lp<float>(L_ML + (c & 3) * 4);
#pragma unroll
        for (int w = 1; w < NCONS; ++w) M = fmaxf(M, *lp<float>(L_ML + w * 16 + (c & 3) * 4));
        float l = 0.f;
#pragma nounroll
        for (int j = cw + d * NCONS; j < nb; j += 4 * NCONS) {
          const v4u s = *lp<v4u>(sca + j * 256);
          float p[8];
#pragma unroll
          for (int i = 0; i < 4; ++i) { p[2 * i] = ex2((lo16(s[i]) - M) * 1.4426950408889634f); p[2 * i + 1] = ex2((bfhi(s[i]) - M) * 1.4426950408889634f); }
          l += ((p[0] + p[1]) + (p[2] + p[3])) + ((p[4] + p[5]) + (p[6] + p[7]));
          *lp<v4u>(sca + j * 256) = v4u{pk2(p[0], p[1]), pk2(p[2], p[3]), pk2(p[4], p[5]), pk2(p[6], p[7])};
        }
        l += dpp<0x124>(l); l += dpp<0x128>(l);
        l = swap32_add(swap16_add(l));
        if (lane < 4) *lp<float>(L_ML + 128 + cw * 16 + lane * 4) = l;
        FENCE();
        arrive(C_SM);
      }
      {
        u32 va[8];
#pragma unroll
        for (int t = 0; t < 8; ++t) va[t] = trow * 256 + ((((u32)(t ^ (trow & 7)) << 1) | ((c >> 1) & 1)) << 4) + (c & 1) * 8;
        v4f acc[8];
#pragma unroll
        for (int t = 0; t < 8; ++t) acc[t] = v4f{0.f, 0.f, 0.f, 0.f};
        int j = cw;
#pragma nounroll
        for (; j + NCONS < nb; j += NCONS) {
          const int u = uv + j;
          const u32 lb = (u & (NSLOT - 1)) * SLOT;
          wait_full(8 * u + 8);
          const v8bf pf = ldfrag(sca + j * 256);
          v8bf vf[8];
#pragma unroll
          for (int t = 0; t < 8; ++t) {
            v4s lo = __builtin_amdgcn_ds_read_tr16_b64_v4i16(lp<v4s>(lb + va[t])), hi = __builtin_amdgcn_ds_read_tr16_b64_v4i16(lp<v4s>(lb + va[t] + 4096));
            vf[t] = __builtin_bit_cast(v8bf, __builtin_shufflevector(lo, hi, 0, 1, 2, 3, 4, 5, 6, 7));
          }
          FENCE(); stsv(L_CTL + 4 * (C_FREE + (u & (NSLOT - 1))), u + 1);
#pragma unroll
          for (int t = 0; t < 8; ++t) acc[t] = mfma(vf[t], pf, acc[t]);
        }
        const int link = j - (nb - NCONS), u = uv + j;
        const u32 lb = (u & (NSLOT - 1)) * SLOT;
        v4f e0 = d == 0 ? acc[0] : d == 1 ? acc[2] : d == 2 ? acc[4] : acc[6];
        v4f e1 = d == 0 ? acc[1] : d == 1 ? acc[3] : d == 2 ? acc[5] : acc[7];
        const v4u pf = *lp<v4u>(sca + j * 256);
        v4u pm[4];
#pragma unroll
        for (int i = 0; i < 4; ++i) { pm[i] = d == i ? pf : v4u{0u, 0u, 0u, 0u}; PIN(pm[i]); }
        float inv = 1.f, L = 0.f;
        if (link == NCONS - 1) {
          while (ldsv(L_CTL + 4 * C_SM) < NCONS) { }
          FENCE();
          L = *lp<float>(L_ML + 128 + (c & 3) * 4);
#pragma unroll
          for (int w = 1; w < NCONS; ++w) L += *lp<float>(L_ML + 128 + w * 16 + (c & 3) * 4);
          inv = rcp(L);
        }
        v2u ml = {canon(M), canon(L)};
        PIN(ml);
        u32 oa = (c & 3) * 4096 + d * 64 + g * 16, ra = L_MRG + lane * 16;
        u32 vb[8];
#pragma unroll
        for (int t = 0; t < 8; ++t) { vb[t] = lb + va[t]; PIN(vb[t]); }
        PIN(e0); PIN(e1); PIN(oa); PIN(ra); PIN(inv);
        if (link == NCONS - 1) {
          while (ldsv(L_CTL + 4 * C_FULL) < (u32)(8 * u + 8 - HEAD)) { }
          if (lane < 4) st2(ml, rws, lane * 8, W_ML + bid * 128);
        }
        while (ldsv(L_CTL + 4 * C_FULL) < (u32)(8 * u + 8)) { }
        FENCE();
        v8bf vf[8];
#pragma unroll
        for (int t = 0; t < 8; ++t) {
          v4s lo = __builtin_amdgcn_ds_read_tr16_b64_v4i16(lp<v4s>(vb[t])), hi = __builtin_amdgcn_ds_read_tr16_b64_v4i16(lp<v4s>(vb[t] + 4096));
          vf[t] = __builtin_bit_cast(v8bf, __builtin_shufflevector(lo, hi, 0, 1, 2, 3, 4, 5, 6, 7));
        }
        __builtin_amdgcn_sched_barrier(0);
#pragma unroll
        for (int i = 0; i < 4; ++i) { e0 = mfma(vf[2 * i], __builtin_bit_cast(v8bf, pm[i]), e0); e1 = mfma(vf[2 * i + 1], __builtin_bit_cast(v8bf, pm[i]), e1); }
        __builtin_amdgcn_sched_barrier(0);
        v4f r0, r1;
        for (;;) {
          const u32 cnt = ldsv(L_CTL + 4 * C_MCNT);
          FENCE();
          r0 = *lp<v4f>(ra); r1 = *lp<v4f>(ra + 1024);
          FENCE();
          if (cnt >= (u32)link) break;
        }
        e0 = (e0 + r0) * inv; e1 = (e1 + r1) * inv;
        if (link == NCONS - 1) {
          st4(v4u{min(pk2(e0.x, e0.y), SENT - 1), min(pk2(e0.z, e0.w), SENT - 1), min(pk2(e1.x, e1.y), SENT - 1), min(pk2(e1.z, e1.w), SENT - 1)}, rws, oa, W_PART + grp * 16384 + r * 256);
        } else {
          *lp<v4f>(ra) = e0; *lp<v4f>(ra + 1024) = e1;
          FENCE(); stsv(L_CTL + 4 * C_MCNT, link + 1);
        }
        stsv(L_CTL + 4 * (C_FREE + (u & (NSLOT - 1))), u + 1);
      }
    }
  }
  if (!(isX || isY)) return;
  const u32 trow = 4 * g + (c >> 2);
  auto waddr = [&](int t) -> u32 { return trow * 128 + ((((u32)(t ^ ((trow >> 1) & 3)) << 1) | ((c >> 1) & 1)) << 4) + (c & 1) * 8; };
  auto wslot = [&](int t) -> u32 { return ((uw + t) & (NSLOT - 1)) * SLOT; };
  const int half = lane >> 5, i32 = lane & 31;
  const u32 fa = L_ACT + (c & 1) * ASTR + g * 16;
  {
    const rsrc_t rws = __builtin_amdgcn_make_buffer_rsrc(ws, 0, W_END, 0x00020000);
    auto weights_done = [&]() { WAITV(0); if (wave == 0) stsv(L_CTL + 4 * C_FULL, 8 * (uw + ntail)); arrive(C_WIN); };
    auto weights_in = [&]() { while (ldsv(L_CTL + 4 * C_WIN) < 2) __builtin_amdgcn_s_sleep(1); FENCE(); };
    if (isX) {
      const int kvh = xi >> 3, ng = xi & 7;
      const int b = wave >> 2, h = wave & 3;
      u32 wa = L_ACT + b * ASTR + (h * 128 + c * 8 + g * 2) * 2, wb[8];
#pragma unroll
      for (int i = 0; i < 8; ++i) { wb[i] = wslot(i) + waddr(wave & 3); PIN(wb[i]); }
      u32 vo = lane * 16 + W_PART + (b * 8 + kvh) * 16384 + h * 4096;
      u32 vl = ((c & 3) * 4 + ((g + (c >> 2)) & 3)) * 128 + W_ML + (b * 8 + kvh) * 2048 + h * 8;
      u32 vs = c * 2048 + g * 16 + W_AP + kvh * 4096 + ng * 256 + (wave & 3) * 64, fb = fa;
      PIN(wa); PIN(vo); PIN(vl); PIN(vs); PIN(fb);
      while (ldsv(L_CTL + 4 * C_ISS) < 2) __builtin_amdgcn_s_sleep(1);
      FENCE();
      v2u ls = ld2(rws, vl, 0);
      if (wave < 2) weights_done();
      if (!__all(max(ls.x, ls.y) != SENT)) {
#pragma nounroll
        for (int sp = 0; sp < SPIN_CAP; ++sp) {
          __builtin_amdgcn_s_sleep(1); FENCE();
          ls = ld2(rws, vl, 0);
          if (__all(max(ls.x, ls.y) != SENT)) break;
        }
        __builtin_amdgcn_s_sleep(XW);
      }
      FENCE();
      v4u a[4];
#pragma unroll
      for (int j = 0; j < 4; ++j) a[j] = ld4(rws, vo + j * 1024, 0);
      __builtin_amdgcn_sched_barrier(0);
      v8bf W[16];
      if (wave >= 4) {
        weights_in();
#pragma unroll
        for (int ks = 0; ks < 16; ++ks) W[ks] = trfrag(wb[ks >> 1] + (ks & 1) * 4096);
      }
      float M = __uint_as_float(ls.x);
      M = fmaxf(M, dpp<0xB1>(M)); M = fmaxf(M, dpp<0x4E>(M)); M = fmaxf(M, dpp<0x141>(M)); M = fmaxf(M, dpp<0x128>(M));
      float cf = ex2((__uint_as_float(ls.x) - M) * 1.4426950408889634f) * __uint_as_float(ls.y);
      cf *= rcp(row_sum(cf));
      float cj[4] = {dpp<0x150>(cf), dpp<0x151>(cf), dpp<0x152>(cf), dpp<0x153>(cf)};
      PIN(cj[0]); PIN(cj[1]); PIN(cj[2]); PIN(cj[3]);
#pragma nounroll
      for (int sp = 0; sp < SPIN_CAP; ++sp) {
        u32 mx = 0;
#pragma unroll
        for (int j = 0; j < 4; ++j) mx = max(max(mx, max(a[j].x, a[j].y)), max(a[j].z, a[j].w));
        if (__all(mx != SENT)) break;
        FENCE();
#pragma unroll
        for (int j = 0; j < 4; ++j) a[j] = ld4(rws, vo + j * 1024, 0);
      }
      v2f o[4];
#pragma unroll
      for (int i = 0; i < 4; ++i) {
        o[i] = v2f{lo16(a[0][i]), bfhi(a[0][i])} * cj[0];
#pragma unroll
        for (int j = 1; j < 4; ++j) o[i] += v2f{lo16(a[j][i]), bfhi(a[j][i])} * cj[j];
      }
      *lp<u32>(wa) = pk2(fold16(fold32(o[0].x, o[2].x), fold32(o[1].x, o[3].x)), fold16(fold32(o[0].y, o[2].y), fold32(o[1].y, o[3].y)));
      bar();
      if (wave >= 4) {
        v8bf A[16];
#pragma unroll
        for (int ks = 0; ks < 16; ++ks) A[ks] = ldfrag(fb + ks * 64);
        __builtin_amdgcn_sched_barrier(0);
        v4f d0 = {0.f, 0.f, 0.f, 0.f};
#pragma unroll
        for (int ks = 0; ks < 16; ++ks) d0 = mfma(W[ks], A[ks], d0);
        v4u ov = {canon(d0.x), canon(d0.y), canon(d0.z), canon(d0.w)};
        if (c < 2) st4(ov, rws, vs, 0);
      }
    } else {
      const int b = wave >> 2, col = (wave & 3) * 128 + i32 * 4 + half * 2;
      u32 wb[8];
#pragma unroll
      for (int i = 0; i < 8; ++i) { wb[i] = wave >= 4 ? wslot(i) + waddr(wave & 3) : wslot(8 + 2 * wave + (i >> 2)) + waddr(i & 3); PIN(wb[i]); }
      u32 wa = L_ACT + b * ASTR + fpos(col) * 2, ra = L_RED + wave * 8 + half * 4, rb_ = L_RED + b * 32;
      u32 ha = L_H1 + (c & 1) * 128 + fpos((wave & 3) * 16 + g * 4 + ((c >> 1) & 3)) * 2, fb = wave >= 4 ? fa : L_H1 + (c & 1) * 128 + g * 16;
      u32 vo = half * 16384 + i32 * 16 + W_AP + b * 2048 + (wave & 3) * 512, vs = (c & 1) * 2048 + (c >> 1) * 64 + g * 16 + W_OP + yi * 4096 + wave * 512;
      PIN(wa); PIN(ra); PIN(rb_); PIN(ha); PIN(fb); PIN(vo); PIN(vs);
      v2f x0, ga, sb, cb1;
      {
        const u32 xv = *(const u32*)(xin + b * 512 + col);
        const u32 tag = ldsv(L_CTL + 4 * C_TAG);
        v4u mg[3];
#pragma nounroll
        for (int sp = 0; sp < SPIN_CAP; ++sp) {
#pragma unroll
          for (int i = 0; i < 3; ++i) mg[i] = ld4(rws, col * 8, W_MG + i * 4096);
          if (__all(((mg[0].y ^ tag) | (mg[0].w ^ tag) | (mg[1].y ^ tag) | (mg[1].w ^ tag) | (mg[2].y ^ tag) | (mg[2].w ^ tag)) == 0)) break;
          FENCE();
        }
        const int sh = 16 - 16 * b;
        x0 = v2f{lo16(xv), bfhi(xv)};
        ga = v2f{bfhi(mg[0].x << sh), bfhi(mg[0].z << sh)}; sb = v2f{bfhi(mg[1].x << sh), bfhi(mg[1].z << sh)};
        cb1 = rb2(v2f{bfhi(mg[2].x << sh), bfhi(mg[2].z << sh)} + 1.f);
      }
      PIN(x0); PIN(ga); PIN(sb); PIN(cb1);
      if (wave < 2) weights_done();
      weights_in();
      v4u a[4];
#pragma unroll
      for (int j = 0; j < 4; ++j) a[j] = ld4(rws, vo, j * 4096);
      __builtin_amdgcn_sched_barrier(0);
      v8bf W[16];
#pragma unroll
      for (int j = 0; j < 16; ++j) W[j] = trfrag(wb[j >> 1] + (j & 1) * 4096);
#pragma nounroll
      for (int sp = 0; sp < SPIN_CAP; ++sp) {
        u32 mx = 0;
#pragma unroll
        for (int j = 0; j < 4; ++j) mx = max(max(mx, max(a[j].x, a[j].y)), max(a[j].z, a[j].w));
        if (__all(mx != SENT)) break;
        FENCE();
#pragma unroll
        for (int j = 0; j < 4; ++j) a[j] = ld4(rws, vo, j * 4096);
      }
      const v4f at = (__builtin_bit_cast(v4f, a[0]) + __builtin_bit_cast(v4f, a[1])) + (__builtin_bit_cast(v4f, a[2]) + __builtin_bit_cast(v4f, a[3]));
      const v2f x1 = rb2(x0 + rb2(ga * rb2(v2f{fold32(at.x, at.z), fold32(at.y, at.w)})));
      const float st = half_sum(fold32(x1.x + x1.y, fmaf(x1.x, x1.x, x1.y * x1.y)));
      if (i32 == 0) *lp<float>(ra) = st;
      bar();
      {
        const v4f r0 = *lp<v4f>(rb_), r1 = *lp<v4f>(rb_ + 16);
        const float mean = ((r0.x + r0.z) + (r1.x + r1.z)) * (1.f / 512.f);
        const float var = fmaxf(fmaf(-mean, mean, ((r0.y + r0.w) + (r1.y + r1.w)) * (1.f / 512.f)), 0.f);
        const v2f mv = rb2(v2f{mean, var});
        const float ri = rcp(rb(__builtin_amdgcn_sqrtf(rb(mv.y + 1e-5f))));
        const v2f hh = rb2(rb2(rb2(x1 - mv.x) * ri) * cb1) + sb;
        *lp<u32>(wa) = pk2(hh.x, hh.y);
      }
      bar();
      if (wave >= 4) {
        v8bf A[16];
#pragma unroll
        for (int ks = 0; ks < 16; ++ks) A[ks] = ldfrag(fb + ks * 64);
        __builtin_amdgcn_sched_barrier(0);
        v4f d0 = {0.f, 0.f, 0.f, 0.f};
#pragma unroll
        for (int ks = 0; ks < 16; ++ks) d0 = mfma(W[ks], A[ks], d0);
        float e0 = (c & 2) ? d0.y : d0.x, e1 = (c & 2) ? d0.w : d0.z;
        PIN(e0); PIN(e1);
        const float v = silu(rb((c & 4) ? e1 : e0));
        *lp<u16>(ha) = (u16)pk2(v, v);
      }
      bar();
      if (wave < 4) {
        const v8bf a0 = ldfrag(fb), a1 = ldfrag(fb + 64);
        v4f dd[8];
#pragma unroll
        for (int t = 0; t < 8; ++t) dd[t] = mfma(W[2 * t + 1], a1, mfma(W[2 * t], a0, v4f{0.f, 0.f, 0.f, 0.f}));
        v4f rv = dd[0];
#pragma unroll
        for (int t = 1; t < 8; ++t) if ((c >> 1) == t) rv = dd[t];
        v4u ov = {canon(rv.x), canon(rv.y), canon(rv.z), canon(rv.w)};
        st4(ov, rws, vs, 0);
        if (wave == 1 && yi == 27 && lane == 0) st2(v2u{(u32)E, (u32)(E >> 32)}, rws, 0, W_EPOCH);
      }
    }
  }
}

extern "C" void kernel_launch(void* const* d_in, const int* in_sizes, int n_in, void* d_out, int out_size, void* d_ws, size_t ws_size, hipStream_t stream) {
  hipLaunchKernelGGL(fused_block, dim3(256), dim3(512), 0, stream, (const u16*)d_in[1], (const u16*)d_in[2], (const u16*)d_in[9], (const u16*)d_in[3],
                     (const u16*)d_in[4], (const u16*)d_in[6], (const u16*)d_in[7], (const u16*)d_in[0], (const u16*)d_in[8], (u16*)d_out, (unsigned char*)d_ws);
}
```

```cpp
#include <hip/hip_runtime.h>

typedef unsigned int u32;
typedef unsigned long long u64;
typedef unsigned short u16;
typedef u32 v4u __attribute__((ext_vector_type(4)));
typedef u32 v2u __attribute__((ext_vector_type(2)));
typedef int v4i __attribute__((ext_vector_type(4)));
typedef float v4f __attribute__((ext_vector_type(4)));
typedef float v2f __attribute__((ext_vector_type(2)));
typedef short v4s __attribute__((ext_vector_type(4)));
typedef __bf16 v8bf __attribute__((ext_vector_type(8)));
typedef __bf16 v2bf __attribute__((ext_vector_type(2)));
#define LAS __attribute__((address_space(3)))
#define GAS __attribute__((address_space(1)))
#define DEVI __device__ __forceinline__

constexpr int NSLOT = 16, SLOT = 8192;
constexpr int NCONS = 6, NSTASH = 4, NF = 2, NMAX = 72;
#define WA 16
#define WB 20
#define WC 28
#define LA 25
#define LB 50
#define TEND 3705
#define LEADX 130
#define LEADX3 210
#define LEADY 190
#define COFF -20
#define W2W 24
#define HEAD 16
#define XW 5
constexpr int QAT = 12, HDELAY = 440, CODE_BYTES = 23424;
#define BLK_A 65, 65, 65, 63, 63, 63, 63, 63, 63, 63, 64, 64, 65, 65, 65, 65
constexpr int NBLK[2][16] = {{BLK_A}, {65, 65, 65, 63, 62, 62, 62, 62, 62, 62, 67, 67, 65, 65, 65, 65}};
constexpr int BIAS = 11;
constexpr u64 start_table(int k) {
  u64 t = 0; int s = 0;
  for (int r = 0; r < 16; ++r) { const int dl = s - 64 * r + BIAS; if (dl < 0 || dl > 15 || NBLK[k][r] > NMAX) return ~0ull; t |= (u64)dl << (4 * r); s += NBLK[k][r]; }
  return s == 1024 ? t : ~0ull;
}
constexpr u64 ST_A = start_table(0), ST_B = start_table(1);
static_assert(ST_A != ~0ull && ST_B != ~0ull, "block table");
constexpr u64 pace(int k, int r, int extra, int lead) { return ((TEND - lead) * 16 + NBLK[k][r]) / (2 * NBLK[k][r] + extra) - 384; }
constexpr u64 pace_table(int k) {
  return pace(k, 0, 0, LEADX) | pace(k, 3, 0, LEADX3) << 8 | pace(k, 3, 0, LEADX) << 16 | pace(k, 4, 8, 0) << 24 | pace(k, 8, 8, 0) << 32 |
         pace(k, 10, 0, k ? 0 : LEADY) << 40 | pace(k, 12, 2, 0) << 48;
}
constexpr u64 PACE_A = pace_table(0), PACE_B = pace_table(1);
constexpr int L_CTL = NSLOT * SLOT;
constexpr int L_Q = L_CTL + 256;
constexpr int L_ACT = L_Q + 1024;
constexpr int ASTR = 1024 + 64;
constexpr int L_MRG = L_ACT + 2 * ASTR;
constexpr int L_ML = L_MRG + 2048;
constexpr int L_SC = L_ML + 256;
constexpr int L_JUNK = L_SC + NMAX * 256;
constexpr int L_END = L_JUNK + 256;
constexpr int L_H1 = L_SC;
constexpr int L_RED = L_H1 + 256;
static_assert(L_END <= 160 * 1024, "LDS");
constexpr int C_FULL = 0, C_QRDY = 1, C_MCNT = 2, C_CLR = 3, C_ARDY = 4, C_TAG = 5, C_WRD = 6, C_MX = 7, C_SM = 8, C_T0 = 9, C_ISS = 10, C_VISS = 11, C_WIN = 12, C_FREE = 16;
constexpr u32 W_EPOCH = 0, W_T0 = 4096, W_SP = 8192, W_HG = W_SP + 4 * 1024 * 16, W_QG = W_HG + 512 * 8, W_MG = W_QG + 4096 * 8, W_PART = W_MG + 2048 * 8;
constexpr u32 W_ML = W_PART + 64 * 4096;
constexpr u32 W_AP = W_ML + 256 * 128, W_OP = W_AP + 8 * 4096, W_END = W_OP + 28 * 4096;
constexpr u32 CLRB = (W_END - W_PART) / 64;
static_assert(CLRB * 64 == W_END - W_PART && CLRB % 16 == 0, "clear split");
constexpr u32 SENT = 0xFFFFFFFFu;
constexpr int SPIN_CAP = 1 << 16;

DEVI float lo16(u32 v) { return __uint_as_float(v << 16); }
DEVI float bfhi(u32 v) { return __uint_as_float(v & 0xffff0000u); }
DEVI u32 pk2(float a, float b) { v2f x = {a, b}; return __builtin_bit_cast(u32, __builtin_convertvector(x, v2bf)); }
DEVI float rb(float a) { return lo16(pk2(a, a)); }
DEVI float ex2(float x) { return __builtin_amdgcn_exp2f(x); }
DEVI float rcp(float x) { return __builtin_amdgcn_rcpf(x); }
DEVI float silu(float v) { return v * rcp(1.f + ex2(v * -1.4426950408889634f)); }
DEVI float silu_exact(float v) {
  const float t = fminf(v * -1.4426950216293335f, 126.f);
  const float r = fmaf(v, -1.92596299112662e-08f, fmaf(v, -1.4426950216293335f, -t));
  float e = ex2(t);
  e = fmaf(e, fminf(r, 1.f) * 0.6931471805599453f, e);
  const float d = 1.f + e, rc = rcp(d), q = v * rc;
  return fmaf(fmaf(-q, d, v), rc, q);
}
DEVI v2f rb2(v2f a) { const u32 p = pk2(a.x, a.y); return v2f{lo16(p), bfhi(p)}; }
DEVI float fold32(float a, float b) { auto s = __builtin_amdgcn_permlane32_swap(__float_as_uint(a), __float_as_uint(b), false, false); return __uint_as_float(s[0]) + __uint_as_float(s[1]); }
DEVI float fold16(float a, float b) { auto s = __builtin_amdgcn_permlane16_swap(__float_as_uint(a), __float_as_uint(b), false, false); return __uint_as_float(s[0]) + __uint_as_float(s[1]); }
#define PIN(x) asm volatile("" : "+v"(x))
DEVI float swap16_add(float x) { auto s = __builtin_amdgcn_permlane16_swap(__float_as_uint(x), __float_as_uint(x), false, false); return __uint_as_float(s[0]) + __uint_as_float(s[1]); }
DEVI float swap32_add(float x) { auto s = __builtin_amdgcn_permlane32_swap(__float_as_uint(x), __float_as_uint(x), false, false); return __uint_as_float(s[0]) + __uint_as_float(s[1]); }
DEVI float swap16_max(float x) { auto s = __builtin_amdgcn_permlane16_swap(__float_as_uint(x), __float_as_uint(x), false, false); return fmaxf(__uint_as_float(s[0]), __uint_as_float(s[1])); }
DEVI float swap32_max(float x) { auto s = __builtin_amdgcn_permlane32_swap(__float_as_uint(x), __float_as_uint(x), false, false); return fmaxf(__uint_as_float(s[0]), __uint_as_float(s[1])); }
template <int CTRL> DEVI float dpp(float x) { return __builtin_bit_cast(float, __builtin_amdgcn_mov_dpp(__builtin_bit_cast(int, x), CTRL, 0xf, 0xf, true)); }
DEVI float row_sum(float x) { x += dpp<0xB1>(x); x += dpp<0x4E>(x); x += dpp<0x141>(x); x += dpp<0x128>(x); return x; }
DEVI float half_sum(float x) { return swap16_add(row_sum(x)); }
DEVI float wave_sum(float x) { return swap32_add(half_sum(x)); }
template <typename T> DEVI LAS T* lp(u32 a) { return (LAS T*)(size_t)a; }
DEVI u32 ldsv(u32 a) { return *(volatile LAS u32*)(size_t)a; }
DEVI void stsv(u32 a, u32 v) { *(volatile LAS u32*)(size_t)a = v; }
#define FENCE() asm volatile("" ::: "memory")
#define WAITV(n) asm volatile("s_waitcnt vmcnt(%0)" ::"i"(n) : "memory")
#define WAITL() asm volatile("s_waitcnt lgkmcnt(0)" ::: "memory")
DEVI void bar() { WAITL(); __builtin_amdgcn_s_barrier(); FENCE(); }
typedef __amdgpu_buffer_rsrc_t rsrc_t;
DEVI v4u ld4(rsrc_t r, u32 vo, u32 so) { return __builtin_bit_cast(v4u, __builtin_amdgcn_raw_buffer_load_b128(r, vo, so, 16)); }
DEVI v2u ld2(rsrc_t r, u32 vo, u32 so) { return __builtin_bit_cast(v2u, __builtin_amdgcn_raw_buffer_load_b64(r, vo, so, 16)); }
DEVI void st4(v4u v, rsrc_t r, u32 vo, u32 so) { __builtin_amdgcn_raw_buffer_store_b128(v, r, vo, so, 16); }
DEVI void st2(v2u v, rsrc_t r, u32 vo, u32 so) { __builtin_amdgcn_raw_buffer_store_b64(v, r, vo, so, 16); }
DEVI u32 canon(float f) { u32 b = __float_as_uint(f); return b < 0xFFFFFFFEu ? b : 0xFFFFFFFEu; }
DEVI v8bf trfrag(u32 a) {
  v4s lo = __builtin_amdgcn_ds_read_tr16_b64_v4i16(lp<v4s>(a)), hi = __builtin_amdgcn_ds_read_tr16_b64_v4i16(lp<v4s>(a + 2048));
  return __builtin_bit_cast(v8bf, __builtin_shufflevector(lo, hi, 0, 1, 2, 3, 4, 5, 6, 7));
}
DEVI v4f mfma(v8bf a, v8bf b, v4f c) { return __builtin_amdgcn_mfma_f32_16x16x32_bf16(a, b, c, 0, 0, 0); }
DEVI v8bf ldfrag(u32 a) { return __builtin_bit_cast(v8bf, *lp<v4u>(a)); }
DEVI int fpos(int k) { return (k & ~31) + ((k >> 2) & 3) * 8 + ((k >> 4) & 1) * 4 + (k & 3); }
DEVI v4i mksrd(const void* p, u32 bytes) { u64 a = (u64)p; return v4i{(int)(u32)a, (int)(u32)(a >> 32), (int)bytes, 0x00020000}; }

#define DMA4(m, v0, v1, v2, v3, srd, so)                                                                                   \
  asm volatile("s_mov_b32 m0, %0\n\ts_nop 0\n\tbuffer_load_dwordx4 %1, %5, %6 offen nt lds\n\t"                           \
               "buffer_load_dwordx4 %2, %5, %6 offen offset:1024 nt lds\n\tbuffer_load_dwordx4 %3, %5, %6 offen offset:2048 nt lds\n\t" \
               "buffer_load_dwordx4 %4, %5, %6 offen offset:3072 nt lds" ::"s"(m), "v"(v0), "v"(v1), "v"(v2), "v"(v3), "s"(srd), "s"(so) : "memory")

__global__ void __launch_bounds__(512, 2)
fused_block(const u16* __restrict__ Kc, const u16* __restrict__ Vc, const u16* __restrict__ wmod, const u16* __restrict__ wq,
            const u16* __restrict__ wo, const u16* __restrict__ w1, const u16* __restrict__ w2, const u16* __restrict__ xin,
            const u16* __restrict__ cin, u16* __restrict__ out, unsigned char* ws) {
  __shared__ __attribute__((aligned(16))) unsigned char smem[L_END];
  asm volatile("" ::"s"((u32)(size_t)(LAS unsigned char*)smem));
  const int tid = threadIdx.x, lane = tid & 63, wave = __builtin_amdgcn_readfirstlane(tid >> 6);
  if (wave == 1 && blockIdx.x < 32) {
    u64 pc; asm volatile("s_getpc_b64 %0" : "=s"(pc));
    const v4i rc = mksrd((const void*)(pc & ~127ull), CODE_BYTES);
    const u32 vo = lane * 128, m = L_JUNK, s1 = 8192, s2 = 16384;
    asm volatile("s_mov_b32 m0, %0\n\ts_nop 0\n\tbuffer_load_dword %1, %2, 0 offen lds\n\tbuffer_load_dword %1, %2, %3 offen lds\n\tbuffer_load_dword %1, %2, %4 offen lds"
                 ::"s"(m), "v"(vo), "s"(rc), "s"(s1), "s"(s2) : "memory");
  }
  const int bid = (blockIdx.x & ~31) | ((blockIdx.x & 7) << 2) | ((blockIdx.x >> 3) & 3);
  const int grp = bid >> 4, r = bid & 15;
  const int g = lane >> 4, c = lane & 15;
  const u64 stt = (grp < 14 ? ST_A : ST_B) >> (4 * r);
  const int kv0 = 64 * r + ((int)stt & 15) - BIAS;
  const bool isS = r >= 12;
  if (__builtin_expect(wave == 0, 1)) {
    const v4i srd = isS ? mksrd(wmod, 512 * 6144) : mksrd(Kc, 1u << 27);
    const u32 k0 = g * 256 + ((c ^ g) << 4), w0 = (lane >> 3) * 6144 + (((lane & 7) ^ ((lane >> 3) & 6)) << 4);
    const u32 a0 = isS ? w0 : k0, a1 = isS ? w0 + 48128 : k0 ^ 64, a2 = isS ? w0 + 2 * 48128 : k0 ^ 128, a3 = isS ? w0 + 3 * 48128 : k0 ^ 192;
    const u32 so = isS ? (r - 12) * 128 * 6144 + grp * 128 : (u32)(grp * 1024 + kv0) << 13, hs = isS ? 32 * 6144 : 4096;
#pragma unroll
    for (int i = 0; i < 4; ++i) { const u32 m = i * 4096, s1 = so + i * hs; DMA4(m, a0, a1, a2, a3, srd, s1); }
  }
  if (wave == 2) { stsv(L_CTL + 4 * lane, 0); WAITL(); }
  __builtin_amdgcn_s_barrier(); FENCE();
  const u64 tstart = __builtin_amdgcn_s_memrealtime();
  FENCE();
  const bool isX = r < 4, isQ = r >= 4 && r < 8, isM = r == 8 || r == 9, isY = (r == 10 || r == 11) && grp < 14, isR = r == 0 && (grp & 1);
  const int xi = grp * 4 + r, qi = grp * 4 + r - 4, mi = grp * 2 + r - 8, yi = grp * 2 + r - 10;
  const int u0 = (int)(0x2222008888880000ull >> (4 * r)) & 15;
  const int nb = 64 + (r == 15 ? BIAS : (int)(stt >> 4) & 15) - ((int)stt & 15);
  const int nf = isS ? 0 : NF;
  const int ntail = isX ? 8 : isY ? 16 : 0;
  const int uv = u0 + nb, uw = uv + nb;
  u64 E = 0;
  auto arrive = [&](int w) { if (lane == 0) __hip_atomic_fetch_add(lp<u32>(L_CTL + 4 * w), 1u, __ATOMIC_RELAXED, __HIP_MEMORY_SCOPE_WORKGROUP); };
  int wv = wave;
  asm volatile("" : "+s"(wv));
  if (__builtin_expect(wv == 0, 1)) {
    __builtin_amdgcn_s_setprio(3);
    int iss, pub = 0;
    auto publish = [&](int v) { pub = max(pub, v); stsv(L_CTL + 4 * C_FULL, pub); };
#define PACE(W_) do { WAITV(W_); publish(iss - (W_)); } while (0)
    const u32 cls = ((u32)(0x6666554433331000ull >> (4 * r)) & 7) + (r == 3 && (grp & 1));
    const u32 tau = 384 + ((u32)((grp < 14 ? PACE_A : PACE_B) >> (8 * cls)) & 255);
    u32 due = ((u32)tstart + COFF) * 16 + NF * tau;
    const u32 wrow = lane >> 3, wlc = ((lane & 7) ^ (((wrow >> 1) & 3) << 1)) << 4;
    int u = NF;
#pragma nounroll
    for (int seg = u0 == 8 ? 0 : 1; seg < (ntail ? 4 : 3); ++seg) {
      const bool kv = seg == 1 || seg == 2;
      const u16* base = seg == 0 ? (isQ ? wq : wmod) : seg == 1 ? Kc : seg == 2 ? Vc : isX ? wo : w1;
      const u32 rs = seg == 0 ? (isQ ? 8192 : 6144) : (seg == 3 && !isX) ? 3584 : 1024;
      const v4i srd = mksrd(base, kv ? 1u << 27 : (seg == 3 && isX) ? 4096 * 1024 : 512 * rs);
      u32 so = kv ? (u32)(grp * 1024 + kv0 + (seg == 1 ? nf : 0)) << 13
                  : seg == 0 ? (isQ ? qi : 16 + mi) * 128
                             : isX ? ((xi >> 3) << 19) + (xi & 7) * 128 : yi * 128;
      const u32 hstep = kv ? 4096 : 32 * rs, ustep = 2 * hstep;
      const u32 sw = seg == 2 ? 2 * g : g, sx = seg == 1 ? 4 : 8;
      const u32 w0 = wrow * rs + wlc, wd = 8 * rs - 1024;
      const u32 a0 = kv ? g * 256 + ((c ^ sw) << 4) : w0, a1 = kv ? g * 256 + ((c ^ sw ^ sx) << 4) : w0 + wd;
      const u32 a2 = kv ? g * 256 + ((c ^ sw ^ ((2 * sx) & 15)) << 4) : w0 + 2 * wd, a3 = kv ? g * 256 + ((c ^ sw ^ ((3 * sx) & 15)) << 4) : w0 + 3 * wd;
      const int ue = seg == 0 ? NF + u0 : seg == 1 ? uv : seg == 2 ? uw : uw + (ntail >> 1);
#pragma nounroll
      while (u < ue) {
        const bool paced = u >= 4 && seg < 3;
        const int stop = min(ue, u < 4 ? 4 : u < 48 ? 48 : 1 << 20);
        u32 dd = paced ? due : due - (1u << 28);
        const int u_in = u, la = (seg == 3 && isX) ? 0x7fffffff : LA * 16;
        u32 t0, t1, lag; u32 vf, vt;
#define DMAS(so) "buffer_load_dwordx4 %[a0], %[srd], " so " offen nt lds\n\tbuffer_load_dwordx4 %[a1], %[srd], " so " offen offset:1024 nt lds\n\t" \
                 "buffer_load_dwordx4 %[a2], %[srd], " so " offen offset:2048 nt lds\n\tbuffer_load_dwordx4 %[a3], %[srd], " so " offen offset:3072 nt lds\n\t"
#define RDFREE "s_lshl_b32 %[t1], %[u], 2\n\ts_and_b32 %[t1], %[t1], 60\n\ts_or_b32 %[t1], %[t1], %[fb]\n\tv_mov_b32 %[vf], %[t1]\n\tds_read_b32 %[vf], %[vf]\n\t"
#define REST(W) "s_waitcnt vmcnt(" #W ")\n\ts_mov_b32 m0, %[t0]\n\ts_lshl_b32 %[t0], %[u], 3\n\t" DMAS("%[t1]")                                   \
                "s_sub_i32 %[t0], %[t0], " #W " - 4\n\ts_max_i32 %[pub], %[pub], %[t0]\n\tv_mov_b32 %[vt], %[pub]\n\tds_write_b32 %[va], %[vt]\n\t"                                      \
                "s_add_i32 %[u], %[u], 1\n\ts_add_u32 %[so], %[so], %[us]\n\ts_add_u32 %[dd], %[dd], %[tau]\n\t" RDFREE                             \
                "s_memrealtime s[98:99]\n\ts_cmp_lt_i32 %[u], %[stop]\n\ts_waitcnt vmcnt(" #W ")\n\ts_cbranch_scc1 1b\n\ts_branch 9f\n"
#define XREST(W) REST(W)
        asm volatile(RDFREE "s_memrealtime s[98:99]\n"
                     "1:\n\ts_sub_i32 %[t1], %[u], 15\n\ts_waitcnt lgkmcnt(0)\n\tv_cmp_gt_i32 vcc, %[t1], %[vf]\n\ts_lshl_b32 s98, s98, 4\n\t"
                     "s_cbranch_vccnz 8f\n"
                     "2:\n\ts_sub_i32 %[lag], s98, %[dd]\n\ts_cmp_lt_i32 %[lag], 0\n\ts_cbranch_scc1 7f\n\t"
                     "s_lshl_b32 %[t0], %[u], 13\n\ts_and_b32 %[t0], %[t0], 0x1e000\n\ts_mov_b32 m0, %[t0]\n\ts_add_u32 %[t1], %[so], %[hs]\n\t" DMAS("%[so]")
                     "s_or_b32 %[t0], %[t0], 0x1000\n\ts_cmp_lt_i32 %[lag], %[la]\n\ts_cbranch_scc1 4f\n\ts_cmp_lt_i32 %[lag], %[lb]\n\ts_cbranch_scc1 5f\n\t"
                     XREST(WC) "4:\n\t" XREST(WA) "5:\n\t" XREST(WB)
                     "8:\n\t" RDFREE "s_sub_i32 %[t1], %[u], 15\n\ts_waitcnt lgkmcnt(0)\n\tv_cmp_gt_i32 vcc, %[t1], %[vf]\n\ts_nop 0\n\ts_cbranch_vccz 7f\n\t"
                     "s_waitcnt vmcnt(0)\n\ts_lshl_b32 %[pub], %[u], 3\n\tv_mov_b32 %[vt], %[pub]\n\tds_write_b32 %[va], %[vt]\n"
                     "6:\n\t" RDFREE "s_sub_i32 %[t1], %[u], 15\n\ts_waitcnt lgkmcnt(0)\n\tv_cmp_gt_i32 vcc, %[t1], %[vf]\n\ts_sleep 1\n\ts_cbranch_vccnz 6b\n"
                     "7:\n\ts_memrealtime s[98:99]\n\ts_waitcnt lgkmcnt(0)\n\ts_lshl_b32 s98, s98, 4\n\ts_branch 2b\n"
                     "9:\n\ts_waitcnt lgkmcnt(0)"
                     : [u] "+s"(u), [so] "+s"(so), [dd] "+s"(dd), [pub] "+s"(pub), [t0] "=&s"(t0), [t1] "=&s"(t1), [lag] "=&s"(lag), [vf] "=&v"(vf), [vt] "=&v"(vt)
                     : [a0] "v"(a0), [a1] "v"(a1), [a2] "v"(a2), [a3] "v"(a3), [srd] "s"(srd), [hs] "s"(hstep), [us] "s"(ustep), [stop] "s"(stop),
                       [va] "v"(L_CTL + 4 * C_FULL), [fb] "i"(L_CTL + 4 * C_FREE), [tau] "s"(tau), [la] "s"(la), [lb] "i"(LB * 16)
                     : "memory", "vcc", "scc", "s98", "s99");
        due += (u - u_in) * tau;
        if (u == 4 && isS) { WAITV(16); publish(16); }
        if (u == uw) stsv(L_CTL + 4 * C_VISS, 1);
        if (u == 48) { const u32 t = __builtin_amdgcn_readfirstlane(ldsv(L_CTL + 4 * C_T0)); if (t) due = t * 16 + u * tau; }
      }
    }
    iss = 8 * u;
    if (!ntail) { PACE(24); PACE(16); PACE(8); PACE(0); }
    else arrive(C_ISS);
    __builtin_amdgcn_s_setprio(0);
  } else {
    const rsrc_t rws = __builtin_amdgcn_make_buffer_rsrc(ws, 0, W_END, 0x00020000);
    auto wait_full = [&](u32 need) { while (ldsv(L_CTL + 4 * C_FULL) < need) __builtin_amdgcn_s_sleep(1); FENCE(); };
    const u32 trow = 4 * g + (c >> 2);
    auto waddr = [&](int t) -> u32 { return trow * 128 + ((((u32)(t ^ ((trow >> 1) & 3)) << 1) | ((c >> 1) & 1)) << 4) + (c & 1) * 8; };

    if (wave == 1) {
      const u64 E0 = __hip_atomic_load((GAS u64*)(ws + W_EPOCH), __ATOMIC_RELAXED, __HIP_MEMORY_SCOPE_AGENT);
      u32 tag;
#define CHAIN_START() do { E = E0 + 1; tag = (u32)E; } while (0)
      auto silu4 = [&](u32 x, u32 y) { return v2u{pk2(silu_exact(lo16(x)), silu_exact(bfhi(x))), pk2(silu_exact(lo16(y)), silu_exact(bfhi(y)))}; };
      if (isS) {
        const v2u cv = *(const v2u*)(cin + (lane >> 5) * 512 + (r - 12) * 128 + (lane & 31) * 4);
        CHAIN_START();
        *lp<v2u>(L_ACT + (lane >> 5) * ASTR + fpos((lane & 31) * 4) * 2) = silu4(cv.x, cv.y);
        FENCE();
        v8bf act[4];
#pragma unroll
        for (int ks = 0; ks < 4; ++ks) { act[ks] = ldfrag(L_ACT + (c & 1) * ASTR + g * 16 + ks * 64); PIN(act[ks]); }
        wait_full(16);
        v8bf W[16];
#pragma unroll
        for (int j = 0; j < 16; ++j) W[j] = trfrag(waddr(j & 3) + (j >> 2) * 4096);
        FENCE(); stsv(L_CTL + 4 * C_FREE, 1); stsv(L_CTL + 4 * (C_FREE + 1), 2);
        v4f acc[4];
#pragma unroll
        for (int t = 0; t < 4; ++t) acc[t] = v4f{0.f, 0.f, 0.f, 0.f};
#pragma unroll
        for (int j = 0; j < 16; ++j) acc[j & 3] = mfma(act[j >> 2], W[j], acc[j & 3]);
        const float r0 = g == 0 ? acc[0][0] : g == 1 ? acc[1][0] : g == 2 ? acc[2][0] : acc[3][0];
        const float r1 = g == 0 ? acc[0][1] : g == 1 ? acc[1][1] : g == 2 ? acc[2][1] : acc[3][1];
        while (ldsv(L_CTL + 4 * C_CLR) == 0) { }
        FENCE();
        st4(v4u{__float_as_uint(r0), tag, __float_as_uint(r1), tag}, rws, lane * 16, W_SP + ((r - 12) * 1024 + grp * 64) * 16);
      } else if (isR) {
        const v4u in0 = *(const v4u*)(xin + lane * 8), in1 = *(const v4u*)(xin + 512 + lane * 8);
        u32 xs0 = *(const u32*)(xin + (grp >> 1) * 64 + (lane & ~1)), xs1 = *(const u32*)(xin + 512 + (grp >> 1) * 64 + (lane & ~1));
        CHAIN_START();
        PIN(xs0); PIN(xs1);
        xs0 >>= 16 * (lane & 1); xs1 >>= 16 * (lane & 1);
        float mr[2], ri[2];
#pragma unroll
        for (int b = 0; b < 2; ++b) {
          const v4u xv = b ? in1 : in0;
          const float x[8] = {lo16(xv.x), bfhi(xv.x), lo16(xv.y), bfhi(xv.y), lo16(xv.z), bfhi(xv.z), lo16(xv.w), bfhi(xv.w)};
          float s = 0.f;
#pragma unroll
          for (int i = 0; i < 8; ++i) s += x[i];
          const float mean = wave_sum(s) * (1.f / 512.f);
          float q2 = 0.f;
#pragma unroll
          for (int i = 0; i < 8; ++i) q2 += (x[i] - mean) * (x[i] - mean);
          const float var = wave_sum(q2) * (1.f / 512.f);
          mr[b] = rb(mean); ri[b] = rcp(rb(__builtin_amdgcn_sqrtf(rb(rb(var) + 1e-5f))));
        }
        v2f y = rb2(rb2(v2f{lo16(xs0) - mr[0], lo16(xs1) - mr[1]}) * v2f{ri[0], ri[1]});
        PIN(y);
        v4u a[8];
#pragma nounroll
        for (int sp = 0; sp < SPIN_CAP; ++sp) {
          u32 bad = 0;
#pragma unroll
          for (int i = 0; i < 8; ++i) { a[i] = ld4(rws, lane * 16, W_SP + ((i & 3) * 1024 + (i >> 2) * 512 + (grp >> 1) * 64) * 16); bad |= (a[i].y ^ tag) | (a[i].w ^ tag); }
          if (__all(bad == 0)) break;
          FENCE();
        }
        v2f sc[2];
#pragma unroll
        for (int pt = 0; pt < 2; ++pt) {
          sc[pt] = v2f{0.f, 0.f};
#pragma unroll
          for (int k4 = 0; k4 < 4; ++k4) sc[pt] += v2f{__uint_as_float(a[pt * 4 + k4].x), __uint_as_float(a[pt * 4 + k4].z)};
        }
        const v2f h = rb2(y * rb2(rb2(sc[1]) + 1.f)) + rb2(sc[0]);
        st2(v2u{pk2(h.x, h.y), tag}, rws, lane * 8, W_HG + (grp >> 1) * 512);
      } else if (isM || isQ) {
        if (isM) {
          const v4u in0 = *(const v4u*)(cin + lane * 8), in1 = *(const v4u*)(cin + 512 + lane * 8);
          CHAIN_START();
          *lp<v2u>(L_ACT + fpos(lane * 8) * 2) = silu4(in0.x, in0.y); *lp<v2u>(L_ACT + fpos(lane * 8 + 4) * 2) = silu4(in0.z, in0.w);
          *lp<v2u>(L_ACT + ASTR + fpos(lane * 8) * 2) = silu4(in1.x, in1.y); *lp<v2u>(L_ACT + ASTR + fpos(lane * 8 + 4) * 2) = silu4(in1.z, in1.w);
        } else {
          CHAIN_START();
          while (__builtin_amdgcn_s_memrealtime() < tstart + HDELAY) __builtin_amdgcn_s_sleep(4);
          v4u a[4];
#pragma nounroll
          for (int sp = 0; sp < SPIN_CAP; ++sp) {
            u32 bad = 0;
#pragma unroll
            for (int i = 0; i < 4; ++i) { a[i] = ld4(rws, lane * 16 + i * 1024, W_HG); bad |= (a[i].y ^ tag) | (a[i].w ^ tag); }
            if (__all(bad == 0)) break;
            FENCE();
          }
#pragma unroll
          for (int i = 0; i < 4; ++i) {
            const u32 wa = L_ACT + fpos(i * 128 + lane * 2) * 2;
            *lp<u32>(wa) = (a[i].x & 0xffffu) | (a[i].z << 16); *lp<u32>(wa + ASTR) = (a[i].x >> 16) | (a[i].z & 0xffff0000u);
          }
        }
        FENCE(); stsv(L_CTL + 4 * C_TAG, tag); stsv(L_CTL + 4 * C_ARDY, 1);
      } else CHAIN_START();
      if (blockIdx.x < 8 && lane == 0) st2(v2u{(u32)tstart, tag}, rws, blockIdx.x * 8, W_T0);
      {
        wait_full(8 * u0 + 16 * QAT);
        v4u a[2];
#pragma nounroll
        for (int sp = 0; sp < SPIN_CAP; ++sp) {
          a[0] = ld4(rws, lane * 16, W_QG + grp * 2048); a[1] = ld4(rws, lane * 16 + 1024, W_QG + grp * 2048);
          if (__all(((a[0].y ^ tag) | (a[0].w ^ tag) | (a[1].y ^ tag) | (a[1].w ^ tag)) == 0)) break;
          __builtin_amdgcn_s_sleep(16); FENCE();
        }
        *lp<v2u>(L_Q + lane * 8) = v2u{a[0].x, a[0].z}; *lp<v2u>(L_Q + 512 + lane * 8) = v2u{a[1].x, a[1].z};
        FENCE(); stsv(L_CTL + 4 * C_TAG, tag); stsv(L_CTL + 4 * C_QRDY, 1);
        {
          const v2u t = ld2(rws, (lane & 7) * 8, W_T0);
          if (__all(t.y == tag)) {
            int dt = (int)(t.x - (u32)tstart);
            dt += __builtin_amdgcn_mov_dpp(dt, 0xB1, 0xf, 0xf, true); dt += __builtin_amdgcn_mov_dpp(dt, 0x4E, 0xf, 0xf, true); dt += __builtin_amdgcn_mov_dpp(dt, 0x141, 0xf, 0xf, true);
            if (dt > -2400 && dt < 2400) stsv(L_CTL + 4 * C_T0, ((u32)tstart + (dt >> 3) + COFF) | 1);
          }
        }
      }
      if (ntail) {
        const v4i srd = mksrd(isX ? wo : w2, isX ? 4096 * 1024 : 1792 * 1024);
        const u32 a0 = (lane >> 3) * 1024 + (((lane & 7) ^ ((lane >> 3) & 6)) << 4), a1 = a0 + 7168, a2 = a0 + 2 * 7168, a3 = a0 + 3 * 7168;
        u32 so = isX ? ((xi >> 3) << 19) + (xi & 7) * 128 + 256 * 1024 : yi << 16;
        while (ldsv(L_CTL + 4 * C_VISS) == 0) __builtin_amdgcn_s_sleep(2);
#pragma nounroll
        for (int u = uw + (ntail >> 1); u < uw + ntail; ++u, so += isX ? 65536 : 128) {
          while ((int)ldsv(L_CTL + 4 * (C_FREE + (u & (NSLOT - 1)))) < u + 1 - NSLOT) __builtin_amdgcn_s_sleep(1);
          const u32 lb = (u & (NSLOT - 1)) * SLOT, m = lb + 4096, s1 = so + 32768;
          DMA4(lb, a0, a1, a2, a3, srd, so);
          DMA4(m, a0, a1, a2, a3, srd, s1);
          WAITV(W2W);
        }
        arrive(C_ISS);
      }
      if (r == 8) {
        wait_full(8 * uw);
        const int zb = grp >> 3, zc = (grp & 7) * 64 + c * 4 + g;
        const u32 xv = xin[zb * 512 + zc];
        v2u m0, m1;
#pragma nounroll
        for (int sp = 0; sp < SPIN_CAP; ++sp) {
          m0 = ld2(rws, zc * 8, W_MG); m1 = ld2(rws, zc * 8, W_MG + 3 * 4096);
          if (__all(((m0.y ^ tag) | (m1.y ^ tag)) == 0)) break;
          FENCE();
        }
        v4u z[7];
#pragma nounroll
        for (int sp = 0; sp < SPIN_CAP; ++sp) {
          z[0] = ld4(rws, g * 4096 + c * 16, W_AP + grp * 256); z[1] = ld4(rws, g * 4096 + c * 16, W_AP + 16384 + grp * 256);
          if (__all(max(max(max(z[0].x, z[0].y), max(z[0].z, z[0].w)), max(max(z[1].x, z[1].y), max(z[1].z, z[1].w))) != SENT)) break;
          __builtin_amdgcn_s_sleep(2); FENCE();
        }
        v4f s0 = __builtin_bit_cast(v4f, z[0]), s1 = __builtin_bit_cast(v4f, z[1]);
        const float at = fold32(fold16(s0.x, s0.y), fold16(s0.z, s0.w)) + fold32(fold16(s1.x, s1.y), fold16(s1.z, s1.w));
        float x1 = rb(lo16(xv) + rb(bfhi(m0.x << (16 - 16 * zb)) * rb(at))), gb = bfhi(m1.x << (16 - 16 * zb));
        GAS u16* op = (GAS u16*)out + zb * 512 + zc;
        PIN(x1); PIN(gb); PIN(op);
#pragma nounroll
        for (int sp = 0; sp < SPIN_CAP; ++sp) {
#pragma unroll
          for (int j = 0; j < 7; ++j) z[j] = ld4(rws, g * 4096 + c * 16, W_OP + j * 16384 + grp * 256);
          u32 mx = 0;
#pragma unroll
          for (int j = 0; j < 7; ++j) mx = max(mx, max(max(z[j].x, z[j].y), max(z[j].z, z[j].w)));
          if (__all(mx != SENT)) break;
          FENCE();
        }
        const v4f s = ((__builtin_bit_cast(v4f, z[0]) + __builtin_bit_cast(v4f, z[1])) + (__builtin_bit_cast(v4f, z[2]) + __builtin_bit_cast(v4f, z[3]))) +
                      ((__builtin_bit_cast(v4f, z[4]) + __builtin_bit_cast(v4f, z[5])) + __builtin_bit_cast(v4f, z[6]));
        const float rr = x1 + rb(gb * rb(fold16(fold32(s.x, s.z), fold32(s.y, s.w))));
        *op = (u16)pk2(rr, rr);
        __builtin_amdgcn_endpgm();
      }
    } else {
      if (isS && wave == 7) {
        const v4u s4 = {SENT, SENT, SENT, SENT};
        const u32 so = W_PART + (grp * 4 + r - 12) * CLRB;
#pragma unroll
        for (int i = 0; i < CLRB / 1024; ++i) st4(s4, rws, lane * 16 + (i & 3) * 1024, so + (i >> 2) * 4096);
        if (lane < (CLRB % 1024) / 16) st4(s4, rws, lane * 16, so + CLRB / 1024 * 1024);
        WAITV(0); stsv(L_CTL + 4 * C_CLR, 1);
      }

      if (wave == 6) { *lp<v4f>(L_MRG + lane * 16) = v4f{0.f, 0.f, 0.f, 0.f}; *lp<v4f>(L_MRG + 1024 + lane * 16) = v4f{0.f, 0.f, 0.f, 0.f}; }
      const int d = c >> 2, cw = wave - 2;
      const u32 sca = L_SC + (g * 4 + (c & 3)) * 16;
      {
        u32 ka[4];
#pragma unroll
        for (int s = 0; s < 4; ++s) ka[s] = c * 256 + (((4 * s + g) ^ c) << 4);
        v8bf qf[4];
        float mx = -INFINITY;
        auto rdk = [&](v8bf (&kf)[8], int j) {
          const int u = j < nf ? j : u0 + j;
          const u32 lb = (u & (NSLOT - 1)) * SLOT;
          wait_full(8 * u + 8);
#pragma unroll
          for (int s = 0; s < 4; ++s) { kf[s] = ldfrag(lb + ka[s]); kf[4 + s] = ldfrag(lb + ka[s] + 4096); }
          FENCE(); stsv(L_CTL + 4 * (C_FREE + (u & (NSLOT - 1))), u + 1);
        };
        auto qk = [&](v8bf (&kf)[8], int j) {
          v4f S0 = {0.f, 0.f, 0.f, 0.f}, S1 = {0.f, 0.f, 0.f, 0.f};
#pragma unroll
          for (int s = 0; s < 4; ++s) { S0 = mfma(kf[s], qf[s], S0); S1 = mfma(kf[4 + s], qf[s], S1); }
          const v2f a = rb2(v2f{S0.x, S0.y}) * 0.08838834764831845f, b = rb2(v2f{S0.z, S0.w}) * 0.08838834764831845f;
          const v2f e = rb2(v2f{S1.x, S1.y}) * 0.08838834764831845f, f = rb2(v2f{S1.z, S1.w}) * 0.08838834764831845f;
          mx = fmaxf(fmaxf(fmaxf(mx, fmaxf(a.x, a.y)), fmaxf(b.x, b.y)), fmaxf(fmaxf(e.x, e.y), fmaxf(f.x, f.y)));
          if (c < 4) *lp<v4u>(sca + j * 256) = v4u{pk2(a.x, a.y), pk2(b.x, b.y), pk2(e.x, e.y), pk2(f.x, f.y)};
        };
        v8bf sk[NSTASH][8];
        rdk(sk[0], cw);
        if (u0 == 8 && wave < 6) {
          wait_full(8 * NF + 64);
          v8bf W[16];
#pragma unroll
          for (int ks = 0; ks < 16; ++ks) W[ks] = trfrag(NF * SLOT + ks * 4096 + waddr(wave - 2));
          WAITL();
          u32 old = 0;
          if (lane == 0) old = __hip_atomic_fetch_add(lp<u32>(L_CTL + 4 * C_WRD), 1u, __ATOMIC_RELAXED, __HIP_MEMORY_SCOPE_WORKGROUP);
          if (__builtin_amdgcn_readfirstlane(old) == 3 && lane < 8) stsv(L_CTL + 4 * (C_FREE + NF + lane), NF + lane + 1);
          while (ldsv(L_CTL + 4 * C_ARDY) == 0) __builtin_amdgcn_s_sleep(1);
          FENCE();
          const u32 tag = ldsv(L_CTL + 4 * C_TAG);
          v8bf A[16];
#pragma unroll
          for (int ks = 0; ks < 16; ++ks) A[ks] = ldfrag(L_ACT + (c & 1) * ASTR + g * 16 + ks * 64);
          __builtin_amdgcn_sched_barrier(0);
          v4f d0 = {0.f, 0.f, 0.f, 0.f}, d1 = {0.f, 0.f, 0.f, 0.f};
#pragma unroll
          for (int ks = 0; ks < 16; ks += 2) { d0 = mfma(A[ks], W[ks], d0); d1 = mfma(A[ks + 1], W[ks + 1], d1); }
          const float r0 = d0[0] + d1[0], r1 = d0[1] + d1[1];
          const float n0 = dpp<0xB1>(r0), n1 = dpp<0xB1>(r1);
          const bool odd = lane & 1;
          if (lane < 16) {
            if (isM) st2(v2u{pk2(r0, r1), tag}, rws, lane * 8, W_MG + mi * 512 + (wave - 2) * 128);
            else st2(v2u{pk2(odd ? n1 : r0, odd ? r1 : n0), tag}, rws, (lane & 1) * 16384 + (lane >> 1) * 8, W_QG + qi * 256 + (wave - 2) * 64);
          }
        }
#pragma unroll
        for (int i = 1; i < NSTASH; ++i) rdk(sk[i], cw + i * NCONS);
        while (ldsv(L_CTL + 4 * C_QRDY) == 0) __builtin_amdgcn_s_sleep(2);
        FENCE();
#pragma unroll
        for (int s = 0; s < 4; ++s) qf[s] = ldfrag(L_Q + (c & 3) * 256 + (4 * s + g) * 16);
#pragma unroll
        for (int i = 0; i < NSTASH; ++i) qk(sk[i], cw + i * NCONS);
#pragma nounroll
        for (int j = cw + NSTASH * NCONS; j < nb; j += NCONS) { v8bf kf[8]; rdk(kf, j); qk(kf, j); }
        mx = swap32_max(swap16_max(rb(mx)));
        if (lane < 4) *lp<float>(L_ML + cw * 16 + lane * 4) = mx;
        FENCE();
        arrive(C_MX);
      }
      float M;
      {
        while (ldsv(L_CTL + 4 * C_MX) < NCONS) __builtin_amdgcn_s_sleep(1);
        FENCE();
        M = *lp<float>(L_ML + (c & 3) * 4);
#pragma unroll
        for (int w = 1; w < NCONS; ++w) M = fmaxf(M, *lp<float>(L_ML + w * 16 + (c & 3) * 4));
        float l = 0.f;
#pragma nounroll
        for (int j = cw + d * NCONS; j < nb; j += 4 * NCONS) {
          const v4u s = *lp<v4u>(sca + j * 256);
          float p[8];
#pragma unroll
          for (int i = 0; i < 4; ++i) { p[2 * i] = ex2((lo16(s[i]) - M) * 1.4426950408889634f); p[2 * i + 1] = ex2((bfhi(s[i]) - M) * 1.4426950408889634f); }
          l += ((p[0] + p[1]) + (p[2] + p[3])) + ((p[4] + p[5]) + (p[6] + p[7]));
          *lp<v4u>(sca + j * 256) = v4u{pk2(p[0], p[1]), pk2(p[2], p[3]), pk2(p[4], p[5]), pk2(p[6], p[7])};
        }
        l += dpp<0x124>(l); l += dpp<0x128>(l);
        l = swap32_add(swap16_add(l));
        if (lane < 4) *lp<float>(L_ML + 128 + cw * 16 + lane * 4) = l;
        FENCE();
        arrive(C_SM);
      }
      {
        u32 va[8];
#pragma unroll
        for (int t = 0; t < 8; ++t) va[t] = trow * 256 + ((((u32)(t ^ (trow & 7)) << 1) | ((c >> 1) & 1)) << 4) + (c & 1) * 8;
        v4f acc[8];
#pragma unroll
        for (int t = 0; t < 8; ++t) acc[t] = v4f{0.f, 0.f, 0.f, 0.f};
        int j = cw;
#pragma nounroll
        for (; j + NCONS < nb; j += NCONS) {
          const int u = uv + j;
          const u32 lb = (u & (NSLOT - 1)) * SLOT;
          wait_full(8 * u + 8);
          const v8bf pf = ldfrag(sca + j * 256);
          v8bf vf[8];
#pragma unroll
          for (int t = 0; t < 8; ++t) {
            v4s lo = __builtin_amdgcn_ds_read_tr16_b64_v4i16(lp<v4s>(lb + va[t])), hi = __builtin_amdgcn_ds_read_tr16_b64_v4i16(lp<v4s>(lb + va[t] + 4096));
            vf[t] = __builtin_bit_cast(v8bf, __builtin_shufflevector(lo, hi, 0, 1, 2, 3, 4, 5, 6, 7));
          }
          FENCE(); stsv(L_CTL + 4 * (C_FREE + (u & (NSLOT - 1))), u + 1);
#pragma unroll
          for (int t = 0; t < 8; ++t) acc[t] = mfma(vf[t], pf, acc[t]);
        }
        const int link = j - (nb - NCONS), u = uv + j;
        const u32 lb = (u & (NSLOT - 1)) * SLOT;
        v4f e0 = d == 0 ? acc[0] : d == 1 ? acc[2] : d == 2 ? acc[4] : acc[6];
        v4f e1 = d == 0 ? acc[1] : d == 1 ? acc[3] : d == 2 ? acc[5] : acc[7];
        const v4u pf = *lp<v4u>(sca + j * 256);
        v4u pm[4];
#pragma unroll
        for (int i = 0; i < 4; ++i) { pm[i] = d == i ? pf : v4u{0u, 0u, 0u, 0u}; PIN(pm[i]); }
        float inv = 1.f, L = 0.f;
        if (link == NCONS - 1) {
          while (ldsv(L_CTL + 4 * C_SM) < NCONS) { }
          FENCE();
          L = *lp<float>(L_ML + 128 + (c & 3) * 4);
#pragma unroll
          for (int w = 1; w < NCONS; ++w) L += *lp<float>(L_ML + 128 + w * 16 + (c & 3) * 4);
          inv = rcp(L);
        }
        v2u ml = {canon(M), canon(L)};
        PIN(ml);
        u32 oa = (c & 3) * 4096 + d * 64 + g * 16, ra = L_MRG + lane * 16;
        u32 vb[8];
#pragma unroll
        for (int t = 0; t < 8; ++t) { vb[t] = lb + va[t]; PIN(vb[t]); }
        PIN(e0); PIN(e1); PIN(oa); PIN(ra); PIN(inv);
        if (link == NCONS - 1) {
          while (ldsv(L_CTL + 4 * C_FULL) < (u32)(8 * u + 8 - HEAD)) { }
          if (lane < 4) st2(ml, rws, lane * 8, W_ML + bid * 128);
        }
        while (ldsv(L_CTL + 4 * C_FULL) < (u32)(8 * u + 8)) { }
        FENCE();
        v8bf vf[8];
#pragma unroll
        for (int t = 0; t < 8; ++t) {
          v4s lo = __builtin_amdgcn_ds_read_tr16_b64_v4i16(lp<v4s>(vb[t])), hi = __builtin_amdgcn_ds_read_tr16_b64_v4i16(lp<v4s>(vb[t] + 4096));
          vf[t] = __builtin_bit_cast(v8bf, __builtin_shufflevector(lo, hi, 0, 1, 2, 3, 4, 5, 6, 7));
        }
        __builtin_amdgcn_sched_barrier(0);
#pragma unroll
        for (int i = 0; i < 4; ++i) { e0 = mfma(vf[2 * i], __builtin_bit_cast(v8bf, pm[i]), e0); e1 = mfma(vf[2 * i + 1], __builtin_bit_cast(v8bf, pm[i]), e1); }
        __builtin_amdgcn_sched_barrier(0);
        v4f r0, r1;
        for (;;) {
          const u32 cnt = ldsv(L_CTL + 4 * C_MCNT);
          FENCE();
          r0 = *lp<v4f>(ra); r1 = *lp<v4f>(ra + 1024);
          FENCE();
          if (cnt >= (u32)link) break;
        }
        e0 = (e0 + r0) * inv; e1 = (e1 + r1) * inv;
        if (link == NCONS - 1) {
          st4(v4u{min(pk2(e0.x, e0.y), SENT - 1), min(pk2(e0.z, e0.w), SENT - 1), min(pk2(e1.x, e1.y), SENT - 1), min(pk2(e1.z, e1.w), SENT - 1)}, rws, oa, W_PART + grp * 16384 + r * 256);
        } else {
          *lp<v4f>(ra) = e0; *lp<v4f>(ra + 1024) = e1;
          FENCE(); stsv(L_CTL + 4 * C_MCNT, link + 1);
        }
        stsv(L_CTL + 4 * (C_FREE + (u & (NSLOT - 1))), u + 1);
      }
    }
  }
  if (!(isX || isY)) return;
  const u32 trow = 4 * g + (c >> 2);
  auto waddr = [&](int t) -> u32 { return trow * 128 + ((((u32)(t ^ ((trow >> 1) & 3)) << 1) | ((c >> 1) & 1)) << 4) + (c & 1) * 8; };
  auto wslot = [&](int t) -> u32 { return ((uw + t) & (NSLOT - 1)) * SLOT; };
  const int half = lane >> 5, i32 = lane & 31;
  const u32 fa = L_ACT + (c & 1) * ASTR + g * 16;
  {
    const rsrc_t rws = __builtin_amdgcn_make_buffer_rsrc(ws, 0, W_END, 0x00020000);
    auto weights_done = [&]() { WAITV(0); if (wave == 0) stsv(L_CTL + 4 * C_FULL, 8 * (uw + ntail)); arrive(C_WIN); };
    auto weights_in = [&]() { while (ldsv(L_CTL + 4 * C_WIN) < 2) __builtin_amdgcn_s_sleep(1); FENCE(); };
    if (isX) {
      const int kvh = xi >> 3, ng = xi & 7;
      const int b = wave >> 2, h = wave & 3;
      u32 wa = L_ACT + b * ASTR + (h * 128 + c * 8 + g * 2) * 2, wb[8];
#pragma unroll
      for (int i = 0; i < 8; ++i) { wb[i] = wslot(i) + waddr(wave & 3); PIN(wb[i]); }
      u32 vo = lane * 16 + W_PART + (b * 8 + kvh) * 16384 + h * 4096;
      u32 vl = ((c & 3) * 4 + ((g + (c >> 2)) & 3)) * 128 + W_ML + (b * 8 + kvh) * 2048 + h * 8;
      u32 vs = c * 2048 + g * 16 + W_AP + kvh * 4096 + ng * 256 + (wave & 3) * 64, fb = fa;
      PIN(wa); PIN(vo); PIN(vl); PIN(vs); PIN(fb);
      while (ldsv(L_CTL + 4 * C_ISS) < 2) __builtin_amdgcn_s_sleep(1);
      FENCE();
      v2u ls = ld2(rws, vl, 0);
      if (wave < 2) weights_done();
      if (!__all(max(ls.x, ls.y) != SENT)) {
#pragma nounroll
        for (int sp = 0; sp < SPIN_CAP; ++sp) {
          __builtin_amdgcn_s_sleep(1); FENCE();
          ls = ld2(rws, vl, 0);
          if (__all(max(ls.x, ls.y) != SENT)) break;
        }
        __builtin_amdgcn_s_sleep(XW);
      }
      FENCE();
      v4u a[4];
#pragma unroll
      for (int j = 0; j < 4; ++j) a[j] = ld4(rws, vo + j * 1024, 0);
      __builtin_amdgcn_sched_barrier(0);
      v8bf W[16];
      if (wave >= 4) {
        weights_in();
#pragma unroll
        for (int ks = 0; ks < 16; ++ks) W[ks] = trfrag(wb[ks >> 1] + (ks & 1) * 4096);
      }
      float M = __uint_as_float(ls.x);
      M = fmaxf(M, dpp<0xB1>(M)); M = fmaxf(M, dpp<0x4E>(M)); M = fmaxf(M, dpp<0x141>(M)); M = fmaxf(M, dpp<0x128>(M));
      float cf = ex2((__uint_as_float(ls.x) - M) * 1.4426950408889634f) * __uint_as_float(ls.y);
      cf *= rcp(row_sum(cf));
      float cj[4] = {dpp<0x150>(cf), dpp<0x151>(cf), dpp<0x152>(cf), dpp<0x153>(cf)};
      PIN(cj[0]); PIN(cj[1]); PIN(cj[2]); PIN(cj[3]);
#pragma nounroll
      for (int sp = 0; sp < SPIN_CAP; ++sp) {
        u32 mx = 0;
#pragma unroll
        for (int j = 0; j < 4; ++j) mx = max(max(mx, max(a[j].x, a[j].y)), max(a[j].z, a[j].w));
        if (__all(mx != SENT)) break;
        FENCE();
#pragma unroll
        for (int j = 0; j < 4; ++j) a[j] = ld4(rws, vo + j * 1024, 0);
      }
      v2f o[4];
#pragma unroll
      for (int i = 0; i < 4; ++i) {
        o[i] = v2f{lo16(a[0][i]), bfhi(a[0][i])} * cj[0];
#pragma unroll
        for (int j = 1; j < 4; ++j) o[i] += v2f{lo16(a[j][i]), bfhi(a[j][i])} * cj[j];
      }
      *lp<u32>(wa) = pk2(fold16(fold32(o[0].x, o[2].x), fold32(o[1].x, o[3].x)), fold16(fold32(o[0].y, o[2].y), fold32(o[1].y, o[3].y)));
      bar();
      if (wave >= 4) {
        v8bf A[16];
#pragma unroll
        for (int ks = 0; ks < 16; ++ks) A[ks] = ldfrag(fb + ks * 64);
        __builtin_amdgcn_sched_barrier(0);
        v4f d0 = {0.f, 0.f, 0.f, 0.f};
#pragma unroll
        for (int ks = 0; ks < 16; ++ks) d0 = mfma(W[ks], A[ks], d0);
        v4u ov = {canon(d0.x), canon(d0.y), canon(d0.z), canon(d0.w)};
        if (c < 2) st4(ov, rws, vs, 0);
      }
    } else {
      const int b = wave >> 2, col = (wave & 3) * 128 + i32 * 4 + half * 2;
      u32 wb[8];
#pragma unroll
      for (int i = 0; i < 8; ++i) { wb[i] = wave >= 4 ? wslot(i) + waddr(wave & 3) : wslot(8 + 2 * wave + (i >> 2)) + waddr(i & 3); PIN(wb[i]); }
      u32 wa = L_ACT + b * ASTR + fpos(col) * 2, ra = L_RED + wave * 8 + half * 4, rb_ = L_RED + b * 32;
      u32 ha = L_H1 + (c & 1) * 128 + fpos((wave & 3) * 16 + g * 4 + ((c >> 1) & 3)) * 2, fb = wave >= 4 ? fa : L_H1 + (c & 1) * 128 + g * 16;
      u32 vo = half * 16384 + i32 * 16 + W_AP + b * 2048 + (wave & 3) * 512, vs = (c & 1) * 2048 + (c >> 1) * 64 + g * 16 + W_OP + yi * 4096 + wave * 512;
      PIN(wa); PIN(ra); PIN(rb_); PIN(ha); PIN(fb); PIN(vo); PIN(vs);
      v2f x0, ga, sb, cb1;
      {
        const u32 xv = *(const u32*)(xin + b * 512 + col);
        const u32 tag = ldsv(L_CTL + 4 * C_TAG);
        v4u mg[3];
#pragma nounroll
        for (int sp = 0; sp < SPIN_CAP; ++sp) {
#pragma unroll
          for (int i = 0; i < 3; ++i) mg[i] = ld4(rws, col * 8, W_MG + i * 4096);
          if (__all(((mg[0].y ^ tag) | (mg[0].w ^ tag) | (mg[1].y ^ tag) | (mg[1].w ^ tag) | (mg[2].y ^ tag) | (mg[2].w ^ tag)) == 0)) break;
          FENCE();
        }
        const int sh = 16 - 16 * b;
        x0 = v2f{lo16(xv), bfhi(xv)};
        ga = v2f{bfhi(mg[0].x << sh), bfhi(mg[0].z << sh)}; sb = v2f{bfhi(mg[1].x << sh), bfhi(mg[1].z << sh)};
        cb1 = rb2(v2f{bfhi(mg[2].x << sh), bfhi(mg[2].z << sh)} + 1.f);
      }
      PIN(x0); PIN(ga); PIN(sb); PIN(cb1);
      if (wave < 2) weights_done();
      weights_in();
      v4u a[4];
#pragma unroll
      for (int j = 0; j < 4; ++j) a[j] = ld4(rws, vo, j * 4096);
      __builtin_amdgcn_sched_barrier(0);
      v8bf W[16];
#pragma unroll
      for (int j = 0; j < 16; ++j) W[j] = trfrag(wb[j >> 1] + (j & 1) * 4096);
#pragma nounroll
      for (int sp = 0; sp < SPIN_CAP; ++sp) {
        u32 mx = 0;
#pragma unroll
        for (int j = 0; j < 4; ++j) mx = max(max(mx, max(a[j].x, a[j].y)), max(a[j].z, a[j].w));
        if (__all(mx != SENT)) break;
        FENCE();
#pragma unroll
        for (int j = 0; j < 4; ++j) a[j] = ld4(rws, vo, j * 4096);
      }
      const v4f at = (__builtin_bit_cast(v4f, a[0]) + __builtin_bit_cast(v4f, a[1])) + (__builtin_bit_cast(v4f, a[2]) + __builtin_bit_cast(v4f, a[3]));
      const v2f x1 = rb2(x0 + rb2(ga * rb2(v2f{fold32(at.x, at.z), fold32(at.y, at.w)})));
      const float st = half_sum(fold32(x1.x + x1.y, fmaf(x1.x, x1.x, x1.y * x1.y)));
      if (i32 == 0) *lp<float>(ra) = st;
      bar();
      {
        const v4f r0 = *lp<v4f>(rb_), r1 = *lp<v4f>(rb_ + 16);
        const float mean = ((r0.x + r0.z) + (r1.x + r1.z)) * (1.f / 512.f);
        const float var = fmaxf(fmaf(-mean, mean, ((r0.y + r0.w) + (r1.y + r1.w)) * (1.f / 512.f)), 0.f);
        const v2f mv = rb2(v2f{mean, var});
        const float ri = rcp(rb(__builtin_amdgcn_sqrtf(rb(mv.y + 1e-5f))));
        const v2f hh = rb2(rb2(rb2(x1 - mv.x) * ri) * cb1) + sb;
        *lp<u32>(wa) = pk2(hh.x, hh.y);
      }
      bar();
      if (wave >= 4) {
        v8bf A[16];
#pragma unroll
        for (int ks = 0; ks < 16; ++ks) A[ks] = ldfrag(fb + ks * 64);
        __builtin_amdgcn_sched_barrier(0);
        v4f d0 = {0.f, 0.f, 0.f, 0.f};
#pragma unroll
        for (int ks = 0; ks < 16; ++ks) d0 = mfma(W[ks], A[ks], d0);
        float e0 = (c & 2) ? d0.y : d0.x, e1 = (c & 2) ? d0.w : d0.z;
        PIN(e0); PIN(e1);
        const float v = silu(rb((c & 4) ? e1 : e0));
        *lp<u16>(ha) = (u16)pk2(v, v);
      }
      bar();
      if (wave < 4) {
        const v8bf a0 = ldfrag(fb), a1 = ldfrag(fb + 64);
        v4f dd[8];
#pragma unroll
        for (int t = 0; t < 8; ++t) dd[t] = mfma(W[2 * t + 1], a1, mfma(W[2 * t], a0, v4f{0.f, 0.f, 0.f, 0.f}));
        v4f rv = dd[0];
#pragma unroll
        for (int t = 1; t < 8; ++t) if ((c >> 1) == t) rv = dd[t];
        v4u ov = {canon(rv.x), canon(rv.y), canon(rv.z), canon(rv.w)};
        st4(ov, rws, vs, 0);
        if (wave == 1 && yi == 27 && lane == 0) st2(v2u{(u32)E, (u32)(E >> 32)}, rws, 0, W_EPOCH);
      }
    }
  }
}

extern "C" void kernel_launch(void* const* d_in, const int* in_sizes, int n_in, void* d_out, int out_size, void* d_ws, size_t ws_size, hipStream_t stream) {
  hipLaunchKernelGGL(fused_block, dim3(256), dim3(512), 0, stream, (const u16*)d_in[1], (const u16*)d_in[2], (const u16*)d_in[9], (const u16*)d_in[3],
                     (const u16*)d_in[4], (const u16*)d_in[6], (const u16*)d_in[7], (const u16*)d_in[0], (const u16*)d_in[8], (u16*)d_out, (unsigned char*)d_ws);
}
```

```cpp
#define DIAG 14
#include <hip/hip_runtime.h>

typedef unsigned int u32;
typedef unsigned long long u64;
typedef unsigned short u16;
typedef u32 v4u __attribute__((ext_vector_type(4)));
typedef u32 v2u __attribute__((ext_vector_type(2)));
typedef int v4i __attribute__((ext_vector_type(4)));
typedef float v4f __attribute__((ext_vector_type(4)));
typedef float v2f __attribute__((ext_vector_type(2)));
typedef short v4s __attribute__((ext_vector_type(4)));
typedef __bf16 v8bf __attribute__((ext_vector_type(8)));
typedef __bf16 v2bf __attribute__((ext_vector_type(2)));
#define LAS __attribute__((address_space(3)))
#define GAS __attribute__((address_space(1)))
#define DEVI __device__ __forceinline__

constexpr int NSLOT = 16, SLOT = 8192;
constexpr int NCONS = 6, NSTASH = 4, NF = 2, NMAX = 72;
#define WA 16
#define WB 20
#define WC 28
#define LA 25
#define LB 50
#define TEND 3705
#define LEADX 130
#define LEADX3 210
#define LEADY 190
#define COFF -20
#define W2W 24
#define HEAD 16
#define XW 5
constexpr int QAT = 12, HDELAY = 440, CODE_BYTES = 23424;
#define BLK_A 65, 65, 65, 63, 63, 63, 63, 63, 63, 63, 64, 64, 65, 65, 65, 65
constexpr int NBLK[2][16] = {{BLK_A}, {65, 65, 65, 63, 62, 62, 62, 62, 62, 62, 67, 67, 65, 65, 65, 65}};
constexpr int BIAS = 11;
constexpr u64 start_table(int k) {
  u64 t = 0; int s = 0;
  for (int r = 0; r < 16; ++r) { const int dl = s - 64 * r + BIAS; if (dl < 0 || dl > 15 || NBLK[k][r] > NMAX) return ~0ull; t |= (u64)dl << (4 * r); s += NBLK[k][r]; }
  return s == 1024 ? t : ~0ull;
}
constexpr u64 ST_A = start_table(0), ST_B = start_table(1);
static_assert(ST_A != ~0ull && ST_B != ~0ull, "block table");
constexpr u64 pace(int k, int r, int extra, int lead) { return ((TEND - lead) * 16 + NBLK[k][r]) / (2 * NBLK[k][r] + extra) - 384; }
constexpr u64 pace_table(int k) {
  return pace(k, 0, 0, LEADX) | pace(k, 3, 0, LEADX3) << 8 | pace(k, 3, 0, LEADX) << 16 | pace(k, 4, 8, 0) << 24 | pace(k, 8, 8, 0) << 32 |
         pace(k, 10, 0, k ? 0 : LEADY) << 40 | pace(k, 12, 2, 0) << 48;
}
constexpr u64 PACE_A = pace_table(0), PACE_B = pace_table(1);
constexpr int L_CTL = NSLOT * SLOT;
constexpr int L_Q = L_CTL + 256;
constexpr int L_ACT = L_Q + 1024;
constexpr int ASTR = 1024 + 64;
constexpr int L_MRG = L_ACT + 2 * ASTR;
constexpr int L_ML = L_MRG + 2048;
constexpr int L_SC = L_ML + 256;
constexpr int L_JUNK = L_SC + NMAX * 256;
constexpr int L_END = L_JUNK + 256;
constexpr int L_H1 = L_SC;
constexpr int L_RED = L_H1 + 256;
static_assert(L_END <= 160 * 1024, "LDS");
constexpr int C_FULL = 0, C_QRDY = 1, C_MCNT = 2, C_CLR = 3, C_ARDY = 4, C_TAG = 5, C_WRD = 6, C_MX = 7, C_SM = 8, C_T0 = 9, C_ISS = 10, C_VISS = 11, C_WIN = 12, C_FREE = 16;
constexpr u32 W_EPOCH = 0, W_T0 = 4096, W_SP = 8192, W_HG = W_SP + 4 * 1024 * 16, W_QG = W_HG + 512 * 8, W_MG = W_QG + 4096 * 8, W_PART = W_MG + 2048 * 8;
constexpr u32 W_ML = W_PART + 64 * 4096;
constexpr u32 W_AP = W_ML + 256 * 128, W_OP = W_AP + 8 * 4096, W_END = W_OP + 28 * 4096;
constexpr u32 CLRB = (W_END - W_PART) / 64;
static_assert(CLRB * 64 == W_END - W_PART && CLRB % 16 == 0, "clear split");
constexpr u32 W_STAMP = 2u << 20;
constexpr u32 SENT = 0xFFFFFFFFu;
constexpr int SPIN_CAP = 1 << 16;

DEVI float lo16(u32 v) { return __uint_as_float(v << 16); }
DEVI float bfhi(u32 v) { return __uint_as_float(v & 0xffff0000u); }
DEVI u32 pk2(float a, float b) { v2f x = {a, b}; return __builtin_bit_cast(u32, __builtin_convertvector(x, v2bf)); }
DEVI float rb(float a) { return lo16(pk2(a, a)); }
DEVI float ex2(float x) { return __builtin_amdgcn_exp2f(x); }
DEVI float rcp(float x) { return __builtin_amdgcn_rcpf(x); }
DEVI float silu(float v) { return v * rcp(1.f + ex2(v * -1.4426950408889634f)); }
DEVI float silu_exact(float v) {
  const float t = fminf(v * -1.4426950216293335f, 126.f);
  const float r = fmaf(v, -1.92596299112662e-08f, fmaf(v, -1.4426950216293335f, -t));
  float e = ex2(t);
  e = fmaf(e, fminf(r, 1.f) * 0.6931471805599453f, e);
  const float d = 1.f + e, rc = rcp(d), q = v * rc;
  return fmaf(fmaf(-q, d, v), rc, q);
}
DEVI v2f rb2(v2f a) { const u32 p = pk2(a.x, a.y); return v2f{lo16(p), bfhi(p)}; }
DEVI float fold32(float a, float b) { auto s = __builtin_amdgcn_permlane32_swap(__float_as_uint(a), __float_as_uint(b), false, false); return __uint_as_float(s[0]) + __uint_as_float(s[1]); }
DEVI float fold16(float a, float b) { auto s = __builtin_amdgcn_permlane16_swap(__float_as_uint(a), __float_as_uint(b), false, false); return __uint_as_float(s[0]) + __uint_as_float(s[1]); }
#define PIN(x) asm volatile("" : "+v"(x))
DEVI float swap16_add(float x) { auto s = __builtin_amdgcn_permlane16_swap(__float_as_uint(x), __float_as_uint(x), false, false); return __uint_as_float(s[0]) + __uint_as_float(s[1]); }
DEVI float swap32_add(float x) { auto s = __builtin_amdgcn_permlane32_swap(__float_as_uint(x), __float_as_uint(x), false, false); return __uint_as_float(s[0]) + __uint_as_float(s[1]); }
DEVI float swap16_max(float x) { auto s = __builtin_amdgcn_permlane16_swap(__float_as_uint(x), __float_as_uint(x), false, false); return fmaxf(__uint_as_float(s[0]), __uint_as_float(s[1])); }
DEVI float swap32_max(float x) { auto s = __builtin_amdgcn_permlane32_swap(__float_as_uint(x), __float_as_uint(x), false, false); return fmaxf(__uint_as_float(s[0]), __uint_as_float(s[1])); }
template <int CTRL> DEVI float dpp(float x) { return __builtin_bit_cast(float, __builtin_amdgcn_mov_dpp(__builtin_bit_cast(int, x), CTRL, 0xf, 0xf, true)); }
DEVI float row_sum(float x) { x += dpp<0xB1>(x); x += dpp<0x4E>(x); x += dpp<0x141>(x); x += dpp<0x128>(x); return x; }
DEVI float half_sum(float x) { return swap16_add(row_sum(x)); }
DEVI float wave_sum(float x) { return swap32_add(half_sum(x)); }
template <typename T> DEVI LAS T* lp(u32 a) { return (LAS T*)(size_t)a; }
DEVI u32 ldsv(u32 a) { return *(volatile LAS u32*)(size_t)a; }
DEVI void stsv(u32 a, u32 v) { *(volatile LAS u32*)(size_t)a = v; }
#define FENCE() asm volatile("" ::: "memory")
#define WAITV(n) asm volatile("s_waitcnt vmcnt(%0)" ::"i"(n) : "memory")
#define WAITL() asm volatile("s_waitcnt lgkmcnt(0)" ::: "memory")
DEVI void bar() { WAITL(); __builtin_amdgcn_s_barrier(); FENCE(); }
typedef __amdgpu_buffer_rsrc_t rsrc_t;
DEVI v4u ld4(rsrc_t r, u32 vo, u32 so) { return __builtin_bit_cast(v4u, __builtin_amdgcn_raw_buffer_load_b128(r, vo, so, 16)); }
DEVI v2u ld2(rsrc_t r, u32 vo, u32 so) { return __builtin_bit_cast(v2u, __builtin_amdgcn_raw_buffer_load_b64(r, vo, so, 16)); }
DEVI void st4(v4u v, rsrc_t r, u32 vo, u32 so) { __builtin_amdgcn_raw_buffer_store_b128(v, r, vo, so, 16); }
DEVI void st2(v2u v, rsrc_t r, u32 vo, u32 so) { __builtin_amdgcn_raw_buffer_store_b64(v, r, vo, so, 16); }
DEVI u32 canon(float f) { u32 b = __float_as_uint(f); return b < 0xFFFFFFFEu ? b : 0xFFFFFFFEu; }
DEVI v8bf trfrag(u32 a) {
  v4s lo = __builtin_amdgcn_ds_read_tr16_b64_v4i16(lp<v4s>(a)), hi = __builtin_amdgcn_ds_read_tr16_b64_v4i16(lp<v4s>(a + 2048));
  return __builtin_bit_cast(v8bf, __builtin_shufflevector(lo, hi, 0, 1, 2, 3, 4, 5, 6, 7));
}
DEVI v4f mfma(v8bf a, v8bf b, v4f c) { return __builtin_amdgcn_mfma_f32_16x16x32_bf16(a, b, c, 0, 0, 0); }
DEVI v8bf ldfrag(u32 a) { return __builtin_bit_cast(v8bf, *lp<v4u>(a)); }
DEVI int fpos(int k) { return (k & ~31) + ((k >> 2) & 3) * 8 + ((k >> 4) & 1) * 4 + (k & 3); }
DEVI v4i mksrd(const void* p, u32 bytes) { u64 a = (u64)p; return v4i{(int)(u32)a, (int)(u32)(a >> 32), (int)bytes, 0x00020000}; }

#define DMA4(m, v0, v1, v2, v3, srd, so)                                                                                   \
  asm volatile("s_mov_b32 m0, %0\n\ts_nop 0\n\tbuffer_load_dwordx4 %1, %5, %6 offen nt lds\n\t"                           \
               "buffer_load_dwordx4 %2, %5, %6 offen offset:1024 nt lds\n\tbuffer_load_dwordx4 %3, %5, %6 offen offset:2048 nt lds\n\t" \
               "buffer_load_dwordx4 %4, %5, %6 offen offset:3072 nt lds" ::"s"(m), "v"(v0), "v"(v1), "v"(v2), "v"(v3), "s"(srd), "s"(so) : "memory")

#define STAMP(i) do { if ((i) == DIAG) tdiag = __builtin_amdgcn_s_memrealtime(); } while (0)
#define FIN() do { if (tdiag) while (__builtin_amdgcn_s_memrealtime() < tdiag + 10000) __builtin_amdgcn_s_sleep(8); } while (0)

#if defined(__HIP_DEVICE_COMPILE__)
asm(".text\n.p2align 8\n.fill 448, 4, 0xBF800000");
#endif
__global__ void __launch_bounds__(512, 2)
fused_block(const u16* __restrict__ Kc, const u16* __restrict__ Vc, const u16* __restrict__ wmod, const u16* __restrict__ wq,
            const u16* __restrict__ wo, const u16* __restrict__ w1, const u16* __restrict__ w2, const u16* __restrict__ xin,
            const u16* __restrict__ cin, u16* __restrict__ out, unsigned char* ws) {
  __shared__ __attribute__((aligned(16))) unsigned char smem[L_END];
  asm volatile("" ::"s"((u32)(size_t)(LAS unsigned char*)smem));
  u64 tdiag = 0;
  const int tid = threadIdx.x, lane = tid & 63, wave = __builtin_amdgcn_readfirstlane(tid >> 6);
  if (wave == 1 && blockIdx.x < 32) {
    u64 pc; asm volatile("s_getpc_b64 %0" : "=s"(pc));
    const v4i rc = mksrd((const void*)(pc & ~127ull), CODE_BYTES);
    const u32 vo = lane * 128, m = L_JUNK, s1 = 8192, s2 = 16384;
    asm volatile("s_mov_b32 m0, %0\n\ts_nop 0\n\tbuffer_load_dword %1, %2, 0 offen lds\n\tbuffer_load_dword %1, %2, %3 offen lds\n\tbuffer_load_dword %1, %2, %4 offen lds"
                 ::"s"(m), "v"(vo), "s"(rc), "s"(s1), "s"(s2) : "memory");
  }
  const int bid = (blockIdx.x & ~31) | ((blockIdx.x & 7) << 2) | ((blockIdx.x >> 3) & 3);
  const int grp = bid >> 4, r = bid & 15;
  const int g = lane >> 4, c = lane & 15;
  const u64 stt = (grp < 14 ? ST_A : ST_B) >> (4 * r);
  const int kv0 = 64 * r + ((int)stt & 15) - BIAS;
  const bool isS = r >= 12;
  if (__builtin_expect(wave == 0, 1)) {
    const v4i srd = isS ? mksrd(wmod, 512 * 6144) : mksrd(Kc, 1u << 27);
    const u32 k0 = g * 256 + ((c ^ g) << 4), w0 = (lane >> 3) * 6144 + (((lane & 7) ^ ((lane >> 3) & 6)) << 4);
    const u32 a0 = isS ? w0 : k0, a1 = isS ? w0 + 48128 : k0 ^ 64, a2 = isS ? w0 + 2 * 48128 : k0 ^ 128, a3 = isS ? w0 + 3 * 48128 : k0 ^ 192;
    const u32 so = isS ? (r - 12) * 128 * 6144 + grp * 128 : (u32)(grp * 1024 + kv0) << 13, hs = isS ? 32 * 6144 : 4096;
#pragma unroll
    for (int i = 0; i < 4; ++i) { const u32 m = i * 4096, s1 = so + i * hs; DMA4(m, a0, a1, a2, a3, srd, s1); }
  }
  if (wave == 2) { stsv(L_CTL + 4 * lane, 0); WAITL(); }
  __builtin_amdgcn_s_barrier(); FENCE();
  const u64 tstart = __builtin_amdgcn_s_memrealtime();
  FENCE();
  const bool isX = r < 4, isQ = r >= 4 && r < 8, isM = r == 8 || r == 9, isY = (r == 10 || r == 11) && grp < 14, isR = r == 0 && (grp & 1);
  const int xi = grp * 4 + r, qi = grp * 4 + r - 4, mi = grp * 2 + r - 8, yi = grp * 2 + r - 10;
  const int u0 = (int)(0x2222008888880000ull >> (4 * r)) & 15;
  const int nb = 64 + (r == 15 ? BIAS : (int)(stt >> 4) & 15) - ((int)stt & 15);
  const int nf = isS ? 0 : NF;
  const int ntail = isX ? 8 : isY ? 16 : 0;
  const int uv = u0 + nb, uw = uv + nb;
  u64 E = 0;
  auto arrive = [&](int w) { if (lane == 0) __hip_atomic_fetch_add(lp<u32>(L_CTL + 4 * w), 1u, __ATOMIC_RELAXED, __HIP_MEMORY_SCOPE_WORKGROUP); };
  int wv = wave;
  asm volatile("" : "+s"(wv));
  if (__builtin_expect(wv == 0, 1)) {
    __builtin_amdgcn_s_setprio(3);
    STAMP(0);
    int iss, pub = 0;
    auto publish = [&](int v) { pub = max(pub, v); stsv(L_CTL + 4 * C_FULL, pub); };
#define PACE(W_) do { WAITV(W_); publish(iss - (W_)); } while (0)
    const u32 cls = ((u32)(0x6666554433331000ull >> (4 * r)) & 7) + (r == 3 && (grp & 1));
    const u32 tau = 384 + ((u32)((grp < 14 ? PACE_A : PACE_B) >> (8 * cls)) & 255);
    u32 due = ((u32)tstart + COFF) * 16 + NF * tau;
    const u32 wrow = lane >> 3, wlc = ((lane & 7) ^ (((wrow >> 1) & 3) << 1)) << 4;
    int u = NF;
#pragma nounroll
    for (int seg = u0 == 8 ? 0 : 1; seg < (ntail ? 4 : 3); ++seg) {
      const bool kv = seg == 1 || seg == 2;
      const u16* base = seg == 0 ? (isQ ? wq : wmod) : seg == 1 ? Kc : seg == 2 ? Vc : isX ? wo : w1;
      const u32 rs = seg == 0 ? (isQ ? 8192 : 6144) : (seg == 3 && !isX) ? 3584 : 1024;
      const v4i srd = mksrd(base, kv ? 1u << 27 : (seg == 3 && isX) ? 4096 * 1024 : 512 * rs);
      u32 so = kv ? (u32)(grp * 1024 + kv0 + (seg == 1 ? nf : 0)) << 13
                  : seg == 0 ? (isQ ? qi : 16 + mi) * 128
                             : isX ? ((xi >> 3) << 19) + (xi & 7) * 128 : yi * 128;
      const u32 hstep = kv ? 4096 : 32 * rs, ustep = 2 * hstep;
      const u32 sw = seg == 2 ? 2 * g : g, sx = seg == 1 ? 4 : 8;
      const u32 w0 = wrow * rs + wlc, wd = 8 * rs - 1024;
      const u32 a0 = kv ? g * 256 + ((c ^ sw) << 4) : w0, a1 = kv ? g * 256 + ((c ^ sw ^ sx) << 4) : w0 + wd;
      const u32 a2 = kv ? g * 256 + ((c ^ sw ^ ((2 * sx) & 15)) << 4) : w0 + 2 * wd, a3 = kv ? g * 256 + ((c ^ sw ^ ((3 * sx) & 15)) << 4) : w0 + 3 * wd;
      const int ue = seg == 0 ? NF + u0 : seg == 1 ? uv : seg == 2 ? uw : uw + (ntail >> 1);
#pragma nounroll
      while (u < ue) {
        const bool paced = u >= 4 && seg < 3;
        const int stop = min(ue, u < 4 ? 4 : u < 48 ? 48 : 1 << 20);
        u32 dd = paced ? due : due - (1u << 28);
        const int u_in = u, la = (seg == 3 && isX) ? 0x7fffffff : LA * 16;
        u32 t0, t1, lag; u32 vf, vt;
#define DMAS(so) "buffer_load_dwordx4 %[a0], %[srd], " so " offen nt lds\n\tbuffer_load_dwordx4 %[a1], %[srd], " so " offen offset:1024 nt lds\n\t" \
                 "buffer_load_dwordx4 %[a2], %[srd], " so " offen offset:2048 nt lds\n\tbuffer_load_dwordx4 %[a3], %[srd], " so " offen offset:3072 nt lds\n\t"
#define RDFREE "s_lshl_b32 %[t1], %[u], 2\n\ts_and_b32 %[t1], %[t1], 60\n\ts_or_b32 %[t1], %[t1], %[fb]\n\tv_mov_b32 %[vf], %[t1]\n\tds_read_b32 %[vf], %[vf]\n\t"
#define REST(W) "s_waitcnt vmcnt(" #W ")\n\ts_mov_b32 m0, %[t0]\n\ts_lshl_b32 %[t0], %[u], 3\n\t" DMAS("%[t1]")                                   \
                "s_sub_i32 %[t0], %[t0], " #W " - 4\n\ts_max_i32 %[pub], %[pub], %[t0]\n\tv_mov_b32 %[vt], %[pub]\n\tds_write_b32 %[va], %[vt]\n\t"                                      \
                "s_add_i32 %[u], %[u], 1\n\ts_add_u32 %[so], %[so], %[us]\n\ts_add_u32 %[dd], %[dd], %[tau]\n\t" RDFREE                             \
                "s_memrealtime s[98:99]\n\ts_cmp_lt_i32 %[u], %[stop]\n\ts_waitcnt vmcnt(" #W ")\n\ts_cbranch_scc1 1b\n\ts_branch 9f\n"
#define XREST(W) REST(W)
        asm volatile(RDFREE "s_memrealtime s[98:99]\n"
                     "1:\n\ts_sub_i32 %[t1], %[u], 15\n\ts_waitcnt lgkmcnt(0)\n\tv_cmp_gt_i32 vcc, %[t1], %[vf]\n\ts_lshl_b32 s98, s98, 4\n\t"
                     "s_cbranch_vccnz 8f\n"
                     "2:\n\ts_sub_i32 %[lag], s98, %[dd]\n\ts_cmp_lt_i32 %[lag], 0\n\ts_cbranch_scc1 7f\n\t"
                     "s_lshl_b32 %[t0], %[u], 13\n\ts_and_b32 %[t0], %[t0], 0x1e000\n\ts_mov_b32 m0, %[t0]\n\ts_add_u32 %[t1], %[so], %[hs]\n\t" DMAS("%[so]")
                     "s_or_b32 %[t0], %[t0], 0x1000\n\ts_cmp_lt_i32 %[lag], %[la]\n\ts_cbranch_scc1 4f\n\ts_cmp_lt_i32 %[lag], %[lb]\n\ts_cbranch_scc1 5f\n\t"
                     XREST(WC) "4:\n\t" XREST(WA) "5:\n\t" XREST(WB)
                     "8:\n\t" RDFREE "s_sub_i32 %[t1], %[u], 15\n\ts_waitcnt lgkmcnt(0)\n\tv_cmp_gt_i32 vcc, %[t1], %[vf]\n\ts_nop 0\n\ts_cbranch_vccz 7f\n\t"
                     "s_waitcnt vmcnt(0)\n\ts_lshl_b32 %[pub], %[u], 3\n\tv_mov_b32 %[vt], %[pub]\n\tds_write_b32 %[va], %[vt]\n"
                     "6:\n\t" RDFREE "s_sub_i32 %[t1], %[u], 15\n\ts_waitcnt lgkmcnt(0)\n\tv_cmp_gt_i32 vcc, %[t1], %[vf]\n\ts_sleep 1\n\ts_cbranch_vccnz 6b\n"
                     "7:\n\ts_memrealtime s[98:99]\n\ts_waitcnt lgkmcnt(0)\n\ts_lshl_b32 s98, s98, 4\n\ts_branch 2b\n"
                     "9:\n\ts_waitcnt lgkmcnt(0)"
                     : [u] "+s"(u), [so] "+s"(so), [dd] "+s"(dd), [pub] "+s"(pub), [t0] "=&s"(t0), [t1] "=&s"(t1), [lag] "=&s"(lag), [vf] "=&v"(vf), [vt] "=&v"(vt)
                     : [a0] "v"(a0), [a1] "v"(a1), [a2] "v"(a2), [a3] "v"(a3), [srd] "s"(srd), [hs] "s"(hstep), [us] "s"(ustep), [stop] "s"(stop),
                       [va] "v"(L_CTL + 4 * C_FULL), [fb] "i"(L_CTL + 4 * C_FREE), [tau] "s"(tau), [la] "s"(la), [lb] "i"(LB * 16)
                     : "memory", "vcc", "scc", "s98", "s99");
        due += (u - u_in) * tau;
        if (u == 4) STAMP(14);
        if (u == 4 && isS) { WAITV(16); publish(16); }
        if (u == uw) stsv(L_CTL + 4 * C_VISS, 1);
        if (u == 48) { const u32 t = __builtin_amdgcn_readfirstlane(ldsv(L_CTL + 4 * C_T0)); if (t) due = t * 16 + u * tau; }
      }
    }
    iss = 8 * u;
    if (!ntail) { PACE(24); PACE(16); PACE(8); PACE(0); }
    else arrive(C_ISS);
    if (!ntail) STAMP(1);
    __builtin_amdgcn_s_setprio(0);
  } else {
    const rsrc_t rws = __builtin_amdgcn_make_buffer_rsrc(ws, 0, W_END, 0x00020000);
    auto wait_full = [&](u32 need) { while (ldsv(L_CTL + 4 * C_FULL) < need) __builtin_amdgcn_s_sleep(1); FENCE(); };
    const u32 trow = 4 * g + (c >> 2);
    auto waddr = [&](int t) -> u32 { return trow * 128 + ((((u32)(t ^ ((trow >> 1) & 3)) << 1) | ((c >> 1) & 1)) << 4) + (c & 1) * 8; };

    if (wave == 1) {
      const u64 E0 = __hip_atomic_load((GAS u64*)(ws + W_EPOCH), __ATOMIC_RELAXED, __HIP_MEMORY_SCOPE_AGENT);
      u32 tag;
#define CHAIN_START() do { E = E0 + 1; tag = (u32)E; } while (0)
      auto silu4 = [&](u32 x, u32 y) { return v2u{pk2(silu_exact(lo16(x)), silu_exact(bfhi(x))), pk2(silu_exact(lo16(y)), silu_exact(bfhi(y)))}; };
      if (isS) {
        const v2u cv = *(const v2u*)(cin + (lane >> 5) * 512 + (r - 12) * 128 + (lane & 31) * 4);
        CHAIN_START();
        *lp<v2u>(L_ACT + (lane >> 5) * ASTR + fpos((lane & 31) * 4) * 2) = silu4(cv.x, cv.y);
        FENCE();
        v8bf act[4];
#pragma unroll
        for (int ks = 0; ks < 4; ++ks) { act[ks] = ldfrag(L_ACT + (c & 1) * ASTR + g * 16 + ks * 64); PIN(act[ks]); }
        wait_full(16);
        v8bf W[16];
#pragma unroll
        for (int j = 0; j < 16; ++j) W[j] = trfrag(waddr(j & 3) + (j >> 2) * 4096);
        FENCE(); stsv(L_CTL + 4 * C_FREE, 1); stsv(L_CTL + 4 * (C_FREE + 1), 2);
        v4f acc[4];
#pragma unroll
        for (int t = 0; t < 4; ++t) acc[t] = v4f{0.f, 0.f, 0.f, 0.f};
#pragma unroll
        for (int j = 0; j < 16; ++j) acc[j & 3] = mfma(act[j >> 2], W[j], acc[j & 3]);
        const float r0 = g == 0 ? acc[0][0] : g == 1 ? acc[1][0] : g == 2 ? acc[2][0] : acc[3][0];
        const float r1 = g == 0 ? acc[0][1] : g == 1 ? acc[1][1] : g == 2 ? acc[2][1] : acc[3][1];
        while (ldsv(L_CTL + 4 * C_CLR) == 0) { }
        FENCE();
        st4(v4u{__float_as_uint(r0), tag, __float_as_uint(r1), tag}, rws, lane * 16, W_SP + ((r - 12) * 1024 + grp * 64) * 16);
        STAMP(3);
      } else if (isR) {
        const v4u in0 = *(const v4u*)(xin + lane * 8), in1 = *(const v4u*)(xin + 512 + lane * 8);
        u32 xs0 = *(const u32*)(xin + (grp >> 1) * 64 + (lane & ~1)), xs1 = *(const u32*)(xin + 512 + (grp >> 1) * 64 + (lane & ~1));
        CHAIN_START();
        PIN(xs0); PIN(xs1);
        xs0 >>= 16 * (lane & 1); xs1 >>= 16 * (lane & 1);
        float mr[2], ri[2];
#pragma unroll
        for (int b = 0; b < 2; ++b) {
          const v4u xv = b ? in1 : in0;
          const float x[8] = {lo16(xv.x), bfhi(xv.x), lo16(xv.y), bfhi(xv.y), lo16(xv.z), bfhi(xv.z), lo16(xv.w), bfhi(xv.w)};
          float s = 0.f;
#pragma unroll
          for (int i = 0; i < 8; ++i) s += x[i];
          const float mean = wave_sum(s) * (1.f / 512.f);
          float q2 = 0.f;
#pragma unroll
          for (int i = 0; i < 8; ++i) q2 += (x[i] - mean) * (x[i] - mean);
          const float var = wave_sum(q2) * (1.f / 512.f);
          mr[b] = rb(mean); ri[b] = rcp(rb(__builtin_amdgcn_sqrtf(rb(rb(var) + 1e-5f))));
        }
        v2f y = rb2(rb2(v2f{lo16(xs0) - mr[0], lo16(xs1) - mr[1]}) * v2f{ri[0], ri[1]});
        PIN(y);
        v4u a[8];
#pragma nounroll
        for (int sp = 0; sp < SPIN_CAP; ++sp) {
          u32 bad = 0;
#pragma unroll
          for (int i = 0; i < 8; ++i) { a[i] = ld4(rws, lane * 16, W_SP + ((i & 3) * 1024 + (i >> 2) * 512 + (grp >> 1) * 64) * 16); bad |= (a[i].y ^ tag) | (a[i].w ^ tag); }
          if (__all(bad == 0)) break;
          FENCE();
        }
        v2f sc[2];
#pragma unroll
        for (int pt = 0; pt < 2; ++pt) {
          sc[pt] = v2f{0.f, 0.f};
#pragma unroll
          for (int k4 = 0; k4 < 4; ++k4) sc[pt] += v2f{__uint_as_float(a[pt * 4 + k4].x), __uint_as_float(a[pt * 4 + k4].z)};
        }
        const v2f h = rb2(y * rb2(rb2(sc[1]) + 1.f)) + rb2(sc[0]);
        st2(v2u{pk2(h.x, h.y), tag}, rws, lane * 8, W_HG + (grp >> 1) * 512);
        STAMP(23);
      } else if (isM || isQ) {
        if (isM) {
          const v4u in0 = *(const v4u*)(cin + lane * 8), in1 = *(const v4u*)(cin + 512 + lane * 8);
          CHAIN_START();
          *lp<v2u>(L_ACT + fpos(lane * 8) * 2) = silu4(in0.x, in0.y); *lp<v2u>(L_ACT + fpos(lane * 8 + 4) * 2) = silu4(in0.z, in0.w);
          *lp<v2u>(L_ACT + ASTR + fpos(lane * 8) * 2) = silu4(in1.x, in1.y); *lp<v2u>(L_ACT + ASTR + fpos(lane * 8 + 4) * 2) = silu4(in1.z, in1.w);
        } else {
          CHAIN_START();
          while (__builtin_amdgcn_s_memrealtime() < tstart + HDELAY) __builtin_amdgcn_s_sleep(4);
          v4u a[4];
#pragma nounroll
          for (int sp = 0; sp < SPIN_CAP; ++sp) {
            u32 bad = 0;
#pragma unroll
            for (int i = 0; i < 4; ++i) { a[i] = ld4(rws, lane * 16 + i * 1024, W_HG); bad |= (a[i].y ^ tag) | (a[i].w ^ tag); }
            if (__all(bad == 0)) break;
            FENCE();
          }
#pragma unroll
          for (int i = 0; i < 4; ++i) {
            const u32 wa = L_ACT + fpos(i * 128 + lane * 2) * 2;
            *lp<u32>(wa) = (a[i].x & 0xffffu) | (a[i].z << 16); *lp<u32>(wa + ASTR) = (a[i].x >> 16) | (a[i].z & 0xffff0000u);
          }
          STAMP(4);
        }
        FENCE(); stsv(L_CTL + 4 * C_TAG, tag); stsv(L_CTL + 4 * C_ARDY, 1);
      } else CHAIN_START();
      if (blockIdx.x < 8 && lane == 0) st2(v2u{(u32)tstart, tag}, rws, blockIdx.x * 8, W_T0);
      {
        wait_full(8 * u0 + 16 * QAT);
        v4u a[2];
#pragma nounroll
        for (int sp = 0; sp < SPIN_CAP; ++sp) {
          a[0] = ld4(rws, lane * 16, W_QG + grp * 2048); a[1] = ld4(rws, lane * 16 + 1024, W_QG + grp * 2048);
          if (__all(((a[0].y ^ tag) | (a[0].w ^ tag) | (a[1].y ^ tag) | (a[1].w ^ tag)) == 0)) break;
          __builtin_amdgcn_s_sleep(16); FENCE();
        }
        *lp<v2u>(L_Q + lane * 8) = v2u{a[0].x, a[0].z}; *lp<v2u>(L_Q + 512 + lane * 8) = v2u{a[1].x, a[1].z};
        FENCE(); stsv(L_CTL + 4 * C_TAG, tag); stsv(L_CTL + 4 * C_QRDY, 1);
        STAMP(6);
        {
          const v2u t = ld2(rws, (lane & 7) * 8, W_T0);
          if (__all(t.y == tag)) {
            int dt = (int)(t.x - (u32)tstart);
            dt += __builtin_amdgcn_mov_dpp(dt, 0xB1, 0xf, 0xf, true); dt += __builtin_amdgcn_mov_dpp(dt, 0x4E, 0xf, 0xf, true); dt += __builtin_amdgcn_mov_dpp(dt, 0x141, 0xf, 0xf, true);
            if (dt > -2400 && dt < 2400) stsv(L_CTL + 4 * C_T0, ((u32)tstart + (dt >> 3) + COFF) | 1);
          }
        }
      }
      STAMP(2);
      if (ntail) {
        const v4i srd = mksrd(isX ? wo : w2, isX ? 4096 * 1024 : 1792 * 1024);
        const u32 a0 = (lane >> 3) * 1024 + (((lane & 7) ^ ((lane >> 3) & 6)) << 4), a1 = a0 + 7168, a2 = a0 + 2 * 7168, a3 = a0 + 3 * 7168;
        u32 so = isX ? ((xi >> 3) << 19) + (xi & 7) * 128 + 256 * 1024 : yi << 16;
        while (ldsv(L_CTL + 4 * C_VISS) == 0) __builtin_amdgcn_s_sleep(2);
#pragma nounroll
        for (int u = uw + (ntail >> 1); u < uw + ntail; ++u, so += isX ? 65536 : 128) {
          while ((int)ldsv(L_CTL + 4 * (C_FREE + (u & (NSLOT - 1)))) < u + 1 - NSLOT) __builtin_amdgcn_s_sleep(1);
          const u32 lb = (u & (NSLOT - 1)) * SLOT, m = lb + 4096, s1 = so + 32768;
          DMA4(lb, a0, a1, a2, a3, srd, so);
          DMA4(m, a0, a1, a2, a3, srd, s1);
          WAITV(W2W);
        }
        arrive(C_ISS);
      }
      if (r == 8) {
        wait_full(8 * uw);
        const int zb = grp >> 3, zc = (grp & 7) * 64 + c * 4 + g;
        const u32 xv = xin[zb * 512 + zc];
        v2u m0, m1;
#pragma nounroll
        for (int sp = 0; sp < SPIN_CAP; ++sp) {
          m0 = ld2(rws, zc * 8, W_MG); m1 = ld2(rws, zc * 8, W_MG + 3 * 4096);
          if (__all(((m0.y ^ tag) | (m1.y ^ tag)) == 0)) break;
          FENCE();
        }
        v4u z[7];
#pragma nounroll
        for (int sp = 0; sp < SPIN_CAP; ++sp) {
          z[0] = ld4(rws, g * 4096 + c * 16, W_AP + grp * 256); z[1] = ld4(rws, g * 4096 + c * 16, W_AP + 16384 + grp * 256);
          if (__all(max(max(max(z[0].x, z[0].y), max(z[0].z, z[0].w)), max(max(z[1].x, z[1].y), max(z[1].z, z[1].w))) != SENT)) break;
          __builtin_amdgcn_s_sleep(2); FENCE();
        }
        v4f s0 = __builtin_bit_cast(v4f, z[0]), s1 = __builtin_bit_cast(v4f, z[1]);
        const float at = fold32(fold16(s0.x, s0.y), fold16(s0.z, s0.w)) + fold32(fold16(s1.x, s1.y), fold16(s1.z, s1.w));
        float x1 = rb(lo16(xv) + rb(bfhi(m0.x << (16 - 16 * zb)) * rb(at))), gb = bfhi(m1.x << (16 - 16 * zb));
        GAS u16* op = (GAS u16*)out + zb * 512 + zc;
        PIN(x1); PIN(gb); PIN(op);
#pragma nounroll
        for (int sp = 0; sp < SPIN_CAP; ++sp) {
#pragma unroll
          for (int j = 0; j < 7; ++j) z[j] = ld4(rws, g * 4096 + c * 16, W_OP + j * 16384 + grp * 256);
          u32 mx = 0;
#pragma unroll
          for (int j = 0; j < 7; ++j) mx = max(mx, max(max(z[j].x, z[j].y), max(z[j].z, z[j].w)));
          if (__all(mx != SENT)) break;
          FENCE();
        }
        const v4f s = ((__builtin_bit_cast(v4f, z[0]) + __builtin_bit_cast(v4f, z[1])) + (__builtin_bit_cast(v4f, z[2]) + __builtin_bit_cast(v4f, z[3]))) +
                      ((__builtin_bit_cast(v4f, z[4]) + __builtin_bit_cast(v4f, z[5])) + __builtin_bit_cast(v4f, z[6]));
        const float rr = x1 + rb(gb * rb(fold16(fold32(s.x, s.z), fold32(s.y, s.w))));
        *op = (u16)pk2(rr, rr);
        STAMP(13);
      }
    } else {
      if (isS && wave == 7) {
        const v4u s4 = {SENT, SENT, SENT, SENT};
        const u32 so = W_PART + (grp * 4 + r - 12) * CLRB;
#pragma unroll
        for (int i = 0; i < CLRB / 1024; ++i) st4(s4, rws, lane * 16 + (i & 3) * 1024, so + (i >> 2) * 4096);
        if (lane < (CLRB % 1024) / 16) st4(s4, rws, lane * 16, so + CLRB / 1024 * 1024);
        WAITV(0); stsv(L_CTL + 4 * C_CLR, 1);
      }

      if (wave == 6) { *lp<v4f>(L_MRG + lane * 16) = v4f{0.f, 0.f, 0.f, 0.f}; *lp<v4f>(L_MRG + 1024 + lane * 16) = v4f{0.f, 0.f, 0.f, 0.f}; }
      const int d = c >> 2, cw = wave - 2;
      const u32 sca = L_SC + (g * 4 + (c & 3)) * 16;
      {
        u32 ka[4];
#pragma unroll
        for (int s = 0; s < 4; ++s) ka[s] = c * 256 + (((4 * s + g) ^ c) << 4);
        v8bf qf[4];
        float mx = -INFINITY;
        auto rdk = [&](v8bf (&kf)[8], int j) {
          const int u = j < nf ? j : u0 + j;
          const u32 lb = (u & (NSLOT - 1)) * SLOT;
          wait_full(8 * u + 8);
#pragma unroll
          for (int s = 0; s < 4; ++s) { kf[s] = ldfrag(lb + ka[s]); kf[4 + s] = ldfrag(lb + ka[s] + 4096); }
          FENCE(); stsv(L_CTL + 4 * (C_FREE + (u & (NSLOT - 1))), u + 1);
        };
        auto qk = [&](v8bf (&kf)[8], int j) {
          v4f S0 = {0.f, 0.f, 0.f, 0.f}, S1 = {0.f, 0.f, 0.f, 0.f};
#pragma unroll
          for (int s = 0; s < 4; ++s) { S0 = mfma(kf[s], qf[s], S0); S1 = mfma(kf[4 + s], qf[s], S1); }
          const v2f a = rb2(v2f{S0.x, S0.y}) * 0.08838834764831845f, b = rb2(v2f{S0.z, S0.w}) * 0.08838834764831845f;
          const v2f e = rb2(v2f{S1.x, S1.y}) * 0.08838834764831845f, f = rb2(v2f{S1.z, S1.w}) * 0.08838834764831845f;
          mx = fmaxf(fmaxf(fmaxf(mx, fmaxf(a.x, a.y)), fmaxf(b.x, b.y)), fmaxf(fmaxf(e.x, e.y), fmaxf(f.x, f.y)));
          if (c < 4) *lp<v4u>(sca + j * 256) = v4u{pk2(a.x, a.y), pk2(b.x, b.y), pk2(e.x, e.y), pk2(f.x, f.y)};
        };
        v8bf sk[NSTASH][8];
        rdk(sk[0], cw);
        if (u0 == 8 && wave < 6) {
          wait_full(8 * NF + 64);
          v8bf W[16];
#pragma unroll
          for (int ks = 0; ks < 16; ++ks) W[ks] = trfrag(NF * SLOT + ks * 4096 + waddr(wave - 2));
          WAITL();
          u32 old = 0;
          if (lane == 0) old = __hip_atomic_fetch_add(lp<u32>(L_CTL + 4 * C_WRD), 1u, __ATOMIC_RELAXED, __HIP_MEMORY_SCOPE_WORKGROUP);
          if (__builtin_amdgcn_readfirstlane(old) == 3 && lane < 8) stsv(L_CTL + 4 * (C_FREE + NF + lane), NF + lane + 1);
          while (ldsv(L_CTL + 4 * C_ARDY) == 0) __builtin_amdgcn_s_sleep(1);
          FENCE();
          const u32 tag = ldsv(L_CTL + 4 * C_TAG);
          v8bf A[16];
#pragma unroll
          for (int ks = 0; ks < 16; ++ks) A[ks] = ldfrag(L_ACT + (c & 1) * ASTR + g * 16 + ks * 64);
          __builtin_amdgcn_sched_barrier(0);
          v4f d0 = {0.f, 0.f, 0.f, 0.f}, d1 = {0.f, 0.f, 0.f, 0.f};
#pragma unroll
          for (int ks = 0; ks < 16; ks += 2) { d0 = mfma(A[ks], W[ks], d0); d1 = mfma(A[ks + 1], W[ks + 1], d1); }
          const float r0 = d0[0] + d1[0], r1 = d0[1] + d1[1];
          const float n0 = dpp<0xB1>(r0), n1 = dpp<0xB1>(r1);
          const bool odd = lane & 1;
          if (lane < 16) {
            if (isM) st2(v2u{pk2(r0, r1), tag}, rws, lane * 8, W_MG + mi * 512 + (wave - 2) * 128);
            else st2(v2u{pk2(odd ? n1 : r0, odd ? r1 : n0), tag}, rws, (lane & 1) * 16384 + (lane >> 1) * 8, W_QG + qi * 256 + (wave - 2) * 64);
          }
          if (isQ && wave == 2) STAMP(5);
        }
#pragma unroll
        for (int i = 1; i < NSTASH; ++i) rdk(sk[i], cw + i * NCONS);
        while (ldsv(L_CTL + 4 * C_QRDY) == 0) __builtin_amdgcn_s_sleep(2);
        FENCE();
#pragma unroll
        for (int s = 0; s < 4; ++s) qf[s] = ldfrag(L_Q + (c & 3) * 256 + (4 * s + g) * 16);
#pragma unroll
        for (int i = 0; i < NSTASH; ++i) qk(sk[i], cw + i * NCONS);
#pragma nounroll
        for (int j = cw + NSTASH * NCONS; j < nb; j += NCONS) { v8bf kf[8]; rdk(kf, j); qk(kf, j); }
        mx = swap32_max(swap16_max(rb(mx)));
        if (lane < 4) *lp<float>(L_ML + cw * 16 + lane * 4) = mx;
        FENCE();
        arrive(C_MX);
      }
      float M;
      {
        while (ldsv(L_CTL + 4 * C_MX) < NCONS) __builtin_amdgcn_s_sleep(1);
        FENCE();
        M = *lp<float>(L_ML + (c & 3) * 4);
#pragma unroll
        for (int w = 1; w < NCONS; ++w) M = fmaxf(M, *lp<float>(L_ML + w * 16 + (c & 3) * 4));
        float l = 0.f;
#pragma nounroll
        for (int j = cw + d * NCONS; j < nb; j += 4 * NCONS) {
          const v4u s = *lp<v4u>(sca + j * 256);
          float p[8];
#pragma unroll
          for (int i = 0; i < 4; ++i) { p[2 * i] = ex2((lo16(s[i]) - M) * 1.4426950408889634f); p[2 * i + 1] = ex2((bfhi(s[i]) - M) * 1.4426950408889634f); }
          l += ((p[0] + p[1]) + (p[2] + p[3])) + ((p[4] + p[5]) + (p[6] + p[7]));
          *lp<v4u>(sca + j * 256) = v4u{pk2(p[0], p[1]), pk2(p[2], p[3]), pk2(p[4], p[5]), pk2(p[6], p[7])};
        }
        l += dpp<0x124>(l); l += dpp<0x128>(l);
        l = swap32_add(swap16_add(l));
        if (lane < 4) *lp<float>(L_ML + 128 + cw * 16 + lane * 4) = l;
        FENCE();
        arrive(C_SM);
      }
      {
        u32 va[8];
#pragma unroll
        for (int t = 0; t < 8; ++t) va[t] = trow * 256 + ((((u32)(t ^ (trow & 7)) << 1) | ((c >> 1) & 1)) << 4) + (c & 1) * 8;
        v4f acc[8];
#pragma unroll
        for (int t = 0; t < 8; ++t) acc[t] = v4f{0.f, 0.f, 0.f, 0.f};
        int j = cw;
#pragma nounroll
        for (; j + NCONS < nb; j += NCONS) {
          const int u = uv + j;
          const u32 lb = (u & (NSLOT - 1)) * SLOT;
          wait_full(8 * u + 8);
          const v8bf pf = ldfrag(sca + j * 256);
          v8bf vf[8];
#pragma unroll
          for (int t = 0; t < 8; ++t) {
            v4s lo = __builtin_amdgcn_ds_read_tr16_b64_v4i16(lp<v4s>(lb + va[t])), hi = __builtin_amdgcn_ds_read_tr16_b64_v4i16(lp<v4s>(lb + va[t] + 4096));
            vf[t] = __builtin_bit_cast(v8bf, __builtin_shufflevector(lo, hi, 0, 1, 2, 3, 4, 5, 6, 7));
          }
          FENCE(); stsv(L_CTL + 4 * (C_FREE + (u & (NSLOT - 1))), u + 1);
#pragma unroll
          for (int t = 0; t < 8; ++t) acc[t] = mfma(vf[t], pf, acc[t]);
        }
        const int link = j - (nb - NCONS), u = uv + j;
        const u32 lb = (u & (NSLOT - 1)) * SLOT;
        v4f e0 = d == 0 ? acc[0] : d == 1 ? acc[2] : d == 2 ? acc[4] : acc[6];
        v4f e1 = d == 0 ? acc[1] : d == 1 ? acc[3] : d == 2 ? acc[5] : acc[7];
        const v4u pf = *lp<v4u>(sca + j * 256);
        v4u pm[4];
#pragma unroll
        for (int i = 0; i < 4; ++i) { pm[i] = d == i ? pf : v4u{0u, 0u, 0u, 0u}; PIN(pm[i]); }
        float inv = 1.f, L = 0.f;
        if (link == NCONS - 1) {
          while (ldsv(L_CTL + 4 * C_SM) < NCONS) { }
          FENCE();
          L = *lp<float>(L_ML + 128 + (c & 3) * 4);
#pragma unroll
          for (int w = 1; w < NCONS; ++w) L += *lp<float>(L_ML + 128 + w * 16 + (c & 3) * 4);
          inv = rcp(L);
        }
        v2u ml = {canon(M), canon(L)};
        PIN(ml);
        u32 oa = (c & 3) * 4096 + d * 64 + g * 16, ra = L_MRG + lane * 16;
        u32 vb[8];
#pragma unroll
        for (int t = 0; t < 8; ++t) { vb[t] = lb + va[t]; PIN(vb[t]); }
        PIN(e0); PIN(e1); PIN(oa); PIN(ra); PIN(inv);
        if (link == NCONS - 1) {
          while (ldsv(L_CTL + 4 * C_FULL) < (u32)(8 * u + 8 - HEAD)) { }
          if (lane < 4) st2(ml, rws, lane * 8, W_ML + bid * 128);
        }
        while (ldsv(L_CTL + 4 * C_FULL) < (u32)(8 * u + 8)) { }
        FENCE();
        v8bf vf[8];
#pragma unroll
        for (int t = 0; t < 8; ++t) {
          v4s lo = __builtin_amdgcn_ds_read_tr16_b64_v4i16(lp<v4s>(vb[t])), hi = __builtin_amdgcn_ds_read_tr16_b64_v4i16(lp<v4s>(vb[t] + 4096));
          vf[t] = __builtin_bit_cast(v8bf, __builtin_shufflevector(lo, hi, 0, 1, 2, 3, 4, 5, 6, 7));
        }
        __builtin_amdgcn_sched_barrier(0);
#pragma unroll
        for (int i = 0; i < 4; ++i) { e0 = mfma(vf[2 * i], __builtin_bit_cast(v8bf, pm[i]), e0); e1 = mfma(vf[2 * i + 1], __builtin_bit_cast(v8bf, pm[i]), e1); }
        __builtin_amdgcn_sched_barrier(0);
        v4f r0, r1;
        for (;;) {
          const u32 cnt = ldsv(L_CTL + 4 * C_MCNT);
          FENCE();
          r0 = *lp<v4f>(ra); r1 = *lp<v4f>(ra + 1024);
          FENCE();
          if (cnt >= (u32)link) break;
        }
        e0 = (e0 + r0) * inv; e1 = (e1 + r1) * inv;
        if (link == NCONS - 1) {
          st4(v4u{min(pk2(e0.x, e0.y), SENT - 1), min(pk2(e0.z, e0.w), SENT - 1), min(pk2(e1.x, e1.y), SENT - 1), min(pk2(e1.z, e1.w), SENT - 1)}, rws, oa, W_PART + grp * 16384 + r * 256);
          STAMP(8);
        } else {
          *lp<v4f>(ra) = e0; *lp<v4f>(ra + 1024) = e1;
          FENCE(); stsv(L_CTL + 4 * C_MCNT, link + 1);
        }
        stsv(L_CTL + 4 * (C_FREE + (u & (NSLOT - 1))), u + 1);
      }
    }
  }
  if (!(isX || isY)) { FIN(); return; }
#define TS(i) do { } while (0)
  const u32 trow = 4 * g + (c >> 2);
  auto waddr = [&](int t) -> u32 { return trow * 128 + ((((u32)(t ^ ((trow >> 1) & 3)) << 1) | ((c >> 1) & 1)) << 4) + (c & 1) * 8; };
  auto wslot = [&](int t) -> u32 { return ((uw + t) & (NSLOT - 1)) * SLOT; };
  const int half = lane >> 5, i32 = lane & 31;
  const u32 fa = L_ACT + (c & 1) * ASTR + g * 16;
  {
    const rsrc_t rws = __builtin_amdgcn_make_buffer_rsrc(ws, 0, W_END, 0x00020000);
    auto weights_done = [&]() { WAITV(0); if (wave == 0) stsv(L_CTL + 4 * C_FULL, 8 * (uw + ntail)); arrive(C_WIN); };
    auto weights_in = [&]() { while (ldsv(L_CTL + 4 * C_WIN) < 2) __builtin_amdgcn_s_sleep(1); FENCE(); };
    if (isX) {
      const int kvh = xi >> 3, ng = xi & 7;
      const int b = wave >> 2, h = wave & 3;
      u32 wa = L_ACT + b * ASTR + (h * 128 + c * 8 + g * 2) * 2, wb[8];
#pragma unroll
      for (int i = 0; i < 8; ++i) { wb[i] = wslot(i) + waddr(wave & 3); PIN(wb[i]); }
      u32 vo = lane * 16 + W_PART + (b * 8 + kvh) * 16384 + h * 4096;
      u32 vl = ((c & 3) * 4 + ((g + (c >> 2)) & 3)) * 128 + W_ML + (b * 8 + kvh) * 2048 + h * 8;
      u32 vs = c * 2048 + g * 16 + W_AP + kvh * 4096 + ng * 256 + (wave & 3) * 64, fb = fa;
      PIN(wa); PIN(vo); PIN(vl); PIN(vs); PIN(fb);
      while (ldsv(L_CTL + 4 * C_ISS) < 2) __builtin_amdgcn_s_sleep(1);
      FENCE();
      TS(0);
      v2u ls = ld2(rws, vl, 0);
      if (wave < 2) weights_done();
      if (wave == 0) STAMP(1);
      if (!__all(max(ls.x, ls.y) != SENT)) {
#pragma nounroll
        for (int sp = 0; sp < SPIN_CAP; ++sp) {
          __builtin_amdgcn_s_sleep(1); FENCE();
          ls = ld2(rws, vl, 0);
          if (__all(max(ls.x, ls.y) != SENT)) break;
        }
        __builtin_amdgcn_s_sleep(XW);
      }
      TS(5);
      FENCE();
      v4u a[4];
#pragma unroll
      for (int j = 0; j < 4; ++j) a[j] = ld4(rws, vo + j * 1024, 0);
      __builtin_amdgcn_sched_barrier(0);
      v8bf W[16];
      if (wave >= 4) {
        weights_in();
#pragma unroll
        for (int ks = 0; ks < 16; ++ks) W[ks] = trfrag(wb[ks >> 1] + (ks & 1) * 4096);
      }
      float M = __uint_as_float(ls.x);
      M = fmaxf(M, dpp<0xB1>(M)); M = fmaxf(M, dpp<0x4E>(M)); M = fmaxf(M, dpp<0x141>(M)); M = fmaxf(M, dpp<0x128>(M));
      float cf = ex2((__uint_as_float(ls.x) - M) * 1.4426950408889634f) * __uint_as_float(ls.y);
      cf *= rcp(row_sum(cf));
      float cj[4] = {dpp<0x150>(cf), dpp<0x151>(cf), dpp<0x152>(cf), dpp<0x153>(cf)};
      PIN(cj[0]); PIN(cj[1]); PIN(cj[2]); PIN(cj[3]);
#pragma nounroll
      for (int sp = 0; sp < SPIN_CAP; ++sp) {
        u32 mx = 0;
#pragma unroll
        for (int j = 0; j < 4; ++j) mx = max(max(mx, max(a[j].x, a[j].y)), max(a[j].z, a[j].w));
        if (__all(mx != SENT)) break;
        FENCE();
#pragma unroll
        for (int j = 0; j < 4; ++j) a[j] = ld4(rws, vo + j * 1024, 0);
      }
      TS(1);
      v2f o[4];
#pragma unroll
      for (int i = 0; i < 4; ++i) {
        o[i] = v2f{lo16(a[0][i]), bfhi(a[0][i])} * cj[0];
#pragma unroll
        for (int j = 1; j < 4; ++j) o[i] += v2f{lo16(a[j][i]), bfhi(a[j][i])} * cj[j];
      }
      *lp<u32>(wa) = pk2(fold16(fold32(o[0].x, o[2].x), fold32(o[1].x, o[3].x)), fold16(fold32(o[0].y, o[2].y), fold32(o[1].y, o[3].y)));
      if (wave == 4) STAMP(9);
      TS(2);
      bar();
      TS(3);
      if (wave >= 4) {
        v8bf A[16];
#pragma unroll
        for (int ks = 0; ks < 16; ++ks) A[ks] = ldfrag(fb + ks * 64);
        __builtin_amdgcn_sched_barrier(0);
        v4f d0 = {0.f, 0.f, 0.f, 0.f};
#pragma unroll
        for (int ks = 0; ks < 16; ++ks) d0 = mfma(W[ks], A[ks], d0);
        v4u ov = {canon(d0.x), canon(d0.y), canon(d0.z), canon(d0.w)};
        if (c < 2) st4(ov, rws, vs, 0);
        if (wave == 4) STAMP(10);
        TS(4);
      }
    } else {
      const int b = wave >> 2, col = (wave & 3) * 128 + i32 * 4 + half * 2;
      u32 wb[8];
#pragma unroll
      for (int i = 0; i < 8; ++i) { wb[i] = wave >= 4 ? wslot(i) + waddr(wave & 3) : wslot(8 + 2 * wave + (i >> 2)) + waddr(i & 3); PIN(wb[i]); }
      u32 wa = L_ACT + b * ASTR + fpos(col) * 2, ra = L_RED + wave * 8 + half * 4, rb_ = L_RED + b * 32;
      u32 ha = L_H1 + (c & 1) * 128 + fpos((wave & 3) * 16 + g * 4 + ((c >> 1) & 3)) * 2, fb = wave >= 4 ? fa : L_H1 + (c & 1) * 128 + g * 16;
      u32 vo = half * 16384 + i32 * 16 + W_AP + b * 2048 + (wave & 3) * 512, vs = (c & 1) * 2048 + (c >> 1) * 64 + g * 16 + W_OP + yi * 4096 + wave * 512;
      PIN(wa); PIN(ra); PIN(rb_); PIN(ha); PIN(fb); PIN(vo); PIN(vs);
      v2f x0, ga, sb, cb1;
      {
        const u32 xv = *(const u32*)(xin + b * 512 + col);
        const u32 tag = ldsv(L_CTL + 4 * C_TAG);
        v4u mg[3];
#pragma nounroll
        for (int sp = 0; sp < SPIN_CAP; ++sp) {
#pragma unroll
          for (int i = 0; i < 3; ++i) mg[i] = ld4(rws, col * 8, W_MG + i * 4096);
          if (__all(((mg[0].y ^ tag) | (mg[0].w ^ tag) | (mg[1].y ^ tag) | (mg[1].w ^ tag) | (mg[2].y ^ tag) | (mg[2].w ^ tag)) == 0)) break;
          FENCE();
        }
        const int sh = 16 - 16 * b;
        x0 = v2f{lo16(xv), bfhi(xv)};
        ga = v2f{bfhi(mg[0].x << sh), bfhi(mg[0].z << sh)}; sb = v2f{bfhi(mg[1].x << sh), bfhi(mg[1].z << sh)};
        cb1 = rb2(v2f{bfhi(mg[2].x << sh), bfhi(mg[2].z << sh)} + 1.f);
      }
      PIN(x0); PIN(ga); PIN(sb); PIN(cb1);
      if (wave < 2) weights_done();
      weights_in();
      if (wave == 0) STAMP(1);
      TS(0);
      v4u a[4];
#pragma unroll
      for (int j = 0; j < 4; ++j) a[j] = ld4(rws, vo, j * 4096);
      __builtin_amdgcn_sched_barrier(0);
      v8bf W[16];
#pragma unroll
      for (int j = 0; j < 16; ++j) W[j] = trfrag(wb[j >> 1] + (j & 1) * 4096);
#pragma nounroll
      for (int sp = 0; sp < SPIN_CAP; ++sp) {
        u32 mx = 0;
#pragma unroll
        for (int j = 0; j < 4; ++j) mx = max(max(mx, max(a[j].x, a[j].y)), max(a[j].z, a[j].w));
        if (__all(mx != SENT)) break;
        FENCE();
#pragma unroll
        for (int j = 0; j < 4; ++j) a[j] = ld4(rws, vo, j * 4096);
      }
      if (wave == 4) STAMP(11);
      TS(1);
      const v4f at = (__builtin_bit_cast(v4f, a[0]) + __builtin_bit_cast(v4f, a[1])) + (__builtin_bit_cast(v4f, a[2]) + __builtin_bit_cast(v4f, a[3]));
      const v2f x1 = rb2(x0 + rb2(ga * rb2(v2f{fold32(at.x, at.z), fold32(at.y, at.w)})));
      const float st = half_sum(fold32(x1.x + x1.y, fmaf(x1.x, x1.x, x1.y * x1.y)));
      if (i32 == 0) *lp<float>(ra) = st;
      bar();
      TS(2);
      {
        const v4f r0 = *lp<v4f>(rb_), r1 = *lp<v4f>(rb_ + 16);
        const float mean = ((r0.x + r0.z) + (r1.x + r1.z)) * (1.f / 512.f);
        const float var = fmaxf(fmaf(-mean, mean, ((r0.y + r0.w) + (r1.y + r1.w)) * (1.f / 512.f)), 0.f);
        const v2f mv = rb2(v2f{mean, var});
        const float ri = rcp(rb(__builtin_amdgcn_sqrtf(rb(mv.y + 1e-5f))));
        const v2f hh = rb2(rb2(rb2(x1 - mv.x) * ri) * cb1) + sb;
        *lp<u32>(wa) = pk2(hh.x, hh.y);
      }
      bar();
      TS(3);
      if (wave >= 4) {
        v8bf A[16];
#pragma unroll
        for (int ks = 0; ks < 16; ++ks) A[ks] = ldfrag(fb + ks * 64);
        __builtin_amdgcn_sched_barrier(0);
        v4f d0 = {0.f, 0.f, 0.f, 0.f};
#pragma unroll
        for (int ks = 0; ks < 16; ++ks) d0 = mfma(W[ks], A[ks], d0);
        float e0 = (c & 2) ? d0.y : d0.x, e1 = (c & 2) ? d0.w : d0.z;
        PIN(e0); PIN(e1);
        const float v = silu(rb((c & 4) ? e1 : e0));
        *lp<u16>(ha) = (u16)pk2(v, v);
      }
      bar();
      TS(4);
      if (wave < 4) {
        const v8bf a0 = ldfrag(fb), a1 = ldfrag(fb + 64);
        v4f dd[8];
#pragma unroll
        for (int t = 0; t < 8; ++t) dd[t] = mfma(W[2 * t + 1], a1, mfma(W[2 * t], a0, v4f{0.f, 0.f, 0.f, 0.f}));
        v4f rv = dd[0];
#pragma unroll
        for (int t = 1; t < 8; ++t) if ((c >> 1) == t) rv = dd[t];
        v4u ov = {canon(rv.x), canon(rv.y), canon(rv.z), canon(rv.w)};
        st4(ov, rws, vs, 0);
        if (wave == 1 && yi == 27 && lane == 0) st2(v2u{(u32)E, (u32)(E >> 32)}, rws, 0, W_EPOCH);
        if (wave == 0) STAMP(12);
        TS(5);
      }
    }
  }
  FIN();
}

extern "C" void kernel_launch(void* const* d_in, const int* in_sizes, int n_in, void* d_out, int out_size, void* d_ws, size_t ws_size, hipStream_t stream) {
  hipLaunchKernelGGL(fused_block, dim3(256), dim3(512), 0, stream, (const u16*)d_in[1], (const u16*)d_in[2], (const u16*)d_in[9], (const u16*)d_in[3],
                     (const u16*)d_in[4], (const u16*)d_in[6], (const u16*)d_in[7], (const u16*)d_in[0], (const u16*)d_in[8], (u16*)d_out, (unsigned char*)d_ws);
}
```

```cpp
#include <hip/hip_runtime.h>

typedef unsigned int u32;
typedef unsigned long long u64;
typedef unsigned short u16;
typedef u32 v4u __attribute__((ext_vector_type(4)));
typedef u32 v2u __attribute__((ext_vector_type(2)));
typedef int v4i __attribute__((ext_vector_type(4)));
typedef float v4f __attribute__((ext_vector_type(4)));
typedef float v2f __attribute__((ext_vector_type(2)));
typedef short v4s __attribute__((ext_vector_type(4)));
typedef __bf16 v8bf __attribute__((ext_vector_type(8)));
typedef __bf16 v2bf __attribute__((ext_vector_type(2)));
#define LAS __attribute__((address_space(3)))
#define GAS __attribute__((address_space(1)))
#define DEVI __device__ __forceinline__

constexpr int NSLOT = 16, SLOT = 8192;
constexpr int NCONS = 6, NSTASH = 4, NF = 2, NMAX = 72;
#define WA 16
#define WB 20
#define WC 28
#define LA 25
#define LB 50
#define TEND 3705
#define LEADX 130
#define LEADX3 210
#define LEADY 190
#define COFF -20
#define W2W 24
#define HEAD 8
#define XW 1
constexpr int QAT = 12, HDELAY = 440, CODE_BYTES = 23424;
#define BLK_A 65, 65, 65, 63, 63, 63, 63, 63, 63, 63, 64, 64, 65, 65, 65, 65
constexpr int NBLK[2][16] = {{BLK_A}, {65, 65, 65, 63, 62, 62, 62, 62, 62, 62, 67, 67, 65, 65, 65, 65}};
constexpr int BIAS = 11;
constexpr u64 start_table(int k) {
  u64 t = 0; int s = 0;
  for (int r = 0; r < 16; ++r) { const int dl = s - 64 * r + BIAS; if (dl < 0 || dl > 15 || NBLK[k][r] > NMAX) return ~0ull; t |= (u64)dl << (4 * r); s += NBLK[k][r]; }
  return s == 1024 ? t : ~0ull;
}
constexpr u64 ST_A = start_table(0), ST_B = start_table(1);
static_assert(ST_A != ~0ull && ST_B != ~0ull, "block table");
constexpr u64 pace(int k, int r, int extra, int lead) { return ((TEND - lead) * 16 + NBLK[k][r]) / (2 * NBLK[k][r] + extra) - 384; }
constexpr u64 pace_table(int k) {
  return pace(k, 0, 0, LEADX) | pace(k, 3, 0, LEADX3) << 8 | pace(k, 3, 0, LEADX) << 16 | pace(k, 4, 8, 0) << 24 | pace(k, 8, 8, 0) << 32 |
         pace(k, 10, 0, k ? 0 : LEADY) << 40 | pace(k, 12, 2, 0) << 48;
}
constexpr u64 PACE_A = pace_table(0), PACE_B = pace_table(1);
constexpr int L_CTL = NSLOT * SLOT;
constexpr int L_Q = L_CTL + 256;
constexpr int L_ACT = L_Q + 1024;
constexpr int ASTR = 1024 + 64;
constexpr int L_MRG = L_ACT + 2 * ASTR;
constexpr int L_ML = L_MRG + 2048;
constexpr int L_SC = L_ML + 256;
constexpr int L_JUNK = L_SC + NMAX * 256;
constexpr int L_END = L_JUNK + 256;
constexpr int L_H1 = L_SC;
constexpr int L_RED = L_H1 + 256;
static_assert(L_END <= 160 * 1024, "LDS");
constexpr int C_FULL = 0, C_QRDY = 1, C_MCNT = 2, C_CLR = 3, C_ARDY = 4, C_TAG = 5, C_WRD = 6, C_MX = 7, C_SM = 8, C_T0 = 9, C_ISS = 10, C_VISS = 11, C_WIN = 12, C_FREE = 16;
constexpr u32 W_EPOCH = 0, W_T0 = 4096, W_SP = 8192, W_HG = W_SP + 4 * 1024 * 16, W_QG = W_HG + 512 * 8, W_MG = W_QG + 4096 * 8, W_PART = W_MG + 2048 * 8;
constexpr u32 W_ML = W_PART + 64 * 4096;
constexpr u32 W_AP = W_ML + 256 * 128, W_OP = W_AP + 8 * 4096, W_END = W_OP + 28 * 4096;
constexpr u32 CLRB = (W_END - W_PART) / 64;
static_assert(CLRB * 64 == W_END - W_PART && CLRB % 16 == 0, "clear split");
constexpr u32 SENT = 0xFFFFFFFFu;
constexpr int SPIN_CAP = 1 << 16;

DEVI float lo16(u32 v) { return __uint_as_float(v << 16); }
DEVI float bfhi(u32 v) { return __uint_as_float(v & 0xffff0000u); }
DEVI u32 pk2(float a, float b) { v2f x = {a, b}; return __builtin_bit_cast(u32, __builtin_convertvector(x, v2bf)); }
DEVI float rb(float a) { return lo16(pk2(a, a)); }
DEVI float ex2(float x) { return __builtin_amdgcn_exp2f(x); }
DEVI float rcp(float x) { return __builtin_amdgcn_rcpf(x); }
DEVI float silu(float v) { return v * rcp(1.f + ex2(v * -1.4426950408889634f)); }
DEVI float silu_exact(float v) {
  const float t = fminf(v * -1.4426950216293335f, 126.f);
  const float r = fmaf(v, -1.92596299112662e-08f, fmaf(v, -1.4426950216293335f, -t));
  float e = ex2(t);
  e = fmaf(e, fminf(r, 1.f) * 0.6931471805599453f, e);
  const float d = 1.f + e, rc = rcp(d), q = v * rc;
  return fmaf(fmaf(-q, d, v), rc, q);
}
DEVI v2f rb2(v2f a) { const u32 p = pk2(a.x, a.y); return v2f{lo16(p), bfhi(p)}; }
DEVI float fold32(float a, float b) { auto s = __builtin_amdgcn_permlane32_swap(__float_as_uint(a), __float_as_uint(b), false, false); return __uint_as_float(s[0]) + __uint_as_float(s[1]); }
DEVI float fold16(float a, float b) { auto s = __builtin_amdgcn_permlane16_swap(__float_as_uint(a), __float_as_uint(b), false, false); return __uint_as_float(s[0]) + __uint_as_float(s[1]); }
#define PIN(x) asm volatile("" : "+v"(x))
DEVI float swap16_add(float x) { auto s = __builtin_amdgcn_permlane16_swap(__float_as_uint(x), __float_as_uint(x), false, false); return __uint_as_float(s[0]) + __uint_as_float(s[1]); }
DEVI float swap32_add(float x) { auto s = __builtin_amdgcn_permlane32_swap(__float_as_uint(x), __float_as_uint(x), false, false); return __uint_as_float(s[0]) + __uint_as_float(s[1]); }
DEVI float swap16_max(float x) { auto s = __builtin_amdgcn_permlane16_swap(__float_as_uint(x), __float_as_uint(x), false, false); return fmaxf(__uint_as_float(s[0]), __uint_as_float(s[1])); }
DEVI float swap32_max(float x) { auto s = __builtin_amdgcn_permlane32_swap(__float_as_uint(x), __float_as_uint(x), false, false); return fmaxf(__uint_as_float(s[0]), __uint_as_float(s[1])); }
template <int CTRL> DEVI float dpp(float x) { return __builtin_bit_cast(float, __builtin_amdgcn_mov_dpp(__builtin_bit_cast(int, x), CTRL, 0xf, 0xf, true)); }
DEVI float row_sum(float x) { x += dpp<0xB1>(x); x += dpp<0x4E>(x); x += dpp<0x141>(x); x += dpp<0x128>(x); return x; }
DEVI float half_sum(float x) { return swap16_add(row_sum(x)); }
DEVI float wave_sum(float x) { return swap32_add(half_sum(x)); }
template <typename T> DEVI LAS T* lp(u32 a) { return (LAS T*)(size_t)a; }
DEVI u32 ldsv(u32 a) { return *(volatile LAS u32*)(size_t)a; }
DEVI void stsv(u32 a, u32 v) { *(volatile LAS u32*)(size_t)a = v; }
#define FENCE() asm volatile("" ::: "memory")
#define WAITV(n) asm volatile("s_waitcnt vmcnt(%0)" ::"i"(n) : "memory")
#define WAITL() asm volatile("s_waitcnt lgkmcnt(0)" ::: "memory")
DEVI void bar() { WAITL(); __builtin_amdgcn_s_barrier(); FENCE(); }
typedef __amdgpu_buffer_rsrc_t rsrc_t;
DEVI v4u ld4(rsrc_t r, u32 vo, u32 so) { return __builtin_bit_cast(v4u, __builtin_amdgcn_raw_buffer_load_b128(r, vo, so, 16)); }
DEVI v2u ld2(rsrc_t r, u32 vo, u32 so) { return __builtin_bit_cast(v2u, __builtin_amdgcn_raw_buffer_load_b64(r, vo, so, 16)); }
DEVI void st4(v4u v, rsrc_t r, u32 vo, u32 so) { __builtin_amdgcn_raw_buffer_store_b128(v, r, vo, so, 16); }
DEVI void st2(v2u v, rsrc_t r, u32 vo, u32 so) { __builtin_amdgcn_raw_buffer_store_b64(v, r, vo, so, 16); }
DEVI u32 canon(float f) { u32 b = __float_as_uint(f); return b < 0xFFFFFFFEu ? b : 0xFFFFFFFEu; }
DEVI v8bf trfrag(u32 a) {
  v4s lo = __builtin_amdgcn_ds_read_tr16_b64_v4i16(lp<v4s>(a)), hi = __builtin_amdgcn_ds_read_tr16_b64_v4i16(lp<v4s>(a + 2048));
  return __builtin_bit_cast(v8bf, __builtin_shufflevector(lo, hi, 0, 1, 2, 3, 4, 5, 6, 7));
}
DEVI v4f mfma(v8bf a, v8bf b, v4f c) { return __builtin_amdgcn_mfma_f32_16x16x32_bf16(a, b, c, 0, 0, 0); }
DEVI v8bf ldfrag(u32 a) { return __builtin_bit_cast(v8bf, *lp<v4u>(a)); }
DEVI int fpos(int k) { return (k & ~31) + ((k >> 2) & 3) * 8 + ((k >> 4) & 1) * 4 + (k & 3); }
DEVI v4i mksrd(const void* p, u32 bytes) { u64 a = (u64)p; return v4i{(int)(u32)a, (int)(u32)(a >> 32), (int)bytes, 0x00020000}; }

#define DMA4(m, v0, v1, v2, v3, srd, so)                                                                                   \
  asm volatile("s_mov_b32 m0, %0\n\ts_nop 0\n\tbuffer_load_dwordx4 %1, %5, %6 offen nt lds\n\t"                           \
               "buffer_load_dwordx4 %2, %5, %6 offen offset:1024 nt lds\n\tbuffer_load_dwordx4 %3, %5, %6 offen offset:2048 nt lds\n\t" \
               "buffer_load_dwordx4 %4, %5, %6 offen offset:3072 nt lds" ::"s"(m), "v"(v0), "v"(v1), "v"(v2), "v"(v3), "s"(srd), "s"(so) : "memory")

__global__ void __launch_bounds__(512, 2)
fused_block(const u16* __restrict__ Kc, const u16* __restrict__ Vc, const u16* __restrict__ wmod, const u16* __restrict__ wq,
            const u16* __restrict__ wo, const u16* __restrict__ w1, const u16* __restrict__ w2, const u16* __restrict__ xin,
            const u16* __restrict__ cin, u16* __restrict__ out, unsigned char* ws) {
  __shared__ __attribute__((aligned(16))) unsigned char smem[L_END];
  asm volatile("" ::"s"((u32)(size_t)(LAS unsigned char*)smem));
  const int tid = threadIdx.x, lane = tid & 63, wave = __builtin_amdgcn_readfirstlane(tid >> 6);
  if (wave == 1 && blockIdx.x < 32) {
    u64 pc; asm volatile("s_getpc_b64 %0" : "=s"(pc));
    const v4i rc = mksrd((const void*)(pc & ~127ull), CODE_BYTES);
    const u32 vo = lane * 128, m = L_JUNK, s1 = 8192, s2 = 16384;
    asm volatile("s_mov_b32 m0, %0\n\ts_nop 0\n\tbuffer_load_dword %1, %2, 0 offen lds\n\tbuffer_load_dword %1, %2, %3 offen lds\n\tbuffer_load_dword %1, %2, %4 offen lds"
                 ::"s"(m), "v"(vo), "s"(rc), "s"(s1), "s"(s2) : "memory");
  }
  const int bid = (blockIdx.x & ~31) | ((blockIdx.x & 7) << 2) | ((blockIdx.x >> 3) & 3);
  const int grp = bid >> 4, r = bid & 15;
  const int g = lane >> 4, c = lane & 15;
  const u64 stt = (grp < 14 ? ST_A : ST_B) >> (4 * r);
  const int kv0 = 64 * r + ((int)stt & 15) - BIAS;
  const bool isS = r >= 12;
  if (__builtin_expect(wave == 0, 1)) {
    const v4i srd = isS ? mksrd(wmod, 512 * 6144) : mksrd(Kc, 1u << 27);
    const u32 k0 = g * 256 + ((c ^ g) << 4), w0 = (lane >> 3) * 6144 + (((lane & 7) ^ ((lane >> 3) & 6)) << 4);
    const u32 a0 = isS ? w0 : k0, a1 = isS ? w0 + 48128 : k0 ^ 64, a2 = isS ? w0 + 2 * 48128 : k0 ^ 128, a3 = isS ? w0 + 3 * 48128 : k0 ^ 192;
    const u32 so = isS ? (r - 12) * 128 * 6144 + grp * 128 : (u32)(grp * 1024 + kv0) << 13, hs = isS ? 32 * 6144 : 4096;
#pragma unroll
    for (int i = 0; i < 4; ++i) { const u32 m = i * 4096, s1 = so + i * hs; DMA4(m, a0, a1, a2, a3, srd, s1); }
  }
  if (wave == 2) { stsv(L_CTL + 4 * lane, 0); WAITL(); }
  __builtin_amdgcn_s_barrier(); FENCE();
  const u64 tstart = __builtin_amdgcn_s_memrealtime();
  FENCE();
  const bool isX = r < 4, isQ = r >= 4 && r < 8, isM = r == 8 || r == 9, isY = (r == 10 || r == 11) && grp < 14, isR = r == 0 && (grp & 1);
  const int xi = grp * 4 + r, qi = grp * 4 + r - 4, mi = grp * 2 + r - 8, yi = grp * 2 + r - 10;
  const int u0 = (int)(0x2222008888880000ull >> (4 * r)) & 15;
  const int nb = 64 + (r == 15 ? BIAS : (int)(stt >> 4) & 15) - ((int)stt & 15);
  const int nf = isS ? 0 : NF;
  const int ntail = isX ? 8 : isY ? 16 : 0;
  const int uv = u0 + nb, uw = uv + nb;
  u64 E = 0;
  auto arrive = [&](int w) { if (lane == 0) __hip_atomic_fetch_add(lp<u32>(L_CTL + 4 * w), 1u, __ATOMIC_RELAXED, __HIP_MEMORY_SCOPE_WORKGROUP); };
  int wv = wave;
  asm volatile("" : "+s"(wv));
  if (__builtin_expect(wv == 0, 1)) {
    __builtin_amdgcn_s_setprio(3);
    int iss, pub = 0;
    auto publish = [&](int v) { pub = max(pub, v); stsv(L_CTL + 4 * C_FULL, pub); };
#define PACE(W_) do { WAITV(W_); publish(iss - (W_)); } while (0)
    const u32 cls = ((u32)(0x6666554433331000ull >> (4 * r)) & 7) + (r == 3 && (grp & 1));
    const u32 tau = 384 + ((u32)((grp < 14 ? PACE_A : PACE_B) >> (8 * cls)) & 255);
    u32 due = ((u32)tstart + COFF) * 16 + NF * tau;
    const u32 wrow = lane >> 3, wlc = ((lane & 7) ^ (((wrow >> 1) & 3) << 1)) << 4;
    int u = NF;
#pragma nounroll
    for (int seg = u0 == 8 ? 0 : 1; seg < (ntail ? 4 : 3); ++seg) {
      const bool kv = seg == 1 || seg == 2;
      const u16* base = seg == 0 ? (isQ ? wq : wmod) : seg == 1 ? Kc : seg == 2 ? Vc : isX ? wo : w1;
      const u32 rs = seg == 0 ? (isQ ? 8192 : 6144) : (seg == 3 && !isX) ? 3584 : 1024;
      const v4i srd = mksrd(base, kv ? 1u << 27 : (seg == 3 && isX) ? 4096 * 1024 : 512 * rs);
      u32 so = kv ? (u32)(grp * 1024 + kv0 + (seg == 1 ? nf : 0)) << 13
                  : seg == 0 ? (isQ ? qi : 16 + mi) * 128
                             : isX ? ((xi >> 3) << 19) + (xi & 7) * 128 : yi * 128;
      const u32 hstep = kv ? 4096 : 32 * rs, ustep = 2 * hstep;
      const u32 sw = seg == 2 ? 2 * g : g, sx = seg == 1 ? 4 : 8;
      const u32 w0 = wrow * rs + wlc, wd = 8 * rs - 1024;
      const u32 a0 = kv ? g * 256 + ((c ^ sw) << 4) : w0, a1 = kv ? g * 256 + ((c ^ sw ^ sx) << 4) : w0 + wd;
      const u32 a2 = kv ? g * 256 + ((c ^ sw ^ ((2 * sx) & 15)) << 4) : w0 + 2 * wd, a3 = kv ? g * 256 + ((c ^ sw ^ ((3 * sx) & 15)) << 4) : w0 + 3 * wd;
      const int ue = seg == 0 ? NF + u0 : seg == 1 ? uv : seg == 2 ? uw : uw + (ntail >> 1);
#pragma nounroll
      while (u < ue) {
        const bool paced = u >= 4 && seg < 3;
        const int stop = min(ue, u < 4 ? 4 : u < 48 ? 48 : 1 << 20);
        u32 dd = paced ? due : due - (1u << 28);
        const int u_in = u, la = (seg == 3 && isX) ? 0x7fffffff : LA * 16;
        u32 t0, t1, lag; u32 vf, vt;
#define DMAS(so) "buffer_load_dwordx4 %[a0], %[srd], " so " offen nt lds\n\tbuffer_load_dwordx4 %[a1], %[srd], " so " offen offset:1024 nt lds\n\t" \
                 "buffer_load_dwordx4 %[a2], %[srd], " so " offen offset:2048 nt lds\n\tbuffer_load_dwordx4 %[a3], %[srd], " so " offen offset:3072 nt lds\n\t"
#define RDFREE "s_lshl_b32 %[t1], %[u], 2\n\ts_and_b32 %[t1], %[t1], 60\n\ts_or_b32 %[t1], %[t1], %[fb]\n\tv_mov_b32 %[vf], %[t1]\n\tds_read_b32 %[vf], %[vf]\n\t"
#define REST(W) "s_waitcnt vmcnt(" #W ")\n\ts_mov_b32 m0, %[t0]\n\ts_lshl_b32 %[t0], %[u], 3\n\t" DMAS("%[t1]")                                   \
                "s_sub_i32 %[t0], %[t0], " #W " - 4\n\ts_max_i32 %[pub], %[pub], %[t0]\n\tv_mov_b32 %[vt], %[pub]\n\tds_write_b32 %[va], %[vt]\n\t"                                      \
                "s_add_i32 %[u], %[u], 1\n\ts_add_u32 %[so], %[so], %[us]\n\ts_add_u32 %[dd], %[dd], %[tau]\n\t" RDFREE                             \
                "s_memrealtime s[98:99]\n\ts_cmp_lt_i32 %[u], %[stop]\n\ts_waitcnt vmcnt(" #W ")\n\ts_cbranch_scc1 1b\n\ts_branch 9f\n"
#define XREST(W) REST(W)
        asm volatile(RDFREE "s_memrealtime s[98:99]\n"
                     "1:\n\ts_sub_i32 %[t1], %[u], 15\n\ts_waitcnt lgkmcnt(0)\n\tv_cmp_gt_i32 vcc, %[t1], %[vf]\n\ts_lshl_b32 s98, s98, 4\n\t"
                     "s_cbranch_vccnz 8f\n"
                     "2:\n\ts_sub_i32 %[lag], s98, %[dd]\n\ts_cmp_lt_i32 %[lag], 0\n\ts_cbranch_scc1 7f\n\t"
                     "s_lshl_b32 %[t0], %[u], 13\n\ts_and_b32 %[t0], %[t0], 0x1e000\n\ts_mov_b32 m0, %[t0]\n\ts_add_u32 %[t1], %[so], %[hs]\n\t" DMAS("%[so]")
                     "s_or_b32 %[t0], %[t0], 0x1000\n\ts_cmp_lt_i32 %[lag], %[la]\n\ts_cbranch_scc1 4f\n\ts_cmp_lt_i32 %[lag], %[lb]\n\ts_cbranch_scc1 5f\n\t"
                     XREST(WC) "4:\n\t" XREST(WA) "5:\n\t" XREST(WB)
                     "8:\n\t" RDFREE "s_sub_i32 %[t1], %[u], 15\n\ts_waitcnt lgkmcnt(0)\n\tv_cmp_gt_i32 vcc, %[t1], %[vf]\n\ts_nop 0\n\ts_cbranch_vccz 7f\n\t"
                     "s_waitcnt vmcnt(0)\n\ts_lshl_b32 %[pub], %[u], 3\n\tv_mov_b32 %[vt], %[pub]\n\tds_write_b32 %[va], %[vt]\n"
                     "6:\n\t" RDFREE "s_sub_i32 %[t1], %[u], 15\n\ts_waitcnt lgkmcnt(0)\n\tv_cmp_gt_i32 vcc, %[t1], %[vf]\n\ts_sleep 1\n\ts_cbranch_vccnz 6b\n"
                     "7:\n\ts_memrealtime s[98:99]\n\ts_waitcnt lgkmcnt(0)\n\ts_lshl_b32 s98, s98, 4\n\ts_branch 2b\n"
                     "9:\n\ts_waitcnt lgkmcnt(0)"
                     : [u] "+s"(u), [so] "+s"(so), [dd] "+s"(dd), [pub] "+s"(pub), [t0] "=&s"(t0), [t1] "=&s"(t1), [lag] "=&s"(lag), [vf] "=&v"(vf), [vt] "=&v"(vt)
                     : [a0] "v"(a0), [a1] "v"(a1), [a2] "v"(a2), [a3] "v"(a3), [srd] "s"(srd), [hs] "s"(hstep), [us] "s"(ustep), [stop] "s"(stop),
                       [va] "v"(L_CTL + 4 * C_FULL), [fb] "i"(L_CTL + 4 * C_FREE), [tau] "s"(tau), [la] "s"(la), [lb] "i"(LB * 16)
                     : "memory", "vcc", "scc", "s98", "s99");
        due += (u - u_in) * tau;
        if (u == 4 && isS) { WAITV(16); publish(16); }
        if (u == uw) stsv(L_CTL + 4 * C_VISS, 1);
        if (u == 48) { const u32 t = __builtin_amdgcn_readfirstlane(ldsv(L_CTL + 4 * C_T0)); if (t) due = t * 16 + u * tau; }
      }
    }
    iss = 8 * u;
    if (!ntail) { PACE(24); PACE(16); PACE(8); PACE(0); }
    else arrive(C_ISS);
    __builtin_amdgcn_s_setprio(0);
  } else {
    const rsrc_t rws = __builtin_amdgcn_make_buffer_rsrc(ws, 0, W_END, 0x00020000);
    auto wait_full = [&](u32 need) { while (ldsv(L_CTL + 4 * C_FULL) < need) __builtin_amdgcn_s_sleep(1); FENCE(); };
    const u32 trow = 4 * g + (c >> 2);
    auto waddr = [&](int t) -> u32 { return trow * 128 + ((((u32)(t ^ ((trow >> 1) & 3)) << 1) | ((c >> 1) & 1)) << 4) + (c & 1) * 8; };

    if (wave == 1) {
      const u64 E0 = __hip_atomic_load((GAS u64*)(ws + W_EPOCH), __ATOMIC_RELAXED, __HIP_MEMORY_SCOPE_AGENT);
      u32 tag;
#define CHAIN_START() do { E = E0 + 1; tag = (u32)E; } while (0)
      auto silu4 = [&](u32 x, u32 y) { return v2u{pk2(silu_exact(lo16(x)), silu_exact(bfhi(x))), pk2(silu_exact(lo16(y)), silu_exact(bfhi(y)))}; };
      if (isS) {
        const v2u cv = *(const v2u*)(cin + (lane >> 5) * 512 + (r - 12) * 128 + (lane & 31) * 4);
        CHAIN_START();
        *lp<v2u>(L_ACT + (lane >> 5) * ASTR + fpos((lane & 31) * 4) * 2) = silu4(cv.x, cv.y);
        FENCE();
        v8bf act[4];
#pragma unroll
        for (int ks = 0; ks < 4; ++ks) { act[ks] = ldfrag(L_ACT + (c & 1) * ASTR + g * 16 + ks * 64); PIN(act[ks]); }
        wait_full(16);
        v8bf W[16];
#pragma unroll
        for (int j = 0; j < 16; ++j) W[j] = trfrag(waddr(j & 3) + (j >> 2) * 4096);
        FENCE(); stsv(L_CTL + 4 * C_FREE, 1); stsv(L_CTL + 4 * (C_FREE + 1), 2);
        v4f acc[4];
#pragma unroll
        for (int t = 0; t < 4; ++t) acc[t] = v4f{0.f, 0.f, 0.f, 0.f};
#pragma unroll
        for (int j = 0; j < 16; ++j) acc[j & 3] = mfma(act[j >> 2], W[j], acc[j & 3]);
        const float r0 = g == 0 ? acc[0][0] : g == 1 ? acc[1][0] : g == 2 ? acc[2][0] : acc[3][0];
        const float r1 = g == 0 ? acc[0][1] : g == 1 ? acc[1][1] : g == 2 ? acc[2][1] : acc[3][1];
        while (ldsv(L_CTL + 4 * C_CLR) == 0) { }
        FENCE();
        st4(v4u{__float_as_uint(r0), tag, __float_as_uint(r1), tag}, rws, lane * 16, W_SP + ((r - 12) * 1024 + grp * 64) * 16);
      } else if (isR) {
        const v4u in0 = *(const v4u*)(xin + lane * 8), in1 = *(const v4u*)(xin + 512 + lane * 8);
        u32 xs0 = *(const u32*)(xin + (grp >> 1) * 64 + (lane & ~1)), xs1 = *(const u32*)(xin + 512 + (grp >> 1) * 64 + (lane & ~1));
        CHAIN_START();
        PIN(xs0); PIN(xs1);
        xs0 >>= 16 * (lane & 1); xs1 >>= 16 * (lane & 1);
        float mr[2], ri[2];
#pragma unroll
        for (int b = 0; b < 2; ++b) {
          const v4u xv = b ? in1 : in0;
          const float x[8] = {lo16(xv.x), bfhi(xv.x), lo16(xv.y), bfhi(xv.y), lo16(xv.z), bfhi(xv.z), lo16(xv.w), bfhi(xv.w)};
          float s = 0.f;
#pragma unroll
          for (int i = 0; i < 8; ++i) s += x[i];
          const float mean = wave_sum(s) * (1.f / 512.f);
          float q2 = 0.f;
#pragma unroll
          for (int i = 0; i < 8; ++i) q2 += (x[i] - mean) * (x[i] - mean);
          const float var = wave_sum(q2) * (1.f / 512.f);
          mr[b] = rb(mean); ri[b] = rcp(rb(__builtin_amdgcn_sqrtf(rb(rb(var) + 1e-5f))));
        }
        v2f y = rb2(rb2(v2f{lo16(xs0) - mr[0], lo16(xs1) - mr[1]}) * v2f{ri[0], ri[1]});
        PIN(y);
        v4u a[8];
#pragma nounroll
        for (int sp = 0; sp < SPIN_CAP; ++sp) {
          u32 bad = 0;
#pragma unroll
          for (int i = 0; i < 8; ++i) { a[i] = ld4(rws, lane * 16, W_SP + ((i & 3) * 1024 + (i >> 2) * 512 + (grp >> 1) * 64) * 16); bad |= (a[i].y ^ tag) | (a[i].w ^ tag); }
          if (__all(bad == 0)) break;
          FENCE();
        }
        v2f sc[2];
#pragma unroll
        for (int pt = 0; pt < 2; ++pt) {
          sc[pt] = v2f{0.f, 0.f};
#pragma unroll
          for (int k4 = 0; k4 < 4; ++k4) sc[pt] += v2f{__uint_as_float(a[pt * 4 + k4].x), __uint_as_float(a[pt * 4 + k4].z)};
        }
        const v2f h = rb2(y * rb2(rb2(sc[1]) + 1.f)) + rb2(sc[0]);
        st2(v2u{pk2(h.x, h.y), tag}, rws, lane * 8, W_HG + (grp >> 1) * 512);
      } else if (isM || isQ) {
        if (isM) {
          const v4u in0 = *(const v4u*)(cin + lane * 8), in1 = *(const v4u*)(cin + 512 + lane * 8);
          CHAIN_START();
          *lp<v2u>(L_ACT + fpos(lane * 8) * 2) = silu4(in0.x, in0.y); *lp<v2u>(L_ACT + fpos(lane * 8 + 4) * 2) = silu4(in0.z, in0.w);
          *lp<v2u>(L_ACT + ASTR + fpos(lane * 8) * 2) = silu4(in1.x, in1.y); *lp<v2u>(L_ACT + ASTR + fpos(lane * 8 + 4) * 2) = silu4(in1.z, in1.w);
        } else {
          CHAIN_START();
          while (__builtin_amdgcn_s_memrealtime() < tstart + HDELAY) __builtin_amdgcn_s_sleep(4);
          v4u a[4];
#pragma nounroll
          for (int sp = 0; sp < SPIN_CAP; ++sp) {
            u32 bad = 0;
#pragma unroll
            for (int i = 0; i < 4; ++i) { a[i] = ld4(rws, lane * 16 + i * 1024, W_HG); bad |= (a[i].y ^ tag) | (a[i].w ^ tag); }
            if (__all(bad == 0)) break;
            FENCE();
          }
#pragma unroll
          for (int i = 0; i < 4; ++i) {
            const u32 wa = L_ACT + fpos(i * 128 + lane * 2) * 2;
            *lp<u32>(wa) = (a[i].x & 0xffffu) | (a[i].z << 16); *lp<u32>(wa + ASTR) = (a[i].x >> 16) | (a[i].z & 0xffff0000u);
          }
        }
        FENCE(); stsv(L_CTL + 4 * C_TAG, tag); stsv(L_CTL + 4 * C_ARDY, 1);
      } else CHAIN_START();
      if (blockIdx.x < 8 && lane == 0) st2(v2u{(u32)tstart, tag}, rws, blockIdx.x * 8, W_T0);
      {
        wait_full(8 * u0 + 16 * QAT);
        v4u a[2];
#pragma nounroll
        for (int sp = 0; sp < SPIN_CAP; ++sp) {
          a[0] = ld4(rws, lane * 16, W_QG + grp * 2048); a[1] = ld4(rws, lane * 16 + 1024, W_QG + grp * 2048);
          if (__all(((a[0].y ^ tag) | (a[0].w ^ tag) | (a[1].y ^ tag) | (a[1].w ^ tag)) == 0)) break;
          __builtin_amdgcn_s_sleep(16); FENCE();
        }
        *lp<v2u>(L_Q + lane * 8) = v2u{a[0].x, a[0].z}; *lp<v2u>(L_Q + 512 + lane * 8) = v2u{a[1].x, a[1].z};
        FENCE(); stsv(L_CTL + 4 * C_TAG, tag); stsv(L_CTL + 4 * C_QRDY, 1);
        {
          const v2u t = ld2(rws, (lane & 7) * 8, W_T0);
          if (__all(t.y == tag)) {
            int dt = (int)(t.x - (u32)tstart);
            dt += __builtin_amdgcn_mov_dpp(dt, 0xB1, 0xf, 0xf, true); dt += __builtin_amdgcn_mov_dpp(dt, 0x4E, 0xf, 0xf, true); dt += __builtin_amdgcn_mov_dpp(dt, 0x141, 0xf, 0xf, true);
            if (dt > -2400 && dt < 2400) stsv(L_CTL + 4 * C_T0, ((u32)tstart + (dt >> 3) + COFF) | 1);
          }
        }
      }
      if (ntail) {
        const v4i srd = mksrd(isX ? wo : w2, isX ? 4096 * 1024 : 1792 * 1024);
        const u32 a0 = (lane >> 3) * 1024 + (((lane & 7) ^ ((lane >> 3) & 6)) << 4), a1 = a0 + 7168, a2 = a0 + 2 * 7168, a3 = a0 + 3 * 7168;
        u32 so = isX ? ((xi >> 3) << 19) + (xi & 7) * 128 + 256 * 1024 : yi << 16;
        while (ldsv(L_CTL + 4 * C_VISS) == 0) __builtin_amdgcn_s_sleep(2);
#pragma nounroll
        for (int u = uw + (ntail >> 1); u < uw + ntail; ++u, so += isX ? 65536 : 128) {
          while ((int)ldsv(L_CTL + 4 * (C_FREE + (u & (NSLOT - 1)))) < u + 1 - NSLOT) __builtin_amdgcn_s_sleep(1);
          const u32 lb = (u & (NSLOT - 1)) * SLOT, m = lb + 4096, s1 = so + 32768;
          DMA4(lb, a0, a1, a2, a3, srd, so);
          DMA4(m, a0, a1, a2, a3, srd, s1);
          WAITV(W2W);
        }
        arrive(C_ISS);
      }
      if (r == 8) {
        wait_full(8 * uw);
        const int zb = grp >> 3, zc = (grp & 7) * 64 + c * 4 + g;
        const u32 xv = xin[zb * 512 + zc];
        v2u m0, m1;
#pragma nounroll
        for (int sp = 0; sp < SPIN_CAP; ++sp) {
          m0 = ld2(rws, zc * 8, W_MG); m1 = ld2(rws, zc * 8, W_MG + 3 * 4096);
          if (__all(((m0.y ^ tag) | (m1.y ^ tag)) == 0)) break;
          FENCE();
        }
        v4u z[7];
#pragma nounroll
        for (int sp = 0; sp < SPIN_CAP; ++sp) {
          z[0] = ld4(rws, g * 4096 + c * 16, W_AP + grp * 256); z[1] = ld4(rws, g * 4096 + c * 16, W_AP + 16384 + grp * 256);
          if (__all(max(max(max(z[0].x, z[0].y), max(z[0].z, z[0].w)), max(max(z[1].x, z[1].y), max(z[1].z, z[1].w))) != SENT)) break;
          __builtin_amdgcn_s_sleep(2); FENCE();
        }
        v4f s0 = __builtin_bit_cast(v4f, z[0]), s1 = __builtin_bit_cast(v4f, z[1]);
        const float at = fold32(fold16(s0.x, s0.y), fold16(s0.z, s0.w)) + fold32(fold16(s1.x, s1.y), fold16(s1.z, s1.w));
        float x1 = rb(lo16(xv) + rb(bfhi(m0.x << (16 - 16 * zb)) * rb(at))), gb = bfhi(m1.x << (16 - 16 * zb));
        GAS u16* op = (GAS u16*)out + zb * 512 + zc;
        PIN(x1); PIN(gb); PIN(op);
#pragma nounroll
        for (int sp = 0; sp < SPIN_CAP; ++sp) {
#pragma unroll
          for (int j = 0; j < 7; ++j) z[j] = ld4(rws, g * 4096 + c * 16, W_OP + j * 16384 + grp * 256);
          u32 mx = 0;
#pragma unroll
          for (int j = 0; j < 7; ++j) mx = max(mx, max(max(z[j].x, z[j].y), max(z[j].z, z[j].w)));
          if (__all(mx != SENT)) break;
          FENCE();
        }
        const v4f s = ((__builtin_bit_cast(v4f, z[0]) + __builtin_bit_cast(v4f, z[1])) + (__builtin_bit_cast(v4f, z[2]) + __builtin_bit_cast(v4f, z[3]))) +
                      ((__builtin_bit_cast(v4f, z[4]) + __builtin_bit_cast(v4f, z[5])) + __builtin_bit_cast(v4f, z[6]));
        const float rr = x1 + rb(gb * rb(fold16(fold32(s.x, s.z), fold32(s.y, s.w))));
        *op = (u16)pk2(rr, rr);
        __builtin_amdgcn_endpgm();
      }
    } else {
      if (isS && wave == 7) {
        const v4u s4 = {SENT, SENT, SENT, SENT};
        const u32 so = W_PART + (grp * 4 + r - 12) * CLRB;
#pragma unroll
        for (int i = 0; i < CLRB / 1024; ++i) st4(s4, rws, lane * 16 + (i & 3) * 1024, so + (i >> 2) * 4096);
        if (lane < (CLRB % 1024) / 16) st4(s4, rws, lane * 16, so + CLRB / 1024 * 1024);
        WAITV(0); stsv(L_CTL + 4 * C_CLR, 1);
      }

      if (wave == 6) { *lp<v4f>(L_MRG + lane * 16) = v4f{0.f, 0.f, 0.f, 0.f}; *lp<v4f>(L_MRG + 1024 + lane * 16) = v4f{0.f, 0.f, 0.f, 0.f}; }
      const int d = c >> 2, cw = wave - 2;
      const u32 sca = L_SC + (g * 4 + (c & 3)) * 16;
      {
        u32 ka[4];
#pragma unroll
        for (int s = 0; s < 4; ++s) ka[s] = c * 256 + (((4 * s + g) ^ c) << 4);
        v8bf qf[4];
        float mx = -INFINITY;
        auto rdk = [&](v8bf (&kf)[8], int j) {
          const int u = j < nf ? j : u0 + j;
          const u32 lb = (u & (NSLOT - 1)) * SLOT;
          wait_full(8 * u + 8);
#pragma unroll
          for (int s = 0; s < 4; ++s) { kf[s] = ldfrag(lb + ka[s]); kf[4 + s] = ldfrag(lb + ka[s] + 4096); }
          FENCE(); stsv(L_CTL + 4 * (C_FREE + (u & (NSLOT - 1))), u + 1);
        };
        auto qk = [&](v8bf (&kf)[8], int j) {
          v4f S0 = {0.f, 0.f, 0.f, 0.f}, S1 = {0.f, 0.f, 0.f, 0.f};
#pragma unroll
          for (int s = 0; s < 4; ++s) { S0 = mfma(kf[s], qf[s], S0); S1 = mfma(kf[4 + s], qf[s], S1); }
          const v2f a = rb2(v2f{S0.x, S0.y}) * 0.08838834764831845f, b = rb2(v2f{S0.z, S0.w}) * 0.08838834764831845f;
          const v2f e = rb2(v2f{S1.x, S1.y}) * 0.08838834764831845f, f = rb2(v2f{S1.z, S1.w}) * 0.08838834764831845f;
          mx = fmaxf(fmaxf(fmaxf(mx, fmaxf(a.x, a.y)), fmaxf(b.x, b.y)), fmaxf(fmaxf(e.x, e.y), fmaxf(f.x, f.y)));
          if (c < 4) *lp<v4u>(sca + j * 256) = v4u{pk2(a.x, a.y), pk2(b.x, b.y), pk2(e.x, e.y), pk2(f.x, f.y)};
        };
        v8bf sk[NSTASH][8];
        rdk(sk[0], cw);
        if (u0 == 8 && wave < 6) {
          wait_full(8 * NF + 64);
          v8bf W[16];
#pragma unroll
          for (int ks = 0; ks < 16; ++ks) W[ks] = trfrag(NF * SLOT + ks * 4096 + waddr(wave - 2));
          WAITL();
          u32 old = 0;
          if (lane == 0) old = __hip_atomic_fetch_add(lp<u32>(L_CTL + 4 * C_WRD), 1u, __ATOMIC_RELAXED, __HIP_MEMORY_SCOPE_WORKGROUP);
          if (__builtin_amdgcn_readfirstlane(old) == 3 && lane < 8) stsv(L_CTL + 4 * (C_FREE + NF + lane), NF + lane + 1);
          while (ldsv(L_CTL + 4 * C_ARDY) == 0) __builtin_amdgcn_s_sleep(1);
          FENCE();
          const u32 tag = ldsv(L_CTL + 4 * C_TAG);
          v8bf A[16];
#pragma unroll
          for (int ks = 0; ks < 16; ++ks) A[ks] = ldfrag(L_ACT + (c & 1) * ASTR + g * 16 + ks * 64);
          __builtin_amdgcn_sched_barrier(0);
          v4f d0 = {0.f, 0.f, 0.f, 0.f}, d1 = {0.f, 0.f, 0.f, 0.f};
#pragma unroll
          for (int ks = 0; ks < 16; ks += 2) { d0 = mfma(A[ks], W[ks], d0); d1 = mfma(A[ks + 1], W[ks + 1], d1); }
          const float r0 = d0[0] + d1[0], r1 = d0[1] + d1[1];
          const float n0 = dpp<0xB1>(r0), n1 = dpp<0xB1>(r1);
          const bool odd = lane & 1;
          if (lane < 16) {
            if (isM) st2(v2u{pk2(r0, r1), tag}, rws, lane * 8, W_MG + mi * 512 + (wave - 2) * 128);
            else st2(v2u{pk2(odd ? n1 : r0, odd ? r1 : n0), tag}, rws, (lane & 1) * 16384 + (lane >> 1) * 8, W_QG + qi * 256 + (wave - 2) * 64);
          }
        }
#pragma unroll
        for (int i = 1; i < NSTASH; ++i) rdk(sk[i], cw + i * NCONS);
        while (ldsv(L_CTL + 4 * C_QRDY) == 0) __builtin_amdgcn_s_sleep(2);
        FENCE();
#pragma unroll
        for (int s = 0; s < 4; ++s) qf[s] = ldfrag(L_Q + (c & 3) * 256 + (4 * s + g) * 16);
#pragma unroll
        for (int i = 0; i < NSTASH; ++i) qk(sk[i], cw + i * NCONS);
#pragma nounroll
        for (int j = cw + NSTASH * NCONS; j < nb; j += NCONS) { v8bf kf[8]; rdk(kf, j); qk(kf, j); }
        mx = swap32_max(swap16_max(rb(mx)));
        if (lane < 4) *lp<float>(L_ML + cw * 16 + lane * 4) = mx;
        FENCE();
        arrive(C_MX);
      }
      float M;
      {
        while (ldsv(L_CTL + 4 * C_MX) < NCONS) __builtin_amdgcn_s_sleep(1);
        FENCE();
        M = *lp<float>(L_ML + (c & 3) * 4);
#pragma unroll
        for (int w = 1; w < NCONS; ++w) M = fmaxf(M, *lp<float>(L_ML + w * 16 + (c & 3) * 4));
        float l = 0.f;
#pragma nounroll
        for (int j = cw + d * NCONS; j < nb; j += 4 * NCONS) {
          const v4u s = *lp<v4u>(sca + j * 256);
          float p[8];
#pragma unroll
          for (int i = 0; i < 4; ++i) { p[2 * i] = ex2((lo16(s[i]) - M) * 1.4426950408889634f); p[2 * i + 1] = ex2((bfhi(s[i]) - M) * 1.4426950408889634f); }
          l += ((p[0] + p[1]) + (p[2] + p[3])) + ((p[4] + p[5]) + (p[6] + p[7]));
          *lp<v4u>(sca + j * 256) = v4u{pk2(p[0], p[1]), pk2(p[2], p[3]), pk2(p[4], p[5]), pk2(p[6], p[7])};
        }
        l += dpp<0x124>(l); l += dpp<0x128>(l);
        l = swap32_add(swap16_add(l));
        if (lane < 4) *lp<float>(L_ML + 128 + cw * 16 + lane * 4) = l;
        FENCE();
        arrive(C_SM);
      }
      {
        u32 va[8];
#pragma unroll
        for (int t = 0; t < 8; ++t) va[t] = trow * 256 + ((((u32)(t ^ (trow & 7)) << 1) | ((c >> 1) & 1)) << 4) + (c & 1) * 8;
        v4f acc[8];
#pragma unroll
        for (int t = 0; t < 8; ++t) acc[t] = v4f{0.f, 0.f, 0.f, 0.f};
        int j = cw;
#pragma nounroll
        for (; j + NCONS < nb; j += NCONS) {
          const int u = uv + j;
          const u32 lb = (u & (NSLOT - 1)) * SLOT;
          wait_full(8 * u + 8);
          const v8bf pf = ldfrag(sca + j * 256);
          v8bf vf[8];
#pragma unroll
          for (int t = 0; t < 8; ++t) {
            v4s lo = __builtin_amdgcn_ds_read_tr16_b64_v4i16(lp<v4s>(lb + va[t])), hi = __builtin_amdgcn_ds_read_tr16_b64_v4i16(lp<v4s>(lb + va[t] + 4096));
            vf[t] = __builtin_bit_cast(v8bf, __builtin_shufflevector(lo, hi, 0, 1, 2, 3, 4, 5, 6, 7));
          }
          FENCE(); stsv(L_CTL + 4 * (C_FREE + (u & (NSLOT - 1))), u + 1);
#pragma unroll
          for (int t = 0; t < 8; ++t) acc[t] = mfma(vf[t], pf, acc[t]);
        }
        const int link = j - (nb - NCONS), u = uv + j;
        const u32 lb = (u & (NSLOT - 1)) * SLOT;
        v4f e0 = d == 0 ? acc[0] : d == 1 ? acc[2] : d == 2 ? acc[4] : acc[6];
        v4f e1 = d == 0 ? acc[1] : d == 1 ? acc[3] : d == 2 ? acc[5] : acc[7];
        const v4u pf = *lp<v4u>(sca + j * 256);
        v4u pm[4];
#pragma unroll
        for (int i = 0; i < 4; ++i) { pm[i] = d == i ? pf : v4u{0u, 0u, 0u, 0u}; PIN(pm[i]); }
        float inv = 1.f, L = 0.f;
        if (link == NCONS - 1) {
          while (ldsv(L_CTL + 4 * C_SM) < NCONS) { }
          FENCE();
          L = *lp<float>(L_ML + 128 + (c & 3) * 4);
#pragma unroll
          for (int w = 1; w < NCONS; ++w) L += *lp<float>(L_ML + 128 + w * 16 + (c & 3) * 4);
          inv = rcp(L);
        }
        v2u ml = {canon(M), canon(L)};
        PIN(ml);
        u32 oa = (c & 3) * 4096 + d * 64 + g * 16, ra = L_MRG + lane * 16;
        u32 vb[8];
#pragma unroll
        for (int t = 0; t < 8; ++t) { vb[t] = lb + va[t]; PIN(vb[t]); }
        PIN(e0); PIN(e1); PIN(oa); PIN(ra); PIN(inv);
        if (link == NCONS - 1) {
          while (ldsv(L_CTL + 4 * C_FULL) < (u32)(8 * u + 8 - HEAD)) { }
          if (lane < 4) st2(ml, rws, lane * 8, W_ML + bid * 128);
        }
        while (ldsv(L_CTL + 4 * C_FULL) < (u32)(8 * u + 8)) { }
        FENCE();
        v8bf vf[8];
#pragma unroll
        for (int t = 0; t < 8; ++t) {
          v4s lo = __builtin_amdgcn_ds_read_tr16_b64_v4i16(lp<v4s>(vb[t])), hi = __builtin_amdgcn_ds_read_tr16_b64_v4i16(lp<v4s>(vb[t] + 4096));
          vf[t] = __builtin_bit_cast(v8bf, __builtin_shufflevector(lo, hi, 0, 1, 2, 3, 4, 5, 6, 7));
        }
        __builtin_amdgcn_sched_barrier(0);
#pragma unroll
        for (int i = 0; i < 4; ++i) { e0 = mfma(vf[2 * i], __builtin_bit_cast(v8bf, pm[i]), e0); e1 = mfma(vf[2 * i + 1], __builtin_bit_cast(v8bf, pm[i]), e1); }
        __builtin_amdgcn_sched_barrier(0);
        v4f r0, r1;
        for (;;) {
          const u32 cnt = ldsv(L_CTL + 4 * C_MCNT);
          FENCE();
          r0 = *lp<v4f>(ra); r1 = *lp<v4f>(ra + 1024);
          FENCE();
          if (cnt >= (u32)link) break;
        }
        e0 = (e0 + r0) * inv; e1 = (e1 + r1) * inv;
        if (link == NCONS - 1) {
          st4(v4u{min(pk2(e0.x, e0.y), SENT - 1), min(pk2(e0.z, e0.w), SENT - 1), min(pk2(e1.x, e1.y), SENT - 1), min(pk2(e1.z, e1.w), SENT - 1)}, rws, oa, W_PART + grp * 16384 + r * 256);
        } else {
          *lp<v4f>(ra) = e0; *lp<v4f>(ra + 1024) = e1;
          FENCE(); stsv(L_CTL + 4 * C_MCNT, link + 1);
        }
        stsv(L_CTL + 4 * (C_FREE + (u & (NSLOT - 1))), u + 1);
      }
    }
  }
  if (!(isX || isY)) return;
  const u32 trow = 4 * g + (c >> 2);
  auto waddr = [&](int t) -> u32 { return trow * 128 + ((((u32)(t ^ ((trow >> 1) & 3)) << 1) | ((c >> 1) & 1)) << 4) + (c & 1) * 8; };
  auto wslot = [&](int t) -> u32 { return ((uw + t) & (NSLOT - 1)) * SLOT; };
  const int half = lane >> 5, i32 = lane & 31;
  const u32 fa = L_ACT + (c & 1) * ASTR + g * 16;
  {
    const rsrc_t rws = __builtin_amdgcn_make_buffer_rsrc(ws, 0, W_END, 0x00020000);
    auto weights_done = [&]() { WAITV(0); if (wave == 0) stsv(L_CTL + 4 * C_FULL, 8 * (uw + ntail)); arrive(C_WIN); };
    auto weights_in = [&]() { while (ldsv(L_CTL + 4 * C_WIN) < 2) __builtin_amdgcn_s_sleep(1); FENCE(); };
    if (isX) {
      const int kvh = xi >> 3, ng = xi & 7;
      const int b = wave >> 2, h = wave & 3;
      u32 wa = L_ACT + b * ASTR + (h * 128 + c * 8 + g * 2) * 2, wb[8];
#pragma unroll
      for (int i = 0; i < 8; ++i) { wb[i] = wslot(i) + waddr(wave & 3); PIN(wb[i]); }
      u32 vo = lane * 16 + W_PART + (b * 8 + kvh) * 16384 + h * 4096;
      u32 vl = ((c & 3) * 4 + ((g + (c >> 2)) & 3)) * 128 + W_ML + (b * 8 + kvh) * 2048 + h * 8;
      u32 vs = c * 2048 + g * 16 + W_AP + kvh * 4096 + ng * 256 + (wave & 3) * 64, fb = fa;
      PIN(wa); PIN(vo); PIN(vl); PIN(vs); PIN(fb);
      while (ldsv(L_CTL + 4 * C_ISS) < 2) __builtin_amdgcn_s_sleep(1);
      FENCE();
      v2u ls = ld2(rws, vl, 0);
      if (wave < 2) weights_done();
      if (!__all(max(ls.x, ls.y) != SENT)) {
#pragma nounroll
        for (int sp = 0; sp < SPIN_CAP; ++sp) {
          __builtin_amdgcn_s_sleep(1); FENCE();
          ls = ld2(rws, vl, 0);
          if (__all(max(ls.x, ls.y) != SENT)) break;
        }
        __builtin_amdgcn_s_sleep(XW);
      }
      FENCE();
      v4u a[4];
#pragma unroll
      for (int j = 0; j < 4; ++j) a[j] = ld4(rws, vo + j * 1024, 0);
      __builtin_amdgcn_sched_barrier(0);
      v8bf W[16];
      if (wave >= 4) {
        weights_in();
#pragma unroll
        for (int ks = 0; ks < 16; ++ks) W[ks] = trfrag(wb[ks >> 1] + (ks & 1) * 4096);
      }
      float M = __uint_as_float(ls.x);
      M = fmaxf(M, dpp<0xB1>(M)); M = fmaxf(M, dpp<0x4E>(M)); M = fmaxf(M, dpp<0x141>(M)); M = fmaxf(M, dpp<0x128>(M));
      float cf = ex2((__uint_as_float(ls.x) - M) * 1.4426950408889634f) * __uint_as_float(ls.y);
      cf *= rcp(row_sum(cf));
      float cj[4] = {dpp<0x150>(cf), dpp<0x151>(cf), dpp<0x152>(cf), dpp<0x153>(cf)};
      PIN(cj[0]); PIN(cj[1]); PIN(cj[2]); PIN(cj[3]);
#pragma nounroll
      for (int sp = 0; sp < SPIN_CAP; ++sp) {
        u32 mx = 0;
#pragma unroll
        for (int j = 0; j < 4; ++j) mx = max(max(mx, max(a[j].x, a[j].y)), max(a[j].z, a[j].w));
        if (__all(mx != SENT)) break;
        FENCE();
#pragma unroll
        for (int j = 0; j < 4; ++j) a[j] = ld4(rws, vo + j * 1024, 0);
      }
      v2f o[4];
#pragma unroll
      for (int i = 0; i < 4; ++i) {
        o[i] = v2f{lo16(a[0][i]), bfhi(a[0][i])} * cj[0];
#pragma unroll
        for (int j = 1; j < 4; ++j) o[i] += v2f{lo16(a[j][i]), bfhi(a[j][i])} * cj[j];
      }
      *lp<u32>(wa) = pk2(fold16(fold32(o[0].x, o[2].x), fold32(o[1].x, o[3].x)), fold16(fold32(o[0].y, o[2].y), fold32(o[1].y, o[3].y)));
      bar();
      if (wave >= 4) {
        v8bf A[16];
#pragma unroll
        for (int ks = 0; ks < 16; ++ks) A[ks] = ldfrag(fb + ks * 64);
        __builtin_amdgcn_sched_barrier(0);
        v4f d0 = {0.f, 0.f, 0.f, 0.f};
#pragma unroll
        for (int ks = 0; ks < 16; ++ks) d0 = mfma(W[ks], A[ks], d0);
        v4u ov = {canon(d0.x), canon(d0.y), canon(d0.z), canon(d0.w)};
        if (c < 2) st4(ov, rws, vs, 0);
      }
    } else {
      const int b = wave >> 2, col = (wave & 3) * 128 + i32 * 4 + half * 2;
      u32 wb[8];
#pragma unroll
      for (int i = 0; i < 8; ++i) { wb[i] = wave >= 4 ? wslot(i) + waddr(wave & 3) : wslot(8 + 2 * wave + (i >> 2)) + waddr(i & 3); PIN(wb[i]); }
      u32 wa = L_ACT + b * ASTR + fpos(col) * 2, ra = L_RED + wave * 8 + half * 4, rb_ = L_RED + b * 32;
      u32 ha = L_H1 + (c & 1) * 128 + fpos((wave & 3) * 16 + g * 4 + ((c >> 1) & 3)) * 2, fb = wave >= 4 ? fa : L_H1 + (c & 1) * 128 + g * 16;
      u32 vo = half * 16384 + i32 * 16 + W_AP + b * 2048 + (wave & 3) * 512, vs = (c & 1) * 2048 + (c >> 1) * 64 + g * 16 + W_OP + yi * 4096 + wave * 512;
      PIN(wa); PIN(ra); PIN(rb_); PIN(ha); PIN(fb); PIN(vo); PIN(vs);
      v2f x0, ga, sb, cb1;
      {
        const u32 xv = *(const u32*)(xin + b * 512 + col);
        const u32 tag = ldsv(L_CTL + 4 * C_TAG);
        v4u mg[3];
#pragma nounroll
        for (int sp = 0; sp < SPIN_CAP; ++sp) {
#pragma unroll
          for (int i = 0; i < 3; ++i) mg[i] = ld4(rws, col * 8, W_MG + i * 4096);
          if (__all(((mg[0].y ^ tag) | (mg[0].w ^ tag) | (mg[1].y ^ tag) | (mg[1].w ^ tag) | (mg[2].y ^ tag) | (mg[2].w ^ tag)) == 0)) break;
          FENCE();
        }
        const int sh = 16 - 16 * b;
        x0 = v2f{lo16(xv), bfhi(xv)};
        ga = v2f{bfhi(mg[0].x << sh), bfhi(mg[0].z << sh)}; sb = v2f{bfhi(mg[1].x << sh), bfhi(mg[1].z << sh)};
        cb1 = rb2(v2f{bfhi(mg[2].x << sh), bfhi(mg[2].z << sh)} + 1.f);
      }
      PIN(x0); PIN(ga); PIN(sb); PIN(cb1);
      if (wave < 2) weights_done();
      weights_in();
      v4u a[4];
#pragma unroll
      for (int j = 0; j < 4; ++j) a[j] = ld4(rws, vo, j * 4096);
      __builtin_amdgcn_sched_barrier(0);
      v8bf W[16];
#pragma unroll
      for (int j = 0; j < 16; ++j) W[j] = trfrag(wb[j >> 1] + (j & 1) * 4096);
#pragma nounroll
      for (int sp = 0; sp < SPIN_CAP; ++sp) {
        u32 mx = 0;
#pragma unroll
        for (int j = 0; j < 4; ++j) mx = max(max(mx, max(a[j].x, a[j].y)), max(a[j].z, a[j].w));
        if (__all(mx != SENT)) break;
        FENCE();
#pragma unroll
        for (int j = 0; j < 4; ++j) a[j] = ld4(rws, vo, j * 4096);
      }
      const v4f at = (__builtin_bit_cast(v4f, a[0]) + __builtin_bit_cast(v4f, a[1])) + (__builtin_bit_cast(v4f, a[2]) + __builtin_bit_cast(v4f, a[3]));
      const v2f x1 = rb2(x0 + rb2(ga * rb2(v2f{fold32(at.x, at.z), fold32(at.y, at.w)})));
      const float st = half_sum(fold32(x1.x + x1.y, fmaf(x1.x, x1.x, x1.y * x1.y)));
      if (i32 == 0) *lp<float>(ra) = st;
      bar();
      {
        const v4f r0 = *lp<v4f>(rb_), r1 = *lp<v4f>(rb_ + 16);
        const float mean = ((r0.x + r0.z) + (r1.x + r1.z)) * (1.f / 512.f);
        const float var = fmaxf(fmaf(-mean, mean, ((r0.y + r0.w) + (r1.y + r1.w)) * (1.f / 512.f)), 0.f);
        const v2f mv = rb2(v2f{mean, var});
        const float ri = rcp(rb(__builtin_amdgcn_sqrtf(rb(mv.y + 1e-5f))));
        const v2f hh = rb2(rb2(rb2(x1 - mv.x) * ri) * cb1) + sb;
        *lp<u32>(wa) = pk2(hh.x, hh.y);
      }
      bar();
      if (wave >= 4) {
        v8bf A[16];
#pragma unroll
        for (int ks = 0; ks < 16; ++ks) A[ks] = ldfrag(fb + ks * 64);
        __builtin_amdgcn_sched_barrier(0);
        v4f d0 = {0.f, 0.f, 0.f, 0.f};
#pragma unroll
        for (int ks = 0; ks < 16; ++ks) d0 = mfma(W[ks], A[ks], d0);
        float e0 = (c & 2) ? d0.y : d0.x, e1 = (c & 2) ? d0.w : d0.z;
        PIN(e0); PIN(e1);
        const float v = silu(rb((c & 4) ? e1 : e0));
        *lp<u16>(ha) = (u16)pk2(v, v);
      }
      bar();
      if (wave < 4) {
        const v8bf a0 = ldfrag(fb), a1 = ldfrag(fb + 64);
        v4f dd[8];
#pragma unroll
        for (int t = 0; t < 8; ++t) dd[t] = mfma(W[2 * t + 1], a1, mfma(W[2 * t], a0, v4f{0.f, 0.f, 0.f, 0.f}));
        v4f rv = dd[0];
#pragma unroll
        for (int t = 1; t < 8; ++t) if ((c >> 1) == t) rv = dd[t];
        v4u ov = {canon(rv.x), canon(rv.y), canon(rv.z), canon(rv.w)};
        st4(ov, rws, vs, 0);
        if (wave == 1 && yi == 27 && lane == 0) st2(v2u{(u32)E, (u32)(E >> 32)}, rws, 0, W_EPOCH);
      }
    }
  }
}

extern "C" void kernel_launch(void* const* d_in, const int* in_sizes, int n_in, void* d_out, int out_size, void* d_ws, size_t ws_size, hipStream_t stream) {
  hipLaunchKernelGGL(fused_block, dim3(256), dim3(512), 0, stream, (const u16*)d_in[1], (const u16*)d_in[2], (const u16*)d_in[9], (const u16*)d_in[3],
                     (const u16*)d_in[4], (const u16*)d_in[6], (const u16*)d_in[7], (const u16*)d_in[0], (const u16*)d_in[8], (u16*)d_out, (unsigned char*)d_ws);
}
```

```cpp
#include <hip/hip_runtime.h>

typedef unsigned int u32;
typedef unsigned long long u64;
typedef unsigned short u16;
typedef u32 v4u __attribute__((ext_vector_type(4)));
typedef u32 v2u __attribute__((ext_vector_type(2)));
typedef int v4i __attribute__((ext_vector_type(4)));
typedef float v4f __attribute__((ext_vector_type(4)));
typedef float v2f __attribute__((ext_vector_type(2)));
typedef short v4s __attribute__((ext_vector_type(4)));
typedef __bf16 v8bf __attribute__((ext_vector_type(8)));
typedef __bf16 v2bf __attribute__((ext_vector_type(2)));
#define LAS __attribute__((address_space(3)))
#define GAS __attribute__((address_space(1)))
#define DEVI __device__ __forceinline__

constexpr int NSLOT = 16, SLOT = 8192;
constexpr int NCONS = 6, NSTASH = 4, NF = 2, NMAX = 72;
#define WA 16
#define WB 20
#define WC 24
#define WD 28
#define LA 25
#define LB 50
#define LC 90
#define TEND 3705
#define LEADX 130
#define LEADX3 210
#define LEADY 190
#define COFF -20
#define W2W 24
#define HEAD 16
#define XW 5
constexpr int QAT = 12, HDELAY = 440, CODE_BYTES = 23552;
#define BLK_A 65, 65, 65, 63, 63, 63, 63, 63, 63, 63, 64, 64, 65, 65, 65, 65
constexpr int NBLK[2][16] = {{BLK_A}, {65, 65, 65, 63, 62, 62, 62, 62, 62, 62, 67, 67, 65, 65, 65, 65}};
constexpr int BIAS = 11;
constexpr u64 start_table(int k) {
  u64 t = 0; int s = 0;
  for (int r = 0; r < 16; ++r) { const int dl = s - 64 * r + BIAS; if (dl < 0 || dl > 15 || NBLK[k][r] > NMAX) return ~0ull; t |= (u64)dl << (4 * r); s += NBLK[k][r]; }
  return s == 1024 ? t : ~0ull;
}
constexpr u64 ST_A = start_table(0), ST_B = start_table(1);
static_assert(ST_A != ~0ull && ST_B != ~0ull, "block table");
constexpr u64 pace(int k, int r, int extra, int lead) { return ((TEND - lead) * 16 + NBLK[k][r]) / (2 * NBLK[k][r] + extra) - 384; }
constexpr u64 pace_table(int k) {
  return pace(k, 0, 0, LEADX) | pace(k, 3, 0, LEADX3) << 8 | pace(k, 3, 0, LEADX) << 16 | pace(k, 4, 8, 0) << 24 | pace(k, 8, 8, 0) << 32 |
         pace(k, 10, 0, k ? 0 : LEADY) << 40 | pace(k, 12, 2, 0) << 48;
}
constexpr u64 PACE_A = pace_table(0), PACE_B = pace_table(1);
constexpr int L_CTL = NSLOT * SLOT;
constexpr int L_Q = L_CTL + 256;
constexpr int L_ACT = L_Q + 1024;
constexpr int ASTR = 1024 + 64;
constexpr int L_MRG = L_ACT + 2 * ASTR;
constexpr int L_ML = L_MRG + 2048;
constexpr int L_SC = L_ML + 256;
constexpr int L_JUNK = L_SC + NMAX * 256;
constexpr int L_END = L_JUNK + 256;
constexpr int L_H1 = L_SC;
constexpr int L_RED = L_H1 + 256;
static_assert(L_END <= 160 * 1024, "LDS");
constexpr int C_FULL = 0, C_QRDY = 1, C_MCNT = 2, C_CLR = 3, C_ARDY = 4, C_TAG = 5, C_WRD = 6, C_MX = 7, C_SM = 8, C_T0 = 9, C_ISS = 10, C_VISS = 11, C_WIN = 12, C_FREE = 16;
constexpr u32 W_EPOCH = 0, W_T0 = 4096, W_SP = 8192, W_HG = W_SP + 4 * 1024 * 16, W_QG = W_HG + 512 * 8, W_MG = W_QG + 4096 * 8, W_PART = W_MG + 2048 * 8;
constexpr u32 W_ML = W_PART + 64 * 4096;
constexpr u32 W_AP = W_ML + 256 * 128, W_OP = W_AP + 8 * 4096, W_END = W_OP + 28 * 4096;
constexpr u32 CLRB = (W_END - W_PART) / 64;
static_assert(CLRB * 64 == W_END - W_PART && CLRB % 16 == 0, "clear split");
constexpr u32 SENT = 0xFFFFFFFFu;
constexpr int SPIN_CAP = 1 << 16;

DEVI float lo16(u32 v) { return __uint_as_float(v << 16); }
DEVI float bfhi(u32 v) { return __uint_as_float(v & 0xffff0000u); }
DEVI u32 pk2(float a, float b) { v2f x = {a, b}; return __builtin_bit_cast(u32, __builtin_convertvector(x, v2bf)); }
DEVI float rb(float a) { return lo16(pk2(a, a)); }
DEVI float ex2(float x) { return __builtin_amdgcn_exp2f(x); }
DEVI float rcp(float x) { return __builtin_amdgcn_rcpf(x); }
DEVI float silu(float v) { return v * rcp(1.f + ex2(v * -1.4426950408889634f)); }
DEVI float silu_exact(float v) {
  const float t = fminf(v * -1.4426950216293335f, 126.f);
  const float r = fmaf(v, -1.92596299112662e-08f, fmaf(v, -1.4426950216293335f, -t));
  float e = ex2(t);
  e = fmaf(e, fminf(r, 1.f) * 0.6931471805599453f, e);
  const float d = 1.f + e, rc = rcp(d), q = v * rc;
  return fmaf(fmaf(-q, d, v), rc, q);
}
DEVI v2f rb2(v2f a) { const u32 p = pk2(a.x, a.y); return v2f{lo16(p), bfhi(p)}; }
DEVI float fold32(float a, float b) { auto s = __builtin_amdgcn_permlane32_swap(__float_as_uint(a), __float_as_uint(b), false, false); return __uint_as_float(s[0]) + __uint_as_float(s[1]); }
DEVI float fold16(float a, float b) { auto s = __builtin_amdgcn_permlane16_swap(__float_as_uint(a), __float_as_uint(b), false, false); return __uint_as_float(s[0]) + __uint_as_float(s[1]); }
#define PIN(x) asm volatile("" : "+v"(x))
DEVI float swap16_add(float x) { auto s = __builtin_amdgcn_permlane16_swap(__float_as_uint(x), __float_as_uint(x), false, false); return __uint_as_float(s[0]) + __uint_as_float(s[1]); }
DEVI float swap32_add(float x) { auto s = __builtin_amdgcn_permlane32_swap(__float_as_uint(x), __float_as_uint(x), false, false); return __uint_as_float(s[0]) + __uint_as_float(s[1]); }
DEVI float swap16_max(float x) { auto s = __builtin_amdgcn_permlane16_swap(__float_as_uint(x), __float_as_uint(x), false, false); return fmaxf(__uint_as_float(s[0]), __uint_as_float(s[1])); }
DEVI float swap32_max(float x) { auto s = __builtin_amdgcn_permlane32_swap(__float_as_uint(x), __float_as_uint(x), false, false); return fmaxf(__uint_as_float(s[0]), __uint_as_float(s[1])); }
template <int CTRL> DEVI float dpp(float x) { return __builtin_bit_cast(float, __builtin_amdgcn_mov_dpp(__builtin_bit_cast(int, x), CTRL, 0xf, 0xf, true)); }
DEVI float row_sum(float x) { x += dpp<0xB1>(x); x += dpp<0x4E>(x); x += dpp<0x141>(x); x += dpp<0x128>(x); return x; }
DEVI float half_sum(float x) { return swap16_add(row_sum(x)); }
DEVI float wave_sum(float x) { return swap32_add(half_sum(x)); }
template <typename T> DEVI LAS T* lp(u32 a) { return (LAS T*)(size_t)a; }
DEVI u32 ldsv(u32 a) { return *(volatile LAS u32*)(size_t)a; }
DEVI void stsv(u32 a, u32 v) { *(volatile LAS u32*)(size_t)a = v; }
#define FENCE() asm volatile("" ::: "memory")
#define WAITV(n) asm volatile("s_waitcnt vmcnt(%0)" ::"i"(n) : "memory")
#define WAITL() asm volatile("s_waitcnt lgkmcnt(0)" ::: "memory")
DEVI void bar() { WAITL(); __builtin_amdgcn_s_barrier(); FENCE(); }
typedef __amdgpu_buffer_rsrc_t rsrc_t;
DEVI v4u ld4(rsrc_t r, u32 vo, u32 so) { return __builtin_bit_cast(v4u, __builtin_amdgcn_raw_buffer_load_b128(r, vo, so, 16)); }
DEVI v2u ld2(rsrc_t r, u32 vo, u32 so) { return __builtin_bit_cast(v2u, __builtin_amdgcn_raw_buffer_load_b64(r, vo, so, 16)); }
DEVI void st4(v4u v, rsrc_t r, u32 vo, u32 so) { __builtin_amdgcn_raw_buffer_store_b128(v, r, vo, so, 16); }
DEVI void st2(v2u v, rsrc_t r, u32 vo, u32 so) { __builtin_amdgcn_raw_buffer_store_b64(v, r, vo, so, 16); }
DEVI u32 canon(float f) { u32 b = __float_as_uint(f); return b < 0xFFFFFFFEu ? b : 0xFFFFFFFEu; }
DEVI v8bf trfrag(u32 a) {
  v4s lo = __builtin_amdgcn_ds_read_tr16_b64_v4i16(lp<v4s>(a)), hi = __builtin_amdgcn_ds_read_tr16_b64_v4i16(lp<v4s>(a + 2048));
  return __builtin_bit_cast(v8bf, __builtin_shufflevector(lo, hi, 0, 1, 2, 3, 4, 5, 6, 7));
}
DEVI v4f mfma(v8bf a, v8bf b, v4f c) { return __builtin_amdgcn_mfma_f32_16x16x32_bf16(a, b, c, 0, 0, 0); }
DEVI v8bf ldfrag(u32 a) { return __builtin_bit_cast(v8bf, *lp<v4u>(a)); }
DEVI int fpos(int k) { return (k & ~31) + ((k >> 2) & 3) * 8 + ((k >> 4) & 1) * 4 + (k & 3); }
DEVI v4i mksrd(const void* p, u32 bytes) { u64 a = (u64)p; return v4i{(int)(u32)a, (int)(u32)(a >> 32), (int)bytes, 0x00020000}; }

#define DMA4(m, v0, v1, v2, v3, srd, so)                                                                                   \
  asm volatile("s_mov_b32 m0, %0\n\ts_nop 0\n\tbuffer_load_dwordx4 %1, %5, %6 offen nt lds\n\t"                           \
               "buffer_load_dwordx4 %2, %5, %6 offen offset:1024 nt lds\n\tbuffer_load_dwordx4 %3, %5, %6 offen offset:2048 nt lds\n\t" \
               "buffer_load_dwordx4 %4, %5, %6 offen offset:3072 nt lds" ::"s"(m), "v"(v0), "v"(v1), "v"(v2), "v"(v3), "s"(srd), "s"(so) : "memory")

__global__ void __launch_bounds__(512, 2)
fused_block(const u16* __restrict__ Kc, const u16* __restrict__ Vc, const u16* __restrict__ wmod, const u16* __restrict__ wq,
            const u16* __restrict__ wo, const u16* __restrict__ w1, const u16* __restrict__ w2, const u16* __restrict__ xin,
            const u16* __restrict__ cin, u16* __restrict__ out, unsigned char* ws) {
  __shared__ __attribute__((aligned(16))) unsigned char smem[L_END];
  asm volatile("" ::"s"((u32)(size_t)(LAS unsigned char*)smem));
  const int tid = threadIdx.x, lane = tid & 63, wave = __builtin_amdgcn_readfirstlane(tid >> 6);
  if (wave == 1 && blockIdx.x < 32) {
    u64 pc; asm volatile("s_getpc_b64 %0" : "=s"(pc));
    const v4i rc = mksrd((const void*)(pc & ~127ull), CODE_BYTES);
    const u32 vo = lane * 128, m = L_JUNK, s1 = 8192, s2 = 16384;
    asm volatile("s_mov_b32 m0, %0\n\ts_nop 0\n\tbuffer_load_dword %1, %2, 0 offen lds\n\tbuffer_load_dword %1, %2, %3 offen lds\n\tbuffer_load_dword %1, %2, %4 offen lds"
                 ::"s"(m), "v"(vo), "s"(rc), "s"(s1), "s"(s2) : "memory");
  }
  const int bid = (blockIdx.x & ~31) | ((blockIdx.x & 7) << 2) | ((blockIdx.x >> 3) & 3);
  const int grp = bid >> 4, r = bid & 15;
  const int g = lane >> 4, c = lane & 15;
  const u64 stt = (grp < 14 ? ST_A : ST_B) >> (4 * r);
  const int kv0 = 64 * r + ((int)stt & 15) - BIAS;
  const bool isS = r >= 12;
  if (__builtin_expect(wave == 0, 1)) {
    const v4i srd = isS ? mksrd(wmod, 512 * 6144) : mksrd(Kc, 1u << 27);
    const u32 k0 = g * 256 + ((c ^ g) << 4), w0 = (lane >> 3) * 6144 + (((lane & 7) ^ ((lane >> 3) & 6)) << 4);
    const u32 a0 = isS ? w0 : k0, a1 = isS ? w0 + 48128 : k0 ^ 64, a2 = isS ? w0 + 2 * 48128 : k0 ^ 128, a3 = isS ? w0 + 3 * 48128 : k0 ^ 192;
    const u32 so = isS ? (r - 12) * 128 * 6144 + grp * 128 : (u32)(grp * 1024 + kv0) << 13, hs = isS ? 32 * 6144 : 4096;
#pragma unroll
    for (int i = 0; i < 4; ++i) { const u32 m = i * 4096, s1 = so + i * hs; DMA4(m, a0, a1, a2, a3, srd, s1); }
  }
  if (wave == 2) { stsv(L_CTL + 4 * lane, 0); WAITL(); }
  __builtin_amdgcn_s_barrier(); FENCE();
  const u64 tstart = __builtin_amdgcn_s_memrealtime();
  FENCE();
  const bool isX = r < 4, isQ = r >= 4 && r < 8, isM = r == 8 || r == 9, isY = (r == 10 || r == 11) && grp < 14, isR = r == 0 && (grp & 1);
  const int xi = grp * 4 + r, qi = grp * 4 + r - 4, mi = grp * 2 + r - 8, yi = grp * 2 + r - 10;
  const int u0 = (int)(0x2222008888880000ull >> (4 * r)) & 15;
  const int nb = 64 + (r == 15 ? BIAS : (int)(stt >> 4) & 15) - ((int)stt & 15);
  const int nf = isS ? 0 : NF;
  const int ntail = isX ? 8 : isY ? 16 : 0;
  const int uv = u0 + nb, uw = uv + nb;
  u64 E = 0;
  auto arrive = [&](int w) { if (lane == 0) __hip_atomic_fetch_add(lp<u32>(L_CTL + 4 * w), 1u, __ATOMIC_RELAXED, __HIP_MEMORY_SCOPE_WORKGROUP); };
  int wv = wave;
  asm volatile("" : "+s"(wv));
  if (__builtin_expect(wv == 0, 1)) {
    __builtin_amdgcn_s_setprio(3);
    int iss, pub = 0;
    auto publish = [&](int v) { pub = max(pub, v); stsv(L_CTL + 4 * C_FULL, pub); };
#define PACE(W_) do { WAITV(W_); publish(iss - (W_)); } while (0)
    const u32 cls = ((u32)(0x6666554433331000ull >> (4 * r)) & 7) + (r == 3 && (grp & 1));
    const u32 tau = 384 + ((u32)((grp < 14 ? PACE_A : PACE_B) >> (8 * cls)) & 255);
    u32 due = ((u32)tstart + COFF) * 16 + NF * tau;
    const u32 wrow = lane >> 3, wlc = ((lane & 7) ^ (((wrow >> 1) & 3) << 1)) << 4;
    int u = NF;
#pragma nounroll
    for (int seg = u0 == 8 ? 0 : 1; seg < (ntail ? 4 : 3); ++seg) {
      const bool kv = seg == 1 || seg == 2;
      const u16* base = seg == 0 ? (isQ ? wq : wmod) : seg == 1 ? Kc : seg == 2 ? Vc : isX ? wo : w1;
      const u32 rs = seg == 0 ? (isQ ? 8192 : 6144) : (seg == 3 && !isX) ? 3584 : 1024;
      const v4i srd = mksrd(base, kv ? 1u << 27 : (seg == 3 && isX) ? 4096 * 1024 : 512 * rs);
      u32 so = kv ? (u32)(grp * 1024 + kv0 + (seg == 1 ? nf : 0)) << 13
                  : seg == 0 ? (isQ ? qi : 16 + mi) * 128
                             : isX ? ((xi >> 3) << 19) + (xi & 7) * 128 : yi * 128;
      const u32 hstep = kv ? 4096 : 32 * rs, ustep = 2 * hstep;
      const u32 sw = seg == 2 ? 2 * g : g, sx = seg == 1 ? 4 : 8;
      const u32 w0 = wrow * rs + wlc, wd = 8 * rs - 1024;
      const u32 a0 = kv ? g * 256 + ((c ^ sw) << 4) : w0, a1 = kv ? g * 256 + ((c ^ sw ^ sx) << 4) : w0 + wd;
      const u32 a2 = kv ? g * 256 + ((c ^ sw ^ ((2 * sx) & 15)) << 4) : w0 + 2 * wd, a3 = kv ? g * 256 + ((c ^ sw ^ ((3 * sx) & 15)) << 4) : w0 + 3 * wd;
      const int ue = seg == 0 ? NF + u0 : seg == 1 ? uv : seg == 2 ? uw : uw + (ntail >> 1);
#pragma nounroll
      while (u < ue) {
        const bool paced = u >= 4 && seg < 3;
        const int stop = min(ue, u < 4 ? 4 : u < 48 ? 48 : 1 << 20);
        u32 dd = paced ? due : due - (1u << 28);
        const int u_in = u, la = (seg == 3 && isX) ? 0x7fffffff : LA * 16;
        u32 t0, t1, lag; u32 vf, vt;
#define DMAS(so) "buffer_load_dwordx4 %[a0], %[srd], " so " offen nt lds\n\tbuffer_load_dwordx4 %[a1], %[srd], " so " offen offset:1024 nt lds\n\t" \
                 "buffer_load_dwordx4 %[a2], %[srd], " so " offen offset:2048 nt lds\n\tbuffer_load_dwordx4 %[a3], %[srd], " so " offen offset:3072 nt lds\n\t"
#define RDFREE "s_lshl_b32 %[t1], %[u], 2\n\ts_and_b32 %[t1], %[t1], 60\n\ts_or_b32 %[t1], %[t1], %[fb]\n\tv_mov_b32 %[vf], %[t1]\n\tds_read_b32 %[vf], %[vf]\n\t"
#define REST(W) "s_waitcnt vmcnt(" #W ")\n\ts_mov_b32 m0, %[t0]\n\ts_lshl_b32 %[t0], %[u], 3\n\t" DMAS("%[t1]")                                   \
                "s_sub_i32 %[t0], %[t0], " #W " - 4\n\ts_max_i32 %[pub], %[pub], %[t0]\n\tv_mov_b32 %[vt], %[pub]\n\tds_write_b32 %[va], %[vt]\n\t"                                      \
                "s_add_i32 %[u], %[u], 1\n\ts_add_u32 %[so], %[so], %[us]\n\ts_add_u32 %[dd], %[dd], %[tau]\n\t" RDFREE                             \
                "s_memrealtime s[98:99]\n\ts_cmp_lt_i32 %[u], %[stop]\n\ts_waitcnt vmcnt(" #W ")\n\ts_cbranch_scc1 1b\n\ts_branch 9f\n"
#define XREST(W) REST(W)
        asm volatile(RDFREE "s_memrealtime s[98:99]\n"
                     "1:\n\ts_sub_i32 %[t1], %[u], 15\n\ts_waitcnt lgkmcnt(0)\n\tv_cmp_gt_i32 vcc, %[t1], %[vf]\n\ts_lshl_b32 s98, s98, 4\n\t"
                     "s_cbranch_vccnz 8f\n"
                     "2:\n\ts_sub_i32 %[lag], s98, %[dd]\n\ts_cmp_lt_i32 %[lag], 0\n\ts_cbranch_scc1 7f\n\t"
                     "s_lshl_b32 %[t0], %[u], 13\n\ts_and_b32 %[t0], %[t0], 0x1e000\n\ts_mov_b32 m0, %[t0]\n\ts_add_u32 %[t1], %[so], %[hs]\n\t" DMAS("%[so]")
                     "s_or_b32 %[t0], %[t0], 0x1000\n\ts_cmp_lt_i32 %[lag], %[la]\n\ts_cbranch_scc1 4f\n\ts_cmp_lt_i32 %[lag], %[lb]\n\ts_cbranch_scc1 5f\n\t"
                     "s_cmp_lt_i32 %[lag], %[lc]\n\ts_cbranch_scc1 3f\n\t"
                     XREST(WD) "3:\n\t" XREST(WC) "4:\n\t" XREST(WA) "5:\n\t" XREST(WB)
                     "8:\n\t" RDFREE "s_sub_i32 %[t1], %[u], 15\n\ts_waitcnt lgkmcnt(0)\n\tv_cmp_gt_i32 vcc, %[t1], %[vf]\n\ts_nop 0\n\ts_cbranch_vccz 7f\n\t"
                     "s_waitcnt vmcnt(0)\n\ts_lshl_b32 %[pub], %[u], 3\n\tv_mov_b32 %[vt], %[pub]\n\tds_write_b32 %[va], %[vt]\n"
                     "6:\n\t" RDFREE "s_sub_i32 %[t1], %[u], 15\n\ts_waitcnt lgkmcnt(0)\n\tv_cmp_gt_i32 vcc, %[t1], %[vf]\n\ts_sleep 1\n\ts_cbranch_vccnz 6b\n"
                     "7:\n\ts_memrealtime s[98:99]\n\ts_waitcnt lgkmcnt(0)\n\ts_lshl_b32 s98, s98, 4\n\ts_branch 2b\n"
                     "9:\n\ts_waitcnt lgkmcnt(0)"
                     : [u] "+s"(u), [so] "+s"(so), [dd] "+s"(dd), [pub] "+s"(pub), [t0] "=&s"(t0), [t1] "=&s"(t1), [lag] "=&s"(lag), [vf] "=&v"(vf), [vt] "=&v"(vt)
                     : [a0] "v"(a0), [a1] "v"(a1), [a2] "v"(a2), [a3] "v"(a3), [srd] "s"(srd), [hs] "s"(hstep), [us] "s"(ustep), [stop] "s"(stop),
                       [va] "v"(L_CTL + 4 * C_FULL), [fb] "i"(L_CTL + 4 * C_FREE), [tau] "s"(tau), [la] "s"(la), [lb] "i"(LB * 16), [lc] "i"(LC * 16)
                     : "memory", "vcc", "scc", "s98", "s99");
        due += (u - u_in) * tau;
        if (u == 4 && isS) { WAITV(16); publish(16); }
        if (u == uw) stsv(L_CTL + 4 * C_VISS, 1);
        if (u == 48) { const u32 t = __builtin_amdgcn_readfirstlane(ldsv(L_CTL + 4 * C_T0)); if (t) due = t * 16 + u * tau; }
      }
    }
    iss = 8 * u;
    if (!ntail) { PACE(24); PACE(16); PACE(8); PACE(0); }
    else arrive(C_ISS);
    __builtin_amdgcn_s_setprio(0);
  } else {
    const rsrc_t rws = __builtin_amdgcn_make_buffer_rsrc(ws, 0, W_END, 0x00020000);
    auto wait_full = [&](u32 need) { while (ldsv(L_CTL + 4 * C_FULL) < need) __builtin_amdgcn_s_sleep(1); FENCE(); };
    const u32 trow = 4 * g + (c >> 2);
    auto waddr = [&](int t) -> u32 { return trow * 128 + ((((u32)(t ^ ((trow >> 1) & 3)) << 1) | ((c >> 1) & 1)) << 4) + (c & 1) * 8; };

    if (wave == 1) {
      const u64 E0 = __hip_atomic_load((GAS u64*)(ws + W_EPOCH), __ATOMIC_RELAXED, __HIP_MEMORY_SCOPE_AGENT);
      u32 tag;
#define CHAIN_START() do { E = E0 + 1; tag = (u32)E; } while (0)
      auto silu4 = [&](u32 x, u32 y) { return v2u{pk2(silu_exact(lo16(x)), silu_exact(bfhi(x))), pk2(silu_exact(lo16(y)), silu_exact(bfhi(y)))}; };
      if (isS) {
        const v2u cv = *(const v2u*)(cin + (lane >> 5) * 512 + (r - 12) * 128 + (lane & 31) * 4);
        CHAIN_START();
        *lp<v2u>(L_ACT + (lane >> 5) * ASTR + fpos((lane & 31) * 4) * 2) = silu4(cv.x, cv.y);
        FENCE();
        v8bf act[4];
#pragma unroll
        for (int ks = 0; ks < 4; ++ks) { act[ks] = ldfrag(L_ACT + (c & 1) * ASTR + g * 16 + ks * 64); PIN(act[ks]); }
        wait_full(16);
        v8bf W[16];
#pragma unroll
        for (int j = 0; j < 16; ++j) W[j] = trfrag(waddr(j & 3) + (j >> 2) * 4096);
        FENCE(); stsv(L_CTL + 4 * C_FREE, 1); stsv(L_CTL + 4 * (C_FREE + 1), 2);
        v4f acc[4];
#pragma unroll
        for (int t = 0; t < 4; ++t) acc[t] = v4f{0.f, 0.f, 0.f, 0.f};
#pragma unroll
        for (int j = 0; j < 16; ++j) acc[j & 3] = mfma(act[j >> 2], W[j], acc[j & 3]);
        const float r0 = g == 0 ? acc[0][0] : g == 1 ? acc[1][0] : g == 2 ? acc[2][0] : acc[3][0];
        const float r1 = g == 0 ? acc[0][1] : g == 1 ? acc[1][1] : g == 2 ? acc[2][1] : acc[3][1];
        while (ldsv(L_CTL + 4 * C_CLR) == 0) { }
        FENCE();
        st4(v4u{__float_as_uint(r0), tag, __float_as_uint(r1), tag}, rws, lane * 16, W_SP + ((r - 12) * 1024 + grp * 64) * 16);
      } else if (isR) {
        const v4u in0 = *(const v4u*)(xin + lane * 8), in1 = *(const v4u*)(xin + 512 + lane * 8);
        u32 xs0 = *(const u32*)(xin + (grp >> 1) * 64 + (lane & ~1)), xs1 = *(const u32*)(xin + 512 + (grp >> 1) * 64 + (lane & ~1));
        CHAIN_START();
        PIN(xs0); PIN(xs1);
        xs0 >>= 16 * (lane & 1); xs1 >>= 16 * (lane & 1);
        float mr[2], ri[2];
#pragma unroll
        for (int b = 0; b < 2; ++b) {
          const v4u xv = b ? in1 : in0;
          const float x[8] = {lo16(xv.x), bfhi(xv.x), lo16(xv.y), bfhi(xv.y), lo16(xv.z), bfhi(xv.z), lo16(xv.w), bfhi(xv.w)};
          float s = 0.f;
#pragma unroll
          for (int i = 0; i < 8; ++i) s += x[i];
          const float mean = wave_sum(s) * (1.f / 512.f);
          float q2 = 0.f;
#pragma unroll
          for (int i = 0; i < 8; ++i) q2 += (x[i] - mean) * (x[i] - mean);
          const float var = wave_sum(q2) * (1.f / 512.f);
          mr[b] = rb(mean); ri[b] = rcp(rb(__builtin_amdgcn_sqrtf(rb(rb(var) + 1e-5f))));
        }
        v2f y = rb2(rb2(v2f{lo16(xs0) - mr[0], lo16(xs1) - mr[1]}) * v2f{ri[0], ri[1]});
        PIN(y);
        v4u a[8];
#pragma nounroll
        for (int sp = 0; sp < SPIN_CAP; ++sp) {
          u32 bad = 0;
#pragma unroll
          for (int i = 0; i < 8; ++i) { a[i] = ld4(rws, lane * 16, W_SP + ((i & 3) * 1024 + (i >> 2) * 512 + (grp >> 1) * 64) * 16); bad |= (a[i].y ^ tag) | (a[i].w ^ tag); }
          if (__all(bad == 0)) break;
          FENCE();
        }
        v2f sc[2];
#pragma unroll
        for (int pt = 0; pt < 2; ++pt) {
          sc[pt] = v2f{0.f, 0.f};
#pragma unroll
          for (int k4 = 0; k4 < 4; ++k4) sc[pt] += v2f{__uint_as_float(a[pt * 4 + k4].x), __uint_as_float(a[pt * 4 + k4].z)};
        }
        const v2f h = rb2(y * rb2(rb2(sc[1]) + 1.f)) + rb2(sc[0]);
        st2(v2u{pk2(h.x, h.y), tag}, rws, lane * 8, W_HG + (grp >> 1) * 512);
      } else if (isM || isQ) {
        if (isM) {
          const v4u in0 = *(const v4u*)(cin + lane * 8), in1 = *(const v4u*)(cin + 512 + lane * 8);
          CHAIN_START();
          *lp<v2u>(L_ACT + fpos(lane * 8) * 2) = silu4(in0.x, in0.y); *lp<v2u>(L_ACT + fpos(lane * 8 + 4) * 2) = silu4(in0.z, in0.w);
          *lp<v2u>(L_ACT + ASTR + fpos(lane * 8) * 2) = silu4(in1.x, in1.y); *lp<v2u>(L_ACT + ASTR + fpos(lane * 8 + 4) * 2) = silu4(in1.z, in1.w);
        } else {
          CHAIN_START();
          while (__builtin_amdgcn_s_memrealtime() < tstart + HDELAY) __builtin_amdgcn_s_sleep(4);
          v4u a[4];
#pragma nounroll
          for (int sp = 0; sp < SPIN_CAP; ++sp) {
            u32 bad = 0;
#pragma unroll
            for (int i = 0; i < 4; ++i) { a[i] = ld4(rws, lane * 16 + i * 1024, W_HG); bad |= (a[i].y ^ tag) | (a[i].w ^ tag); }
            if (__all(bad == 0)) break;
            FENCE();
          }
#pragma unroll
          for (int i = 0; i < 4; ++i) {
            const u32 wa = L_ACT + fpos(i * 128 + lane * 2) * 2;
            *lp<u32>(wa) = (a[i].x & 0xffffu) | (a[i].z << 16); *lp<u32>(wa + ASTR) = (a[i].x >> 16) | (a[i].z & 0xffff0000u);
          }
        }
        FENCE(); stsv(L_CTL + 4 * C_TAG, tag); stsv(L_CTL + 4 * C_ARDY, 1);
      } else CHAIN_START();
      if (blockIdx.x < 8 && lane == 0) st2(v2u{(u32)tstart, tag}, rws, blockIdx.x * 8, W_T0);
      {
        wait_full(8 * u0 + 16 * QAT);
        v4u a[2];
#pragma nounroll
        for (int sp = 0; sp < SPIN_CAP; ++sp) {
          a[0] = ld4(rws, lane * 16, W_QG + grp * 2048); a[1] = ld4(rws, lane * 16 + 1024, W_QG + grp * 2048);
          if (__all(((a[0].y ^ tag) | (a[0].w ^ tag) | (a[1].y ^ tag) | (a[1].w ^ tag)) == 0)) break;
          __builtin_amdgcn_s_sleep(16); FENCE();
        }
        *lp<v2u>(L_Q + lane * 8) = v2u{a[0].x, a[0].z}; *lp<v2u>(L_Q + 512 + lane * 8) = v2u{a[1].x, a[1].z};
        FENCE(); stsv(L_CTL + 4 * C_TAG, tag); stsv(L_CTL + 4 * C_QRDY, 1);
        {
          const v2u t = ld2(rws, (lane & 7) * 8, W_T0);
          if (__all(t.y == tag)) {
            int dt = (int)(t.x - (u32)tstart);
            dt += __builtin_amdgcn_mov_dpp(dt, 0xB1, 0xf, 0xf, true); dt += __builtin_amdgcn_mov_dpp(dt, 0x4E, 0xf, 0xf, true); dt += __builtin_amdgcn_mov_dpp(dt, 0x141, 0xf, 0xf, true);
            if (dt > -2400 && dt < 2400) stsv(L_CTL + 4 * C_T0, ((u32)tstart + (dt >> 3) + COFF) | 1);
          }
        }
      }
      if (ntail) {
        const v4i srd = mksrd(isX ? wo : w2, isX ? 4096 * 1024 : 1792 * 1024);
        const u32 a0 = (lane >> 3) * 1024 + (((lane & 7) ^ ((lane >> 3) & 6)) << 4), a1 = a0 + 7168, a2 = a0 + 2 * 7168, a3 = a0 + 3 * 7168;
        u32 so = isX ? ((xi >> 3) << 19) + (xi & 7) * 128 + 256 * 1024 : yi << 16;
        while (ldsv(L_CTL + 4 * C_VISS) == 0) __builtin_amdgcn_s_sleep(2);
#pragma nounroll
        for (int u = uw + (ntail >> 1); u < uw + ntail; ++u, so += isX ? 65536 : 128) {
          while ((int)ldsv(L_CTL + 4 * (C_FREE + (u & (NSLOT - 1)))) < u + 1 - NSLOT) __builtin_amdgcn_s_sleep(1);
          const u32 lb = (u & (NSLOT - 1)) * SLOT, m = lb + 4096, s1 = so + 32768;
          DMA4(lb, a0, a1, a2, a3, srd, so);
          DMA4(m, a0, a1, a2, a3, srd, s1);
          WAITV(W2W);
        }
        arrive(C_ISS);
      }
      if (r == 8) {
        wait_full(8 * uw);
        const int zb = grp >> 3, zc = (grp & 7) * 64 + c * 4 + g;
        const u32 xv = xin[zb * 512 + zc];
        v2u m0, m1;
#pragma nounroll
        for (int sp = 0; sp < SPIN_CAP; ++sp) {
          m0 = ld2(rws, zc * 8, W_MG); m1 = ld2(rws, zc * 8, W_MG + 3 * 4096);
          if (__all(((m0.y ^ tag) | (m1.y ^ tag)) == 0)) break;
          FENCE();
        }
        v4u z[7];
#pragma nounroll
        for (int sp = 0; sp < SPIN_CAP; ++sp) {
          z[0] = ld4(rws, g * 4096 + c * 16, W_AP + grp * 256); z[1] = ld4(rws, g * 4096 + c * 16, W_AP + 16384 + grp * 256);
          if (__all(max(max(max(z[0].x, z[0].y), max(z[0].z, z[0].w)), max(max(z[1].x, z[1].y), max(z[1].z, z[1].w))) != SENT)) break;
          __builtin_amdgcn_s_sleep(2); FENCE();
        }
        v4f s0 = __builtin_bit_cast(v4f, z[0]), s1 = __builtin_bit_cast(v4f, z[1]);
        const float at = fold32(fold16(s0.x, s0.y), fold16(s0.z, s0.w)) + fold32(fold16(s1.x, s1.y), fold16(s1.z, s1.w));
        float x1 = rb(lo16(xv) + rb(bfhi(m0.x << (16 - 16 * zb)) * rb(at))), gb = bfhi(m1.x << (16 - 16 * zb));
        GAS u16* op = (GAS u16*)out + zb * 512 + zc;
        PIN(x1); PIN(gb); PIN(op);
#pragma nounroll
        for (int sp = 0; sp < SPIN_CAP; ++sp) {
#pragma unroll
          for (int j = 0; j < 7; ++j) z[j] = ld4(rws, g * 4096 + c * 16, W_OP + j * 16384 + grp * 256);
          u32 mx = 0;
#pragma unroll
          for (int j = 0; j < 7; ++j) mx = max(mx, max(max(z[j].x, z[j].y), max(z[j].z, z[j].w)));
          if (__all(mx != SENT)) break;
          FENCE();
        }
        const v4f s = ((__builtin_bit_cast(v4f, z[0]) + __builtin_bit_cast(v4f, z[1])) + (__builtin_bit_cast(v4f, z[2]) + __builtin_bit_cast(v4f, z[3]))) +
                      ((__builtin_bit_cast(v4f, z[4]) + __builtin_bit_cast(v4f, z[5])) + __builtin_bit_cast(v4f, z[6]));
        const float rr = x1 + rb(gb * rb(fold16(fold32(s.x, s.z), fold32(s.y, s.w))));
        *op = (u16)pk2(rr, rr);
        __builtin_amdgcn_endpgm();
      }
    } else {
      if (isS && wave == 7) {
        const v4u s4 = {SENT, SENT, SENT, SENT};
        const u32 so = W_PART + (grp * 4 + r - 12) * CLRB;
#pragma unroll
        for (int i = 0; i < CLRB / 1024; ++i) st4(s4, rws, lane * 16 + (i & 3) * 1024, so + (i >> 2) * 4096);
        if (lane < (CLRB % 1024) / 16) st4(s4, rws, lane * 16, so + CLRB / 1024 * 1024);
        WAITV(0); stsv(L_CTL + 4 * C_CLR, 1);
      }

      if (wave == 6) { *lp<v4f>(L_MRG + lane * 16) = v4f{0.f, 0.f, 0.f, 0.f}; *lp<v4f>(L_MRG + 1024 + lane * 16) = v4f{0.f, 0.f, 0.f, 0.f}; }
      const int d = c >> 2, cw = wave - 2;
      const u32 sca = L_SC + (g * 4 + (c & 3)) * 16;
      {
        u32 ka[4];
#pragma unroll
        for (int s = 0; s < 4; ++s) ka[s] = c * 256 + (((4 * s + g) ^ c) << 4);
        v8bf qf[4];
        float mx = -INFINITY;
        auto rdk = [&](v8bf (&kf)[8], int j) {
          const int u = j < nf ? j : u0 + j;
          const u32 lb = (u & (NSLOT - 1)) * SLOT;
          wait_full(8 * u + 8);
#pragma unroll
          for (int s = 0; s < 4; ++s) { kf[s] = ldfrag(lb + ka[s]); kf[4 + s] = ldfrag(lb + ka[s] + 4096); }
          FENCE(); stsv(L_CTL + 4 * (C_FREE + (u & (NSLOT - 1))), u + 1);
        };
        auto qk = [&](v8bf (&kf)[8], int j) {
          v4f S0 = {0.f, 0.f, 0.f, 0.f}, S1 = {0.f, 0.f, 0.f, 0.f};
#pragma unroll
          for (int s = 0; s < 4; ++s) { S0 = mfma(kf[s], qf[s], S0); S1 = mfma(kf[4 + s], qf[s], S1); }
          const v2f a = rb2(v2f{S0.x, S0.y}) * 0.08838834764831845f, b = rb2(v2f{S0.z, S0.w}) * 0.08838834764831845f;
          const v2f e = rb2(v2f{S1.x, S1.y}) * 0.08838834764831845f, f = rb2(v2f{S1.z, S1.w}) * 0.08838834764831845f;
          mx = fmaxf(fmaxf(fmaxf(mx, fmaxf(a.x, a.y)), fmaxf(b.x, b.y)), fmaxf(fmaxf(e.x, e.y), fmaxf(f.x, f.y)));
          if (c < 4) *lp<v4u>(sca + j * 256) = v4u{pk2(a.x, a.y), pk2(b.x, b.y), pk2(e.x, e.y), pk2(f.x, f.y)};
        };
        v8bf sk[NSTASH][8];
        rdk(sk[0], cw);
        if (u0 == 8 && wave < 6) {
          wait_full(8 * NF + 64);
          v8bf W[16];
#pragma unroll
          for (int ks = 0; ks < 16; ++ks) W[ks] = trfrag(NF * SLOT + ks * 4096 + waddr(wave - 2));
          WAITL();
          u32 old = 0;
          if (lane == 0) old = __hip_atomic_fetch_add(lp<u32>(L_CTL + 4 * C_WRD), 1u, __ATOMIC_RELAXED, __HIP_MEMORY_SCOPE_WORKGROUP);
          if (__builtin_amdgcn_readfirstlane(old) == 3 && lane < 8) stsv(L_CTL + 4 * (C_FREE + NF + lane), NF + lane + 1);
          while (ldsv(L_CTL + 4 * C_ARDY) == 0) __builtin_amdgcn_s_sleep(1);
          FENCE();
          const u32 tag = ldsv(L_CTL + 4 * C_TAG);
          v8bf A[16];
#pragma unroll
          for (int ks = 0; ks < 16; ++ks) A[ks] = ldfrag(L_ACT + (c & 1) * ASTR + g * 16 + ks * 64);
          __builtin_amdgcn_sched_barrier(0);
          v4f d0 = {0.f, 0.f, 0.f, 0.f}, d1 = {0.f, 0.f, 0.f, 0.f};
#pragma unroll
          for (int ks = 0; ks < 16; ks += 2) { d0 = mfma(A[ks], W[ks], d0); d1 = mfma(A[ks + 1], W[ks + 1], d1); }
          const float r0 = d0[0] + d1[0], r1 = d0[1] + d1[1];
          const float n0 = dpp<0xB1>(r0), n1 = dpp<0xB1>(r1);
          const bool odd = lane & 1;
          if (lane < 16) {
            if (isM) st2(v2u{pk2(r0, r1), tag}, rws, lane * 8, W_MG + mi * 512 + (wave - 2) * 128);
            else st2(v2u{pk2(odd ? n1 : r0, odd ? r1 : n0), tag}, rws, (lane & 1) * 16384 + (lane >> 1) * 8, W_QG + qi * 256 + (wave - 2) * 64);
          }
        }
#pragma unroll
        for (int i = 1; i < NSTASH; ++i) rdk(sk[i], cw + i * NCONS);
        while (ldsv(L_CTL + 4 * C_QRDY) == 0) __builtin_amdgcn_s_sleep(2);
        FENCE();
#pragma unroll
        for (int s = 0; s < 4; ++s) qf[s] = ldfrag(L_Q + (c & 3) * 256 + (4 * s + g) * 16);
#pragma unroll
        for (int i = 0; i < NSTASH; ++i) qk(sk[i], cw + i * NCONS);
#pragma nounroll
        for (int j = cw + NSTASH * NCONS; j < nb; j += NCONS) { v8bf kf[8]; rdk(kf, j); qk(kf, j); }
        mx = swap32_max(swap16_max(rb(mx)));
        if (lane < 4) *lp<float>(L_ML + cw * 16 + lane * 4) = mx;
        FENCE();
        arrive(C_MX);
      }
      float M;
      {
        while (ldsv(L_CTL + 4 * C_MX) < NCONS) __builtin_amdgcn_s_sleep(1);
        FENCE();
        M = *lp<float>(L_ML + (c & 3) * 4);
#pragma unroll
        for (int w = 1; w < NCONS; ++w) M = fmaxf(M, *lp<float>(L_ML + w * 16 + (c & 3) * 4));
        float l = 0.f;
#pragma nounroll
        for (int j = cw + d * NCONS; j < nb; j += 4 * NCONS) {
          const v4u s = *lp<v4u>(sca + j * 256);
          float p[8];
#pragma unroll
          for (int i = 0; i < 4; ++i) { p[2 * i] = ex2((lo16(s[i]) - M) * 1.4426950408889634f); p[2 * i + 1] = ex2((bfhi(s[i]) - M) * 1.4426950408889634f); }
          l += ((p[0] + p[1]) + (p[2] + p[3])) + ((p[4] + p[5]) + (p[6] + p[7]));
          *lp<v4u>(sca + j * 256) = v4u{pk2(p[0], p[1]), pk2(p[2], p[3]), pk2(p[4], p[5]), pk2(p[6], p[7])};
        }
        l += dpp<0x124>(l); l += dpp<0x128>(l);
        l = swap32_add(swap16_add(l));
        if (lane < 4) *lp<float>(L_ML + 128 + cw * 16 + lane * 4) = l;
        FENCE();
        arrive(C_SM);
      }
      {
        u32 va[8];
#pragma unroll
        for (int t = 0; t < 8; ++t) va[t] = trow * 256 + ((((u32)(t ^ (trow & 7)) << 1) | ((c >> 1) & 1)) << 4) + (c & 1) * 8;
        v4f acc[8];
#pragma unroll
        for (int t = 0; t < 8; ++t) acc[t] = v4f{0.f, 0.f, 0.f, 0.f};
        int j = cw;
#pragma nounroll
        for (; j + NCONS < nb; j += NCONS) {
          const int u = uv + j;
          const u32 lb = (u & (NSLOT - 1)) * SLOT;
          wait_full(8 * u + 8);
          const v8bf pf = ldfrag(sca + j * 256);
          v8bf vf[8];
#pragma unroll
          for (int t = 0; t < 8; ++t) {
            v4s lo = __builtin_amdgcn_ds_read_tr16_b64_v4i16(lp<v4s>(lb + va[t])), hi = __builtin_amdgcn_ds_read_tr16_b64_v4i16(lp<v4s>(lb + va[t] + 4096));
            vf[t] = __builtin_bit_cast(v8bf, __builtin_shufflevector(lo, hi, 0, 1, 2, 3, 4, 5, 6, 7));
          }
          FENCE(); stsv(L_CTL + 4 * (C_FREE + (u & (NSLOT - 1))), u + 1);
#pragma unroll
          for (int t = 0; t < 8; ++t) acc[t] = mfma(vf[t], pf, acc[t]);
        }
        const int link = j - (nb - NCONS), u = uv + j;
        const u32 lb = (u & (NSLOT - 1)) * SLOT;
        v4f e0 = d == 0 ? acc[0] : d == 1 ? acc[2] : d == 2 ? acc[4] : acc[6];
        v4f e1 = d == 0 ? acc[1] : d == 1 ? acc[3] : d == 2 ? acc[5] : acc[7];
        const v4u pf = *lp<v4u>(sca + j * 256);
        v4u pm[4];
#pragma unroll
        for (int i = 0; i < 4; ++i) { pm[i] = d == i ? pf : v4u{0u, 0u, 0u, 0u}; PIN(pm[i]); }
        float inv = 1.f, L = 0.f;
        if (link == NCONS - 1) {
          while (ldsv(L_CTL + 4 * C_SM) < NCONS) { }
          FENCE();
          L = *lp<float>(L_ML + 128 + (c & 3) * 4);
#pragma unroll
          for (int w = 1; w < NCONS; ++w) L += *lp<float>(L_ML + 128 + w * 16 + (c & 3) * 4);
          inv = rcp(L);
        }
        v2u ml = {canon(M), canon(L)};
        PIN(ml);
        u32 oa = (c & 3) * 4096 + d * 64 + g * 16, ra = L_MRG + lane * 16;
        u32 vb[8];
#pragma unroll
        for (int t = 0; t < 8; ++t) { vb[t] = lb + va[t]; PIN(vb[t]); }
        PIN(e0); PIN(e1); PIN(oa); PIN(ra); PIN(inv);
        if (link == NCONS - 1) {
          while (ldsv(L_CTL + 4 * C_FULL) < (u32)(8 * u + 8 - HEAD)) { }
          if (lane < 4) st2(ml, rws, lane * 8, W_ML + bid * 128);
        }
        while (ldsv(L_CTL + 4 * C_FULL) < (u32)(8 * u + 8)) { }
        FENCE();
        v8bf vf[8];
#pragma unroll
        for (int t = 0; t < 8; ++t) {
          v4s lo = __builtin_amdgcn_ds_read_tr16_b64_v4i16(lp<v4s>(vb[t])), hi = __builtin_amdgcn_ds_read_tr16_b64_v4i16(lp<v4s>(vb[t] + 4096));
          vf[t] = __builtin_bit_cast(v8bf, __builtin_shufflevector(lo, hi, 0, 1, 2, 3, 4, 5, 6, 7));
        }
        __builtin_amdgcn_sched_barrier(0);
#pragma unroll
        for (int i = 0; i < 4; ++i) { e0 = mfma(vf[2 * i], __builtin_bit_cast(v8bf, pm[i]), e0); e1 = mfma(vf[2 * i + 1], __builtin_bit_cast(v8bf, pm[i]), e1); }
        __builtin_amdgcn_sched_barrier(0);
        v4f r0, r1;
        for (;;) {
          const u32 cnt = ldsv(L_CTL + 4 * C_MCNT);
          FENCE();
          r0 = *lp<v4f>(ra); r1 = *lp<v4f>(ra + 1024);
          FENCE();
          if (cnt >= (u32)link) break;
        }
        e0 = (e0 + r0) * inv; e1 = (e1 + r1) * inv;
        if (link == NCONS - 1) {
          st4(v4u{min(pk2(e0.x, e0.y), SENT - 1), min(pk2(e0.z, e0.w), SENT - 1), min(pk2(e1.x, e1.y), SENT - 1), min(pk2(e1.z, e1.w), SENT - 1)}, rws, oa, W_PART + grp * 16384 + r * 256);
        } else {
          *lp<v4f>(ra) = e0; *lp<v4f>(ra + 1024) = e1;
          FENCE(); stsv(L_CTL + 4 * C_MCNT, link + 1);
        }
        stsv(L_CTL + 4 * (C_FREE + (u & (NSLOT - 1))), u + 1);
      }
    }
  }
  if (!(isX || isY)) return;
  const u32 trow = 4 * g + (c >> 2);
  auto waddr = [&](int t) -> u32 { return trow * 128 + ((((u32)(t ^ ((trow >> 1) & 3)) << 1) | ((c >> 1) & 1)) << 4) + (c & 1) * 8; };
  auto wslot = [&](int t) -> u32 { return ((uw + t) & (NSLOT - 1)) * SLOT; };
  const int half = lane >> 5, i32 = lane & 31;
  const u32 fa = L_ACT + (c & 1) * ASTR + g * 16;
  {
    const rsrc_t rws = __builtin_amdgcn_make_buffer_rsrc(ws, 0, W_END, 0x00020000);
    auto weights_done = [&]() { WAITV(0); if (wave == 0) stsv(L_CTL + 4 * C_FULL, 8 * (uw + ntail)); arrive(C_WIN); };
    auto weights_in = [&]() { while (ldsv(L_CTL + 4 * C_WIN) < 2) __builtin_amdgcn_s_sleep(1); FENCE(); };
    if (isX) {
      const int kvh = xi >> 3, ng = xi & 7;
      const int b = wave >> 2, h = wave & 3;
      u32 wa = L_ACT + b * ASTR + (h * 128 + c * 8 + g * 2) * 2, wb[8];
#pragma unroll
      for (int i = 0; i < 8; ++i) { wb[i] = wslot(i) + waddr(wave & 3); PIN(wb[i]); }
      u32 vo = lane * 16 + W_PART + (b * 8 + kvh) * 16384 + h * 4096;
      u32 vl = ((c & 3) * 4 + ((g + (c >> 2)) & 3)) * 128 + W_ML + (b * 8 + kvh) * 2048 + h * 8;
      u32 vs = c * 2048 + g * 16 + W_AP + kvh * 4096 + ng * 256 + (wave & 3) * 64, fb = fa;
      PIN(wa); PIN(vo); PIN(vl); PIN(vs); PIN(fb);
      while (ldsv(L_CTL + 4 * C_ISS) < 2) __builtin_amdgcn_s_sleep(1);
      FENCE();
      v2u ls = ld2(rws, vl, 0);
      if (wave < 2) weights_done();
      if (!__all(max(ls.x, ls.y) != SENT)) {
#pragma nounroll
        for (int sp = 0; sp < SPIN_CAP; ++sp) {
          __builtin_amdgcn_s_sleep(1); FENCE();
          ls = ld2(rws, vl, 0);
          if (__all(max(ls.x, ls.y) != SENT)) break;
        }
        __builtin_amdgcn_s_sleep(XW);
      }
      FENCE();
      v4u a[4];
#pragma unroll
      for (int j = 0; j < 4; ++j) a[j] = ld4(rws, vo + j * 1024, 0);
      __builtin_amdgcn_sched_barrier(0);
      v8bf W[16];
      if (wave >= 4) {
        weights_in();
#pragma unroll
        for (int ks = 0; ks < 16; ++ks) W[ks] = trfrag(wb[ks >> 1] + (ks & 1) * 4096);
      }
      float M = __uint_as_float(ls.x);
      M = fmaxf(M, dpp<0xB1>(M)); M = fmaxf(M, dpp<0x4E>(M)); M = fmaxf(M, dpp<0x141>(M)); M = fmaxf(M, dpp<0x128>(M));
      float cf = ex2((__uint_as_float(ls.x) - M) * 1.4426950408889634f) * __uint_as_float(ls.y);
      cf *= rcp(row_sum(cf));
      float cj[4] = {dpp<0x150>(cf), dpp<0x151>(cf), dpp<0x152>(cf), dpp<0x153>(cf)};
      PIN(cj[0]); PIN(cj[1]); PIN(cj[2]); PIN(cj[3]);
#pragma nounroll
      for (int sp = 0; sp < SPIN_CAP; ++sp) {
        u32 mx = 0;
#pragma unroll
        for (int j = 0; j < 4; ++j) mx = max(max(mx, max(a[j].x, a[j].y)), max(a[j].z, a[j].w));
        if (__all(mx != SENT)) break;
        FENCE();
#pragma unroll
        for (int j = 0; j < 4; ++j) a[j] = ld4(rws, vo + j * 1024, 0);
      }
      v2f o[4];
#pragma unroll
      for (int i = 0; i < 4; ++i) {
        o[i] = v2f{lo16(a[0][i]), bfhi(a[0][i])} * cj[0];
#pragma unroll
        for (int j = 1; j < 4; ++j) o[i] += v2f{lo16(a[j][i]), bfhi(a[j][i])} * cj[j];
      }
      *lp<u32>(wa) = pk2(fold16(fold32(o[0].x, o[2].x), fold32(o[1].x, o[3].x)), fold16(fold32(o[0].y, o[2].y), fold32(o[1].y, o[3].y)));
      bar();
      if (wave >= 4) {
        v8bf A[16];
#pragma unroll
        for (int ks = 0; ks < 16; ++ks) A[ks] = ldfrag(fb + ks * 64);
        __builtin_amdgcn_sched_barrier(0);
        v4f d0 = {0.f, 0.f, 0.f, 0.f};
#pragma unroll
        for (int ks = 0; ks < 16; ++ks) d0 = mfma(W[ks], A[ks], d0);
        v4u ov = {canon(d0.x), canon(d0.y), canon(d0.z), canon(d0.w)};
        if (c < 2) st4(ov, rws, vs, 0);
      }
    } else {
      const int b = wave >> 2, col = (wave & 3) * 128 + i32 * 4 + half * 2;
      u32 wb[8];
#pragma unroll
      for (int i = 0; i < 8; ++i) { wb[i] = wave >= 4 ? wslot(i) + waddr(wave & 3) : wslot(8 + 2 * wave + (i >> 2)) + waddr(i & 3); PIN(wb[i]); }
      u32 wa = L_ACT + b * ASTR + fpos(col) * 2, ra = L_RED + wave * 8 + half * 4, rb_ = L_RED + b * 32;
      u32 ha = L_H1 + (c & 1) * 128 + fpos((wave & 3) * 16 + g * 4 + ((c >> 1) & 3)) * 2, fb = wave >= 4 ? fa : L_H1 + (c & 1) * 128 + g * 16;
      u32 vo = half * 16384 + i32 * 16 + W_AP + b * 2048 + (wave & 3) * 512, vs = (c & 1) * 2048 + (c >> 1) * 64 + g * 16 + W_OP + yi * 4096 + wave * 512;
      PIN(wa); PIN(ra); PIN(rb_); PIN(ha); PIN(fb); PIN(vo); PIN(vs);
      v2f x0, ga, sb, cb1;
      {
        const u32 xv = *(const u32*)(xin + b * 512 + col);
        const u32 tag = ldsv(L_CTL + 4 * C_TAG);
        v4u mg[3];
#pragma nounroll
        for (int sp = 0; sp < SPIN_CAP; ++sp) {
#pragma unroll
          for (int i = 0; i < 3; ++i) mg[i] = ld4(rws, col * 8, W_MG + i * 4096);
          if (__all(((mg[0].y ^ tag) | (mg[0].w ^ tag) | (mg[1].y ^ tag) | (mg[1].w ^ tag) | (mg[2].y ^ tag) | (mg[2].w ^ tag)) == 0)) break;
          FENCE();
        }
        const int sh = 16 - 16 * b;
        x0 = v2f{lo16(xv), bfhi(xv)};
        ga = v2f{bfhi(mg[0].x << sh), bfhi(mg[0].z << sh)}; sb = v2f{bfhi(mg[1].x << sh), bfhi(mg[1].z << sh)};
        cb1 = rb2(v2f{bfhi(mg[2].x << sh), bfhi(mg[2].z << sh)} + 1.f);
      }
      PIN(x0); PIN(ga); PIN(sb); PIN(cb1);
      if (wave < 2) weights_done();
      weights_in();
      v4u a[4];
#pragma unroll
      for (int j = 0; j < 4; ++j) a[j] = ld4(rws, vo, j * 4096);
      __builtin_amdgcn_sched_barrier(0);
      v8bf W[16];
#pragma unroll
      for (int j = 0; j < 16; ++j) W[j] = trfrag(wb[j >> 1] + (j & 1) * 4096);
#pragma nounroll
      for (int sp = 0; sp < SPIN_CAP; ++sp) {
        u32 mx = 0;
#pragma unroll
        for (int j = 0; j < 4; ++j) mx = max(max(mx, max(a[j].x, a[j].y)), max(a[j].z, a[j].w));
        if (__all(mx != SENT)) break;
        FENCE();
#pragma unroll
        for (int j = 0; j < 4; ++j) a[j] = ld4(rws, vo, j * 4096);
      }
      const v4f at = (__builtin_bit_cast(v4f, a[0]) + __builtin_bit_cast(v4f, a[1])) + (__builtin_bit_cast(v4f, a[2]) + __builtin_bit_cast(v4f, a[3]));
      const v2f x1 = rb2(x0 + rb2(ga * rb2(v2f{fold32(at.x, at.z), fold32(at.y, at.w)})));
      const float st = half_sum(fold32(x1.x + x1.y, fmaf(x1.x, x1.x, x1.y * x1.y)));
      if (i32 == 0) *lp<float>(ra) = st;
      bar();
      {
        const v4f r0 = *lp<v4f>(rb_), r1 = *lp<v4f>(rb_ + 16);
        const float mean = ((r0.x + r0.z) + (r1.x + r1.z)) * (1.f / 512.f);
        const float var = fmaxf(fmaf(-mean, mean, ((r0.y + r0.w) + (r1.y + r1.w)) * (1.f / 512.f)), 0.f);
        const v2f mv = rb2(v2f{mean, var});
        const float ri = rcp(rb(__builtin_amdgcn_sqrtf(rb(mv.y + 1e-5f))));
        const v2f hh = rb2(rb2(rb2(x1 - mv.x) * ri) * cb1) + sb;
        *lp<u32>(wa) = pk2(hh.x, hh.y);
      }
      bar();
      if (wave >= 4) {
        v8bf A[16];
#pragma unroll
        for (int ks = 0; ks < 16; ++ks) A[ks] = ldfrag(fb + ks * 64);
        __builtin_amdgcn_sched_barrier(0);
        v4f d0 = {0.f, 0.f, 0.f, 0.f};
#pragma unroll
        for (int ks = 0; ks < 16; ++ks) d0 = mfma(W[ks], A[ks], d0);
        float e0 = (c & 2) ? d0.y : d0.x, e1 = (c & 2) ? d0.w : d0.z;
        PIN(e0); PIN(e1);
        const float v = silu(rb((c & 4) ? e1 : e0));
        *lp<u16>(ha) = (u16)pk2(v, v);
      }
      bar();
      if (wave < 4) {
        const v8bf a0 = ldfrag(fb), a1 = ldfrag(fb + 64);
        v4f dd[8];
#pragma unroll
        for (int t = 0; t < 8; ++t) dd[t] = mfma(W[2 * t + 1], a1, mfma(W[2 * t], a0, v4f{0.f, 0.f, 0.f, 0.f}));
        v4f rv = dd[0];
#pragma unroll
        for (int t = 1; t < 8; ++t) if ((c >> 1) == t) rv = dd[t];
        v4u ov = {canon(rv.x), canon(rv.y), canon(rv.z), canon(rv.w)};
        st4(ov, rws, vs, 0);
        if (wave == 1 && yi == 27 && lane == 0) st2(v2u{(u32)E, (u32)(E >> 32)}, rws, 0, W_EPOCH);
      }
    }
  }
}

extern "C" void kernel_launch(void* const* d_in, const int* in_sizes, int n_in, void* d_out, int out_size, void* d_ws, size_t ws_size, hipStream_t stream) {
  hipLaunchKernelGGL(fused_block, dim3(256), dim3(512), 0, stream, (const u16*)d_in[1], (const u16*)d_in[2], (const u16*)d_in[9], (const u16*)d_in[3],
                     (const u16*)d_in[4], (const u16*)d_in[6], (const u16*)d_in[7], (const u16*)d_in[0], (const u16*)d_in[8], (u16*)d_out, (unsigned char*)d_ws);
}
```

```cpp
#include <hip/hip_runtime.h>

typedef unsigned int u32;
typedef unsigned long long u64;
typedef unsigned short u16;
typedef u32 v4u __attribute__((ext_vector_type(4)));
typedef u32 v2u __attribute__((ext_vector_type(2)));
typedef int v4i __attribute__((ext_vector_type(4)));
typedef float v4f __attribute__((ext_vector_type(4)));
typedef float v2f __attribute__((ext_vector_type(2)));
typedef short v4s __attribute__((ext_vector_type(4)));
typedef __bf16 v8bf __attribute__((ext_vector_type(8)));
typedef __bf16 v2bf __attribute__((ext_vector_type(2)));
#define LAS __attribute__((address_space(3)))
#define GAS __attribute__((address_space(1)))
#define DEVI __device__ __forceinline__

constexpr int NSLOT = 16, SLOT = 8192;
constexpr int NCONS = 6, NSTASH = 4, NF = 2, NMAX = 72;
#define WA 16
#define WB 20
#define WC 24
#define WD 28
#define LA 25
#define LB 50
#define LC 90
#define TEND 3705
#define LEADX 130
#define LEADX3 210
#define LEADY 190
#define COFF -20
#define W2W 24
#define ZDRY 1
#define HEAD 16
#define XW 5
constexpr int QAT = 12, HDELAY = 440, CODE_BYTES = 23552;
#define BLK_A 65, 65, 65, 63, 63, 63, 63, 63, 63, 63, 64, 64, 65, 65, 65, 65
constexpr int NBLK[2][16] = {{BLK_A}, {65, 65, 65, 63, 62, 62, 62, 62, 62, 62, 67, 67, 65, 65, 65, 65}};
constexpr int BIAS = 11;
constexpr u64 start_table(int k) {
  u64 t = 0; int s = 0;
  for (int r = 0; r < 16; ++r) { const int dl = s - 64 * r + BIAS; if (dl < 0 || dl > 15 || NBLK[k][r] > NMAX) return ~0ull; t |= (u64)dl << (4 * r); s += NBLK[k][r]; }
  return s == 1024 ? t : ~0ull;
}
constexpr u64 ST_A = start_table(0), ST_B = start_table(1);
static_assert(ST_A != ~0ull && ST_B != ~0ull, "block table");
constexpr u64 pace(int k, int r, int extra, int lead) { return ((TEND - lead) * 16 + NBLK[k][r]) / (2 * NBLK[k][r] + extra) - 384; }
constexpr u64 pace_table(int k) {
  return pace(k, 0, 0, LEADX) | pace(k, 3, 0, LEADX3) << 8 | pace(k, 3, 0, LEADX) << 16 | pace(k, 4, 8, 0) << 24 | pace(k, 8, 8, 0) << 32 |
         pace(k, 10, 0, k ? 0 : LEADY) << 40 | pace(k, 12, 2, 0) << 48;
}
constexpr u64 PACE_A = pace_table(0), PACE_B = pace_table(1);
constexpr int L_CTL = NSLOT * SLOT;
constexpr int L_Q = L_CTL + 256;
constexpr int L_ACT = L_Q + 1024;
constexpr int ASTR = 1024 + 64;
constexpr int L_MRG = L_ACT + 2 * ASTR;
constexpr int L_ML = L_MRG + 2048;
constexpr int L_SC = L_ML + 256;
constexpr int L_JUNK = L_SC + NMAX * 256;
constexpr int L_END = L_JUNK + 256;
constexpr int L_H1 = L_SC;
constexpr int L_RED = L_H1 + 256;
static_assert(L_END <= 160 * 1024, "LDS");
constexpr int C_FULL = 0, C_QRDY = 1, C_MCNT = 2, C_CLR = 3, C_ARDY = 4, C_TAG = 5, C_WRD = 6, C_MX = 7, C_SM = 8, C_T0 = 9, C_ISS = 10, C_VISS = 11, C_WIN = 12, C_FREE = 16;
constexpr u32 W_EPOCH = 0, W_T0 = 4096, W_SP = 8192, W_HG = W_SP + 4 * 1024 * 16, W_QG = W_HG + 512 * 8, W_MG = W_QG + 4096 * 8, W_PART = W_MG + 2048 * 8;
constexpr u32 W_ML = W_PART + 64 * 4096;
constexpr u32 W_AP = W_ML + 256 * 128, W_OP = W_AP + 8 * 4096, W_END = W_OP + 28 * 4096;
constexpr u32 CLRB = (W_END - W_PART) / 64;
static_assert(CLRB * 64 == W_END - W_PART && CLRB % 16 == 0, "clear split");
constexpr u32 SENT = 0xFFFFFFFFu;
constexpr int SPIN_CAP = 1 << 16;

DEVI float lo16(u32 v) { return __uint_as_float(v << 16); }
DEVI float bfhi(u32 v) { return __uint_as_float(v & 0xffff0000u); }
DEVI u32 pk2(float a, float b) { v2f x = {a, b}; return __builtin_bit_cast(u32, __builtin_convertvector(x, v2bf)); }
DEVI float rb(float a) { return lo16(pk2(a, a)); }
DEVI float ex2(float x) { return __builtin_amdgcn_exp2f(x); }
DEVI float rcp(float x) { return __builtin_amdgcn_rcpf(x); }
DEVI float silu(float v) { return v * rcp(1.f + ex2(v * -1.4426950408889634f)); }
DEVI float silu_exact(float v) {
  const float t = fminf(v * -1.4426950216293335f, 126.f);
  const float r = fmaf(v, -1.92596299112662e-08f, fmaf(v, -1.4426950216293335f, -t));
  float e = ex2(t);
  e = fmaf(e, fminf(r, 1.f) * 0.6931471805599453f, e);
  const float d = 1.f + e, rc = rcp(d), q = v * rc;
  return fmaf(fmaf(-q, d, v), rc, q);
}
DEVI v2f rb2(v2f a) { const u32 p = pk2(a.x, a.y); return v2f{lo16(p), bfhi(p)}; }
DEVI float fold32(float a, float b) { auto s = __builtin_amdgcn_permlane32_swap(__float_as_uint(a), __float_as_uint(b), false, false); return __uint_as_float(s[0]) + __uint_as_float(s[1]); }
DEVI float fold16(float a, float b) { auto s = __builtin_amdgcn_permlane16_swap(__float_as_uint(a), __float_as_uint(b), false, false); return __uint_as_float(s[0]) + __uint_as_float(s[1]); }
#define PIN(x) asm volatile("" : "+v"(x))
DEVI float swap16_add(float x) { auto s = __builtin_amdgcn_permlane16_swap(__float_as_uint(x), __float_as_uint(x), false, false); return __uint_as_float(s[0]) + __uint_as_float(s[1]); }
DEVI float swap32_add(float x) { auto s = __builtin_amdgcn_permlane32_swap(__float_as_uint(x), __float_as_uint(x), false, false); return __uint_as_float(s[0]) + __uint_as_float(s[1]); }
DEVI float swap16_max(float x) { auto s = __builtin_amdgcn_permlane16_swap(__float_as_uint(x), __float_as_uint(x), false, false); return fmaxf(__uint_as_float(s[0]), __uint_as_float(s[1])); }
DEVI float swap32_max(float x) { auto s = __builtin_amdgcn_permlane32_swap(__float_as_uint(x), __float_as_uint(x), false, false); return fmaxf(__uint_as_float(s[0]), __uint_as_float(s[1])); }
template <int CTRL> DEVI float dpp(float x) { return __builtin_bit_cast(float, __builtin_amdgcn_mov_dpp(__builtin_bit_cast(int, x), CTRL, 0xf, 0xf, true)); }
DEVI float row_sum(float x) { x += dpp<0xB1>(x); x += dpp<0x4E>(x); x += dpp<0x141>(x); x += dpp<0x128>(x); return x; }
DEVI float half_sum(float x) { return swap16_add(row_sum(x)); }
DEVI float wave_sum(float x) { return swap32_add(half_sum(x)); }
template <typename T> DEVI LAS T* lp(u32 a) { return (LAS T*)(size_t)a; }
DEVI u32 ldsv(u32 a) { return *(volatile LAS u32*)(size_t)a; }
DEVI void stsv(u32 a, u32 v) { *(volatile LAS u32*)(size_t)a = v; }
#define FENCE() asm volatile("" ::: "memory")
#define WAITV(n) asm volatile("s_waitcnt vmcnt(%0)" ::"i"(n) : "memory")
#define WAITL() asm volatile("s_waitcnt lgkmcnt(0)" ::: "memory")
DEVI void bar() { WAITL(); __builtin_amdgcn_s_barrier(); FENCE(); }
typedef __amdgpu_buffer_rsrc_t rsrc_t;
DEVI v4u ld4(rsrc_t r, u32 vo, u32 so) { return __builtin_bit_cast(v4u, __builtin_amdgcn_raw_buffer_load_b128(r, vo, so, 16)); }
DEVI v2u ld2(rsrc_t r, u32 vo, u32 so) { return __builtin_bit_cast(v2u, __builtin_amdgcn_raw_buffer_load_b64(r, vo, so, 16)); }
DEVI void st4(v4u v, rsrc_t r, u32 vo, u32 so) { __builtin_amdgcn_raw_buffer_store_b128(v, r, vo, so, 16); }
DEVI void st2(v2u v, rsrc_t r, u32 vo, u32 so) { __builtin_amdgcn_raw_buffer_store_b64(v, r, vo, so, 16); }
DEVI u32 canon(float f) { u32 b = __float_as_uint(f); return b < 0xFFFFFFFEu ? b : 0xFFFFFFFEu; }
DEVI v8bf trfrag(u32 a) {
  v4s lo = __builtin_amdgcn_ds_read_tr16_b64_v4i16(lp<v4s>(a)), hi = __builtin_amdgcn_ds_read_tr16_b64_v4i16(lp<v4s>(a + 2048));
  return __builtin_bit_cast(v8bf, __builtin_shufflevector(lo, hi, 0, 1, 2, 3, 4, 5, 6, 7));
}
DEVI v4f mfma(v8bf a, v8bf b, v4f c) { return __builtin_amdgcn_mfma_f32_16x16x32_bf16(a, b, c, 0, 0, 0); }
DEVI v8bf ldfrag(u32 a) { return __builtin_bit_cast(v8bf, *lp<v4u>(a)); }
DEVI int fpos(int k) { return (k & ~31) + ((k >> 2) & 3) * 8 + ((k >> 4) & 1) * 4 + (k & 3); }
DEVI v4i mksrd(const void* p, u32 bytes) { u64 a = (u64)p; return v4i{(int)(u32)a, (int)(u32)(a >> 32), (int)bytes, 0x00020000}; }

#define DMA4(m, v0, v1, v2, v3, srd, so)                                                                                   \
  asm volatile("s_mov_b32 m0, %0\n\ts_nop 0\n\tbuffer_load_dwordx4 %1, %5, %6 offen nt lds\n\t"                           \
               "buffer_load_dwordx4 %2, %5, %6 offen offset:1024 nt lds\n\tbuffer_load_dwordx4 %3, %5, %6 offen offset:2048 nt lds\n\t" \
               "buffer_load_dwordx4 %4, %5, %6 offen offset:3072 nt lds" ::"s"(m), "v"(v0), "v"(v1), "v"(v2), "v"(v3), "s"(srd), "s"(so) : "memory")

__global__ void __launch_bounds__(512, 2)
fused_block(const u16* __restrict__ Kc, const u16* __restrict__ Vc, const u16* __restrict__ wmod, const u16* __restrict__ wq,
            const u16* __restrict__ wo, const u16* __restrict__ w1, const u16* __restrict__ w2, const u16* __restrict__ xin,
            const u16* __restrict__ cin, u16* __restrict__ out, unsigned char* ws) {
  __shared__ __attribute__((aligned(16))) unsigned char smem[L_END];
  asm volatile("" ::"s"((u32)(size_t)(LAS unsigned char*)smem));
  const int tid = threadIdx.x, lane = tid & 63, wave = __builtin_amdgcn_readfirstlane(tid >> 6);
  if (wave == 1 && blockIdx.x < 32) {
    u64 pc; asm volatile("s_getpc_b64 %0" : "=s"(pc));
    const v4i rc = mksrd((const void*)(pc & ~127ull), CODE_BYTES);
    const u32 vo = lane * 128, m = L_JUNK, s1 = 8192, s2 = 16384;
    asm volatile("s_mov_b32 m0, %0\n\ts_nop 0\n\tbuffer_load_dword %1, %2, 0 offen lds\n\tbuffer_load_dword %1, %2, %3 offen lds\n\tbuffer_load_dword %1, %2, %4 offen lds"
                 ::"s"(m), "v"(vo), "s"(rc), "s"(s1), "s"(s2) : "memory");
  }
  const int bid = (blockIdx.x & ~31) | ((blockIdx.x & 7) << 2) | ((blockIdx.x >> 3) & 3);
  const int grp = bid >> 4, r = bid & 15;
  const int g = lane >> 4, c = lane & 15;
  const u64 stt = (grp < 14 ? ST_A : ST_B) >> (4 * r);
  const int kv0 = 64 * r + ((int)stt & 15) - BIAS;
  const bool isS = r >= 12;
  if (__builtin_expect(wave == 0, 1)) {
    const v4i srd = isS ? mksrd(wmod, 512 * 6144) : mksrd(Kc, 1u << 27);
    const u32 k0 = g * 256 + ((c ^ g) << 4), w0 = (lane >> 3) * 6144 + (((lane & 7) ^ ((lane >> 3) & 6)) << 4);
    const u32 a0 = isS ? w0 : k0, a1 = isS ? w0 + 48128 : k0 ^ 64, a2 = isS ? w0 + 2 * 48128 : k0 ^ 128, a3 = isS ? w0 + 3 * 48128 : k0 ^ 192;
    const u32 so = isS ? (r - 12) * 128 * 6144 + grp * 128 : (u32)(grp * 1024 + kv0) << 13, hs = isS ? 32 * 6144 : 4096;
#pragma unroll
    for (int i = 0; i < 4; ++i) { const u32 m = i * 4096, s1 = so + i * hs; DMA4(m, a0, a1, a2, a3, srd, s1); }
  }
  if (wave == 2) { stsv(L_CTL + 4 * lane, 0); WAITL(); }
  __builtin_amdgcn_s_barrier(); FENCE();
  const u64 tstart = __builtin_amdgcn_s_memrealtime();
  FENCE();
  const bool isX = r < 4, isQ = r >= 4 && r < 8, isM = r == 8 || r == 9, isY = (r == 10 || r == 11) && grp < 14, isR = r == 0 && (grp & 1);
  const int xi = grp * 4 + r, qi = grp * 4 + r - 4, mi = grp * 2 + r - 8, yi = grp * 2 + r - 10;
  const int u0 = (int)(0x2222008888880000ull >> (4 * r)) & 15;
  const int nb = 64 + (r == 15 ? BIAS : (int)(stt >> 4) & 15) - ((int)stt & 15);
  const int nf = isS ? 0 : NF;
  const int ntail = isX ? 8 : isY ? 16 : 0;
  const int uv = u0 + nb, uw = uv + nb;
  u64 E = 0;
  auto arrive = [&](int w) { if (lane == 0) __hip_atomic_fetch_add(lp<u32>(L_CTL + 4 * w), 1u, __ATOMIC_RELAXED, __HIP_MEMORY_SCOPE_WORKGROUP); };
  int wv = wave;
  asm volatile("" : "+s"(wv));
  if (__builtin_expect(wv == 0, 1)) {
    __builtin_amdgcn_s_setprio(3);
    int iss, pub = 0;
    auto publish = [&](int v) { pub = max(pub, v); stsv(L_CTL + 4 * C_FULL, pub); };
#define PACE(W_) do { WAITV(W_); publish(iss - (W_)); } while (0)
    const u32 cls = ((u32)(0x6666554433331000ull >> (4 * r)) & 7) + (r == 3 && (grp & 1));
    const u32 tau = 384 + ((u32)((grp < 14 ? PACE_A : PACE_B) >> (8 * cls)) & 255);
    u32 due = ((u32)tstart + COFF) * 16 + NF * tau;
    const u32 wrow = lane >> 3, wlc = ((lane & 7) ^ (((wrow >> 1) & 3) << 1)) << 4;
    int u = NF;
#pragma nounroll
    for (int seg = u0 == 8 ? 0 : 1; seg < (ntail ? 4 : 3); ++seg) {
      const bool kv = seg == 1 || seg == 2;
      const u16* base = seg == 0 ? (isQ ? wq : wmod) : seg == 1 ? Kc : seg == 2 ? Vc : isX ? wo : w1;
      const u32 rs = seg == 0 ? (isQ ? 8192 : 6144) : (seg == 3 && !isX) ? 3584 : 1024;
      const v4i srd = mksrd(base, kv ? 1u << 27 : (seg == 3 && isX) ? 4096 * 1024 : 512 * rs);
      u32 so = kv ? (u32)(grp * 1024 + kv0 + (seg == 1 ? nf : 0)) << 13
                  : seg == 0 ? (isQ ? qi : 16 + mi) * 128
                             : isX ? ((xi >> 3) << 19) + (xi & 7) * 128 : yi * 128;
      const u32 hstep = kv ? 4096 : 32 * rs, ustep = 2 * hstep;
      const u32 sw = seg == 2 ? 2 * g : g, sx = seg == 1 ? 4 : 8;
      const u32 w0 = wrow * rs + wlc, wd = 8 * rs - 1024;
      const u32 a0 = kv ? g * 256 + ((c ^ sw) << 4) : w0, a1 = kv ? g * 256 + ((c ^ sw ^ sx) << 4) : w0 + wd;
      const u32 a2 = kv ? g * 256 + ((c ^ sw ^ ((2 * sx) & 15)) << 4) : w0 + 2 * wd, a3 = kv ? g * 256 + ((c ^ sw ^ ((3 * sx) & 15)) << 4) : w0 + 3 * wd;
      const int ue = seg == 0 ? NF + u0 : seg == 1 ? uv : seg == 2 ? uw : uw + (ntail >> 1);
#pragma nounroll
      while (u < ue) {
        const bool paced = u >= 4 && seg < 3;
        const int stop = min(ue, u < 4 ? 4 : u < 48 ? 48 : 1 << 20);
        u32 dd = paced ? due : due - (1u << 28);
        const int u_in = u, la = (seg == 3 && isX) ? 0x7fffffff : LA * 16;
        u32 t0, t1, lag; u32 vf, vt;
#define DMAS(so) "buffer_load_dwordx4 %[a0], %[srd], " so " offen nt lds\n\tbuffer_load_dwordx4 %[a1], %[srd], " so " offen offset:1024 nt lds\n\t" \
                 "buffer_load_dwordx4 %[a2], %[srd], " so " offen offset:2048 nt lds\n\tbuffer_load_dwordx4 %[a3], %[srd], " so " offen offset:3072 nt lds\n\t"
#define RDFREE "s_lshl_b32 %[t1], %[u], 2\n\ts_and_b32 %[t1], %[t1], 60\n\ts_or_b32 %[t1], %[t1], %[fb]\n\tv_mov_b32 %[vf], %[t1]\n\tds_read_b32 %[vf], %[vf]\n\t"
#define REST(W) "s_waitcnt vmcnt(" #W ")\n\ts_mov_b32 m0, %[t0]\n\ts_lshl_b32 %[t0], %[u], 3\n\t" DMAS("%[t1]")                                   \
                "s_sub_i32 %[t0], %[t0], " #W " - 4\n\ts_max_i32 %[pub], %[pub], %[t0]\n\tv_mov_b32 %[vt], %[pub]\n\tds_write_b32 %[va], %[vt]\n\t"                                      \
                "s_add_i32 %[u], %[u], 1\n\ts_add_u32 %[so], %[so], %[us]\n\ts_add_u32 %[dd], %[dd], %[tau]\n\t" RDFREE                             \
                "s_memrealtime s[98:99]\n\ts_cmp_lt_i32 %[u], %[stop]\n\ts_waitcnt vmcnt(" #W ")\n\ts_cbranch_scc1 1b\n\ts_branch 9f\n"
#define XREST(W) REST(W)
        asm volatile(RDFREE "s_memrealtime s[98:99]\n"
                     "1:\n\ts_sub_i32 %[t1], %[u], 15\n\ts_waitcnt lgkmcnt(0)\n\tv_cmp_gt_i32 vcc, %[t1], %[vf]\n\ts_lshl_b32 s98, s98, 4\n\t"
                     "s_cbranch_vccnz 8f\n"
                     "2:\n\ts_sub_i32 %[lag], s98, %[dd]\n\ts_cmp_lt_i32 %[lag], 0\n\ts_cbranch_scc1 7f\n\t"
                     "s_lshl_b32 %[t0], %[u], 13\n\ts_and_b32 %[t0], %[t0], 0x1e000\n\ts_mov_b32 m0, %[t0]\n\ts_add_u32 %[t1], %[so], %[hs]\n\t" DMAS("%[so]")
                     "s_or_b32 %[t0], %[t0], 0x1000\n\ts_cmp_lt_i32 %[lag], %[la]\n\ts_cbranch_scc1 4f\n\ts_cmp_lt_i32 %[lag], %[lb]\n\ts_cbranch_scc1 5f\n\t"
                     "s_cmp_lt_i32 %[lag], %[lc]\n\ts_cbranch_scc1 3f\n\t"
                     XREST(WD) "3:\n\t" XREST(WC) "4:\n\t" XREST(WA) "5:\n\t" XREST(WB)
                     "8:\n\t" RDFREE "s_sub_i32 %[t1], %[u], 15\n\ts_waitcnt lgkmcnt(0)\n\tv_cmp_gt_i32 vcc, %[t1], %[vf]\n\ts_nop 0\n\ts_cbranch_vccz 7f\n\t"
                     "s_waitcnt vmcnt(0)\n\ts_lshl_b32 %[pub], %[u], 3\n\tv_mov_b32 %[vt], %[pub]\n\tds_write_b32 %[va], %[vt]\n"
                     "6:\n\t" RDFREE "s_sub_i32 %[t1], %[u], 15\n\ts_waitcnt lgkmcnt(0)\n\tv_cmp_gt_i32 vcc, %[t1], %[vf]\n\ts_sleep 1\n\ts_cbranch_vccnz 6b\n"
                     "7:\n\ts_memrealtime s[98:99]\n\ts_waitcnt lgkmcnt(0)\n\ts_lshl_b32 s98, s98, 4\n\ts_branch 2b\n"
                     "9:\n\ts_waitcnt lgkmcnt(0)"
                     : [u] "+s"(u), [so] "+s"(so), [dd] "+s"(dd), [pub] "+s"(pub), [t0] "=&s"(t0), [t1] "=&s"(t1), [lag] "=&s"(lag), [vf] "=&v"(vf), [vt] "=&v"(vt)
                     : [a0] "v"(a0), [a1] "v"(a1), [a2] "v"(a2), [a3] "v"(a3), [srd] "s"(srd), [hs] "s"(hstep), [us] "s"(ustep), [stop] "s"(stop),
                       [va] "v"(L_CTL + 4 * C_FULL), [fb] "i"(L_CTL + 4 * C_FREE), [tau] "s"(tau), [la] "s"(la), [lb] "i"(LB * 16), [lc] "i"(LC * 16)
                     : "memory", "vcc", "scc", "s98", "s99");
        due += (u - u_in) * tau;
        if (u == 4 && isS) { WAITV(16); publish(16); }
        if (u == uw) stsv(L_CTL + 4 * C_VISS, 1);
        if (u == 48) { const u32 t = __builtin_amdgcn_readfirstlane(ldsv(L_CTL + 4 * C_T0)); if (t) due = t * 16 + u * tau; }
      }
    }
    iss = 8 * u;
    if (!ntail) { PACE(24); PACE(16); PACE(8); PACE(0); }
    else arrive(C_ISS);
    __builtin_amdgcn_s_setprio(0);
  } else {
    const rsrc_t rws = __builtin_amdgcn_make_buffer_rsrc(ws, 0, W_END, 0x00020000);
    auto wait_full = [&](u32 need) { while (ldsv(L_CTL + 4 * C_FULL) < need) __builtin_amdgcn_s_sleep(1); FENCE(); };
    const u32 trow = 4 * g + (c >> 2);
    auto waddr = [&](int t) -> u32 { return trow * 128 + ((((u32)(t ^ ((trow >> 1) & 3)) << 1) | ((c >> 1) & 1)) << 4) + (c & 1) * 8; };

    if (wave == 1) {
      const u64 E0 = __hip_atomic_load((GAS u64*)(ws + W_EPOCH), __ATOMIC_RELAXED, __HIP_MEMORY_SCOPE_AGENT);
      u32 tag;
#define CHAIN_START() do { E = E0 + 1; tag = (u32)E; } while (0)
      auto silu4 = [&](u32 x, u32 y) { return v2u{pk2(silu_exact(lo16(x)), silu_exact(bfhi(x))), pk2(silu_exact(lo16(y)), silu_exact(bfhi(y)))}; };
      if (isS) {
        const v2u cv = *(const v2u*)(cin + (lane >> 5) * 512 + (r - 12) * 128 + (lane & 31) * 4);
        CHAIN_START();
        *lp<v2u>(L_ACT + (lane >> 5) * ASTR + fpos((lane & 31) * 4) * 2) = silu4(cv.x, cv.y);
        FENCE();
        v8bf act[4];
#pragma unroll
        for (int ks = 0; ks < 4; ++ks) { act[ks] = ldfrag(L_ACT + (c & 1) * ASTR + g * 16 + ks * 64); PIN(act[ks]); }
        wait_full(16);
        v8bf W[16];
#pragma unroll
        for (int j = 0; j < 16; ++j) W[j] = trfrag(waddr(j & 3) + (j >> 2) * 4096);
        FENCE(); stsv(L_CTL + 4 * C_FREE, 1); stsv(L_CTL + 4 * (C_FREE + 1), 2);
        v4f acc[4];
#pragma unroll
        for (int t = 0; t < 4; ++t) acc[t] = v4f{0.f, 0.f, 0.f, 0.f};
#pragma unroll
        for (int j = 0; j < 16; ++j) acc[j & 3] = mfma(act[j >> 2], W[j], acc[j & 3]);
        const float r0 = g == 0 ? acc[0][0] : g == 1 ? acc[1][0] : g == 2 ? acc[2][0] : acc[3][0];
        const float r1 = g == 0 ? acc[0][1] : g == 1 ? acc[1][1] : g == 2 ? acc[2][1] : acc[3][1];
        while (ldsv(L_CTL + 4 * C_CLR) == 0) { }
        FENCE();
        st4(v4u{__float_as_uint(r0), tag, __float_as_uint(r1), tag}, rws, lane * 16, W_SP + ((r - 12) * 1024 + grp * 64) * 16);
      } else if (isR) {
        const v4u in0 = *(const v4u*)(xin + lane * 8), in1 = *(const v4u*)(xin + 512 + lane * 8);
        u32 xs0 = *(const u32*)(xin + (grp >> 1) * 64 + (lane & ~1)), xs1 = *(const u32*)(xin + 512 + (grp >> 1) * 64 + (lane & ~1));
        CHAIN_START();
        PIN(xs0); PIN(xs1);
        xs0 >>= 16 * (lane & 1); xs1 >>= 16 * (lane & 1);
        float mr[2], ri[2];
#pragma unroll
        for (int b = 0; b < 2; ++b) {
          const v4u xv = b ? in1 : in0;
          const float x[8] = {lo16(xv.x), bfhi(xv.x), lo16(xv.y), bfhi(xv.y), lo16(xv.z), bfhi(xv.z), lo16(xv.w), bfhi(xv.w)};
          float s = 0.f;
#pragma unroll
          for (int i = 0; i < 8; ++i) s += x[i];
          const float mean = wave_sum(s) * (1.f / 512.f);
          float q2 = 0.f;
#pragma unroll
          for (int i = 0; i < 8; ++i) q2 += (x[i] - mean) * (x[i] - mean);
          const float var = wave_sum(q2) * (1.f / 512.f);
          mr[b] = rb(mean); ri[b] = rcp(rb(__builtin_amdgcn_sqrtf(rb(rb(var) + 1e-5f))));
        }
        v2f y = rb2(rb2(v2f{lo16(xs0) - mr[0], lo16(xs1) - mr[1]}) * v2f{ri[0], ri[1]});
        PIN(y);
        v4u a[8];
#pragma nounroll
        for (int sp = 0; sp < SPIN_CAP; ++sp) {
          u32 bad = 0;
#pragma unroll
          for (int i = 0; i < 8; ++i) { a[i] = ld4(rws, lane * 16, W_SP + ((i & 3) * 1024 + (i >> 2) * 512 + (grp >> 1) * 64) * 16); bad |= (a[i].y ^ tag) | (a[i].w ^ tag); }
          if (__all(bad == 0)) break;
          FENCE();
        }
        v2f sc[2];
#pragma unroll
        for (int pt = 0; pt < 2; ++pt) {
          sc[pt] = v2f{0.f, 0.f};
#pragma unroll
          for (int k4 = 0; k4 < 4; ++k4) sc[pt] += v2f{__uint_as_float(a[pt * 4 + k4].x), __uint_as_float(a[pt * 4 + k4].z)};
        }
        const v2f h = rb2(y * rb2(rb2(sc[1]) + 1.f)) + rb2(sc[0]);
        st2(v2u{pk2(h.x, h.y), tag}, rws, lane * 8, W_HG + (grp >> 1) * 512);
      } else if (isM || isQ) {
        if (isM) {
          const v4u in0 = *(const v4u*)(cin + lane * 8), in1 = *(const v4u*)(cin + 512 + lane * 8);
          CHAIN_START();
          *lp<v2u>(L_ACT + fpos(lane * 8) * 2) = silu4(in0.x, in0.y); *lp<v2u>(L_ACT + fpos(lane * 8 + 4) * 2) = silu4(in0.z, in0.w);
          *lp<v2u>(L_ACT + ASTR + fpos(lane * 8) * 2) = silu4(in1.x, in1.y); *lp<v2u>(L_ACT + ASTR + fpos(lane * 8 + 4) * 2) = silu4(in1.z, in1.w);
        } else {
          CHAIN_START();
          while (__builtin_amdgcn_s_memrealtime() < tstart + HDELAY) __builtin_amdgcn_s_sleep(4);
          v4u a[4];
#pragma nounroll
          for (int sp = 0; sp < SPIN_CAP; ++sp) {
            u32 bad = 0;
#pragma unroll
            for (int i = 0; i < 4; ++i) { a[i] = ld4(rws, lane * 16 + i * 1024, W_HG); bad |= (a[i].y ^ tag) | (a[i].w ^ tag); }
            if (__all(bad == 0)) break;
            FENCE();
          }
#pragma unroll
          for (int i = 0; i < 4; ++i) {
            const u32 wa = L_ACT + fpos(i * 128 + lane * 2) * 2;
            *lp<u32>(wa) = (a[i].x & 0xffffu) | (a[i].z << 16); *lp<u32>(wa + ASTR) = (a[i].x >> 16) | (a[i].z & 0xffff0000u);
          }
        }
        FENCE(); stsv(L_CTL + 4 * C_TAG, tag); stsv(L_CTL + 4 * C_ARDY, 1);
      } else CHAIN_START();
      if (blockIdx.x < 8 && lane == 0) st2(v2u{(u32)tstart, tag}, rws, blockIdx.x * 8, W_T0);
      {
        wait_full(8 * u0 + 16 * QAT);
        v4u a[2];
#pragma nounroll
        for (int sp = 0; sp < SPIN_CAP; ++sp) {
          a[0] = ld4(rws, lane * 16, W_QG + grp * 2048); a[1] = ld4(rws, lane * 16 + 1024, W_QG + grp * 2048);
          if (__all(((a[0].y ^ tag) | (a[0].w ^ tag) | (a[1].y ^ tag) | (a[1].w ^ tag)) == 0)) break;
          __builtin_amdgcn_s_sleep(16); FENCE();
        }
        *lp<v2u>(L_Q + lane * 8) = v2u{a[0].x, a[0].z}; *lp<v2u>(L_Q + 512 + lane * 8) = v2u{a[1].x, a[1].z};
        FENCE(); stsv(L_CTL + 4 * C_TAG, tag); stsv(L_CTL + 4 * C_QRDY, 1);
        {
          const v2u t = ld2(rws, (lane & 7) * 8, W_T0);
          if (__all(t.y == tag)) {
            int dt = (int)(t.x - (u32)tstart);
            dt += __builtin_amdgcn_mov_dpp(dt, 0xB1, 0xf, 0xf, true); dt += __builtin_amdgcn_mov_dpp(dt, 0x4E, 0xf, 0xf, true); dt += __builtin_amdgcn_mov_dpp(dt, 0x141, 0xf, 0xf, true);
            if (dt > -2400 && dt < 2400) stsv(L_CTL + 4 * C_T0, ((u32)tstart + (dt >> 3) + COFF) | 1);
          }
        }
      }
      if (r == 8) {
#pragma nounroll
        for (int pass = ZDRY ? 0 : 1; pass < 2; ++pass) {
        const int cap = pass ? SPIN_CAP : 1;
        const rsrc_t rws = __builtin_amdgcn_make_buffer_rsrc(ws, 0, pass ? W_END : 0, 0x00020000);
        const rsrc_t rout = __builtin_amdgcn_make_buffer_rsrc(out, 0, pass ? 2048 : 0, 0x00020000);
        if (pass) wait_full(8 * uw);
        const int zb = grp >> 3, zc = (grp & 7) * 64 + c * 4 + g;
        const u32 xv = __builtin_amdgcn_raw_buffer_load_b16(__builtin_amdgcn_make_buffer_rsrc((void*)xin, 0, pass ? 2048 : 0, 0x00020000), (zb * 512 + zc) * 2, 0, 0);
        v2u m0, m1;
#pragma nounroll
        for (int sp = 0; sp < cap; ++sp) {
          m0 = ld2(rws, zc * 8, W_MG); m1 = ld2(rws, zc * 8, W_MG + 3 * 4096);
          if (__all(((m0.y ^ tag) | (m1.y ^ tag)) == 0)) break;
          FENCE();
        }
        v4u z[7];
#pragma nounroll
        for (int sp = 0; sp < cap; ++sp) {
          z[0] = ld4(rws, g * 4096 + c * 16, W_AP + grp * 256); z[1] = ld4(rws, g * 4096 + c * 16, W_AP + 16384 + grp * 256);
          if (__all(max(max(max(z[0].x, z[0].y), max(z[0].z, z[0].w)), max(max(z[1].x, z[1].y), max(z[1].z, z[1].w))) != SENT)) break;
          __builtin_amdgcn_s_sleep(2); FENCE();
        }
        v4f s0 = __builtin_bit_cast(v4f, z[0]), s1 = __builtin_bit_cast(v4f, z[1]);
        const float at = fold32(fold16(s0.x, s0.y), fold16(s0.z, s0.w)) + fold32(fold16(s1.x, s1.y), fold16(s1.z, s1.w));
        float x1 = rb(lo16(xv) + rb(bfhi(m0.x << (16 - 16 * zb)) * rb(at))), gb = bfhi(m1.x << (16 - 16 * zb));
        u32 op = (zb * 512 + zc) * 2;
        PIN(x1); PIN(gb); PIN(op);
#pragma nounroll
        for (int sp = 0; sp < cap; ++sp) {
#pragma unroll
          for (int j = 0; j < 7; ++j) z[j] = ld4(rws, g * 4096 + c * 16, W_OP + j * 16384 + grp * 256);
          u32 mx = 0;
#pragma unroll
          for (int j = 0; j < 7; ++j) mx = max(mx, max(max(z[j].x, z[j].y), max(z[j].z, z[j].w)));
          if (__all(mx != SENT)) break;
          FENCE();
        }
        const v4f s = ((__builtin_bit_cast(v4f, z[0]) + __builtin_bit_cast(v4f, z[1])) + (__builtin_bit_cast(v4f, z[2]) + __builtin_bit_cast(v4f, z[3]))) +
                      ((__builtin_bit_cast(v4f, z[4]) + __builtin_bit_cast(v4f, z[5])) + __builtin_bit_cast(v4f, z[6]));
        const float rr = x1 + rb(gb * rb(fold16(fold32(s.x, s.z), fold32(s.y, s.w))));
        __builtin_amdgcn_raw_buffer_store_b16((u16)pk2(rr, rr), rout, op, 0, 0);
        }
      }
    } else {
      if (isS && wave == 7) {
        const v4u s4 = {SENT, SENT, SENT, SENT};
        const u32 so = W_PART + (grp * 4 + r - 12) * CLRB;
#pragma unroll
        for (int i = 0; i < CLRB / 1024; ++i) st4(s4, rws, lane * 16 + (i & 3) * 1024, so + (i >> 2) * 4096);
        if (lane < (CLRB % 1024) / 16) st4(s4, rws, lane * 16, so + CLRB / 1024 * 1024);
        WAITV(0); stsv(L_CTL + 4 * C_CLR, 1);
      }

      if (wave == 6) { *lp<v4f>(L_MRG + lane * 16) = v4f{0.f, 0.f, 0.f, 0.f}; *lp<v4f>(L_MRG + 1024 + lane * 16) = v4f{0.f, 0.f, 0.f, 0.f}; }
      const int d = c >> 2, cw = wave - 2;
      const u32 sca = L_SC + (g * 4 + (c & 3)) * 16;
      {
        u32 ka[4];
#pragma unroll
        for (int s = 0; s < 4; ++s) ka[s] = c * 256 + (((4 * s + g) ^ c) << 4);
        v8bf qf[4];
        float mx = -INFINITY;
        auto rdk = [&](v8bf (&kf)[8], int j) {
          const int u = j < nf ? j : u0 + j;
          const u32 lb = (u & (NSLOT - 1)) * SLOT;
          wait_full(8 * u + 8);
#pragma unroll
          for (int s = 0; s < 4; ++s) { kf[s] = ldfrag(lb + ka[s]); kf[4 + s] = ldfrag(lb + ka[s] + 4096); }
          FENCE(); stsv(L_CTL + 4 * (C_FREE + (u & (NSLOT - 1))), u + 1);
        };
        auto qk = [&](v8bf (&kf)[8], int j) {
          v4f S0 = {0.f, 0.f, 0.f, 0.f}, S1 = {0.f, 0.f, 0.f, 0.f};
#pragma unroll
          for (int s = 0; s < 4; ++s) { S0 = mfma(kf[s], qf[s], S0); S1 = mfma(kf[4 + s], qf[s], S1); }
          const v2f a = rb2(v2f{S0.x, S0.y}) * 0.08838834764831845f, b = rb2(v2f{S0.z, S0.w}) * 0.08838834764831845f;
          const v2f e = rb2(v2f{S1.x, S1.y}) * 0.08838834764831845f, f = rb2(v2f{S1.z, S1.w}) * 0.08838834764831845f;
          mx = fmaxf(fmaxf(fmaxf(mx, fmaxf(a.x, a.y)), fmaxf(b.x, b.y)), fmaxf(fmaxf(e.x, e.y), fmaxf(f.x, f.y)));
          if (c < 4) *lp<v4u>(sca + j * 256) = v4u{pk2(a.x, a.y), pk2(b.x, b.y), pk2(e.x, e.y), pk2(f.x, f.y)};
        };
        v8bf sk[NSTASH][8];
        rdk(sk[0], cw);
        if (u0 == 8 && wave < 6) {
          wait_full(8 * NF + 64);
          v8bf W[16];
#pragma unroll
          for (int ks = 0; ks < 16; ++ks) W[ks] = trfrag(NF * SLOT + ks * 4096 + waddr(wave - 2));
          WAITL();
          u32 old = 0;
          if (lane == 0) old = __hip_atomic_fetch_add(lp<u32>(L_CTL + 4 * C_WRD), 1u, __ATOMIC_RELAXED, __HIP_MEMORY_SCOPE_WORKGROUP);
          if (__builtin_amdgcn_readfirstlane(old) == 3 && lane < 8) stsv(L_CTL + 4 * (C_FREE + NF + lane), NF + lane + 1);
          while (ldsv(L_CTL + 4 * C_ARDY) == 0) __builtin_amdgcn_s_sleep(1);
          FENCE();
          const u32 tag = ldsv(L_CTL + 4 * C_TAG);
          v8bf A[16];
#pragma unroll
          for (int ks = 0; ks < 16; ++ks) A[ks] = ldfrag(L_ACT + (c & 1) * ASTR + g * 16 + ks * 64);
          __builtin_amdgcn_sched_barrier(0);
          v4f d0 = {0.f, 0.f, 0.f, 0.f}, d1 = {0.f, 0.f, 0.f, 0.f};
#pragma unroll
          for (int ks = 0; ks < 16; ks += 2) { d0 = mfma(A[ks], W[ks], d0); d1 = mfma(A[ks + 1], W[ks + 1], d1); }
          const float r0 = d0[0] + d1[0], r1 = d0[1] + d1[1];
          const float n0 = dpp<0xB1>(r0), n1 = dpp<0xB1>(r1);
          const bool odd = lane & 1;
          if (lane < 16) {
            if (isM) st2(v2u{pk2(r0, r1), tag}, rws, lane * 8, W_MG + mi * 512 + (wave - 2) * 128);
            else st2(v2u{pk2(odd ? n1 : r0, odd ? r1 : n0), tag}, rws, (lane & 1) * 16384 + (lane >> 1) * 8, W_QG + qi * 256 + (wave - 2) * 64);
          }
        }
#pragma unroll
        for (int i = 1; i < NSTASH; ++i) rdk(sk[i], cw + i * NCONS);
        while (ldsv(L_CTL + 4 * C_QRDY) == 0) __builtin_amdgcn_s_sleep(2);
        FENCE();
#pragma unroll
        for (int s = 0; s < 4; ++s) qf[s] = ldfrag(L_Q + (c & 3) * 256 + (4 * s + g) * 16);
#pragma unroll
        for (int i = 0; i < NSTASH; ++i) qk(sk[i], cw + i * NCONS);
#pragma nounroll
        for (int j = cw + NSTASH * NCONS; j < nb; j += NCONS) { v8bf kf[8]; rdk(kf, j); qk(kf, j); }
        mx = swap32_max(swap16_max(rb(mx)));
        if (lane < 4) *lp<float>(L_ML + cw * 16 + lane * 4) = mx;
        FENCE();
        arrive(C_MX);
      }
      float M;
      {
        while (ldsv(L_CTL + 4 * C_MX) < NCONS) __builtin_amdgcn_s_sleep(1);
        FENCE();
        M = *lp<float>(L_ML + (c & 3) * 4);
#pragma unroll
        for (int w = 1; w < NCONS; ++w) M = fmaxf(M, *lp<float>(L_ML + w * 16 + (c & 3) * 4));
        float l = 0.f;
#pragma nounroll
        for (int j = cw + d * NCONS; j < nb; j += 4 * NCONS) {
          const v4u s = *lp<v4u>(sca + j * 256);
          float p[8];
#pragma unroll
          for (int i = 0; i < 4; ++i) { p[2 * i] = ex2((lo16(s[i]) - M) * 1.4426950408889634f); p[2 * i + 1] = ex2((bfhi(s[i]) - M) * 1.4426950408889634f); }
          l += ((p[0] + p[1]) + (p[2] + p[3])) + ((p[4] + p[5]) + (p[6] + p[7]));
          *lp<v4u>(sca + j * 256) = v4u{pk2(p[0], p[1]), pk2(p[2], p[3]), pk2(p[4], p[5]), pk2(p[6], p[7])};
        }
        l += dpp<0x124>(l); l += dpp<0x128>(l);
        l = swap32_add(swap16_add(l));
        if (lane < 4) *lp<float>(L_ML + 128 + cw * 16 + lane * 4) = l;
        FENCE();
        arrive(C_SM);
      }
      {
        u32 va[8];
#pragma unroll
        for (int t = 0; t < 8; ++t) va[t] = trow * 256 + ((((u32)(t ^ (trow & 7)) << 1) | ((c >> 1) & 1)) << 4) + (c & 1) * 8;
        v4f acc[8];
#pragma unroll
        for (int t = 0; t < 8; ++t) acc[t] = v4f{0.f, 0.f, 0.f, 0.f};
        int j = cw;
#pragma nounroll
        for (; j + NCONS < nb; j += NCONS) {
          const int u = uv + j;
          const u32 lb = (u & (NSLOT - 1)) * SLOT;
          wait_full(8 * u + 8);
          const v8bf pf = ldfrag(sca + j * 256);
          v8bf vf[8];
#pragma unroll
          for (int t = 0; t < 8; ++t) {
            v4s lo = __builtin_amdgcn_ds_read_tr16_b64_v4i16(lp<v4s>(lb + va[t])), hi = __builtin_amdgcn_ds_read_tr16_b64_v4i16(lp<v4s>(lb + va[t] + 4096));
            vf[t] = __builtin_bit_cast(v8bf, __builtin_shufflevector(lo, hi, 0, 1, 2, 3, 4, 5, 6, 7));
          }
          FENCE(); stsv(L_CTL + 4 * (C_FREE + (u & (NSLOT - 1))), u + 1);
#pragma unroll
          for (int t = 0; t < 8; ++t) acc[t] = mfma(vf[t], pf, acc[t]);
        }
        const int link = j - (nb - NCONS), u = uv + j;
        const u32 lb = (u & (NSLOT - 1)) * SLOT;
        v4f e0 = d == 0 ? acc[0] : d == 1 ? acc[2] : d == 2 ? acc[4] : acc[6];
        v4f e1 = d == 0 ? acc[1] : d == 1 ? acc[3] : d == 2 ? acc[5] : acc[7];
        const v4u pf = *lp<v4u>(sca + j * 256);
        v4u pm[4];
#pragma unroll
        for (int i = 0; i < 4; ++i) { pm[i] = d == i ? pf : v4u{0u, 0u, 0u, 0u}; PIN(pm[i]); }
        float inv = 1.f, L = 0.f;
        if (link == NCONS - 1) {
          while (ldsv(L_CTL + 4 * C_SM) < NCONS) { }
          FENCE();
          L = *lp<float>(L_ML + 128 + (c & 3) * 4);
#pragma unroll
          for (int w = 1; w < NCONS; ++w) L += *lp<float>(L_ML + 128 + w * 16 + (c & 3) * 4);
          inv = rcp(L);
        }
        v2u ml = {canon(M), canon(L)};
        PIN(ml);
        u32 oa = (c & 3) * 4096 + d * 64 + g * 16, ra = L_MRG + lane * 16;
        u32 vb[8];
#pragma unroll
        for (int t = 0; t < 8; ++t) { vb[t] = lb + va[t]; PIN(vb[t]); }
        PIN(e0); PIN(e1); PIN(oa); PIN(ra); PIN(inv);
        if (link == NCONS - 1) {
          while (ldsv(L_CTL + 4 * C_FULL) < (u32)(8 * u + 8 - HEAD)) { }
          if (lane < 4) st2(ml, rws, lane * 8, W_ML + bid * 128);
        }
        while (ldsv(L_CTL + 4 * C_FULL) < (u32)(8 * u + 8)) { }
        FENCE();
        v8bf vf[8];
#pragma unroll
        for (int t = 0; t < 8; ++t) {
          v4s lo = __builtin_amdgcn_ds_read_tr16_b64_v4i16(lp<v4s>(vb[t])), hi = __builtin_amdgcn_ds_read_tr16_b64_v4i16(lp<v4s>(vb[t] + 4096));
          vf[t] = __builtin_bit_cast(v8bf, __builtin_shufflevector(lo, hi, 0, 1, 2, 3, 4, 5, 6, 7));
        }
        __builtin_amdgcn_sched_barrier(0);
#pragma unroll
        for (int i = 0; i < 4; ++i) { e0 = mfma(vf[2 * i], __builtin_bit_cast(v8bf, pm[i]), e0); e1 = mfma(vf[2 * i + 1], __builtin_bit_cast(v8bf, pm[i]), e1); }
        __builtin_amdgcn_sched_barrier(0);
        v4f r0, r1;
        for (;;) {
          const u32 cnt = ldsv(L_CTL + 4 * C_MCNT);
          FENCE();
          r0 = *lp<v4f>(ra); r1 = *lp<v4f>(ra + 1024);
          FENCE();
          if (cnt >= (u32)link) break;
        }
        e0 = (e0 + r0) * inv; e1 = (e1 + r1) * inv;
        if (link == NCONS - 1) {
          st4(v4u{min(pk2(e0.x, e0.y), SENT - 1), min(pk2(e0.z, e0.w), SENT - 1), min(pk2(e1.x, e1.y), SENT - 1), min(pk2(e1.z, e1.w), SENT - 1)}, rws, oa, W_PART + grp * 16384 + r * 256);
        } else {
          *lp<v4f>(ra) = e0; *lp<v4f>(ra + 1024) = e1;
          FENCE(); stsv(L_CTL + 4 * C_MCNT, link + 1);
        }
        stsv(L_CTL + 4 * (C_FREE + (u & (NSLOT - 1))), u + 1);
      }
    }
  }
  if (!(isX || isY)) return;
  const u32 trow = 4 * g + (c >> 2);
  auto waddr = [&](int t) -> u32 { return trow * 128 + ((((u32)(t ^ ((trow >> 1) & 3)) << 1) | ((c >> 1) & 1)) << 4) + (c & 1) * 8; };
  auto wslot = [&](int t) -> u32 { return ((uw + t) & (NSLOT - 1)) * SLOT; };
  const int half = lane >> 5, i32 = lane & 31;
  const u32 fa = L_ACT + (c & 1) * ASTR + g * 16;
#pragma nounroll
  for (int pass = wave == 1 ? 0 : 1; pass < 2; ++pass) {
    const bool dry = pass == 0;
    const int cap = dry ? 1 : SPIN_CAP;
    const rsrc_t rws = __builtin_amdgcn_make_buffer_rsrc(ws, 0, dry ? 0 : W_END, 0x00020000);
    auto tbar = [&]() { WAITL(); if (!dry) __builtin_amdgcn_s_barrier(); FENCE(); };
    if (wave == 1 && !dry) {
        const v4i srd = mksrd(isX ? wo : w2, isX ? 4096 * 1024 : 1792 * 1024);
        const u32 a0 = (lane >> 3) * 1024 + (((lane & 7) ^ ((lane >> 3) & 6)) << 4), a1 = a0 + 7168, a2 = a0 + 2 * 7168, a3 = a0 + 3 * 7168;
        u32 so = isX ? ((xi >> 3) << 19) + (xi & 7) * 128 + 256 * 1024 : yi << 16;
        while (ldsv(L_CTL + 4 * C_VISS) == 0) __builtin_amdgcn_s_sleep(2);
#pragma nounroll
        for (int u = uw + (ntail >> 1); u < uw + ntail; ++u, so += isX ? 65536 : 128) {
          while ((int)ldsv(L_CTL + 4 * (C_FREE + (u & (NSLOT - 1)))) < u + 1 - NSLOT) __builtin_amdgcn_s_sleep(1);
          const u32 lb = (u & (NSLOT - 1)) * SLOT, m = lb + 4096, s1 = so + 32768;
          DMA4(lb, a0, a1, a2, a3, srd, so);
          DMA4(m, a0, a1, a2, a3, srd, s1);
          WAITV(W2W);
        }
        arrive(C_ISS);
      }
    auto weights_done = [&]() { WAITV(0); if (wave == 0) stsv(L_CTL + 4 * C_FULL, 8 * (uw + ntail)); arrive(C_WIN); };
    auto weights_in = [&]() { while (!dry && ldsv(L_CTL + 4 * C_WIN) < 2) __builtin_amdgcn_s_sleep(1); FENCE(); };
    if (isX) {
      const int kvh = xi >> 3, ng = xi & 7;
      const int b = wave >> 2, h = wave & 3;
      u32 wa = L_ACT + b * ASTR + (h * 128 + c * 8 + g * 2) * 2, wb[8];
#pragma unroll
      for (int i = 0; i < 8; ++i) { wb[i] = wslot(i) + waddr(wave & 3); PIN(wb[i]); }
      u32 vo = lane * 16 + W_PART + (b * 8 + kvh) * 16384 + h * 4096;
      u32 vl = ((c & 3) * 4 + ((g + (c >> 2)) & 3)) * 128 + W_ML + (b * 8 + kvh) * 2048 + h * 8;
      u32 vs = c * 2048 + g * 16 + W_AP + kvh * 4096 + ng * 256 + (wave & 3) * 64, fb = fa;
      PIN(wa); PIN(vo); PIN(vl); PIN(vs); PIN(fb);
      while (!dry && ldsv(L_CTL + 4 * C_ISS) < 2) __builtin_amdgcn_s_sleep(1);
      FENCE();
      v2u ls = ld2(rws, vl, 0);
      if (wave < 2 && !dry) weights_done();
      if (!__all(max(ls.x, ls.y) != SENT)) {
#pragma nounroll
        for (int sp = 0; sp < cap; ++sp) {
          __builtin_amdgcn_s_sleep(1); FENCE();
          ls = ld2(rws, vl, 0);
          if (__all(max(ls.x, ls.y) != SENT)) break;
        }
        __builtin_amdgcn_s_sleep(XW);
      }
      FENCE();
      v4u a[4];
#pragma unroll
      for (int j = 0; j < 4; ++j) a[j] = ld4(rws, vo + j * 1024, 0);
      __builtin_amdgcn_sched_barrier(0);
      v8bf W[16];
      if (wave >= 4 || dry) {
        weights_in();
#pragma unroll
        for (int ks = 0; ks < 16; ++ks) W[ks] = trfrag(wb[ks >> 1] + (ks & 1) * 4096);
      }
      float M = __uint_as_float(ls.x);
      M = fmaxf(M, dpp<0xB1>(M)); M = fmaxf(M, dpp<0x4E>(M)); M = fmaxf(M, dpp<0x141>(M)); M = fmaxf(M, dpp<0x128>(M));
      float cf = ex2((__uint_as_float(ls.x) - M) * 1.4426950408889634f) * __uint_as_float(ls.y);
      cf *= rcp(row_sum(cf));
      float cj[4] = {dpp<0x150>(cf), dpp<0x151>(cf), dpp<0x152>(cf), dpp<0x153>(cf)};
      PIN(cj[0]); PIN(cj[1]); PIN(cj[2]); PIN(cj[3]);
#pragma nounroll
      for (int sp = 0; sp < cap; ++sp) {
        u32 mx = 0;
#pragma unroll
        for (int j = 0; j < 4; ++j) mx = max(max(mx, max(a[j].x, a[j].y)), max(a[j].z, a[j].w));
        if (__all(mx != SENT)) break;
        FENCE();
#pragma unroll
        for (int j = 0; j < 4; ++j) a[j] = ld4(rws, vo + j * 1024, 0);
      }
      v2f o[4];
#pragma unroll
      for (int i = 0; i < 4; ++i) {
        o[i] = v2f{lo16(a[0][i]), bfhi(a[0][i])} * cj[0];
#pragma unroll
        for (int j = 1; j < 4; ++j) o[i] += v2f{lo16(a[j][i]), bfhi(a[j][i])} * cj[j];
      }
      *lp<u32>(wa) = pk2(fold16(fold32(o[0].x, o[2].x), fold32(o[1].x, o[3].x)), fold16(fold32(o[0].y, o[2].y), fold32(o[1].y, o[3].y)));
      tbar();
      if (wave >= 4 || dry) {
        v8bf A[16];
#pragma unroll
        for (int ks = 0; ks < 16; ++ks) A[ks] = ldfrag(fb + ks * 64);
        __builtin_amdgcn_sched_barrier(0);
        v4f d0 = {0.f, 0.f, 0.f, 0.f};
#pragma unroll
        for (int ks = 0; ks < 16; ++ks) d0 = mfma(W[ks], A[ks], d0);
        v4u ov = {canon(d0.x), canon(d0.y), canon(d0.z), canon(d0.w)};
        if (c < 2) st4(ov, rws, vs, 0);
      }
    } else {
      const int b = wave >> 2, col = (wave & 3) * 128 + i32 * 4 + half * 2;
      u32 wb[8];
#pragma unroll
      for (int i = 0; i < 8; ++i) { wb[i] = wave >= 4 ? wslot(i) + waddr(wave & 3) : wslot(8 + 2 * wave + (i >> 2)) + waddr(i & 3); PIN(wb[i]); }
      u32 wa = L_ACT + b * ASTR + fpos(col) * 2, ra = L_RED + wave * 8 + half * 4, rb_ = L_RED + b * 32;
      u32 ha = L_H1 + (c & 1) * 128 + fpos((wave & 3) * 16 + g * 4 + ((c >> 1) & 3)) * 2, fb = wave >= 4 ? fa : L_H1 + (c & 1) * 128 + g * 16;
      u32 vo = half * 16384 + i32 * 16 + W_AP + b * 2048 + (wave & 3) * 512, vs = (c & 1) * 2048 + (c >> 1) * 64 + g * 16 + W_OP + yi * 4096 + wave * 512;
      if (dry) ra = ha = L_JUNK;
      PIN(wa); PIN(ra); PIN(rb_); PIN(ha); PIN(fb); PIN(vo); PIN(vs);
      v2f x0, ga, sb, cb1;
      {
        const u32 xv = __builtin_amdgcn_raw_buffer_load_b32(__builtin_amdgcn_make_buffer_rsrc((void*)xin, 0, dry ? 0 : 2048, 0x00020000), (b * 512 + col) * 2, 0, 0);
        const u32 tag = ldsv(L_CTL + 4 * C_TAG);
        v4u mg[3];
#pragma nounroll
        for (int sp = 0; sp < cap; ++sp) {
#pragma unroll
          for (int i = 0; i < 3; ++i) mg[i] = ld4(rws, col * 8, W_MG + i * 4096);
          if (__all(((mg[0].y ^ tag) | (mg[0].w ^ tag) | (mg[1].y ^ tag) | (mg[1].w ^ tag) | (mg[2].y ^ tag) | (mg[2].w ^ tag)) == 0)) break;
          FENCE();
        }
        const int sh = 16 - 16 * b;
        x0 = v2f{lo16(xv), bfhi(xv)};
        ga = v2f{bfhi(mg[0].x << sh), bfhi(mg[0].z << sh)}; sb = v2f{bfhi(mg[1].x << sh), bfhi(mg[1].z << sh)};
        cb1 = rb2(v2f{bfhi(mg[2].x << sh), bfhi(mg[2].z << sh)} + 1.f);
      }
      PIN(x0); PIN(ga); PIN(sb); PIN(cb1);
      if (wave < 2 && !dry) weights_done();
      weights_in();
      v4u a[4];
#pragma unroll
      for (int j = 0; j < 4; ++j) a[j] = ld4(rws, vo, j * 4096);
      __builtin_amdgcn_sched_barrier(0);
      v8bf W[16];
#pragma unroll
      for (int j = 0; j < 16; ++j) W[j] = trfrag(wb[j >> 1] + (j & 1) * 4096);
#pragma nounroll
      for (int sp = 0; sp < cap; ++sp) {
        u32 mx = 0;
#pragma unroll
        for (int j = 0; j < 4; ++j) mx = max(max(mx, max(a[j].x, a[j].y)), max(a[j].z, a[j].w));
        if (__all(mx != SENT)) break;
        FENCE();
#pragma unroll
        for (int j = 0; j < 4; ++j) a[j] = ld4(rws, vo, j * 4096);
      }
      const v4f at = (__builtin_bit_cast(v4f, a[0]) + __builtin_bit_cast(v4f, a[1])) + (__builtin_bit_cast(v4f, a[2]) + __builtin_bit_cast(v4f, a[3]));
      const v2f x1 = rb2(x0 + rb2(ga * rb2(v2f{fold32(at.x, at.z), fold32(at.y, at.w)})));
      const float st = half_sum(fold32(x1.x + x1.y, fmaf(x1.x, x1.x, x1.y * x1.y)));
      if (i32 == 0) *lp<float>(ra) = st;
      tbar();
      {
        const v4f r0 = *lp<v4f>(rb_), r1 = *lp<v4f>(rb_ + 16);
        const float mean = ((r0.x + r0.z) + (r1.x + r1.z)) * (1.f / 512.f);
        const float var = fmaxf(fmaf(-mean, mean, ((r0.y + r0.w) + (r1.y + r1.w)) * (1.f / 512.f)), 0.f);
        const v2f mv = rb2(v2f{mean, var});
        const float ri = rcp(rb(__builtin_amdgcn_sqrtf(rb(mv.y + 1e-5f))));
        const v2f hh = rb2(rb2(rb2(x1 - mv.x) * ri) * cb1) + sb;
        *lp<u32>(wa) = pk2(hh.x, hh.y);
      }
      tbar();
      if (wave >= 4 || dry) {
        v8bf A[16];
#pragma unroll
        for (int ks = 0; ks < 16; ++ks) A[ks] = ldfrag(fb + ks * 64);
        __builtin_amdgcn_sched_barrier(0);
        v4f d0 = {0.f, 0.f, 0.f, 0.f};
#pragma unroll
        for (int ks = 0; ks < 16; ++ks) d0 = mfma(W[ks], A[ks], d0);
        float e0 = (c & 2) ? d0.y : d0.x, e1 = (c & 2) ? d0.w : d0.z;
        PIN(e0); PIN(e1);
        const float v = silu(rb((c & 4) ? e1 : e0));
        *lp<u16>(ha) = (u16)pk2(v, v);
      }
      tbar();
      if (wave < 4) {
        const v8bf a0 = ldfrag(fb), a1 = ldfrag(fb + 64);
        v4f dd[8];
#pragma unroll
        for (int t = 0; t < 8; ++t) dd[t] = mfma(W[2 * t + 1], a1, mfma(W[2 * t], a0, v4f{0.f, 0.f, 0.f, 0.f}));
        v4f rv = dd[0];
#pragma unroll
        for (int t = 1; t < 8; ++t) if ((c >> 1) == t) rv = dd[t];
        v4u ov = {canon(rv.x), canon(rv.y), canon(rv.z), canon(rv.w)};
        st4(ov, rws, vs, 0);
        if (wave == 1 && yi == 27 && lane == 0) st2(v2u{(u32)E, (u32)(E >> 32)}, rws, 0, W_EPOCH);
      }
    }
  }
}

extern "C" void kernel_launch(void* const* d_in, const int* in_sizes, int n_in, void* d_out, int out_size, void* d_ws, size_t ws_size, hipStream_t stream) {
  hipLaunchKernelGGL(fused_block, dim3(256), dim3(512), 0, stream, (const u16*)d_in[1], (const u16*)d_in[2], (const u16*)d_in[9], (const u16*)d_in[3],
                     (const u16*)d_in[4], (const u16*)d_in[6], (const u16*)d_in[7], (const u16*)d_in[0], (const u16*)d_in[8], (u16*)d_out, (unsigned char*)d_ws);
}
```

```cpp
#include <hip/hip_runtime.h>

typedef unsigned int u32;
typedef unsigned long long u64;
typedef unsigned short u16;
typedef u32 v4u __attribute__((ext_vector_type(4)));
typedef u32 v2u __attribute__((ext_vector_type(2)));
typedef int v4i __attribute__((ext_vector_type(4)));
typedef float v4f __attribute__((ext_vector_type(4)));
typedef float v2f __attribute__((ext_vector_type(2)));
typedef short v4s __attribute__((ext_vector_type(4)));
typedef __bf16 v8bf __attribute__((ext_vector_type(8)));
typedef __bf16 v2bf __attribute__((ext_vector_type(2)));
#define LAS __attribute__((address_space(3)))
#define GAS __attribute__((address_space(1)))
#define DEVI __device__ __forceinline__

constexpr int NSLOT = 16, SLOT = 8192;
constexpr int NCONS = 6, NSTASH = 4, NF = 2, NMAX = 72;
#define WA 16
#define WB 20
#define WC 24
#define WD 28
#define LA 25
#define LB 50
#define LC 90
#define TEND 3705
#define LEADX 130
#define LEADX3 210
#define LEADY 190
#define COFF -20
#define W2W 24
#define ZW 5
#define HEAD 16
#define XW 5
constexpr int QAT = 12, HDELAY = 440, CODE_BYTES = 23680;
#define BLK_A 65, 65, 65, 63, 63, 63, 63, 63, 63, 63, 64, 64, 65, 65, 65, 65
constexpr int NBLK[2][16] = {{BLK_A}, {65, 65, 65, 63, 62, 62, 62, 62, 62, 62, 67, 67, 65, 65, 65, 65}};
constexpr int BIAS = 11;
constexpr u64 start_table(int k) {
  u64 t = 0; int s = 0;
  for (int r = 0; r < 16; ++r) { const int dl = s - 64 * r + BIAS; if (dl < 0 || dl > 15 || NBLK[k][r] > NMAX) return ~0ull; t |= (u64)dl << (4 * r); s += NBLK[k][r]; }
  return s == 1024 ? t : ~0ull;
}
constexpr u64 ST_A = start_table(0), ST_B = start_table(1);
static_assert(ST_A != ~0ull && ST_B != ~0ull, "block table");
constexpr u64 pace(int k, int r, int extra, int lead) { return ((TEND - lead) * 16 + NBLK[k][r]) / (2 * NBLK[k][r] + extra) - 384; }
constexpr u64 pace_table(int k) {
  return pace(k, 0, 0, LEADX) | pace(k, 3, 0, LEADX3) << 8 | pace(k, 3, 0, LEADX) << 16 | pace(k, 4, 8, 0) << 24 | pace(k, 8, 8, 0) << 32 |
         pace(k, 10, 0, k ? 0 : LEADY) << 40 | pace(k, 12, 2, 0) << 48;
}
constexpr u64 PACE_A = pace_table(0), PACE_B = pace_table(1);
constexpr int L_CTL = NSLOT * SLOT;
constexpr int L_Q = L_CTL + 256;
constexpr int L_ACT = L_Q + 1024;
constexpr int ASTR = 1024 + 64;
constexpr int L_MRG = L_ACT + 2 * ASTR;
constexpr int L_ML = L_MRG + 2048;
constexpr int L_SC = L_ML + 256;
constexpr int L_JUNK = L_SC + NMAX * 256;
constexpr int L_END = L_JUNK + 256;
constexpr int L_H1 = L_SC;
constexpr int L_RED = L_H1 + 256;
static_assert(L_END <= 160 * 1024, "LDS");
constexpr int C_FULL = 0, C_QRDY = 1, C_MCNT = 2, C_CLR = 3, C_ARDY = 4, C_TAG = 5, C_WRD = 6, C_MX = 7, C_SM = 8, C_T0 = 9, C_ISS = 10, C_VISS = 11, C_WIN = 12, C_FREE = 16;
constexpr u32 W_EPOCH = 0, W_T0 = 4096, W_SP = 8192, W_HG = W_SP + 4 * 1024 * 16, W_QG = W_HG + 512 * 8, W_MG = W_QG + 4096 * 8, W_PART = W_MG + 2048 * 8;
constexpr u32 W_ML = W_PART + 64 * 4096;
constexpr u32 W_AP = W_ML + 256 * 128, W_OP = W_AP + 8 * 4096, W_YF = W_OP + 28 * 4096, W_END = W_YF + 32 * 128;
constexpr u32 CLRB = (W_END - W_PART) / 64;
static_assert(CLRB * 64 == W_END - W_PART && CLRB % 16 == 0, "clear split");
constexpr u32 SENT = 0xFFFFFFFFu;
constexpr int SPIN_CAP = 1 << 16;

DEVI float lo16(u32 v) { return __uint_as_float(v << 16); }
DEVI float bfhi(u32 v) { return __uint_as_float(v & 0xffff0000u); }
DEVI u32 pk2(float a, float b) { v2f x = {a, b}; return __builtin_bit_cast(u32, __builtin_convertvector(x, v2bf)); }
DEVI float rb(float a) { return lo16(pk2(a, a)); }
DEVI float ex2(float x) { return __builtin_amdgcn_exp2f(x); }
DEVI float rcp(float x) { return __builtin_amdgcn_rcpf(x); }
DEVI float silu(float v) { return v * rcp(1.f + ex2(v * -1.4426950408889634f)); }
DEVI float silu_exact(float v) {
  const float t = fminf(v * -1.4426950216293335f, 126.f);
  const float r = fmaf(v, -1.92596299112662e-08f, fmaf(v, -1.4426950216293335f, -t));
  float e = ex2(t);
  e = fmaf(e, fminf(r, 1.f) * 0.6931471805599453f, e);
  const float d = 1.f + e, rc = rcp(d), q = v * rc;
  return fmaf(fmaf(-q, d, v), rc, q);
}
DEVI v2f rb2(v2f a) { const u32 p = pk2(a.x, a.y); return v2f{lo16(p), bfhi(p)}; }
DEVI float fold32(float a, float b) { auto s = __builtin_amdgcn_permlane32_swap(__float_as_uint(a), __float_as_uint(b), false, false); return __uint_as_float(s[0]) + __uint_as_float(s[1]); }
DEVI float fold16(float a, float b) { auto s = __builtin_amdgcn_permlane16_swap(__float_as_uint(a), __float_as_uint(b), false, false); return __uint_as_float(s[0]) + __uint_as_float(s[1]); }
#define PIN(x) asm volatile("" : "+v"(x))
DEVI float swap16_add(float x) { auto s = __builtin_amdgcn_permlane16_swap(__float_as_uint(x), __float_as_uint(x), false, false); return __uint_as_float(s[0]) + __uint_as_float(s[1]); }
DEVI float swap32_add(float x) { auto s = __builtin_amdgcn_permlane32_swap(__float_as_uint(x), __float_as_uint(x), false, false); return __uint_as_float(s[0]) + __uint_as_float(s[1]); }
DEVI float swap16_max(float x) { auto s = __builtin_amdgcn_permlane16_swap(__float_as_uint(x), __float_as_uint(x), false, false); return fmaxf(__uint_as_float(s[0]), __uint_as_float(s[1])); }
DEVI float swap32_max(float x) { auto s = __builtin_amdgcn_permlane32_swap(__float_as_uint(x), __float_as_uint(x), false, false); return fmaxf(__uint_as_float(s[0]), __uint_as_float(s[1])); }
template <int CTRL> DEVI float dpp(float x) { return __builtin_bit_cast(float, __builtin_amdgcn_mov_dpp(__builtin_bit_cast(int, x), CTRL, 0xf, 0xf, true)); }
DEVI float row_sum(float x) { x += dpp<0xB1>(x); x += dpp<0x4E>(x); x += dpp<0x141>(x); x += dpp<0x128>(x); return x; }
DEVI float half_sum(float x) { return swap16_add(row_sum(x)); }
DEVI float wave_sum(float x) { return swap32_add(half_sum(x)); }
template <typename T> DEVI LAS T* lp(u32 a) { return (LAS T*)(size_t)a; }
DEVI u32 ldsv(u32 a) { return *(volatile LAS u32*)(size_t)a; }
DEVI void stsv(u32 a, u32 v) { *(volatile LAS u32*)(size_t)a = v; }
#define FENCE() asm volatile("" ::: "memory")
#define WAITV(n) asm volatile("s_waitcnt vmcnt(%0)" ::"i"(n) : "memory")
#define WAITL() asm volatile("s_waitcnt lgkmcnt(0)" ::: "memory")
DEVI void bar() { WAITL(); __builtin_amdgcn_s_barrier(); FENCE(); }
typedef __amdgpu_buffer_rsrc_t rsrc_t;
DEVI v4u ld4(rsrc_t r, u32 vo, u32 so) { return __builtin_bit_cast(v4u, __builtin_amdgcn_raw_buffer_load_b128(r, vo, so, 16)); }
DEVI v2u ld2(rsrc_t r, u32 vo, u32 so) { return __builtin_bit_cast(v2u, __builtin_amdgcn_raw_buffer_load_b64(r, vo, so, 16)); }
DEVI void st4(v4u v, rsrc_t r, u32 vo, u32 so) { __builtin_amdgcn_raw_buffer_store_b128(v, r, vo, so, 16); }
DEVI void st2(v2u v, rsrc_t r, u32 vo, u32 so) { __builtin_amdgcn_raw_buffer_store_b64(v, r, vo, so, 16); }
DEVI u32 canon(float f) { u32 b = __float_as_uint(f); return b < 0xFFFFFFFEu ? b : 0xFFFFFFFEu; }
DEVI v8bf trfrag(u32 a) {
  v4s lo = __builtin_amdgcn_ds_read_tr16_b64_v4i16(lp<v4s>(a)), hi = __builtin_amdgcn_ds_read_tr16_b64_v4i16(lp<v4s>(a + 2048));
  return __builtin_bit_cast(v8bf, __builtin_shufflevector(lo, hi, 0, 1, 2, 3, 4, 5, 6, 7));
}
DEVI v4f mfma(v8bf a, v8bf b, v4f c) { return __builtin_amdgcn_mfma_f32_16x16x32_bf16(a, b, c, 0, 0, 0); }
DEVI v8bf ldfrag(u32 a) { return __builtin_bit_cast(v8bf, *lp<v4u>(a)); }
DEVI int fpos(int k) { return (k & ~31) + ((k >> 2) & 3) * 8 + ((k >> 4) & 1) * 4 + (k & 3); }
DEVI v4i mksrd(const void* p, u32 bytes) { u64 a = (u64)p; return v4i{(int)(u32)a, (int)(u32)(a >> 32), (int)bytes, 0x00020000}; }

#define DMA4(m, v0, v1, v2, v3, srd, so)                                                                                   \
  asm volatile("s_mov_b32 m0, %0\n\ts_nop 0\n\tbuffer_load_dwordx4 %1, %5, %6 offen nt lds\n\t"                           \
               "buffer_load_dwordx4 %2, %5, %6 offen offset:1024 nt lds\n\tbuffer_load_dwordx4 %3, %5, %6 offen offset:2048 nt lds\n\t" \
               "buffer_load_dwordx4 %4, %5, %6 offen offset:3072 nt lds" ::"s"(m), "v"(v0), "v"(v1), "v"(v2), "v"(v3), "s"(srd), "s"(so) : "memory")

__global__ void __launch_bounds__(512, 2)
fused_block(const u16* __restrict__ Kc, const u16* __restrict__ Vc, const u16* __restrict__ wmod, const u16* __restrict__ wq,
            const u16* __restrict__ wo, const u16* __restrict__ w1, const u16* __restrict__ w2, const u16* __restrict__ xin,
            const u16* __restrict__ cin, u16* __restrict__ out, unsigned char* ws) {
  __shared__ __attribute__((aligned(16))) unsigned char smem[L_END];
  asm volatile("" ::"s"((u32)(size_t)(LAS unsigned char*)smem));
  const int tid = threadIdx.x, lane = tid & 63, wave = __builtin_amdgcn_readfirstlane(tid >> 6);
  if (wave == 1 && blockIdx.x < 32) {
    u64 pc; asm volatile("s_getpc_b64 %0" : "=s"(pc));
    const v4i rc = mksrd((const void*)(pc & ~127ull), CODE_BYTES);
    const u32 vo = lane * 128, m = L_JUNK, s1 = 8192, s2 = 16384;
    asm volatile("s_mov_b32 m0, %0\n\ts_nop 0\n\tbuffer_load_dword %1, %2, 0 offen lds\n\tbuffer_load_dword %1, %2, %3 offen lds\n\tbuffer_load_dword %1, %2, %4 offen lds"
                 ::"s"(m), "v"(vo), "s"(rc), "s"(s1), "s"(s2) : "memory");
  }
  const int bid = (blockIdx.x & ~31) | ((blockIdx.x & 7) << 2) | ((blockIdx.x >> 3) & 3);
  const int grp = bid >> 4, r = bid & 15;
  const int g = lane >> 4, c = lane & 15;
  const u64 stt = (grp < 14 ? ST_A : ST_B) >> (4 * r);
  const int kv0 = 64 * r + ((int)stt & 15) - BIAS;
  const bool isS = r >= 12;
  if (__builtin_expect(wave == 0, 1)) {
    const v4i srd = isS ? mksrd(wmod, 512 * 6144) : mksrd(Kc, 1u << 27);
    const u32 k0 = g * 256 + ((c ^ g) << 4), w0 = (lane >> 3) * 6144 + (((lane & 7) ^ ((lane >> 3) & 6)) << 4);
    const u32 a0 = isS ? w0 : k0, a1 = isS ? w0 + 48128 : k0 ^ 64, a2 = isS ? w0 + 2 * 48128 : k0 ^ 128, a3 = isS ? w0 + 3 * 48128 : k0 ^ 192;
    const u32 so = isS ? (r - 12) * 128 * 6144 + grp * 128 : (u32)(grp * 1024 + kv0) << 13, hs = isS ? 32 * 6144 : 4096;
#pragma unroll
    for (int i = 0; i < 4; ++i) { const u32 m = i * 4096, s1 = so + i * hs; DMA4(m, a0, a1, a2, a3, srd, s1); }
  }
  if (wave == 2) { stsv(L_CTL + 4 * lane, 0); WAITL(); }
  __builtin_amdgcn_s_barrier(); FENCE();
  const u64 tstart = __builtin_amdgcn_s_memrealtime();
  FENCE();
  const bool isX = r < 4, isQ = r >= 4 && r < 8, isM = r == 8 || r == 9, isY = (r == 10 || r == 11) && grp < 14, isR = r == 0 && (grp & 1);
  const int xi = grp * 4 + r, qi = grp * 4 + r - 4, mi = grp * 2 + r - 8, yi = grp * 2 + r - 10;
  const int u0 = (int)(0x2222008888880000ull >> (4 * r)) & 15;
  const int nb = 64 + (r == 15 ? BIAS : (int)(stt >> 4) & 15) - ((int)stt & 15);
  const int nf = isS ? 0 : NF;
  const int ntail = isX ? 8 : isY ? 16 : 0;
  const int uv = u0 + nb, uw = uv + nb;
  u64 E = 0;
  auto arrive = [&](int w) { if (lane == 0) __hip_atomic_fetch_add(lp<u32>(L_CTL + 4 * w), 1u, __ATOMIC_RELAXED, __HIP_MEMORY_SCOPE_WORKGROUP); };
  int wv = wave;
  asm volatile("" : "+s"(wv));
  if (__builtin_expect(wv == 0, 1)) {
    __builtin_amdgcn_s_setprio(3);
    int iss, pub = 0;
    auto publish = [&](int v) { pub = max(pub, v); stsv(L_CTL + 4 * C_FULL, pub); };
#define PACE(W_) do { WAITV(W_); publish(iss - (W_)); } while (0)
    const u32 cls = ((u32)(0x6666554433331000ull >> (4 * r)) & 7) + (r == 3 && (grp & 1));
    const u32 tau = 384 + ((u32)((grp < 14 ? PACE_A : PACE_B) >> (8 * cls)) & 255);
    u32 due = ((u32)tstart + COFF) * 16 + NF * tau;
    const u32 wrow = lane >> 3, wlc = ((lane & 7) ^ (((wrow >> 1) & 3) << 1)) << 4;
    int u = NF;
#pragma nounroll
    for (int seg = u0 == 8 ? 0 : 1; seg < (ntail ? 4 : 3); ++seg) {
      const bool kv = seg == 1 || seg == 2;
      const u16* base = seg == 0 ? (isQ ? wq : wmod) : seg == 1 ? Kc : seg == 2 ? Vc : isX ? wo : w1;
      const u32 rs = seg == 0 ? (isQ ? 8192 : 6144) : (seg == 3 && !isX) ? 3584 : 1024;
      const v4i srd = mksrd(base, kv ? 1u << 27 : (seg == 3 && isX) ? 4096 * 1024 : 512 * rs);
      u32 so = kv ? (u32)(grp * 1024 + kv0 + (seg == 1 ? nf : 0)) << 13
                  : seg == 0 ? (isQ ? qi : 16 + mi) * 128
                             : isX ? ((xi >> 3) << 19) + (xi & 7) * 128 : yi * 128;
      const u32 hstep = kv ? 4096 : 32 * rs, ustep = 2 * hstep;
      const u32 sw = seg == 2 ? 2 * g : g, sx = seg == 1 ? 4 : 8;
      const u32 w0 = wrow * rs + wlc, wd = 8 * rs - 1024;
      const u32 a0 = kv ? g * 256 + ((c ^ sw) << 4) : w0, a1 = kv ? g * 256 + ((c ^ sw ^ sx) << 4) : w0 + wd;
      const u32 a2 = kv ? g * 256 + ((c ^ sw ^ ((2 * sx) & 15)) << 4) : w0 + 2 * wd, a3 = kv ? g * 256 + ((c ^ sw ^ ((3 * sx) & 15)) << 4) : w0 + 3 * wd;
      const int ue = seg == 0 ? NF + u0 : seg == 1 ? uv : seg == 2 ? uw : uw + (ntail >> 1);
#pragma nounroll
      while (u < ue) {
        const bool paced = u >= 4 && seg < 3;
        const int stop = min(ue, u < 4 ? 4 : u < 48 ? 48 : 1 << 20);
        u32 dd = paced ? due : due - (1u << 28);
        const int u_in = u, la = (seg == 3 && isX) ? 0x7fffffff : LA * 16;
        u32 t0, t1, lag; u32 vf, vt;
#define DMAS(so) "buffer_load_dwordx4 %[a0], %[srd], " so " offen nt lds\n\tbuffer_load_dwordx4 %[a1], %[srd], " so " offen offset:1024 nt lds\n\t" \
                 "buffer_load_dwordx4 %[a2], %[srd], " so " offen offset:2048 nt lds\n\tbuffer_load_dwordx4 %[a3], %[srd], " so " offen offset:3072 nt lds\n\t"
#define RDFREE "s_lshl_b32 %[t1], %[u], 2\n\ts_and_b32 %[t1], %[t1], 60\n\ts_or_b32 %[t1], %[t1], %[fb]\n\tv_mov_b32 %[vf], %[t1]\n\tds_read_b32 %[vf], %[vf]\n\t"
#define REST(W) "s_waitcnt vmcnt(" #W ")\n\ts_mov_b32 m0, %[t0]\n\ts_lshl_b32 %[t0], %[u], 3\n\t" DMAS("%[t1]")                                   \
                "s_sub_i32 %[t0], %[t0], " #W " - 4\n\ts_max_i32 %[pub], %[pub], %[t0]\n\tv_mov_b32 %[vt], %[pub]\n\tds_write_b32 %[va], %[vt]\n\t"                                      \
                "s_add_i32 %[u], %[u], 1\n\ts_add_u32 %[so], %[so], %[us]\n\ts_add_u32 %[dd], %[dd], %[tau]\n\t" RDFREE                             \
                "s_memrealtime s[98:99]\n\ts_cmp_lt_i32 %[u], %[stop]\n\ts_waitcnt vmcnt(" #W ")\n\ts_cbranch_scc1 1b\n\ts_branch 9f\n"
#define XREST(W) REST(W)
        asm volatile(RDFREE "s_memrealtime s[98:99]\n"
                     "1:\n\ts_sub_i32 %[t1], %[u], 15\n\ts_waitcnt lgkmcnt(0)\n\tv_cmp_gt_i32 vcc, %[t1], %[vf]\n\ts_lshl_b32 s98, s98, 4\n\t"
                     "s_cbranch_vccnz 8f\n"
                     "2:\n\ts_sub_i32 %[lag], s98, %[dd]\n\ts_cmp_lt_i32 %[lag], 0\n\ts_cbranch_scc1 7f\n\t"
                     "s_lshl_b32 %[t0], %[u], 13\n\ts_and_b32 %[t0], %[t0], 0x1e000\n\ts_mov_b32 m0, %[t0]\n\ts_add_u32 %[t1], %[so], %[hs]\n\t" DMAS("%[so]")
                     "s_or_b32 %[t0], %[t0], 0x1000\n\ts_cmp_lt_i32 %[lag], %[la]\n\ts_cbranch_scc1 4f\n\ts_cmp_lt_i32 %[lag], %[lb]\n\ts_cbranch_scc1 5f\n\t"
                     "s_cmp_lt_i32 %[lag], %[lc]\n\ts_cbranch_scc1 3f\n\t"
                     XREST(WD) "3:\n\t" XREST(WC) "4:\n\t" XREST(WA) "5:\n\t" XREST(WB)
                     "8:\n\t" RDFREE "s_sub_i32 %[t1], %[u], 15\n\ts_waitcnt lgkmcnt(0)\n\tv_cmp_gt_i32 vcc, %[t1], %[vf]\n\ts_nop 0\n\ts_cbranch_vccz 7f\n\t"
                     "s_waitcnt vmcnt(0)\n\ts_lshl_b32 %[pub], %[u], 3\n\tv_mov_b32 %[vt], %[pub]\n\tds_write_b32 %[va], %[vt]\n"
                     "6:\n\t" RDFREE "s_sub_i32 %[t1], %[u], 15\n\ts_waitcnt lgkmcnt(0)\n\tv_cmp_gt_i32 vcc, %[t1], %[vf]\n\ts_sleep 1\n\ts_cbranch_vccnz 6b\n"
                     "7:\n\ts_memrealtime s[98:99]\n\ts_waitcnt lgkmcnt(0)\n\ts_lshl_b32 s98, s98, 4\n\ts_branch 2b\n"
                     "9:\n\ts_waitcnt lgkmcnt(0)"
                     : [u] "+s"(u), [so] "+s"(so), [dd] "+s"(dd), [pub] "+s"(pub), [t0] "=&s"(t0), [t1] "=&s"(t1), [lag] "=&s"(lag), [vf] "=&v"(vf), [vt] "=&v"(vt)
                     : [a0] "v"(a0), [a1] "v"(a1), [a2] "v"(a2), [a3] "v"(a3), [srd] "s"(srd), [hs] "s"(hstep), [us] "s"(ustep), [stop] "s"(stop),
                       [va] "v"(L_CTL + 4 * C_FULL), [fb] "i"(L_CTL + 4 * C_FREE), [tau] "s"(tau), [la] "s"(la), [lb] "i"(LB * 16), [lc] "i"(LC * 16)
                     : "memory", "vcc", "scc", "s98", "s99");
        due += (u - u_in) * tau;
        if (u == 4 && isS) { WAITV(16); publish(16); }
        if (u == uw) stsv(L_CTL + 4 * C_VISS, 1);
        if (u == 48) { const u32 t = __builtin_amdgcn_readfirstlane(ldsv(L_CTL + 4 * C_T0)); if (t) due = t * 16 + u * tau; }
      }
    }
    iss = 8 * u;
    if (!ntail) { PACE(24); PACE(16); PACE(8); PACE(0); }
    else arrive(C_ISS);
    __builtin_amdgcn_s_setprio(0);
  } else {
    const rsrc_t rws = __builtin_amdgcn_make_buffer_rsrc(ws, 0, W_END, 0x00020000);
    auto wait_full = [&](u32 need) { while (ldsv(L_CTL + 4 * C_FULL) < need) __builtin_amdgcn_s_sleep(1); FENCE(); };
    const u32 trow = 4 * g + (c >> 2);
    auto waddr = [&](int t) -> u32 { return trow * 128 + ((((u32)(t ^ ((trow >> 1) & 3)) << 1) | ((c >> 1) & 1)) << 4) + (c & 1) * 8; };

    if (wave == 1) {
      const u64 E0 = __hip_atomic_load((GAS u64*)(ws + W_EPOCH), __ATOMIC_RELAXED, __HIP_MEMORY_SCOPE_AGENT);
      u32 tag;
#define CHAIN_START() do { E = E0 + 1; tag = (u32)E; } while (0)
      auto silu4 = [&](u32 x, u32 y) { return v2u{pk2(silu_exact(lo16(x)), silu_exact(bfhi(x))), pk2(silu_exact(lo16(y)), silu_exact(bfhi(y)))}; };
      if (isS) {
        const v2u cv = *(const v2u*)(cin + (lane >> 5) * 512 + (r - 12) * 128 + (lane & 31) * 4);
        CHAIN_START();
        *lp<v2u>(L_ACT + (lane >> 5) * ASTR + fpos((lane & 31) * 4) * 2) = silu4(cv.x, cv.y);
        FENCE();
        v8bf act[4];
#pragma unroll
        for (int ks = 0; ks < 4; ++ks) { act[ks] = ldfrag(L_ACT + (c & 1) * ASTR + g * 16 + ks * 64); PIN(act[ks]); }
        wait_full(16);
        v8bf W[16];
#pragma unroll
        for (int j = 0; j < 16; ++j) W[j] = trfrag(waddr(j & 3) + (j >> 2) * 4096);
        FENCE(); stsv(L_CTL + 4 * C_FREE, 1); stsv(L_CTL + 4 * (C_FREE + 1), 2);
        v4f acc[4];
#pragma unroll
        for (int t = 0; t < 4; ++t) acc[t] = v4f{0.f, 0.f, 0.f, 0.f};
#pragma unroll
        for (int j = 0; j < 16; ++j) acc[j & 3] = mfma(act[j >> 2], W[j], acc[j & 3]);
        const float r0 = g == 0 ? acc[0][0] : g == 1 ? acc[1][0] : g == 2 ? acc[2][0] : acc[3][0];
        const float r1 = g == 0 ? acc[0][1] : g == 1 ? acc[1][1] : g == 2 ? acc[2][1] : acc[3][1];
        while (ldsv(L_CTL + 4 * C_CLR) == 0) { }
        FENCE();
        st4(v4u{__float_as_uint(r0), tag, __float_as_uint(r1), tag}, rws, lane * 16, W_SP + ((r - 12) * 1024 + grp * 64) * 16);
      } else if (isR) {
        const v4u in0 = *(const v4u*)(xin + lane * 8), in1 = *(const v4u*)(xin + 512 + lane * 8);
        u32 xs0 = *(const u32*)(xin + (grp >> 1) * 64 + (lane & ~1)), xs1 = *(const u32*)(xin + 512 + (grp >> 1) * 64 + (lane & ~1));
        CHAIN_START();
        PIN(xs0); PIN(xs1);
        xs0 >>= 16 * (lane & 1); xs1 >>= 16 * (lane & 1);
        float mr[2], ri[2];
#pragma unroll
        for (int b = 0; b < 2; ++b) {
          const v4u xv = b ? in1 : in0;
          const float x[8] = {lo16(xv.x), bfhi(xv.x), lo16(xv.y), bfhi(xv.y), lo16(xv.z), bfhi(xv.z), lo16(xv.w), bfhi(xv.w)};
          float s = 0.f;
#pragma unroll
          for (int i = 0; i < 8; ++i) s += x[i];
          const float mean = wave_sum(s) * (1.f / 512.f);
          float q2 = 0.f;
#pragma unroll
          for (int i = 0; i < 8; ++i) q2 += (x[i] - mean) * (x[i] - mean);
          const float var = wave_sum(q2) * (1.f / 512.f);
          mr[b] = rb(mean); ri[b] = rcp(rb(__builtin_amdgcn_sqrtf(rb(rb(var) + 1e-5f))));
        }
        v2f y = rb2(rb2(v2f{lo16(xs0) - mr[0], lo16(xs1) - mr[1]}) * v2f{ri[0], ri[1]});
        PIN(y);
        v4u a[8];
#pragma nounroll
        for (int sp = 0; sp < SPIN_CAP; ++sp) {
          u32 bad = 0;
#pragma unroll
          for (int i = 0; i < 8; ++i) { a[i] = ld4(rws, lane * 16, W_SP + ((i & 3) * 1024 + (i >> 2) * 512 + (grp >> 1) * 64) * 16); bad |= (a[i].y ^ tag) | (a[i].w ^ tag); }
          if (__all(bad == 0)) break;
          FENCE();
        }
        v2f sc[2];
#pragma unroll
        for (int pt = 0; pt < 2; ++pt) {
          sc[pt] = v2f{0.f, 0.f};
#pragma unroll
          for (int k4 = 0; k4 < 4; ++k4) sc[pt] += v2f{__uint_as_float(a[pt * 4 + k4].x), __uint_as_float(a[pt * 4 + k4].z)};
        }
        const v2f h = rb2(y * rb2(rb2(sc[1]) + 1.f)) + rb2(sc[0]);
        st2(v2u{pk2(h.x, h.y), tag}, rws, lane * 8, W_HG + (grp >> 1) * 512);
      } else if (isM || isQ) {
        if (isM) {
          const v4u in0 = *(const v4u*)(cin + lane * 8), in1 = *(const v4u*)(cin + 512 + lane * 8);
          CHAIN_START();
          *lp<v2u>(L_ACT + fpos(lane * 8) * 2) = silu4(in0.x, in0.y); *lp<v2u>(L_ACT + fpos(lane * 8 + 4) * 2) = silu4(in0.z, in0.w);
          *lp<v2u>(L_ACT + ASTR + fpos(lane * 8) * 2) = silu4(in1.x, in1.y); *lp<v2u>(L_ACT + ASTR + fpos(lane * 8 + 4) * 2) = silu4(in1.z, in1.w);
        } else {
          CHAIN_START();
          while (__builtin_amdgcn_s_memrealtime() < tstart + HDELAY) __builtin_amdgcn_s_sleep(4);
          v4u a[4];
#pragma nounroll
          for (int sp = 0; sp < SPIN_CAP; ++sp) {
            u32 bad = 0;
#pragma unroll
            for (int i = 0; i < 4; ++i) { a[i] = ld4(rws, lane * 16 + i * 1024, W_HG); bad |= (a[i].y ^ tag) | (a[i].w ^ tag); }
            if (__all(bad == 0)) break;
            FENCE();
          }
#pragma unroll
          for (int i = 0; i < 4; ++i) {
            const u32 wa = L_ACT + fpos(i * 128 + lane * 2) * 2;
            *lp<u32>(wa) = (a[i].x & 0xffffu) | (a[i].z << 16); *lp<u32>(wa + ASTR) = (a[i].x >> 16) | (a[i].z & 0xffff0000u);
          }
        }
        FENCE(); stsv(L_CTL + 4 * C_TAG, tag); stsv(L_CTL + 4 * C_ARDY, 1);
      } else CHAIN_START();
      if (blockIdx.x < 8 && lane == 0) st2(v2u{(u32)tstart, tag}, rws, blockIdx.x * 8, W_T0);
      {
        wait_full(8 * u0 + 16 * QAT);
        v4u a[2];
#pragma nounroll
        for (int sp = 0; sp < SPIN_CAP; ++sp) {
          a[0] = ld4(rws, lane * 16, W_QG + grp * 2048); a[1] = ld4(rws, lane * 16 + 1024, W_QG + grp * 2048);
          if (__all(((a[0].y ^ tag) | (a[0].w ^ tag) | (a[1].y ^ tag) | (a[1].w ^ tag)) == 0)) break;
          __builtin_amdgcn_s_sleep(16); FENCE();
        }
        *lp<v2u>(L_Q + lane * 8) = v2u{a[0].x, a[0].z}; *lp<v2u>(L_Q + 512 + lane * 8) = v2u{a[1].x, a[1].z};
        FENCE(); stsv(L_CTL + 4 * C_TAG, tag); stsv(L_CTL + 4 * C_QRDY, 1);
        {
          const v2u t = ld2(rws, (lane & 7) * 8, W_T0);
          if (__all(t.y == tag)) {
            int dt = (int)(t.x - (u32)tstart);
            dt += __builtin_amdgcn_mov_dpp(dt, 0xB1, 0xf, 0xf, true); dt += __builtin_amdgcn_mov_dpp(dt, 0x4E, 0xf, 0xf, true); dt += __builtin_amdgcn_mov_dpp(dt, 0x141, 0xf, 0xf, true);
            if (dt > -2400 && dt < 2400) stsv(L_CTL + 4 * C_T0, ((u32)tstart + (dt >> 3) + COFF) | 1);
          }
        }
      }
      if (ntail) {
        const v4i srd = mksrd(isX ? wo : w2, isX ? 4096 * 1024 : 1792 * 1024);
        const u32 a0 = (lane >> 3) * 1024 + (((lane & 7) ^ ((lane >> 3) & 6)) << 4), a1 = a0 + 7168, a2 = a0 + 2 * 7168, a3 = a0 + 3 * 7168;
        u32 so = isX ? ((xi >> 3) << 19) + (xi & 7) * 128 + 256 * 1024 : yi << 16;
        while (ldsv(L_CTL + 4 * C_VISS) == 0) __builtin_amdgcn_s_sleep(2);
#pragma nounroll
        for (int u = uw + (ntail >> 1); u < uw + ntail; ++u, so += isX ? 65536 : 128) {
          while ((int)ldsv(L_CTL + 4 * (C_FREE + (u & (NSLOT - 1)))) < u + 1 - NSLOT) __builtin_amdgcn_s_sleep(1);
          const u32 lb = (u & (NSLOT - 1)) * SLOT, m = lb + 4096, s1 = so + 32768;
          DMA4(lb, a0, a1, a2, a3, srd, so);
          DMA4(m, a0, a1, a2, a3, srd, s1);
          WAITV(W2W);
        }
        arrive(C_ISS);
      }
      if (r == 8) {
        wait_full(8 * uw);
        const int zb = grp >> 3, zc = (grp & 7) * 64 + c * 4 + g;
        const u32 xv = xin[zb * 512 + zc];
        v2u m0, m1;
#pragma nounroll
        for (int sp = 0; sp < SPIN_CAP; ++sp) {
          m0 = ld2(rws, zc * 8, W_MG); m1 = ld2(rws, zc * 8, W_MG + 3 * 4096);
          if (__all(((m0.y ^ tag) | (m1.y ^ tag)) == 0)) break;
          FENCE();
        }
        v4u z[7];
#pragma nounroll
        for (int sp = 0; sp < SPIN_CAP; ++sp) {
          z[0] = ld4(rws, g * 4096 + c * 16, W_AP + grp * 256); z[1] = ld4(rws, g * 4096 + c * 16, W_AP + 16384 + grp * 256);
          if (__all(max(max(max(z[0].x, z[0].y), max(z[0].z, z[0].w)), max(max(z[1].x, z[1].y), max(z[1].z, z[1].w))) != SENT)) break;
          __builtin_amdgcn_s_sleep(2); FENCE();
        }
        v4f s0 = __builtin_bit_cast(v4f, z[0]), s1 = __builtin_bit_cast(v4f, z[1]);
        const float at = fold32(fold16(s0.x, s0.y), fold16(s0.z, s0.w)) + fold32(fold16(s1.x, s1.y), fold16(s1.z, s1.w));
        float x1 = rb(lo16(xv) + rb(bfhi(m0.x << (16 - 16 * zb)) * rb(at))), gb = bfhi(m1.x << (16 - 16 * zb));
        GAS u16* op = (GAS u16*)out + zb * 512 + zc;
        PIN(x1); PIN(gb); PIN(op);
#pragma nounroll
        for (int sp = 0; sp < SPIN_CAP; ++sp) {
          if (__all(ld2(rws, min(lane, 27) * 128, W_YF).x != SENT)) break;
          FENCE();
        }
        __builtin_amdgcn_s_sleep(ZW);
        FENCE();
#pragma nounroll
        for (int sp = 0; sp < SPIN_CAP; ++sp) {
#pragma unroll
          for (int j = 0; j < 7; ++j) z[j] = ld4(rws, g * 4096 + c * 16, W_OP + j * 16384 + grp * 256);
          u32 mx = 0;
#pragma unroll
          for (int j = 0; j < 7; ++j) mx = max(mx, max(max(z[j].x, z[j].y), max(z[j].z, z[j].w)));
          if (__all(mx != SENT)) break;
          FENCE();
        }
        const v4f s = ((__builtin_bit_cast(v4f, z[0]) + __builtin_bit_cast(v4f, z[1])) + (__builtin_bit_cast(v4f, z[2]) + __builtin_bit_cast(v4f, z[3]))) +
                      ((__builtin_bit_cast(v4f, z[4]) + __builtin_bit_cast(v4f, z[5])) + __builtin_bit_cast(v4f, z[6]));
        const float rr = x1 + rb(gb * rb(fold16(fold32(s.x, s.z), fold32(s.y, s.w))));
        *op = (u16)pk2(rr, rr);
        __builtin_amdgcn_endpgm();
      }
    } else {
      if (isS && wave == 7) {
        const v4u s4 = {SENT, SENT, SENT, SENT};
        const u32 so = W_PART + (grp * 4 + r - 12) * CLRB;
#pragma unroll
        for (int i = 0; i < CLRB / 1024; ++i) st4(s4, rws, lane * 16 + (i & 3) * 1024, so + (i >> 2) * 4096);
        if (lane < (CLRB % 1024) / 16) st4(s4, rws, lane * 16, so + CLRB / 1024 * 1024);
        WAITV(0); stsv(L_CTL + 4 * C_CLR, 1);
      }

      if (wave == 6) { *lp<v4f>(L_MRG + lane * 16) = v4f{0.f, 0.f, 0.f, 0.f}; *lp<v4f>(L_MRG + 1024 + lane * 16) = v4f{0.f, 0.f, 0.f, 0.f}; }
      const int d = c >> 2, cw = wave - 2;
      const u32 sca = L_SC + (g * 4 + (c & 3)) * 16;
      {
        u32 ka[4];
#pragma unroll
        for (int s = 0; s < 4; ++s) ka[s] = c * 256 + (((4 * s + g) ^ c) << 4);
        v8bf qf[4];
        float mx = -INFINITY;
        auto rdk = [&](v8bf (&kf)[8], int j) {
          const int u = j < nf ? j : u0 + j;
          const u32 lb = (u & (NSLOT - 1)) * SLOT;
          wait_full(8 * u + 8);
#pragma unroll
          for (int s = 0; s < 4; ++s) { kf[s] = ldfrag(lb + ka[s]); kf[4 + s] = ldfrag(lb + ka[s] + 4096); }
          FENCE(); stsv(L_CTL + 4 * (C_FREE + (u & (NSLOT - 1))), u + 1);
        };
        auto qk = [&](v8bf (&kf)[8], int j) {
          v4f S0 = {0.f, 0.f, 0.f, 0.f}, S1 = {0.f, 0.f, 0.f, 0.f};
#pragma unroll
          for (int s = 0; s < 4; ++s) { S0 = mfma(kf[s], qf[s], S0); S1 = mfma(kf[4 + s], qf[s], S1); }
          const v2f a = rb2(v2f{S0.x, S0.y}) * 0.08838834764831845f, b = rb2(v2f{S0.z, S0.w}) * 0.08838834764831845f;
          const v2f e = rb2(v2f{S1.x, S1.y}) * 0.08838834764831845f, f = rb2(v2f{S1.z, S1.w}) * 0.08838834764831845f;
          mx = fmaxf(fmaxf(fmaxf(mx, fmaxf(a.x, a.y)), fmaxf(b.x, b.y)), fmaxf(fmaxf(e.x, e.y), fmaxf(f.x, f.y)));
          if (c < 4) *lp<v4u>(sca + j * 256) = v4u{pk2(a.x, a.y), pk2(b.x, b.y), pk2(e.x, e.y), pk2(f.x, f.y)};
        };
        v8bf sk[NSTASH][8];
        rdk(sk[0], cw);
        if (u0 == 8 && wave < 6) {
          wait_full(8 * NF + 64);
          v8bf W[16];
#pragma unroll
          for (int ks = 0; ks < 16; ++ks) W[ks] = trfrag(NF * SLOT + ks * 4096 + waddr(wave - 2));
          WAITL();
          u32 old = 0;
          if (lane == 0) old = __hip_atomic_fetch_add(lp<u32>(L_CTL + 4 * C_WRD), 1u, __ATOMIC_RELAXED, __HIP_MEMORY_SCOPE_WORKGROUP);
          if (__builtin_amdgcn_readfirstlane(old) == 3 && lane < 8) stsv(L_CTL + 4 * (C_FREE + NF + lane), NF + lane + 1);
          while (ldsv(L_CTL + 4 * C_ARDY) == 0) __builtin_amdgcn_s_sleep(1);
          FENCE();
          const u32 tag = ldsv(L_CTL + 4 * C_TAG);
          v8bf A[16];
#pragma unroll
          for (int ks = 0; ks < 16; ++ks) A[ks] = ldfrag(L_ACT + (c & 1) * ASTR + g * 16 + ks * 64);
          __builtin_amdgcn_sched_barrier(0);
          v4f d0 = {0.f, 0.f, 0.f, 0.f}, d1 = {0.f, 0.f, 0.f, 0.f};
#pragma unroll
          for (int ks = 0; ks < 16; ks += 2) { d0 = mfma(A[ks], W[ks], d0); d1 = mfma(A[ks + 1], W[ks + 1], d1); }
          const float r0 = d0[0] + d1[0], r1 = d0[1] + d1[1];
          const float n0 = dpp<0xB1>(r0), n1 = dpp<0xB1>(r1);
          const bool odd = lane & 1;
          if (lane < 16) {
            if (isM) st2(v2u{pk2(r0, r1), tag}, rws, lane * 8, W_MG + mi * 512 + (wave - 2) * 128);
            else st2(v2u{pk2(odd ? n1 : r0, odd ? r1 : n0), tag}, rws, (lane & 1) * 16384 + (lane >> 1) * 8, W_QG + qi * 256 + (wave - 2) * 64);
          }
        }
#pragma unroll
        for (int i = 1; i < NSTASH; ++i) rdk(sk[i], cw + i * NCONS);
        while (ldsv(L_CTL + 4 * C_QRDY) == 0) __builtin_amdgcn_s_sleep(2);
        FENCE();
#pragma unroll
        for (int s = 0; s < 4; ++s) qf[s] = ldfrag(L_Q + (c & 3) * 256 + (4 * s + g) * 16);
#pragma unroll
        for (int i = 0; i < NSTASH; ++i) qk(sk[i], cw + i * NCONS);
#pragma nounroll
        for (int j = cw + NSTASH * NCONS; j < nb; j += NCONS) { v8bf kf[8]; rdk(kf, j); qk(kf, j); }
        mx = swap32_max(swap16_max(rb(mx)));
        if (lane < 4) *lp<float>(L_ML + cw * 16 + lane * 4) = mx;
        FENCE();
        arrive(C_MX);
      }
      float M;
      {
        while (ldsv(L_CTL + 4 * C_MX) < NCONS) __builtin_amdgcn_s_sleep(1);
        FENCE();
        M = *lp<float>(L_ML + (c & 3) * 4);
#pragma unroll
        for (int w = 1; w < NCONS; ++w) M = fmaxf(M, *lp<float>(L_ML + w * 16 + (c & 3) * 4));
        float l = 0.f;
#pragma nounroll
        for (int j = cw + d * NCONS; j < nb; j += 4 * NCONS) {
          const v4u s = *lp<v4u>(sca + j * 256);
          float p[8];
#pragma unroll
          for (int i = 0; i < 4; ++i) { p[2 * i] = ex2((lo16(s[i]) - M) * 1.4426950408889634f); p[2 * i + 1] = ex2((bfhi(s[i]) - M) * 1.4426950408889634f); }
          l += ((p[0] + p[1]) + (p[2] + p[3])) + ((p[4] + p[5]) + (p[6] + p[7]));
          *lp<v4u>(sca + j * 256) = v4u{pk2(p[0], p[1]), pk2(p[2], p[3]), pk2(p[4], p[5]), pk2(p[6], p[7])};
        }
        l += dpp<0x124>(l); l += dpp<0x128>(l);
        l = swap32_add(swap16_add(l));
        if (lane < 4) *lp<float>(L_ML + 128 + cw * 16 + lane * 4) = l;
        FENCE();
        arrive(C_SM);
      }
      {
        u32 va[8];
#pragma unroll
        for (int t = 0; t < 8; ++t) va[t] = trow * 256 + ((((u32)(t ^ (trow & 7)) << 1) | ((c >> 1) & 1)) << 4) + (c & 1) * 8;
        v4f acc[8];
#pragma unroll
        for (int t = 0; t < 8; ++t) acc[t] = v4f{0.f, 0.f, 0.f, 0.f};
        int j = cw;
#pragma nounroll
        for (; j + NCONS < nb; j += NCONS) {
          const int u = uv + j;
          const u32 lb = (u & (NSLOT - 1)) * SLOT;
          wait_full(8 * u + 8);
          const v8bf pf = ldfrag(sca + j * 256);
          v8bf vf[8];
#pragma unroll
          for (int t = 0; t < 8; ++t) {
            v4s lo = __builtin_amdgcn_ds_read_tr16_b64_v4i16(lp<v4s>(lb + va[t])), hi = __builtin_amdgcn_ds_read_tr16_b64_v4i16(lp<v4s>(lb + va[t] + 4096));
            vf[t] = __builtin_bit_cast(v8bf, __builtin_shufflevector(lo, hi, 0, 1, 2, 3, 4, 5, 6, 7));
          }
          FENCE(); stsv(L_CTL + 4 * (C_FREE + (u & (NSLOT - 1))), u + 1);
#pragma unroll
          for (int t = 0; t < 8; ++t) acc[t] = mfma(vf[t], pf, acc[t]);
        }
        const int link = j - (nb - NCONS), u = uv + j;
        const u32 lb = (u & (NSLOT - 1)) * SLOT;
        v4f e0 = d == 0 ? acc[0] : d == 1 ? acc[2] : d == 2 ? acc[4] : acc[6];
        v4f e1 = d == 0 ? acc[1] : d == 1 ? acc[3] : d == 2 ? acc[5] : acc[7];
        const v4u pf = *lp<v4u>(sca + j * 256);
        v4u pm[4];
#pragma unroll
        for (int i = 0; i < 4; ++i) { pm[i] = d == i ? pf : v4u{0u, 0u, 0u, 0u}; PIN(pm[i]); }
        float inv = 1.f, L = 0.f;
        if (link == NCONS - 1) {
          while (ldsv(L_CTL + 4 * C_SM) < NCONS) { }
          FENCE();
          L = *lp<float>(L_ML + 128 + (c & 3) * 4);
#pragma unroll
          for (int w = 1; w < NCONS; ++w) L += *lp<float>(L_ML + 128 + w * 16 + (c & 3) * 4);
          inv = rcp(L);
        }
        v2u ml = {canon(M), canon(L)};
        PIN(ml);
        u32 oa = (c & 3) * 4096 + d * 64 + g * 16, ra = L_MRG + lane * 16;
        u32 vb[8];
#pragma unroll
        for (int t = 0; t < 8; ++t) { vb[t] = lb + va[t]; PIN(vb[t]); }
        PIN(e0); PIN(e1); PIN(oa); PIN(ra); PIN(inv);
        if (link == NCONS - 1) {
          while (ldsv(L_CTL + 4 * C_FULL) < (u32)(8 * u + 8 - HEAD)) { }
          if (lane < 4) st2(ml, rws, lane * 8, W_ML + bid * 128);
        }
        while (ldsv(L_CTL + 4 * C_FULL) < (u32)(8 * u + 8)) { }
        FENCE();
        v8bf vf[8];
#pragma unroll
        for (int t = 0; t < 8; ++t) {
          v4s lo = __builtin_amdgcn_ds_read_tr16_b64_v4i16(lp<v4s>(vb[t])), hi = __builtin_amdgcn_ds_read_tr16_b64_v4i16(lp<v4s>(vb[t] + 4096));
          vf[t] = __builtin_bit_cast(v8bf, __builtin_shufflevector(lo, hi, 0, 1, 2, 3, 4, 5, 6, 7));
        }
        __builtin_amdgcn_sched_barrier(0);
#pragma unroll
        for (int i = 0; i < 4; ++i) { e0 = mfma(vf[2 * i], __builtin_bit_cast(v8bf, pm[i]), e0); e1 = mfma(vf[2 * i + 1], __builtin_bit_cast(v8bf, pm[i]), e1); }
        __builtin_amdgcn_sched_barrier(0);
        v4f r0, r1;
        for (;;) {
          const u32 cnt = ldsv(L_CTL + 4 * C_MCNT);
          FENCE();
          r0 = *lp<v4f>(ra); r1 = *lp<v4f>(ra + 1024);
          FENCE();
          if (cnt >= (u32)link) break;
        }
        e0 = (e0 + r0) * inv; e1 = (e1 + r1) * inv;
        if (link == NCONS - 1) {
          st4(v4u{min(pk2(e0.x, e0.y), SENT - 1), min(pk2(e0.z, e0.w), SENT - 1), min(pk2(e1.x, e1.y), SENT - 1), min(pk2(e1.z, e1.w), SENT - 1)}, rws, oa, W_PART + grp * 16384 + r * 256);
        } else {
          *lp<v4f>(ra) = e0; *lp<v4f>(ra + 1024) = e1;
          FENCE(); stsv(L_CTL + 4 * C_MCNT, link + 1);
        }
        stsv(L_CTL + 4 * (C_FREE + (u & (NSLOT - 1))), u + 1);
      }
    }
  }
  if (!(isX || isY)) return;
  const u32 trow = 4 * g + (c >> 2);
  auto waddr = [&](int t) -> u32 { return trow * 128 + ((((u32)(t ^ ((trow >> 1) & 3)) << 1) | ((c >> 1) & 1)) << 4) + (c & 1) * 8; };
  auto wslot = [&](int t) -> u32 { return ((uw + t) & (NSLOT - 1)) * SLOT; };
  const int half = lane >> 5, i32 = lane & 31;
  const u32 fa = L_ACT + (c & 1) * ASTR + g * 16;
  {
    const rsrc_t rws = __builtin_amdgcn_make_buffer_rsrc(ws, 0, W_END, 0x00020000);
    auto weights_done = [&]() { WAITV(0); if (wave == 0) stsv(L_CTL + 4 * C_FULL, 8 * (uw + ntail)); arrive(C_WIN); };
    auto weights_in = [&]() { while (ldsv(L_CTL + 4 * C_WIN) < 2) __builtin_amdgcn_s_sleep(1); FENCE(); };
    if (isX) {
      const int kvh = xi >> 3, ng = xi & 7;
      const int b = wave >> 2, h = wave & 3;
      u32 wa = L_ACT + b * ASTR + (h * 128 + c * 8 + g * 2) * 2, wb[8];
#pragma unroll
      for (int i = 0; i < 8; ++i) { wb[i] = wslot(i) + waddr(wave & 3); PIN(wb[i]); }
      u32 vo = lane * 16 + W_PART + (b * 8 + kvh) * 16384 + h * 4096;
      u32 vl = ((c & 3) * 4 + ((g + (c >> 2)) & 3)) * 128 + W_ML + (b * 8 + kvh) * 2048 + h * 8;
      u32 vs = c * 2048 + g * 16 + W_AP + kvh * 4096 + ng * 256 + (wave & 3) * 64, fb = fa;
      PIN(wa); PIN(vo); PIN(vl); PIN(vs); PIN(fb);
      while (ldsv(L_CTL + 4 * C_ISS) < 2) __builtin_amdgcn_s_sleep(1);
      FENCE();
      v2u ls = ld2(rws, vl, 0);
      if (wave < 2) weights_done();
      if (!__all(max(ls.x, ls.y) != SENT)) {
#pragma nounroll
        for (int sp = 0; sp < SPIN_CAP; ++sp) {
          __builtin_amdgcn_s_sleep(1); FENCE();
          ls = ld2(rws, vl, 0);
          if (__all(max(ls.x, ls.y) != SENT)) break;
        }
        __builtin_amdgcn_s_sleep(XW);
      }
      FENCE();
      v4u a[4];
#pragma unroll
      for (int j = 0; j < 4; ++j) a[j] = ld4(rws, vo + j * 1024, 0);
      __builtin_amdgcn_sched_barrier(0);
      v8bf W[16];
      if (wave >= 4) {
        weights_in();
#pragma unroll
        for (int ks = 0; ks < 16; ++ks) W[ks] = trfrag(wb[ks >> 1] + (ks & 1) * 4096);
      }
      float M = __uint_as_float(ls.x);
      M = fmaxf(M, dpp<0xB1>(M)); M = fmaxf(M, dpp<0x4E>(M)); M = fmaxf(M, dpp<0x141>(M)); M = fmaxf(M, dpp<0x128>(M));
      float cf = ex2((__uint_as_float(ls.x) - M) * 1.4426950408889634f) * __uint_as_float(ls.y);
      cf *= rcp(row_sum(cf));
      float cj[4] = {dpp<0x150>(cf), dpp<0x151>(cf), dpp<0x152>(cf), dpp<0x153>(cf)};
      PIN(cj[0]); PIN(cj[1]); PIN(cj[2]); PIN(cj[3]);
#pragma nounroll
      for (int sp = 0; sp < SPIN_CAP; ++sp) {
        u32 mx = 0;
#pragma unroll
        for (int j = 0; j < 4; ++j) mx = max(max(mx, max(a[j].x, a[j].y)), max(a[j].z, a[j].w));
        if (__all(mx != SENT)) break;
        FENCE();
#pragma unroll
        for (int j = 0; j < 4; ++j) a[j] = ld4(rws, vo + j * 1024, 0);
      }
      v2f o[4];
#pragma unroll
      for (int i = 0; i < 4; ++i) {
        o[i] = v2f{lo16(a[0][i]), bfhi(a[0][i])} * cj[0];
#pragma unroll
        for (int j = 1; j < 4; ++j) o[i] += v2f{lo16(a[j][i]), bfhi(a[j][i])} * cj[j];
      }
      *lp<u32>(wa) = pk2(fold16(fold32(o[0].x, o[2].x), fold32(o[1].x, o[3].x)), fold16(fold32(o[0].y, o[2].y), fold32(o[1].y, o[3].y)));
      bar();
      if (wave >= 4) {
        v8bf A[16];
#pragma unroll
        for (int ks = 0; ks < 16; ++ks) A[ks] = ldfrag(fb + ks * 64);
        __builtin_amdgcn_sched_barrier(0);
        v4f d0 = {0.f, 0.f, 0.f, 0.f};
#pragma unroll
        for (int ks = 0; ks < 16; ++ks) d0 = mfma(W[ks], A[ks], d0);
        v4u ov = {canon(d0.x), canon(d0.y), canon(d0.z), canon(d0.w)};
        if (c < 2) st4(ov, rws, vs, 0);
      }
    } else {
      const int b = wave >> 2, col = (wave & 3) * 128 + i32 * 4 + half * 2;
      u32 wb[8];
#pragma unroll
      for (int i = 0; i < 8; ++i) { wb[i] = wave >= 4 ? wslot(i) + waddr(wave & 3) : wslot(8 + 2 * wave + (i >> 2)) + waddr(i & 3); PIN(wb[i]); }
      u32 wa = L_ACT + b * ASTR + fpos(col) * 2, ra = L_RED + wave * 8 + half * 4, rb_ = L_RED + b * 32;
      u32 ha = L_H1 + (c & 1) * 128 + fpos((wave & 3) * 16 + g * 4 + ((c >> 1) & 3)) * 2, fb = wave >= 4 ? fa : L_H1 + (c & 1) * 128 + g * 16;
      u32 vo = half * 16384 + i32 * 16 + W_AP + b * 2048 + (wave & 3) * 512, vs = (c & 1) * 2048 + (c >> 1) * 64 + g * 16 + W_OP + yi * 4096 + wave * 512;
      PIN(wa); PIN(ra); PIN(rb_); PIN(ha); PIN(fb); PIN(vo); PIN(vs);
      v2f x0, ga, sb, cb1;
      {
        const u32 xv = *(const u32*)(xin + b * 512 + col);
        const u32 tag = ldsv(L_CTL + 4 * C_TAG);
        v4u mg[3];
#pragma nounroll
        for (int sp = 0; sp < SPIN_CAP; ++sp) {
#pragma unroll
          for (int i = 0; i < 3; ++i) mg[i] = ld4(rws, col * 8, W_MG + i * 4096);
          if (__all(((mg[0].y ^ tag) | (mg[0].w ^ tag) | (mg[1].y ^ tag) | (mg[1].w ^ tag) | (mg[2].y ^ tag) | (mg[2].w ^ tag)) == 0)) break;
          FENCE();
        }
        const int sh = 16 - 16 * b;
        x0 = v2f{lo16(xv), bfhi(xv)};
        ga = v2f{bfhi(mg[0].x << sh), bfhi(mg[0].z << sh)}; sb = v2f{bfhi(mg[1].x << sh), bfhi(mg[1].z << sh)};
        cb1 = rb2(v2f{bfhi(mg[2].x << sh), bfhi(mg[2].z << sh)} + 1.f);
      }
      PIN(x0); PIN(ga); PIN(sb); PIN(cb1);
      if (wave < 2) weights_done();
      weights_in();
      v4u a[4];
#pragma unroll
      for (int j = 0; j < 4; ++j) a[j] = ld4(rws, vo, j * 4096);
      __builtin_amdgcn_sched_barrier(0);
      v8bf W[16];
#pragma unroll
      for (int j = 0; j < 16; ++j) W[j] = trfrag(wb[j >> 1] + (j & 1) * 4096);
#pragma nounroll
      for (int sp = 0; sp < SPIN_CAP; ++sp) {
        u32 mx = 0;
#pragma unroll
        for (int j = 0; j < 4; ++j) mx = max(max(mx, max(a[j].x, a[j].y)), max(a[j].z, a[j].w));
        if (__all(mx != SENT)) break;
        FENCE();
#pragma unroll
        for (int j = 0; j < 4; ++j) a[j] = ld4(rws, vo, j * 4096);
      }
      const v4f at = (__builtin_bit_cast(v4f, a[0]) + __builtin_bit_cast(v4f, a[1])) + (__builtin_bit_cast(v4f, a[2]) + __builtin_bit_cast(v4f, a[3]));
      const v2f x1 = rb2(x0 + rb2(ga * rb2(v2f{fold32(at.x, at.z), fold32(at.y, at.w)})));
      const float st = half_sum(fold32(x1.x + x1.y, fmaf(x1.x, x1.x, x1.y * x1.y)));
      if (i32 == 0) *lp<float>(ra) = st;
      bar();
      {
        const v4f r0 = *lp<v4f>(rb_), r1 = *lp<v4f>(rb_ + 16);
        const float mean = ((r0.x + r0.z) + (r1.x + r1.z)) * (1.f / 512.f);
        const float var = fmaxf(fmaf(-mean, mean, ((r0.y + r0.w) + (r1.y + r1.w)) * (1.f / 512.f)), 0.f);
        const v2f mv = rb2(v2f{mean, var});
        const float ri = rcp(rb(__builtin_amdgcn_sqrtf(rb(mv.y + 1e-5f))));
        const v2f hh = rb2(rb2(rb2(x1 - mv.x) * ri) * cb1) + sb;
        *lp<u32>(wa) = pk2(hh.x, hh.y);
      }
      bar();
      if (wave == 0 && lane == 0) st2(v2u{0u, 0u}, rws, 0, W_YF + yi * 128);
      if (wave >= 4) {
        v8bf A[16];
#pragma unroll
        for (int ks = 0; ks < 16; ++ks) A[ks] = ldfrag(fb + ks * 64);
        __builtin_amdgcn_sched_barrier(0);
        v4f d0 = {0.f, 0.f, 0.f, 0.f};
#pragma unroll
        for (int ks = 0; ks < 16; ++ks) d0 = mfma(W[ks], A[ks], d0);
        float e0 = (c & 2) ? d0.y : d0.x, e1 = (c & 2) ? d0.w : d0.z;
        PIN(e0); PIN(e1);
        const float v = silu(rb((c & 4) ? e1 : e0));
        *lp<u16>(ha) = (u16)pk2(v, v);
      }
      bar();
      if (wave < 4) {
        const v8bf a0 = ldfrag(fb), a1 = ldfrag(fb + 64);
        v4f dd[8];
#pragma unroll
        for (int t = 0; t < 8; ++t) dd[t] = mfma(W[2 * t + 1], a1, mfma(W[2 * t], a0, v4f{0.f, 0.f, 0.f, 0.f}));
        v4f rv = dd[0];
#pragma unroll
        for (int t = 1; t < 8; ++t) if ((c >> 1) == t) rv = dd[t];
        v4u ov = {canon(rv.x), canon(rv.y), canon(rv.z), canon(rv.w)};
        st4(ov, rws, vs, 0);
        if (wave == 1 && yi == 27 && lane == 0) st2(v2u{(u32)E, (u32)(E >> 32)}, rws, 0, W_EPOCH);
      }
    }
  }
}

extern "C" void kernel_launch(void* const* d_in, const int* in_sizes, int n_in, void* d_out, int out_size, void* d_ws, size_t ws_size, hipStream_t stream) {
  hipLaunchKernelGGL(fused_block, dim3(256), dim3(512), 0, stream, (const u16*)d_in[1], (const u16*)d_in[2], (const u16*)d_in[9], (const u16*)d_in[3],
                     (const u16*)d_in[4], (const u16*)d_in[6], (const u16*)d_in[7], (const u16*)d_in[0], (const u16*)d_in[8], (u16*)d_out, (unsigned char*)d_ws);
}
```

```cpp
#include <hip/hip_runtime.h>

typedef unsigned int u32;
typedef unsigned long long u64;
typedef unsigned short u16;
typedef u32 v4u __attribute__((ext_vector_type(4)));
typedef u32 v2u __attribute__((ext_vector_type(2)));
typedef int v4i __attribute__((ext_vector_type(4)));
typedef float v4f __attribute__((ext_vector_type(4)));
typedef float v2f __attribute__((ext_vector_type(2)));
typedef short v4s __attribute__((ext_vector_type(4)));
typedef __bf16 v8bf __attribute__((ext_vector_type(8)));
typedef __bf16 v2bf __attribute__((ext_vector_type(2)));
#define LAS __attribute__((address_space(3)))
#define GAS __attribute__((address_space(1)))
#define DEVI __device__ __forceinline__

constexpr int NSLOT = 16, SLOT = 8192;
constexpr int NCONS = 6, NSTASH = 4, NF = 4, NMAX = 65;
#define WA 18
#define WB 20
#define WC 22
#define WD 24
#define WE 28
#define LD 200
#define LA 25
#define LB 60
#define LC 110
#define TEND 3570
#define LEADX 130
#define LEADX3 210
#define LEADY 190
#define COFF -15
#define COFF1 3
#define W2W 32
#define ZW 5
#define T0AT 260
#define YDL 100
#define HEAD 16
#define XW 5
constexpr int QAT = 12, HDELAY = 440, CODE_BYTES = 24576;
#define NB63 0x2222110000000222ull
#define DONOR 0x9876000000000543ull
constexpr bool tables_ok() {
  int given = 0, taken = 0;
  for (int r = 0; r < 16; ++r) { const int n = (int)(NB63 >> (4 * r)) & 15; if (n == 0) given |= 1 << r; if (n == 2) taken += 1 << ((int)(DONOR >> (4 * r)) & 15); if (n > 2) return false; }
  return given == taken;
}
static_assert(tables_ok(), "block tables");
constexpr u64 pace(int r, int extra, int lead) { return ((TEND - lead) * 16 + 64) / (2 * (63 + ((int)(NB63 >> (4 * r)) & 15)) + extra) - 384; }
constexpr u64 PACES = pace(0, 0, LEADX) | pace(3, 0, LEADX3) << 8 | pace(3, 0, LEADX) << 16 | pace(4, 8, 0) << 24 | pace(10, 0, LEADY) << 32 | pace(10, 0, 0) << 40 | pace(12, 2, 0) << 48;
constexpr int L_CTL = NSLOT * SLOT;
constexpr int L_Q = L_CTL + 256;
constexpr int L_ACT = L_Q + 1024;
constexpr int ASTR = 1024 + 64;
constexpr int L_MRG = L_ACT + 2 * ASTR;
constexpr int L_ML = L_MRG + 2048;
constexpr int L_SC = L_ML + 256;
constexpr int L_JUNK = L_SC + NMAX * 256;
constexpr int L_END = L_JUNK + 256;
constexpr int L_H1 = L_SC;
constexpr int L_RED = L_H1 + 256;
static_assert(L_END <= 160 * 1024, "LDS");
constexpr int C_FULL = 0, C_QRDY = 1, C_MCNT = 2, C_CLR = 3, C_ARDY = 4, C_TAG = 5, C_WRD = 6, C_MX = 7, C_SM = 8, C_T0 = 9, C_ISS = 10, C_VISS = 11, C_WIN = 12, C_FREE = 16;
constexpr u32 W_EPOCH = 0, W_T0 = 4096, W_SP = 8192, W_HG = W_SP + 4 * 1024 * 16, W_QG = W_HG + 512 * 8, W_MG = W_QG + 4096 * 8, W_PART = W_MG + 2048 * 8;
constexpr u32 W_ML = W_PART + 64 * 4096;
constexpr u32 W_AP = W_ML + 256 * 128, W_OP = W_AP + 8 * 4096, W_YF = W_OP + 28 * 4096, W_END = W_YF + 32 * 128;
constexpr u32 CLRB = (W_END - W_PART) / 64;
static_assert(CLRB * 64 == W_END - W_PART && CLRB % 16 == 0, "clear split");
constexpr u32 SENT = 0xFFFFFFFFu;
constexpr int SPIN_CAP = 1 << 18;

DEVI float lo16(u32 v) { return __uint_as_float(v << 16); }
DEVI float bfhi(u32 v) { return __uint_as_float(v & 0xffff0000u); }
DEVI u32 pk2(float a, float b) { v2f x = {a, b}; return __builtin_bit_cast(u32, __builtin_convertvector(x, v2bf)); }
DEVI float rb(float a) { return lo16(pk2(a, a)); }
DEVI float ex2(float x) { return __builtin_amdgcn_exp2f(x); }
DEVI float rcp(float x) { return __builtin_amdgcn_rcpf(x); }
DEVI float silu(float v) { return v * rcp(1.f + ex2(v * -1.4426950408889634f)); }
DEVI float silu_exact(float v) {
  const float t = fminf(v * -1.4426950216293335f, 126.f);
  const float r = fmaf(v, -1.92596299112662e-08f, fmaf(v, -1.4426950216293335f, -t));
  float e = ex2(t);
  e = fmaf(e, fminf(r, 1.f) * 0.6931471805599453f, e);
  const float d = 1.f + e, rc = rcp(d), q = v * rc;
  return fmaf(fmaf(-q, d, v), rc, q);
}
DEVI v2f rb2(v2f a) { const u32 p = pk2(a.x, a.y); return v2f{lo16(p), bfhi(p)}; }
DEVI float fold32(float a, float b) { auto s = __builtin_amdgcn_permlane32_swap(__float_as_uint(a), __float_as_uint(b), false, false); return __uint_as_float(s[0]) + __uint_as_float(s[1]); }
DEVI float fold16(float a, float b) { auto s = __builtin_amdgcn_permlane16_swap(__float_as_uint(a), __float_as_uint(b), false, false); return __uint_as_float(s[0]) + __uint_as_float(s[1]); }
#define PIN(x) asm volatile("" : "+v"(x))
DEVI float swap16_add(float x) { auto s = __builtin_amdgcn_permlane16_swap(__float_as_uint(x), __float_as_uint(x), false, false); return __uint_as_float(s[0]) + __uint_as_float(s[1]); }
DEVI float swap32_add(float x) { auto s = __builtin_amdgcn_permlane32_swap(__float_as_uint(x), __float_as_uint(x), false, false); return __uint_as_float(s[0]) + __uint_as_float(s[1]); }
DEVI float swap16_max(float x) { auto s = __builtin_amdgcn_permlane16_swap(__float_as_uint(x), __float_as_uint(x), false, false); return fmaxf(__uint_as_float(s[0]), __uint_as_float(s[1])); }
DEVI float swap32_max(float x) { auto s = __builtin_amdgcn_permlane32_swap(__float_as_uint(x), __float_as_uint(x), false, false); return fmaxf(__uint_as_float(s[0]), __uint_as_float(s[1])); }
template <int CTRL> DEVI float dpp(float x) { return __builtin_bit_cast(float, __builtin_amdgcn_mov_dpp(__builtin_bit_cast(int, x), CTRL, 0xf, 0xf, true)); }
DEVI float row_sum(float x) { x += dpp<0xB1>(x); x += dpp<0x4E>(x); x += dpp<0x141>(x); x += dpp<0x128>(x); return x; }
DEVI float half_sum(float x) { return swap16_add(row_sum(x)); }
DEVI float wave_sum(float x) { return swap32_add(half_sum(x)); }
template <typename T> DEVI LAS T* lp(u32 a) { return (LAS T*)(size_t)a; }
DEVI u32 ldsv(u32 a) { return *(volatile LAS u32*)(size_t)a; }
DEVI void stsv(u32 a, u32 v) { *(volatile LAS u32*)(size_t)a = v; }
#define FENCE() asm volatile("" ::: "memory")
#define WAITV(n) asm volatile("s_waitcnt vmcnt(%0)" ::"i"(n) : "memory")
#define WAITL() asm volatile("s_waitcnt lgkmcnt(0)" ::: "memory")
DEVI void bar() { WAITL(); __builtin_amdgcn_s_barrier(); FENCE(); }
typedef __amdgpu_buffer_rsrc_t rsrc_t;
DEVI v4u ld4(rsrc_t r, u32 vo, u32 so) { return __builtin_bit_cast(v4u, __builtin_amdgcn_raw_buffer_load_b128(r, vo, so, 16)); }
DEVI v2u ld2(rsrc_t r, u32 vo, u32 so) { return __builtin_bit_cast(v2u, __builtin_amdgcn_raw_buffer_load_b64(r, vo, so, 16)); }
DEVI void st4(v4u v, rsrc_t r, u32 vo, u32 so) { __builtin_amdgcn_raw_buffer_store_b128(v, r, vo, so, 16); }
DEVI void st2(v2u v, rsrc_t r, u32 vo, u32 so) { __builtin_amdgcn_raw_buffer_store_b64(v, r, vo, so, 16); }
DEVI u32 canon(float f) { u32 b = __float_as_uint(f); return b < 0xFFFFFFFEu ? b : 0xFFFFFFFEu; }
DEVI v8bf trfrag(u32 a) {
  v4s lo = __builtin_amdgcn_ds_read_tr16_b64_v4i16(lp<v4s>(a)), hi = __builtin_amdgcn_ds_read_tr16_b64_v4i16(lp<v4s>(a + 2048));
  return __builtin_bit_cast(v8bf, __builtin_shufflevector(lo, hi, 0, 1, 2, 3, 4, 5, 6, 7));
}
DEVI v4f mfma(v8bf a, v8bf b, v4f c) { return __builtin_amdgcn_mfma_f32_16x16x32_bf16(a, b, c, 0, 0, 0); }
DEVI v8bf ldfrag(u32 a) { return __builtin_bit_cast(v8bf, *lp<v4u>(a)); }
DEVI int fpos(int k) { return (k & ~31) + ((k >> 2) & 3) * 8 + ((k >> 4) & 1) * 4 + (k & 3); }
DEVI v4i mksrd(const void* p, u32 bytes) { u64 a = (u64)p; return v4i{(int)(u32)a, (int)(u32)(a >> 32), (int)bytes, 0x00020000}; }

#define DMA4(m, v0, v1, v2, v3, srd, so)                                                                                   \
  asm volatile("s_mov_b32 m0, %0\n\ts_nop 0\n\tbuffer_load_dwordx4 %1, %5, %6 offen nt lds\n\t"                           \
               "buffer_load_dwordx4 %2, %5, %6 offen offset:1024 nt lds\n\tbuffer_load_dwordx4 %3, %5, %6 offen offset:2048 nt lds\n\t" \
               "buffer_load_dwordx4 %4, %5, %6 offen offset:3072 nt lds" ::"s"(m), "v"(v0), "v"(v1), "v"(v2), "v"(v3), "s"(srd), "s"(so) : "memory")

__global__ void __launch_bounds__(512, 2)
fused_block(const u16* __restrict__ Kc, const u16* __restrict__ Vc, const u16* __restrict__ wmod, const u16* __restrict__ wq,
            const u16* __restrict__ wo, const u16* __restrict__ w1, const u16* __restrict__ w2, const u16* __restrict__ xin,
            const u16* __restrict__ cin, u16* __restrict__ out, unsigned char* ws) {
  __shared__ __attribute__((aligned(16))) unsigned char smem[L_END];
  asm volatile("" ::"s"((u32)(size_t)(LAS unsigned char*)smem));
  const int tid = threadIdx.x, lane = tid & 63, wave = __builtin_amdgcn_readfirstlane(tid >> 6);
  if (wave == 1 && blockIdx.x < 32) {
    u64 pc; asm volatile("s_getpc_b64 %0" : "=s"(pc));
    const v4i rc = mksrd((const void*)((pc & ~127ull) + 640), CODE_BYTES);
    const u32 vo = lane * 128, m = L_JUNK, s1 = 8192, s2 = 16384;
    asm volatile("s_mov_b32 m0, %0\n\ts_nop 0\n\tbuffer_load_dword %1, %2, 0 offen lds\n\tbuffer_load_dword %1, %2, %3 offen lds\n\tbuffer_load_dword %1, %2, %4 offen lds"
                 ::"s"(m), "v"(vo), "s"(rc), "s"(s1), "s"(s2) : "memory");
  }
  const int bid = (blockIdx.x & ~31) | ((blockIdx.x & 7) << 2) | ((blockIdx.x >> 3) & 3);
  const int grp = bid >> 4, r = bid & 15;
  const int g = lane >> 4, c = lane & 15;
#define BLKADDR(r_, j_) (((u32)(grp * 1024 + ((((r_) >> 2) << 8) | ((j_) << 2) | (((r_) ^ (j_)) & 3))) << 13) | ((grp & 2) << 11))
  const bool isS = r >= 12;
  const u32 k0 = g * 256 + ((c ^ g) << 4);
  if (__builtin_expect(wave == 0, 1)) {
    const v4i rk = mksrd(Kc, 1u << 27), srd = isS ? mksrd(wmod, 512 * 6144) : rk;
    const u32 w0 = (lane >> 3) * 6144 + (((lane & 7) ^ ((lane >> 3) & 6)) << 4), k1 = k0 ^ 64, k2 = k0 ^ 128, k3 = k0 ^ 192;
    const u32 a0 = isS ? w0 : k0, a1 = isS ? w0 + 48128 : k1, a2 = isS ? w0 + 2 * 48128 : k2, a3 = isS ? w0 + 3 * 48128 : k3;
    const u32 sk = BLKADDR(r, 0), sw = (r - 12) * 128 * 6144 + grp * 128;
#pragma unroll
    for (int i = 0; i < 4; ++i) { const u32 m = i * 4096, s1 = isS ? sw + i * 32 * 6144 : sk ^ ((i & 1) * 4096 + (i >> 1) * (32768 + 8192)); DMA4(m, a0, a1, a2, a3, srd, s1); }
#pragma unroll
    for (int i = 4; i < 8; ++i) { const u32 m = i * 4096, s1 = sk ^ ((i & 1) * 4096 + (i >> 1) * (32768 + 8192)); DMA4(m, k0, k1, k2, k3, rk, s1); }
  }
  if (wave == 2) { stsv(L_CTL + 4 * lane, 0); WAITL(); }
  __builtin_amdgcn_s_barrier(); FENCE();
  const u64 tstart = __builtin_amdgcn_s_memrealtime();
  FENCE();
  const bool isX = r < 4, isQ = r >= 4 && r < 8, isM = r == 8 || r == 9, isY = (r == 10 || r == 11) && grp < 14, isR = r == 0 && (grp & 1);
  const int xi = grp * 4 + r, qi = grp * 4 + r - 4, mi = grp * 2 + r - 8, yi = grp * 2 + r - 10;
  const int u0 = (int)(0x2222008888880000ull >> (4 * r)) & 15;
  const int nb = 63 + ((int)(NB63 >> (4 * r)) & 15);
  const int nf = isS ? 0 : NF;
  const int ntail = isX ? 8 : isY ? 16 : 0;
  const int uv = u0 + nb, uw = uv + nb;
  const int vrot = max(uv - 64, 0);
  auto kblock = [&](int k) { return k < nf ? k : k + u0 < nb ? k + u0 : k + u0 - nb + nf; };
  auto vblock = [&](int k) { return k + vrot < nb ? k + vrot : k + vrot - nb; };
  u64 E = 0;
  auto arrive = [&](int w) { if (lane == 0) __hip_atomic_fetch_add(lp<u32>(L_CTL + 4 * w), 1u, __ATOMIC_RELAXED, __HIP_MEMORY_SCOPE_WORKGROUP); };
  int wv = wave;
  asm volatile("" : "+s"(wv));
  if (__builtin_expect(wv == 0, 1)) {
    __builtin_amdgcn_s_setprio(3);
    int iss, pub = 0;
    auto publish = [&](int v) { pub = max(pub, v); stsv(L_CTL + 4 * C_FULL, pub); };
#define PACE(W_) do { WAITV(W_); publish(iss - (W_)); } while (0)
    const u32 cls = ((u32)(0x6666443333331000ull >> (4 * r)) & 7) + ((r == 3 && (grp & 1)) || ((r >> 1) == 5 && grp >= 14));
    const u32 tau = 384 + ((u32)(PACES >> (8 * cls)) & 255);
    stsv(L_CTL + 4 * C_T0, (u32)tstart + COFF);
    const bool lead = u0 == 8;
    const u32 rs = lead ? (isQ ? 8192 : 6144) : isX ? 1024 : 3584;
    const u32 wcol = lead ? (isQ ? qi : 16 + mi) * 128 : isX ? ((xi >> 3) << 19) + (xi & 7) * 128 : yi * 128;
    const int wb = lead ? NF : uw, dn = (int)(DONOR >> (4 * r)) & 15, ue = uw + (ntail >> 1);
    u32 ta[3], tb[3];
#pragma unroll
    for (int k = 0; k < 3; ++k) {
      const int uu = 64 * k + lane, j = uu < uv ? kblock(uu - u0) : vblock(uu - uv);
      const bool w = (u32)(uu - wb) < 8;
      const u32 kvo = j < 64 ? BLKADDR(r, j) : BLKADDR(dn, 63), wo_ = wcol + (uu - wb) * 64 * rs;
      ta[k] = w ? wo_ : kvo; tb[k] = w ? wo_ + 32 * rs : kvo ^ 4096;
    }
    const u32 w0 = (lane >> 3) * rs + (((lane & 7) ^ ((lane >> 3) & 6)) << 4), wd = 8 * rs - 1024, v0 = g * 256 + ((c ^ (2 * g)) << 4);
    const v4u aw = {w0, w0 + wd, w0 + 2 * wd, w0 + 3 * wd}, ak = {k0, k0 ^ 64, k0 ^ 128, k0 ^ 192}, av = {v0, v0 ^ 128, v0, v0 ^ 128};
    const v4i sw = mksrd(lead ? (isQ ? wq : wmod) : isX ? wo : w1, isX ? 4096 * 1024 : 512 * rs), sk = mksrd(Kc, 1u << 27), sv = mksrd(Vc, 1u << 27);
    v4u A0 = lead ? aw : ak, A1 = lead ? ak : av, A2 = lead ? av : aw;
    v4i S0 = lead ? sw : sk, S1 = lead ? sk : sv, S2 = lead ? sv : sw;
    int u = NF, e1 = lead ? NF + 8 : uv, e2 = lead ? uv : uw, ev = min(e1, 64), la = LA * 16;
    const int lx = isX ? 0x7fffffff : LA * 16;
    const u32 un = isX ? 0x10000000 : 0;
    u32 dd = NF * tau, t0, t1, s0, s1, lag, vf, vb, vt;
    if (isS) {
      asm volatile("" ::"v"(ta[0]), "v"(tb[0]), "v"(ta[1]), "v"(tb[1]), "v"(ta[2]), "v"(tb[2]), "v"(A0), "v"(A1), "v"(A2));
      WAITV(16); publish(16);
    }
#define DMAS(so) "buffer_load_dwordx4 v100, s[88:91], " so " offen nt lds\n\tbuffer_load_dwordx4 v101, s[88:91], " so " offen offset:1024 nt lds\n\t" \
                 "buffer_load_dwordx4 v102, s[88:91], " so " offen offset:2048 nt lds\n\tbuffer_load_dwordx4 v103, s[88:91], " so " offen offset:3072 nt lds\n\t"
#define RDFREE "s_lshl_b32 %[t1], %[u], 2\n\ts_and_b32 %[t1], %[t1], 60\n\ts_or_b32 %[t1], %[t1], %[fb]\n\tv_mov_b32 %[vf], %[t1]\n\tds_read_b32 %[vf], %[vf]\n\t"
#define RDTIME "ds_read_b32 %[vb], %[va] offset:%[ot]\n\ts_memrealtime s[98:99]\n\t"
#define REST(W) "s_waitcnt vmcnt(" #W ")\n\ts_mov_b32 m0, %[t0]\n\ts_lshl_b32 %[t0], %[u], 3\n\t" DMAS("%[s1]")                                    \
                "s_sub_i32 %[t0], %[t0], " #W " - 4\n\ts_max_i32 %[pub], %[pub], %[t0]\n\tv_mov_b32 %[vt], %[pub]\n\tds_write_b32 %[va], %[vt]\n\t" \
                "s_add_i32 %[u], %[u], 1\n\ts_add_u32 %[dd], %[dd], %[tau]\n\t" RDFREE RDTIME                                                       \
                "s_cmp_lg_u32 %[u], %[ev]\n\ts_waitcnt vmcnt(" #W ")\n\ts_cbranch_scc1 1b\n\ts_branch 10f\n"
#define XREST(W) REST(W)
    asm volatile(RDFREE RDTIME "\n"
                 "1:\n\ts_and_b32 %[t0], %[u], 63\n\tv_readlane_b32 %[s0], %[ta0], %[t0]\n\tv_readlane_b32 %[s1], %[tb0], %[t0]\n\t"
                 "s_sub_i32 %[t1], %[u], 15\n\ts_waitcnt lgkmcnt(0)\n\tv_cmp_gt_i32 vcc, %[t1], %[vf]\n\tv_readfirstlane_b32 s99, %[vb]\n\t"
                 "s_cbranch_vccnz 8f\n"
                 "2:\n\ts_sub_i32 %[lag], s98, s99\n\ts_lshl_b32 %[lag], %[lag], 4\n\ts_sub_i32 %[lag], %[lag], %[dd]\n\ts_cmp_lt_i32 %[lag], 0\n\ts_cbranch_scc1 7f\n\t"
                 "s_lshl_b32 %[t0], %[u], 13\n\ts_and_b32 %[t0], %[t0], 0x1e000\n\ts_mov_b32 m0, %[t0]\n\ts_or_b32 %[t0], %[t0], 0x1000\n\t" DMAS("%[s0]")
                 "s_cmp_lt_i32 %[lag], %[la]\n\ts_cbranch_scc1 4f\n\ts_cmp_lt_i32 %[lag], %[lb]\n\ts_cbranch_scc1 5f\n\t"
                 "s_cmp_lt_i32 %[lag], %[lc]\n\ts_cbranch_scc1 3f\n\ts_cmp_lt_i32 %[lag], %[ld]\n\ts_cbranch_scc1 13f\n\t"
                 XREST(WE) "13:\n\t" XREST(WD) "3:\n\t" XREST(WC) "4:\n\t" XREST(WA) "5:\n\t" XREST(WB)
                 "8:\n\t" RDFREE "s_sub_i32 %[t1], %[u], 15\n\ts_waitcnt lgkmcnt(0)\n\tv_cmp_gt_i32 vcc, %[t1], %[vf]\n\ts_nop 0\n\ts_cbranch_vccz 7f\n\t"
                 "s_waitcnt vmcnt(0)\n\ts_lshl_b32 %[pub], %[u], 3\n\tv_mov_b32 %[vt], %[pub]\n\tds_write_b32 %[va], %[vt]\n"
                 "6:\n\t" RDFREE "s_sub_i32 %[t1], %[u], 15\n\ts_waitcnt lgkmcnt(0)\n\tv_cmp_gt_i32 vcc, %[t1], %[vf]\n\ts_sleep 1\n\ts_cbranch_vccnz 6b\n"
                 "7:\n\ts_memrealtime s[98:99]\n\ts_waitcnt lgkmcnt(0)\n\tv_readfirstlane_b32 s99, %[vb]\n\ts_branch 2b\n"
                 "10:\n\ts_cmp_eq_u32 %[u], %[ue]\n\ts_cbranch_scc1 9f\n\t"
                 "s_and_b32 %[t0], %[u], 63\n\ts_cbranch_scc1 11f\n\t"
                 "v_mov_b32 %[ta0], %[ta1]\n\tv_mov_b32 %[tb0], %[tb1]\n\tv_mov_b32 %[ta1], %[ta2]\n\tv_mov_b32 %[tb1], %[tb2]\n"
                 "11:\n\ts_cmp_lg_u32 %[u], %[e1]\n\ts_cbranch_scc1 12f\n\t"
                 "s_mov_b64 s[88:89], s[92:93]\n\ts_mov_b64 s[90:91], s[94:95]\n\ts_mov_b64 s[92:93], s[84:85]\n\ts_mov_b64 s[94:95], s[86:87]\n\t"
                 "v_mov_b64 v[100:101], v[104:105]\n\tv_mov_b64 v[102:103], v[106:107]\n\tv_mov_b64 v[104:105], v[108:109]\n\tv_mov_b64 v[106:107], v[110:111]\n\t"
                 "s_mov_b32 %[e1], %[e2]\n\ts_mov_b32 %[e2], %[ue]\n\t"
                 "s_cmp_lg_u32 %[u], %[uw]\n\ts_cbranch_scc1 12f\n\t"
                 "s_sub_u32 %[dd], %[dd], %[un]\n\ts_mov_b32 %[la], %[lx]\n\tv_mov_b32 %[vt], 1\n\tds_write_b32 %[va], %[vt] offset:%[ov]\n"
                 "12:\n\ts_or_b32 %[t0], %[u], 63\n\ts_add_i32 %[t0], %[t0], 1\n\ts_min_i32 %[ev], %[e1], %[t0]\n\ts_branch 1b\n"
                 "9:\n\ts_waitcnt lgkmcnt(0)"
                 : [u] "+&s"(u), [dd] "+&s"(dd), [pub] "+&s"(pub), [e1] "+&s"(e1), [e2] "+&s"(e2), [ev] "+&s"(ev), [la] "+&s"(la),
                   [ta0] "+&v"(ta[0]), [tb0] "+&v"(tb[0]), [ta1] "+&v"(ta[1]), [tb1] "+&v"(tb[1]),
                   "+{v[100:103]}"(A0), "+{v[104:107]}"(A1), "+{s[88:91]}"(S0), "+{s[92:95]}"(S1),
                   [t0] "=&s"(t0), [t1] "=&s"(t1), [s0] "=&s"(s0), [s1] "=&s"(s1), [lag] "=&s"(lag), [vf] "=&v"(vf), [vb] "=&v"(vb), [vt] "=&v"(vt)
                 : [ta2] "v"(ta[2]), [tb2] "v"(tb[2]), "{v[108:111]}"(A2), "{s[84:87]}"(S2), [ue] "s"(ue), [uw] "s"(uw), [lx] "s"(lx), [un] "s"(un), [tau] "s"(tau),
                   [va] "v"(L_CTL + 4 * C_FULL), [fb] "i"(L_CTL + 4 * C_FREE), [ot] "i"(4 * C_T0), [ov] "i"(4 * C_VISS), [lb] "i"(LB * 16), [lc] "i"(LC * 16), [ld] "i"(LD * 16)
                 : "memory", "vcc", "scc", "s98", "s99");
    iss = 8 * u;
    if (!ntail) { PACE(24); PACE(16); PACE(8); PACE(0); }
    else arrive(C_ISS);
    __builtin_amdgcn_s_setprio(0);
  } else {
    const rsrc_t rws = __builtin_amdgcn_make_buffer_rsrc(ws, 0, W_END, 0x00020000);
    auto wait_full = [&](u32 need) { while (ldsv(L_CTL + 4 * C_FULL) < need) __builtin_amdgcn_s_sleep(1); FENCE(); };
    const u32 trow = 4 * g + (c >> 2);
    auto waddr = [&](int t) -> u32 { return trow * 128 + ((((u32)(t ^ ((trow >> 1) & 3)) << 1) | ((c >> 1) & 1)) << 4) + (c & 1) * 8; };

    if (wave == 1) {
      const u64 E0 = __hip_atomic_load((GAS u64*)(ws + W_EPOCH), __ATOMIC_RELAXED, __HIP_MEMORY_SCOPE_AGENT);
      u32 tag;
#define CHAIN_START() do { E = E0 + 1; tag = (u32)E; if ((blockIdx.x >> 3) == 5 && lane == 0) st2(v2u{(u32)tstart, tag}, rws, (blockIdx.x & 7) * 8, W_T0); } while (0)
      bool have_t0 = false;
      auto clock = [&]() {
        const v2u t = ld2(rws, (lane & 7) * 8, W_T0);
        if (__all(t.y == tag)) {
          int dt = (int)(t.x - (u32)tstart);
          dt += __builtin_amdgcn_mov_dpp(dt, 0xB1, 0xf, 0xf, true); dt += __builtin_amdgcn_mov_dpp(dt, 0x4E, 0xf, 0xf, true); dt += __builtin_amdgcn_mov_dpp(dt, 0x141, 0xf, 0xf, true);
          if (dt > -2400 && dt < 2400) stsv(L_CTL + 4 * C_T0, ((u32)tstart + (dt >> 3) + COFF1));
          have_t0 = true;
        }
      };
      auto silu4 = [&](u32 x, u32 y) { return v2u{pk2(silu_exact(lo16(x)), silu_exact(bfhi(x))), pk2(silu_exact(lo16(y)), silu_exact(bfhi(y)))}; };
      if (isS) {
        const v2u cv = *(const v2u*)(cin + (lane >> 5) * 512 + (r - 12) * 128 + (lane & 31) * 4);
        CHAIN_START();
        *lp<v2u>(L_ACT + (lane >> 5) * ASTR + fpos((lane & 31) * 4) * 2) = silu4(cv.x, cv.y);
        FENCE();
        v8bf act[4];
#pragma unroll
        for (int ks = 0; ks < 4; ++ks) { act[ks] = ldfrag(L_ACT + (c & 1) * ASTR + g * 16 + ks * 64); PIN(act[ks]); }
        wait_full(16);
        v8bf W[16];
#pragma unroll
        for (int j = 0; j < 16; ++j) W[j] = trfrag(waddr(j & 3) + (j >> 2) * 4096);
        FENCE(); stsv(L_CTL + 4 * C_FREE, 1); stsv(L_CTL + 4 * (C_FREE + 1), 2);
        v4f acc[4];
#pragma unroll
        for (int t = 0; t < 4; ++t) acc[t] = v4f{0.f, 0.f, 0.f, 0.f};
#pragma unroll
        for (int j = 0; j < 16; ++j) acc[j & 3] = mfma(act[j >> 2], W[j], acc[j & 3]);
        const float r0 = g == 0 ? acc[0][0] : g == 1 ? acc[1][0] : g == 2 ? acc[2][0] : acc[3][0];
        const float r1 = g == 0 ? acc[0][1] : g == 1 ? acc[1][1] : g == 2 ? acc[2][1] : acc[3][1];
        while (ldsv(L_CTL + 4 * C_CLR) == 0) { }
        FENCE();
        st4(v4u{__float_as_uint(r0), tag, __float_as_uint(r1), tag}, rws, lane * 16, W_SP + ((r - 12) * 1024 + grp * 64) * 16);
      } else if (isR) {
        const v4u in0 = *(const v4u*)(xin + lane * 8), in1 = *(const v4u*)(xin + 512 + lane * 8);
        u32 xs0 = *(const u32*)(xin + (grp >> 1) * 64 + (lane & ~1)), xs1 = *(const u32*)(xin + 512 + (grp >> 1) * 64 + (lane & ~1));
        CHAIN_START();
        PIN(xs0); PIN(xs1);
        xs0 >>= 16 * (lane & 1); xs1 >>= 16 * (lane & 1);
        float mr[2], ri[2];
#pragma unroll
        for (int b = 0; b < 2; ++b) {
          const v4u xv = b ? in1 : in0;
          const float x[8] = {lo16(xv.x), bfhi(xv.x), lo16(xv.y), bfhi(xv.y), lo16(xv.z), bfhi(xv.z), lo16(xv.w), bfhi(xv.w)};
          float s = 0.f;
#pragma unroll
          for (int i = 0; i < 8; ++i) s += x[i];
          const float mean = wave_sum(s) * (1.f / 512.f);
          float q2 = 0.f;
#pragma unroll
          for (int i = 0; i < 8; ++i) q2 += (x[i] - mean) * (x[i] - mean);
          const float var = wave_sum(q2) * (1.f / 512.f);
          mr[b] = rb(mean); ri[b] = rcp(rb(__builtin_amdgcn_sqrtf(rb(rb(var) + 1e-5f))));
        }
        v2f y = rb2(rb2(v2f{lo16(xs0) - mr[0], lo16(xs1) - mr[1]}) * v2f{ri[0], ri[1]});
        PIN(y);
        v4u a[8];
#pragma nounroll
        for (int sp = 0; sp < SPIN_CAP; ++sp) {
          u32 bad = 0;
#pragma unroll
          for (int i = 0; i < 8; ++i) { a[i] = ld4(rws, lane * 16, W_SP + ((i & 3) * 1024 + (i >> 2) * 512 + (grp >> 1) * 64) * 16); bad |= (a[i].y ^ tag) | (a[i].w ^ tag); }
          if (__all(bad == 0)) break;
          FENCE();
        }
        v2f sc[2];
#pragma unroll
        for (int pt = 0; pt < 2; ++pt) {
          sc[pt] = v2f{0.f, 0.f};
#pragma unroll
          for (int k4 = 0; k4 < 4; ++k4) sc[pt] += v2f{__uint_as_float(a[pt * 4 + k4].x), __uint_as_float(a[pt * 4 + k4].z)};
        }
        const v2f h = rb2(y * rb2(rb2(sc[1]) + 1.f)) + rb2(sc[0]);
        st2(v2u{pk2(h.x, h.y), tag}, rws, lane * 8, W_HG + (grp >> 1) * 512);
      } else if (isM || isQ) {
        if (isM) {
          const v4u in0 = *(const v4u*)(cin + lane * 8), in1 = *(const v4u*)(cin + 512 + lane * 8);
          CHAIN_START();
          *lp<v2u>(L_ACT + fpos(lane * 8) * 2) = silu4(in0.x, in0.y); *lp<v2u>(L_ACT + fpos(lane * 8 + 4) * 2) = silu4(in0.z, in0.w);
          *lp<v2u>(L_ACT + ASTR + fpos(lane * 8) * 2) = silu4(in1.x, in1.y); *lp<v2u>(L_ACT + ASTR + fpos(lane * 8 + 4) * 2) = silu4(in1.z, in1.w);
        } else {
          CHAIN_START();
          while (__builtin_amdgcn_s_memrealtime() < tstart + T0AT) __builtin_amdgcn_s_sleep(4);
          clock();
          while (__builtin_amdgcn_s_memrealtime() < tstart + HDELAY) __builtin_amdgcn_s_sleep(4);
          v4u a[4];
#pragma nounroll
          for (int sp = 0; sp < SPIN_CAP; ++sp) {
            u32 bad = 0;
#pragma unroll
            for (int i = 0; i < 4; ++i) { a[i] = ld4(rws, lane * 16 + i * 1024, W_HG); bad |= (a[i].y ^ tag) | (a[i].w ^ tag); }
            if (__all(bad == 0)) break;
            FENCE();
          }
#pragma unroll
          for (int i = 0; i < 4; ++i) {
            const u32 wa = L_ACT + fpos(i * 128 + lane * 2) * 2;
            *lp<u32>(wa) = (a[i].x & 0xffffu) | (a[i].z << 16); *lp<u32>(wa + ASTR) = (a[i].x >> 16) | (a[i].z & 0xffff0000u);
          }
        }
        FENCE(); stsv(L_CTL + 4 * C_TAG, tag); stsv(L_CTL + 4 * C_ARDY, 1);
      } else CHAIN_START();
      if (!isQ) {
        while (__builtin_amdgcn_s_memrealtime() < tstart + T0AT) __builtin_amdgcn_s_sleep(4);
        clock();
      }
      {
        wait_full(8 * u0 + 16 * QAT);
        v4u a[2];
#pragma nounroll
        for (int sp = 0; sp < SPIN_CAP; ++sp) {
          a[0] = ld4(rws, lane * 16, W_QG + grp * 2048); a[1] = ld4(rws, lane * 16 + 1024, W_QG + grp * 2048);
          if (__all(((a[0].y ^ tag) | (a[0].w ^ tag) | (a[1].y ^ tag) | (a[1].w ^ tag)) == 0)) break;
          __builtin_amdgcn_s_sleep(16); FENCE();
        }
        *lp<v2u>(L_Q + lane * 8) = v2u{a[0].x, a[0].z}; *lp<v2u>(L_Q + 512 + lane * 8) = v2u{a[1].x, a[1].z};
        FENCE(); stsv(L_CTL + 4 * C_TAG, tag); stsv(L_CTL + 4 * C_QRDY, 1);
        if (!have_t0) clock();
      }
      if (ntail) {
        const v4i srd = mksrd(isX ? wo : w2, isX ? 4096 * 1024 : 1792 * 1024);
        const u32 a0 = (lane >> 3) * 1024 + (((lane & 7) ^ ((lane >> 3) & 6)) << 4), a1 = a0 + 7168, a2 = a0 + 2 * 7168, a3 = a0 + 3 * 7168;
        u32 so = isX ? ((xi >> 3) << 19) + (xi & 7) * 128 + 256 * 1024 : yi << 16;
        while (ldsv(L_CTL + 4 * C_VISS) == 0) __builtin_amdgcn_s_sleep(2);
        if (!isX) { const u64 tv = __builtin_amdgcn_s_memrealtime(); while (__builtin_amdgcn_s_memrealtime() < tv + YDL) __builtin_amdgcn_s_sleep(2); }
#pragma nounroll
        for (int u = uw + (ntail >> 1); u < uw + ntail; ++u, so += isX ? 65536 : 128) {
          while ((int)ldsv(L_CTL + 4 * (C_FREE + (u & (NSLOT - 1)))) < u + 1 - NSLOT) __builtin_amdgcn_s_sleep(1);
          const u32 lb = (u & (NSLOT - 1)) * SLOT, m = lb + 4096, s1 = so + 32768;
          DMA4(lb, a0, a1, a2, a3, srd, so);
          DMA4(m, a0, a1, a2, a3, srd, s1);
          WAITV(W2W);
        }
        arrive(C_ISS);
      }
      if (r == 8) {
        wait_full(8 * uw);
        const int zb = grp >> 3, zc = (grp & 7) * 64 + c * 4 + g;
        const u32 xv = xin[zb * 512 + zc];
        v2u m0, m1;
#pragma nounroll
        for (int sp = 0; sp < SPIN_CAP; ++sp) {
          m0 = ld2(rws, zc * 8, W_MG); m1 = ld2(rws, zc * 8, W_MG + 3 * 4096);
          if (__all(((m0.y ^ tag) | (m1.y ^ tag)) == 0)) break;
          FENCE();
        }
        v4u z[7];
#pragma nounroll
        for (int sp = 0; sp < SPIN_CAP; ++sp) {
          z[0] = ld4(rws, g * 4096 + c * 16, W_AP + grp * 256); z[1] = ld4(rws, g * 4096 + c * 16, W_AP + 16384 + grp * 256);
          if (__all(max(max(max(z[0].x, z[0].y), max(z[0].z, z[0].w)), max(max(z[1].x, z[1].y), max(z[1].z, z[1].w))) != SENT)) break;
          __builtin_amdgcn_s_sleep(2); FENCE();
        }
        v4f s0 = __builtin_bit_cast(v4f, z[0]), s1 = __builtin_bit_cast(v4f, z[1]);
        const float at = fold32(fold16(s0.x, s0.y), fold16(s0.z, s0.w)) + fold32(fold16(s1.x, s1.y), fold16(s1.z, s1.w));
        float x1 = rb(lo16(xv) + rb(bfhi(m0.x << (16 - 16 * zb)) * rb(at))), gb = bfhi(m1.x << (16 - 16 * zb));
        GAS u16* op = (GAS u16*)out + zb * 512 + zc;
        PIN(x1); PIN(gb); PIN(op);
#pragma nounroll
        for (int sp = 0; sp < SPIN_CAP; ++sp) {
          if (__all(ld2(rws, min(lane, 27) * 128, W_YF).x != SENT)) break;
          FENCE();
        }
        __builtin_amdgcn_s_sleep(ZW);
        FENCE();
#pragma nounroll
        for (int sp = 0; sp < SPIN_CAP; ++sp) {
#pragma unroll
          for (int j = 0; j < 7; ++j) z[j] = ld4(rws, g * 4096 + c * 16, W_OP + j * 16384 + grp * 256);
          u32 mx = 0;
#pragma unroll
          for (int j = 0; j < 7; ++j) mx = max(mx, max(max(z[j].x, z[j].y), max(z[j].z, z[j].w)));
          if (__all(mx != SENT)) break;
          FENCE();
        }
        const v4f s = ((__builtin_bit_cast(v4f, z[0]) + __builtin_bit_cast(v4f, z[1])) + (__builtin_bit_cast(v4f, z[2]) + __builtin_bit_cast(v4f, z[3]))) +
                      ((__builtin_bit_cast(v4f, z[4]) + __builtin_bit_cast(v4f, z[5])) + __builtin_bit_cast(v4f, z[6]));
        const float rr = x1 + rb(gb * rb(fold16(fold32(s.x, s.z), fold32(s.y, s.w))));
        *op = (u16)pk2(rr, rr);
        __builtin_amdgcn_endpgm();
      }
    } else {
      if (isS && wave == 7) {
        const v4u s4 = {SENT, SENT, SENT, SENT};
        const u32 so = W_PART + (grp * 4 + r - 12) * CLRB;
#pragma unroll
        for (int i = 0; i < CLRB / 1024; ++i) st4(s4, rws, lane * 16 + (i & 3) * 1024, so + (i >> 2) * 4096);
        if (lane < (CLRB % 1024) / 16) st4(s4, rws, lane * 16, so + CLRB / 1024 * 1024);
        WAITV(0); stsv(L_CTL + 4 * C_CLR, 1);
      }

      if (wave == 6) { *lp<v4f>(L_MRG + lane * 16) = v4f{0.f, 0.f, 0.f, 0.f}; *lp<v4f>(L_MRG + 1024 + lane * 16) = v4f{0.f, 0.f, 0.f, 0.f}; }
      const int d = c >> 2, cw = wave - 2;
      const u32 sca = L_SC + (g * 4 + (c & 3)) * 16;
      {
        u32 ka[4];
#pragma unroll
        for (int s = 0; s < 4; ++s) ka[s] = c * 256 + (((4 * s + g) ^ c) << 4);
        v8bf qf[4];
        float mx = -INFINITY;
        auto rdk = [&](v8bf (&kf)[8], int j) {
          const int u = j < nf ? j : u0 + j;
          const u32 lb = (u & (NSLOT - 1)) * SLOT;
          wait_full(8 * u + 8);
#pragma unroll
          for (int s = 0; s < 4; ++s) { kf[s] = ldfrag(lb + ka[s]); kf[4 + s] = ldfrag(lb + ka[s] + 4096); }
          FENCE(); stsv(L_CTL + 4 * (C_FREE + (u & (NSLOT - 1))), u + 1);
        };
        auto qk = [&](v8bf (&kf)[8], int j) {
          v4f S0 = {0.f, 0.f, 0.f, 0.f}, S1 = {0.f, 0.f, 0.f, 0.f};
#pragma unroll
          for (int s = 0; s < 4; ++s) { S0 = mfma(kf[s], qf[s], S0); S1 = mfma(kf[4 + s], qf[s], S1); }
          const v2f a = rb2(v2f{S0.x, S0.y}) * 0.08838834764831845f, b = rb2(v2f{S0.z, S0.w}) * 0.08838834764831845f;
          const v2f e = rb2(v2f{S1.x, S1.y}) * 0.08838834764831845f, f = rb2(v2f{S1.z, S1.w}) * 0.08838834764831845f;
          mx = fmaxf(fmaxf(fmaxf(mx, fmaxf(a.x, a.y)), fmaxf(b.x, b.y)), fmaxf(fmaxf(e.x, e.y), fmaxf(f.x, f.y)));
          if (c < 4) *lp<v4u>(sca + kblock(j) * 256) = v4u{pk2(a.x, a.y), pk2(b.x, b.y), pk2(e.x, e.y), pk2(f.x, f.y)};
        };
        v8bf sk[NSTASH][8];
        rdk(sk[0], cw);
        if (u0 == 8 && wave < 6) {
          wait_full(8 * NF + 64);
          v8bf W[16];
#pragma unroll
          for (int ks = 0; ks < 16; ++ks) W[ks] = trfrag(NF * SLOT + ks * 4096 + waddr(wave - 2));
          WAITL();
          u32 old = 0;
          if (lane == 0) old = __hip_atomic_fetch_add(lp<u32>(L_CTL + 4 * C_WRD), 1u, __ATOMIC_RELAXED, __HIP_MEMORY_SCOPE_WORKGROUP);
          if (__builtin_amdgcn_readfirstlane(old) == 3 && lane < 8) stsv(L_CTL + 4 * (C_FREE + NF + lane), NF + lane + 1);
          while (ldsv(L_CTL + 4 * C_ARDY) == 0) __builtin_amdgcn_s_sleep(1);
          FENCE();
          const u32 tag = ldsv(L_CTL + 4 * C_TAG);
          v8bf A[16];
#pragma unroll
          for (int ks = 0; ks < 16; ++ks) A[ks] = ldfrag(L_ACT + (c & 1) * ASTR + g * 16 + ks * 64);
          __builtin_amdgcn_sched_barrier(0);
          v4f d0 = {0.f, 0.f, 0.f, 0.f}, d1 = {0.f, 0.f, 0.f, 0.f};
#pragma unroll
          for (int ks = 0; ks < 16; ks += 2) { d0 = mfma(A[ks], W[ks], d0); d1 = mfma(A[ks + 1], W[ks + 1], d1); }
          const float r0 = d0[0] + d1[0], r1 = d0[1] + d1[1];
          const float n0 = dpp<0xB1>(r0), n1 = dpp<0xB1>(r1);
          const bool odd = lane & 1;
          if (lane < 16) {
            if (isM) st2(v2u{pk2(r0, r1), tag}, rws, lane * 8, W_MG + mi * 512 + (wave - 2) * 128);
            else st2(v2u{pk2(odd ? n1 : r0, odd ? r1 : n0), tag}, rws, (lane & 1) * 16384 + (lane >> 1) * 8, W_QG + qi * 256 + (wave - 2) * 64);
          }
        }
#pragma unroll
        for (int i = 1; i < NSTASH; ++i) rdk(sk[i], cw + i * NCONS);
        while (ldsv(L_CTL + 4 * C_QRDY) == 0) __builtin_amdgcn_s_sleep(2);
        FENCE();
#pragma unroll
        for (int s = 0; s < 4; ++s) qf[s] = ldfrag(L_Q + (c & 3) * 256 + (4 * s + g) * 16);
#pragma unroll
        for (int i = 0; i < NSTASH; ++i) qk(sk[i], cw + i * NCONS);
#pragma nounroll
        for (int j = cw + NSTASH * NCONS; j < nb; j += NCONS) { v8bf kf[8]; rdk(kf, j); qk(kf, j); }
        mx = swap32_max(swap16_max(rb(mx)));
        if (lane < 4) *lp<float>(L_ML + cw * 16 + lane * 4) = mx;
        FENCE();
        arrive(C_MX);
      }
      float M;
      {
        while (ldsv(L_CTL + 4 * C_MX) < NCONS) __builtin_amdgcn_s_sleep(1);
        FENCE();
        M = *lp<float>(L_ML + (c & 3) * 4);
#pragma unroll
        for (int w = 1; w < NCONS; ++w) M = fmaxf(M, *lp<float>(L_ML + w * 16 + (c & 3) * 4));
        float l = 0.f;
#pragma nounroll
        for (int j = cw + d * NCONS; j < nb; j += 4 * NCONS) {
          const v4u s = *lp<v4u>(sca + j * 256);
          float p[8];
#pragma unroll
          for (int i = 0; i < 4; ++i) { p[2 * i] = ex2((lo16(s[i]) - M) * 1.4426950408889634f); p[2 * i + 1] = ex2((bfhi(s[i]) - M) * 1.4426950408889634f); }
          l += ((p[0] + p[1]) + (p[2] + p[3])) + ((p[4] + p[5]) + (p[6] + p[7]));
          *lp<v4u>(sca + j * 256) = v4u{pk2(p[0], p[1]), pk2(p[2], p[3]), pk2(p[4], p[5]), pk2(p[6], p[7])};
        }
        l += dpp<0x124>(l); l += dpp<0x128>(l);
        l = swap32_add(swap16_add(l));
        if (lane < 4) *lp<float>(L_ML + 128 + cw * 16 + lane * 4) = l;
        FENCE();
        arrive(C_SM);
      }
      {
        u32 va[8];
#pragma unroll
        for (int t = 0; t < 8; ++t) va[t] = trow * 256 + ((((u32)(t ^ (trow & 7)) << 1) | ((c >> 1) & 1)) << 4) + (c & 1) * 8;
        v4f acc[8];
#pragma unroll
        for (int t = 0; t < 8; ++t) acc[t] = v4f{0.f, 0.f, 0.f, 0.f};
        while (ldsv(L_CTL + 4 * C_SM) < NCONS) __builtin_amdgcn_s_sleep(1);
        FENCE();
        int j = cw;
#pragma nounroll
        for (; j + NCONS < nb; j += NCONS) {
          const int u = uv + j;
          const u32 lb = (u & (NSLOT - 1)) * SLOT;
          wait_full(8 * u + 8);
          const v8bf pf = ldfrag(sca + vblock(j) * 256);
          v8bf vf[8];
#pragma unroll
          for (int t = 0; t < 8; ++t) {
            v4s lo = __builtin_amdgcn_ds_read_tr16_b64_v4i16(lp<v4s>(lb + va[t])), hi = __builtin_amdgcn_ds_read_tr16_b64_v4i16(lp<v4s>(lb + va[t] + 4096));
            vf[t] = __builtin_bit_cast(v8bf, __builtin_shufflevector(lo, hi, 0, 1, 2, 3, 4, 5, 6, 7));
          }
          FENCE(); stsv(L_CTL + 4 * (C_FREE + (u & (NSLOT - 1))), u + 1);
#pragma unroll
          for (int t = 0; t < 8; ++t) acc[t] = mfma(vf[t], pf, acc[t]);
        }
        const int link = j - (nb - NCONS), u = uv + j;
        const u32 lb = (u & (NSLOT - 1)) * SLOT;
        v4f e0 = d == 0 ? acc[0] : d == 1 ? acc[2] : d == 2 ? acc[4] : acc[6];
        v4f e1 = d == 0 ? acc[1] : d == 1 ? acc[3] : d == 2 ? acc[5] : acc[7];
        const v4u pf = *lp<v4u>(sca + vblock(j) * 256);
        v4u pm[4];
#pragma unroll
        for (int i = 0; i < 4; ++i) { pm[i] = d == i ? pf : v4u{0u, 0u, 0u, 0u}; PIN(pm[i]); }
        float inv = 1.f, L = 0.f;
        if (link == NCONS - 1) {
          L = *lp<float>(L_ML + 128 + (c & 3) * 4);
#pragma unroll
          for (int w = 1; w < NCONS; ++w) L += *lp<float>(L_ML + 128 + w * 16 + (c & 3) * 4);
          inv = rcp(L);
        }
        v2u ml = {canon(M), canon(L)};
        PIN(ml);
        u32 oa = (c & 3) * 4096 + d * 64 + g * 16, ra = L_MRG + lane * 16;
        u32 vb[8];
#pragma unroll
        for (int t = 0; t < 8; ++t) { vb[t] = lb + va[t]; PIN(vb[t]); }
        PIN(e0); PIN(e1); PIN(oa); PIN(ra); PIN(inv);
        if (link == NCONS - 1) {
          while (ldsv(L_CTL + 4 * C_FULL) < (u32)(8 * u + 8 - HEAD)) { }
          if (lane < 4) st2(ml, rws, lane * 8, W_ML + bid * 128);
        }
        while (ldsv(L_CTL + 4 * C_FULL) < (u32)(8 * u + 8)) { }
        FENCE();
        v8bf vf[8];
#pragma unroll
        for (int t = 0; t < 8; ++t) {
          v4s lo = __builtin_amdgcn_ds_read_tr16_b64_v4i16(lp<v4s>(vb[t])), hi = __builtin_amdgcn_ds_read_tr16_b64_v4i16(lp<v4s>(vb[t] + 4096));
          vf[t] = __builtin_bit_cast(v8bf, __builtin_shufflevector(lo, hi, 0, 1, 2, 3, 4, 5, 6, 7));
        }
        __builtin_amdgcn_sched_barrier(0);
#pragma unroll
        for (int i = 0; i < 4; ++i) { e0 = mfma(vf[2 * i], __builtin_bit_cast(v8bf, pm[i]), e0); e1 = mfma(vf[2 * i + 1], __builtin_bit_cast(v8bf, pm[i]), e1); }
        __builtin_amdgcn_sched_barrier(0);
        v4f r0, r1;
        for (;;) {
          const u32 cnt = ldsv(L_CTL + 4 * C_MCNT);
          FENCE();
          r0 = *lp<v4f>(ra); r1 = *lp<v4f>(ra + 1024);
          FENCE();
          if (cnt >= (u32)link) break;
        }
        e0 = (e0 + r0) * inv; e1 = (e1 + r1) * inv;
        if (link == NCONS - 1) {
          st4(v4u{min(pk2(e0.x, e0.y), SENT - 1), min(pk2(e0.z, e0.w), SENT - 1), min(pk2(e1.x, e1.y), SENT - 1), min(pk2(e1.z, e1.w), SENT - 1)}, rws, oa, W_PART + grp * 16384 + r * 256);
        } else {
          *lp<v4f>(ra) = e0; *lp<v4f>(ra + 1024) = e1;
          FENCE(); stsv(L_CTL + 4 * C_MCNT, link + 1);
        }
        stsv(L_CTL + 4 * (C_FREE + (u & (NSLOT - 1))), u + 1);
      }
    }
  }
  if (!(isX || isY)) return;
  const u32 trow = 4 * g + (c >> 2);
  auto waddr = [&](int t) -> u32 { return trow * 128 + ((((u32)(t ^ ((trow >> 1) & 3)) << 1) | ((c >> 1) & 1)) << 4) + (c & 1) * 8; };
  auto wslot = [&](int t) -> u32 { return ((uw + t) & (NSLOT - 1)) * SLOT; };
  const int half = lane >> 5, i32 = lane & 31;
  const u32 fa = L_ACT + (c & 1) * ASTR + g * 16;
  {
    const rsrc_t rws = __builtin_amdgcn_make_buffer_rsrc(ws, 0, W_END, 0x00020000);
    auto weights_done = [&]() { WAITV(0); if (wave == 0) stsv(L_CTL + 4 * C_FULL, 8 * (uw + ntail)); arrive(C_WIN); };
    auto weights_in = [&]() { while (ldsv(L_CTL + 4 * C_WIN) < 2) __builtin_amdgcn_s_sleep(1); FENCE(); };
    if (isX) {
      const int kvh = xi >> 3, ng = xi & 7;
      const int b = wave >> 2, h = wave & 3;
      u32 wa = L_ACT + b * ASTR + (h * 128 + c * 8 + g * 2) * 2, wb[8];
#pragma unroll
      for (int i = 0; i < 8; ++i) { wb[i] = wslot(i) + waddr(wave & 3); PIN(wb[i]); }
      u32 vo = lane * 16 + W_PART + (b * 8 + kvh) * 16384 + h * 4096;
      u32 vl = ((c & 3) * 4 + ((g + (c >> 2)) & 3)) * 128 + W_ML + (b * 8 + kvh) * 2048 + h * 8;
      u32 vs = c * 2048 + g * 16 + W_AP + kvh * 4096 + ng * 256 + (wave & 3) * 64, fb = fa;
      PIN(wa); PIN(vo); PIN(vl); PIN(vs); PIN(fb);
      while (ldsv(L_CTL + 4 * C_ISS) < 2) __builtin_amdgcn_s_sleep(1);
      FENCE();
      v2u ls = ld2(rws, vl, 0);
      if (wave < 2) weights_done();
      if (!__all(max(ls.x, ls.y) != SENT)) {
#pragma nounroll
        for (int sp = 0; sp < SPIN_CAP; ++sp) {
          __builtin_amdgcn_s_sleep(1); FENCE();
          ls = ld2(rws, vl, 0);
          if (__all(max(ls.x, ls.y) != SENT)) break;
        }
        __builtin_amdgcn_s_sleep(XW);
      }
      FENCE();
      v4u a[4];
#pragma unroll
      for (int j = 0; j < 4; ++j) a[j] = ld4(rws, vo + j * 1024, 0);
      __builtin_amdgcn_sched_barrier(0);
      v8bf W[16];
      if (wave >= 4) {
        weights_in();
#pragma unroll
        for (int ks = 0; ks < 16; ++ks) W[ks] = trfrag(wb[ks >> 1] + (ks & 1) * 4096);
      }
      float M = __uint_as_float(ls.x);
      M = fmaxf(M, dpp<0xB1>(M)); M = fmaxf(M, dpp<0x4E>(M)); M = fmaxf(M, dpp<0x141>(M)); M = fmaxf(M, dpp<0x128>(M));
      float cf = ex2((__uint_as_float(ls.x) - M) * 1.4426950408889634f) * __uint_as_float(ls.y);
      cf *= rcp(row_sum(cf));
      float cj[4] = {dpp<0x150>(cf), dpp<0x151>(cf), dpp<0x152>(cf), dpp<0x153>(cf)};
      PIN(cj[0]); PIN(cj[1]); PIN(cj[2]); PIN(cj[3]);
#pragma nounroll
      for (int sp = 0; sp < SPIN_CAP; ++sp) {
        u32 mx = 0;
#pragma unroll
        for (int j = 0; j < 4; ++j) mx = max(max(mx, max(a[j].x, a[j].y)), max(a[j].z, a[j].w));
        if (__all(mx != SENT)) break;
        FENCE();
#pragma unroll
        for (int j = 0; j < 4; ++j) a[j] = ld4(rws, vo + j * 1024, 0);
      }
      v2f o[4];
#pragma unroll
      for (int i = 0; i < 4; ++i) {
        o[i] = v2f{lo16(a[0][i]), bfhi(a[0][i])} * cj[0];
#pragma unroll
        for (int j = 1; j < 4; ++j) o[i] += v2f{lo16(a[j][i]), bfhi(a[j][i])} * cj[j];
      }
      *lp<u32>(wa) = pk2(fold16(fold32(o[0].x, o[2].x), fold32(o[1].x, o[3].x)), fold16(fold32(o[0].y, o[2].y), fold32(o[1].y, o[3].y)));
      bar();
      if (wave >= 4) {
        v8bf A[16];
#pragma unroll
        for (int ks = 0; ks < 16; ++ks) A[ks] = ldfrag(fb + ks * 64);
        __builtin_amdgcn_sched_barrier(0);
        v4f d0 = {0.f, 0.f, 0.f, 0.f};
#pragma unroll
        for (int ks = 0; ks < 16; ++ks) d0 = mfma(W[ks], A[ks], d0);
        v4u ov = {canon(d0.x), canon(d0.y), canon(d0.z), canon(d0.w)};
        if (c < 2) st4(ov, rws, vs, 0);
      }
    } else {
      const int b = wave >> 2, col = (wave & 3) * 128 + i32 * 4 + half * 2;
      u32 wb[8];
#pragma unroll
      for (int i = 0; i < 8; ++i) { wb[i] = wave >= 4 ? wslot(i) + waddr(wave & 3) : wslot(8 + 2 * wave + (i >> 2)) + waddr(i & 3); PIN(wb[i]); }
      u32 wa = L_ACT + b * ASTR + fpos(col) * 2, ra = L_RED + wave * 8 + half * 4, rb_ = L_RED + b * 32;
      u32 ha = L_H1 + (c & 1) * 128 + fpos((wave & 3) * 16 + g * 4 + ((c >> 1) & 3)) * 2, fb = wave >= 4 ? fa : L_H1 + (c & 1) * 128 + g * 16;
      u32 vo = half * 16384 + i32 * 16 + W_AP + b * 2048 + (wave & 3) * 512, vs = (c & 1) * 2048 + (c >> 1) * 64 + g * 16 + W_OP + yi * 4096 + wave * 512;
      PIN(wa); PIN(ra); PIN(rb_); PIN(ha); PIN(fb); PIN(vo); PIN(vs);
      v2f x0, ga, sb, cb1;
      {
        const u32 xv = *(const u32*)(xin + b * 512 + col);
        const u32 tag = ldsv(L_CTL + 4 * C_TAG);
        v4u mg[3];
#pragma nounroll
        for (int sp = 0; sp < SPIN_CAP; ++sp) {
#pragma unroll
          for (int i = 0; i < 3; ++i) mg[i] = ld4(rws, col * 8, W_MG + i * 4096);
          if (__all(((mg[0].y ^ tag) | (mg[0].w ^ tag) | (mg[1].y ^ tag) | (mg[1].w ^ tag) | (mg[2].y ^ tag) | (mg[2].w ^ tag)) == 0)) break;
          FENCE();
        }
        const int sh = 16 - 16 * b;
        x0 = v2f{lo16(xv), bfhi(xv)};
        ga = v2f{bfhi(mg[0].x << sh), bfhi(mg[0].z << sh)}; sb = v2f{bfhi(mg[1].x << sh), bfhi(mg[1].z << sh)};
        cb1 = rb2(v2f{bfhi(mg[2].x << sh), bfhi(mg[2].z << sh)} + 1.f);
      }
      PIN(x0); PIN(ga); PIN(sb); PIN(cb1);
      if (wave < 2) weights_done();
      weights_in();
      v4u a[4];
#pragma unroll
      for (int j = 0; j < 4; ++j) a[j] = ld4(rws, vo, j * 4096);
      __builtin_amdgcn_sched_barrier(0);
      v8bf W[16];
#pragma unroll
      for (int j = 0; j < 16; ++j) W[j] = trfrag(wb[j >> 1] + (j & 1) * 4096);
#pragma nounroll
      for (int sp = 0; sp < SPIN_CAP; ++sp) {
        u32 mx = 0;
#pragma unroll
        for (int j = 0; j < 4; ++j) mx = max(max(mx, max(a[j].x, a[j].y)), max(a[j].z, a[j].w));
        if (__all(mx != SENT)) break;
        FENCE();
#pragma unroll
        for (int j = 0; j < 4; ++j) a[j] = ld4(rws, vo, j * 4096);
      }
      const v4f at = (__builtin_bit_cast(v4f, a[0]) + __builtin_bit_cast(v4f, a[1])) + (__builtin_bit_cast(v4f, a[2]) + __builtin_bit_cast(v4f, a[3]));
      const v2f x1 = rb2(x0 + rb2(ga * rb2(v2f{fold32(at.x, at.z), fold32(at.y, at.w)})));
      const float st = half_sum(fold32(x1.x + x1.y, fmaf(x1.x, x1.x, x1.y * x1.y)));
      if (i32 == 0) *lp<float>(ra) = st;
      bar();
      {
        const v4f r0 = *lp<v4f>(rb_), r1 = *lp<v4f>(rb_ + 16);
        const float mean = ((r0.x + r0.z) + (r1.x + r1.z)) * (1.f / 512.f);
        const float var = fmaxf(fmaf(-mean, mean, ((r0.y + r0.w) + (r1.y + r1.w)) * (1.f / 512.f)), 0.f);
        const v2f mv = rb2(v2f{mean, var});
        const float ri = rcp(rb(__builtin_amdgcn_sqrtf(rb(mv.y + 1e-5f))));
        const v2f hh = rb2(rb2(rb2(x1 - mv.x) * ri) * cb1) + sb;
        *lp<u32>(wa) = pk2(hh.x, hh.y);
      }
      bar();
      if (wave == 0 && lane == 0) st2(v2u{0u, 0u}, rws, 0, W_YF + yi * 128);
      if (wave >= 4) {
        v8bf A[16];
#pragma unroll
        for (int ks = 0; ks < 16; ++ks) A[ks] = ldfrag(fb + ks * 64);
        __builtin_amdgcn_sched_barrier(0);
        v4f d0 = {0.f, 0.f, 0.f, 0.f};
#pragma unroll
        for (int ks = 0; ks < 16; ++ks) d0 = mfma(W[ks], A[ks], d0);
        float e0 = (c & 2) ? d0.y : d0.x, e1 = (c & 2) ? d0.w : d0.z;
        PIN(e0); PIN(e1);
        const float v = silu(rb((c & 4) ? e1 : e0));
        *lp<u16>(ha) = (u16)pk2(v, v);
      }
      bar();
      if (wave < 4) {
        const v8bf a0 = ldfrag(fb), a1 = ldfrag(fb + 64);
        v4f dd[8];
#pragma unroll
        for (int t = 0; t < 8; ++t) dd[t] = mfma(W[2 * t + 1], a1, mfma(W[2 * t], a0, v4f{0.f, 0.f, 0.f, 0.f}));
        v4f rv = dd[0];
#pragma unroll
        for (int t = 1; t < 8; ++t) if ((c >> 1) == t) rv = dd[t];
        v4u ov = {canon(rv.x), canon(rv.y), canon(rv.z), canon(rv.w)};
        st4(ov, rws, vs, 0);
        if (wave == 1 && yi == 27 && lane == 0) st2(v2u{(u32)E, (u32)(E >> 32)}, rws, 0, W_EPOCH);
      }
    }
  }
}

extern "C" void kernel_launch(void* const* d_in, const int* in_sizes, int n_in, void* d_out, int out_size, void* d_ws, size_t ws_size, hipStream_t stream) {
  hipLaunchKernelGGL(fused_block, dim3(256), dim3(512), 0, stream, (const u16*)d_in[1], (const u16*)d_in[2], (const u16*)d_in[9], (const u16*)d_in[3],
                     (const u16*)d_in[4], (const u16*)d_in[6], (const u16*)d_in[7], (const u16*)d_in[0], (const u16*)d_in[8], (u16*)d_out, (unsigned char*)d_ws);
}
```
